```python
import torch

def _softmax(x, axis=-1):
    return torch.softmax(x, dim=axis)
def _gelu(x):
    return torch.nn.functional.gelu(x, approximate="tanh")
def _silu(x):
    return torch.nn.functional.silu(x)
def _sigmoid(x):
    return torch.sigmoid(x)
def _layernorm(x, axis=-1, eps=1e-5):
    m = x.mean(dim=axis, keepdim=True)
    v = x.var(dim=axis, keepdim=True, unbiased=False)
    return (x - m) / torch.sqrt(v + eps)
def _scatter_add(tgt, idx, src, axis=0):
    return tgt.clone().index_add_(axis, idx, src)
def _segment_sum(src, seg, n):
    out = torch.zeros((n,) + src.shape[1:], dtype=src.dtype)
    return out.index_add_(0, seg, src)
def _pad(x, pw):
    flat = tuple(v for pair in reversed(pw) for v in pair)
    return torch.nn.functional.pad(x, flat)

kernel_name = 'gated_mix__sliding_block__ssm_conv_h3__prompt'

def setup_inputs():

    _w = (lambda a: (lambda t: t.to(torch.bfloat16) if t.is_floating_point() else t)(torch.as_tensor(a)))
    def _A_setup():
        B, S, D = 1, 2048, 576
        return {
            "x": _w((lambda *s: torch.randn(*s, dtype=torch.bfloat16) * 0.05)(B, S, D)),
            "wqkv": _w((lambda *s: torch.randn(*s, dtype=torch.bfloat16) * 0.8)(D, 3 * D)),
            "wo": _w((lambda *s: torch.randn(*s, dtype=torch.bfloat16) * 0.05)(D, D)),
        }
    _A_in = _A_setup()

    def _B_setup():
        B, S, D = 1, 2048, 576
        ks = 4
        return {
            "q": _w((lambda *s: torch.randn(*s, dtype=torch.bfloat16) * 0.05)(B, S, D)),
            "k": _w((lambda *s: torch.randn(*s, dtype=torch.bfloat16) * 0.8)(B, S, D)),
            "v": _w((lambda *s: torch.randn(*s, dtype=torch.bfloat16) * 0.8)(B, S, D)),
            "shift_k": _w((lambda *s: torch.randn(*s, dtype=torch.bfloat16) * 0.8)(D, ks) * 0.5),
            "ssm_k": _w((lambda *s: torch.randn(*s, dtype=torch.bfloat16) * 1.6)(D, S) * (1.0 / S ** 0.5)),
            "ssm_D": _w((lambda *s: torch.randn(*s, dtype=torch.bfloat16) * 0.8)(D)),
        }
    _B_in = _B_setup()

    return {
        "A_x": _A_in["x"],
        "A_wqkv": _A_in["wqkv"],
        "A_wo": _A_in["wo"],
        "B_q": _B_in["q"],
        "B_k": _B_in["k"],
        "B_v": _B_in["v"],
        "B_shift_k": _B_in["shift_k"],
        "B_ssm_k": _B_in["ssm_k"],
        "B_ssm_D": _B_in["ssm_D"],
        "gate_w": _w((lambda *s: torch.randn(*s, dtype=torch.bfloat16) * 0.05)(576) * 16.0),
    }

def reference(A_x, A_wqkv, A_wo, B_q, B_k, B_v, B_shift_k, B_ssm_k, B_ssm_D, gate_w):
    def _A_ref(x, wqkv, wo):
        B, S, D = x.shape
        H = 32; Dh = D // H; W = 256
        qkv = (lambda x, *d: x.permute(*d))((x @ wqkv).reshape(B, S, 3, H, Dh), 2, 0, 3, 1, 4)
        q, k, v = qkv[0], qkv[1], qkv[2]
        scores = torch.einsum("bhqd,bhkd->bhqk", q, k) * (Dh ** -0.5)
        idx = torch.arange(S)
        mask = (idx[:, None] - idx[None, :] >= 0) & (idx[:, None] - idx[None, :] < W)
        scores = torch.where(mask[None, None], scores, -1e9)
        p = _softmax(scores, axis=-1)
        o = torch.einsum("bhqk,bhkd->bhqd", p, v)
        return (lambda x, *d: x.permute(*d))(o, 0, 2, 1, 3).reshape(B, S, D) @ wo

    def _B_ref(q, k, v, shift_k, ssm_k, ssm_D):
        B, S, D = q.shape
        ks = shift_k.shape[-1]
        k_pad = _pad(k, ((0, 0), (ks - 1, 0), (0, 0)))
        k_shift = torch.zeros_like(k)
        for t in range(ks):
            k_shift = k_shift + k_pad[:, t:t + S, :] * shift_k[:, ks - 1 - t][None, None, :]
        kv = k_shift * v
        L = 2 * S
        kv_pad = _pad(kv, ((0, 0), (0, S), (0, 0)))
        kf_pad = _pad(ssm_k, ((0, 0), (0, S)))
        kv_f = (lambda x, axes=(-2,-1): torch.fft.rfft2(x.float(), dim=axes))(kv_pad[:, None, :, :], axes=(1, 2))
        kf_f = (lambda x, axes=(-2,-1): torch.fft.rfft2(x.float(), dim=axes))(kf_pad[:, None, :], axes=(1, 2))
        kf_f = (lambda x, *d: x.permute(*d))(kf_f, 1, 2, 0)
        y_f = kv_f * kf_f[None, :, :, :]
        y = (lambda x, s, axes=(-2,-1): torch.fft.irfft2(x, s=s, dim=axes).to(torch.bfloat16))(y_f, (1, L), axes=(1, 2))[:, 0, :S, :]
        y = y + kv * ssm_D[None, None, :]
        return q * y

    x = A_x
    g = _sigmoid((x * gate_w).sum(-1, keepdims=True))
    a = _A_ref(x, A_wqkv, A_wo)
    b = _B_ref(x, B_k, B_v, B_shift_k, B_ssm_k, B_ssm_D)
    return g * a + (1 - g) * b
```

```cpp
#include <hip/hip_runtime.h>
#include <hip/hip_bf16.h>
typedef __hip_bfloat16 bf16;
#define S_ 2048
#define D_ 576
__device__ __forceinline__ float b2f(bf16 v){return __bfloat162float(v);}
__device__ __forceinline__ float rb(float v){return __bfloat162float(__float2bfloat16(v));}
__global__ void k_gemm(const bf16* A,const bf16* B,bf16* C,int M,int N,int K){
  int n=blockIdx.x*64+threadIdx.x%64; int m=blockIdx.y*4+threadIdx.x/64;
  if(n>=N||m>=M)return; float acc=0;
  for(int k=0;k<K;k++)acc+=b2f(A[m*K+k])*b2f(B[k*N+n]);
  C[m*N+n]=__float2bfloat16(acc);
}
__global__ void k_attn(const bf16* qkv,bf16* o){
  int idx=blockIdx.x*256+threadIdx.x; int h=idx%32; int s=idx/32;
  float q[18]; for(int d=0;d<18;d++)q[d]=b2f(qkv[s*1728+h*18+d]);
  float m=-1e30f,l=0,acc[18]; for(int d=0;d<18;d++)acc[d]=0;
  int k0=s-255; if(k0<0)k0=0;
  for(int k=k0;k<=s;k++){ float sc=0;
#pragma unroll
    for(int d=0;d<18;d++)sc+=q[d]*b2f(qkv[k*1728+576+h*18+d]);
    sc*=0.23570226f; float mn=fmaxf(m,sc); float a=__expf(m-mn),p=__expf(sc-mn); l=l*a+p;
#pragma unroll
    for(int d=0;d<18;d++)acc[d]=acc[d]*a+p*b2f(qkv[k*1728+1152+h*18+d]); m=mn; }
  for(int d=0;d<18;d++)o[s*576+h*18+d]=__float2bfloat16(acc[d]/l);
}
__global__ void k_kv(const bf16* k,const bf16* v,const bf16* sh,bf16* kv){
  int idx=blockIdx.x*256+threadIdx.x; int d=idx%576; int s=idx/576;
  float ks=0;
  for(int t=0;t<4;t++){int ss=s+t-3; float kk=ss>=0?b2f(k[ss*576+d]):0.f; ks=rb(ks+rb(kk*b2f(sh[d*4+3-t])));}
  kv[idx]=__float2bfloat16(ks*b2f(v[idx]));
}
__global__ void k_final(const bf16* x,const bf16* a,const bf16* kv,const bf16* sk,const bf16* sD,const bf16* gw,bf16* out){
  int s=blockIdx.x; int d=threadIdx.x; __shared__ float red[576];
  red[d]=rb(b2f(x[s*576+d])*b2f(gw[d])); __syncthreads();
  float g=0; for(int i=0;i<576;i++)g+=red[i]; g=rb(g); g=rb(1.f/(1.f+__expf(-g)));
  float y=0; for(int t=0;t<=s;t++)y+=b2f(kv[t*576+d])*b2f(sk[d*2048+s-t]);
  y=rb(y); y=rb(y+rb(b2f(kv[s*576+d])*b2f(sD[d]))); float b=rb(b2f(x[s*576+d])*y);
  out[s*576+d]=__float2bfloat16(rb(g*b2f(a[s*576+d]))+rb(rb(1.f-g)*b));
}
extern "C" void kernel_launch(void* const* d_in, const int* in_sizes, int n_in, void* d_out, int out_size, void* d_ws, size_t ws_size, hipStream_t stream) {
  const bf16* x=(const bf16*)d_in[0];const bf16* wqkv=(const bf16*)d_in[1];const bf16* wo=(const bf16*)d_in[2];
  const bf16* Bk=(const bf16*)d_in[4];const bf16* Bv=(const bf16*)d_in[5];const bf16* sh=(const bf16*)d_in[6];
  const bf16* sk=(const bf16*)d_in[7];const bf16* sD=(const bf16*)d_in[8];const bf16* gw=(const bf16*)d_in[9];
  bf16* qkv=(bf16*)d_ws; bf16* o=qkv+2048*1728; bf16* a=o+2048*576; bf16* kv=a+2048*576;
  k_gemm<<<dim3(27,512),256,0,stream>>>(x,wqkv,qkv,2048,1728,576);
  k_attn<<<256,256,0,stream>>>(qkv,o);
  k_gemm<<<dim3(9,512),256,0,stream>>>(o,wo,a,2048,576,576);
  k_kv<<<2048*576/256,256,0,stream>>>(Bk,Bv,sh,kv);
  k_final<<<2048,576,0,stream>>>(x,a,kv,sk,sD,gw,(bf16*)d_out);
}
```

```cpp
#include <hip/hip_runtime.h>
#include <hip/hip_bf16.h>
#include <utility>

typedef unsigned short u16;
typedef unsigned int u32;
typedef u32 u32x2 __attribute__((ext_vector_type(2)));
typedef u32 u32x4 __attribute__((ext_vector_type(4)));
typedef short s16x4 __attribute__((ext_vector_type(4)));
typedef __bf16 bf16x8 __attribute__((ext_vector_type(8)));
typedef __bf16 bf16x2 __attribute__((ext_vector_type(2)));
typedef float f32x2 __attribute__((ext_vector_type(2)));
typedef float f32x4 __attribute__((ext_vector_type(4)));
typedef float f32x16 __attribute__((ext_vector_type(16)));
#define LAS __attribute__((address_space(3)))
#define GAS __attribute__((address_space(1)))
#define DEVI __device__ __forceinline__

constexpr int S_ = 2048, D_ = 576;
constexpr size_t WS_QKV = 0;
constexpr size_t WS_O   = WS_QKV + 96ull * 2048 * 40;
constexpr size_t WS_UR  = WS_O + 32ull * 2048 * 40;
constexpr size_t WS_Y2  = WS_UR + 576ull * 4096;
constexpr size_t WS_G   = WS_Y2 + 576ull * 4096;

template <typename F, int... I> DEVI void sfor_impl(F&& f, std::integer_sequence<int, I...>) { (f(std::integral_constant<int, I>{}), ...); }
template <int N, typename F> DEVI void sfor(F&& f) { sfor_impl(f, std::make_integer_sequence<int, N>{}); }

DEVI u32 pk2(float a, float b) { f32x2 v = {a, b}; return __builtin_bit_cast(u32, __builtin_convertvector(v, bf16x2)); }
DEVI u32 f2u(float a) { return __builtin_bit_cast(u32, a); }
DEVI float u2f(u32 a) { return __builtin_bit_cast(float, a); }
DEVI float lo2f(u32 w) { return __builtin_bit_cast(float, w << 16); }
DEVI float hi2f(u32 w) { return __builtin_bit_cast(float, w & 0xffff0000u); }
DEVI float rbf(float a) { return lo2f(pk2(a, a)); }
DEVI f32x4 mfma16(u32x4 a, u32x4 b, f32x4 c) { return __builtin_amdgcn_mfma_f32_16x16x32_bf16(__builtin_bit_cast(bf16x8, a), __builtin_bit_cast(bf16x8, b), c, 0, 0, 0); }
DEVI f32x16 mfma32(u32x4 a, u32x4 b, f32x16 c) { return __builtin_amdgcn_mfma_f32_32x32x16_bf16(__builtin_bit_cast(bf16x8, a), __builtin_bit_cast(bf16x8, b), c, 0, 0, 0); }
DEVI f32x16 mfma32k8(u32x2 a, u32x2 b, f32x16 c) { return __builtin_amdgcn_mfma_f32_32x32x8bf16_1k(__builtin_bit_cast(s16x4, a), __builtin_bit_cast(s16x4, b), c, 0, 0, 0); }
DEVI u32x2 tr_read(u32 addr) { return __builtin_bit_cast(u32x2, __builtin_amdgcn_ds_read_tr16_b64_v4i16((LAS s16x4*)addr)); }
DEVI u32x4 lds128(u32 addr) { return *(LAS u32x4*)addr; }
DEVI u32x2 lds64(u32 addr) { return *(LAS u32x2*)addr; }
DEVI u32 lds32(u32 addr) { return *(LAS u32*)addr; }
DEVI void st128(u32 addr, u32x4 v) { *(LAS u32x4*)addr = v; }
DEVI void st64(u32 addr, u32x2 v) { *(LAS u32x2*)addr = v; }
DEVI void st32(u32 addr, u32 v) { *(LAS u32*)addr = v; }
DEVI void dma16(const void* sbase, u32 voff, u32 ldsbase) {
  asm volatile("s_mov_b32 m0, %2\n\ts_nop 0\n\tglobal_load_lds_dwordx4 %0, %1" ::"v"(voff), "s"(sbase), "s"(ldsbase) : "memory");
}
template <int OFF> DEVI void gl128(u32x4& d, u32 voff, const void* sbase) { asm volatile("global_load_dwordx4 %0, %1, %2 offset:%3" : "=v"(d) : "v"(voff), "s"(sbase), "i"(OFF)); }
template <int OFF> DEVI void gl64(u32x2& d, u32 voff, const void* sbase) { asm volatile("global_load_dwordx2 %0, %1, %2 offset:%3" : "=v"(d) : "v"(voff), "s"(sbase), "i"(OFF)); }
template <int OFF> DEVI void gl32(u32& d, u32 voff, const void* sbase) { asm volatile("global_load_dword %0, %1, %2 offset:%3" : "=v"(d) : "v"(voff), "s"(sbase), "i"(OFF)); }
template <int N> DEVI void wait_vm() { asm volatile("s_waitcnt vmcnt(%0)" ::"i"(N > 63 ? 63 : N) : "memory"); }
DEVI void wait_lgkm() { asm volatile("s_waitcnt lgkmcnt(0)" ::: "memory"); }
DEVI void bar() { asm volatile("s_barrier" ::: "memory"); }
DEVI u32 rfl(u32 v) { return __builtin_amdgcn_readfirstlane(v); }

constexpr int K1_W = 0, K1_P = 129024, K1_KT = K1_P, K1_VT = K1_P + 10080, K1_SH = K1_P + 30720, K1_GW = K1_SH + 576, K1_LDS = K1_GW + 1152;
constexpr int PD = 3;

__global__ __launch_bounds__(256, 1) void k1(const u16* __restrict__ x, const u16* __restrict__ wqkv, const u16* __restrict__ Bk,
                                             const u16* __restrict__ Bv, const u16* __restrict__ shk, const u16* __restrict__ gw, char* __restrict__ ws) {
  extern __shared__ __attribute__((aligned(16))) char smem[];
  const u32 sb = (u32)(size_t)smem;
  const int b = blockIdx.x, xcd = b & 7, idx = b >> 3;
  const int rb = 2 * xcd + (idx & 1), cb = idx >> 1;
  const int rt = 4 * xcd + (idx & 3), cg = idx >> 2;
  const int lane = threadIdx.x & 63;
  const u32 w = rfl(threadIdx.x >> 6);
  const int g = lane >> 4, l15 = lane & 15;
  const int shift = (cb & 1) * 4, nl0 = 108 * cb - shift;

  {
    const int r7 = lane / 9, c9 = lane - 9 * r7;
    if (lane < 63) {
      sfor<5>([&](auto I) {
        const int j = (int)w * 5 + I;
        const bool isv = j >= 10;
        const int jj = isv ? j - 10 : j;
        int row = 64 * rt + 7 * jj + r7 - (isv ? 0 : 3);
        row = row < 0 ? 0 : (row > 2047 ? 2047 : row);
        dma16((isv ? Bv : Bk) + 72 * cg, row * 1152 + 16 * c9, sb + K1_KT + 1008 * j);
      });
    }
    if (w == 0) { if (lane < 36) dma16(shk + 72 * 4 * cg, lane * 16, sb + K1_SH); }
    else if (w == 1) dma16(gw, lane * 16, sb + K1_GW);
    else if (w == 2) { if (lane < 8) dma16(gw + 512, lane * 16, sb + K1_GW + 1024); }
  }
  u32x4 xf[2][18];
  const u32 xo0 = (32 * w + l15) * 1152 + 16 * g, xo1 = xo0 + 16 * 1152;
  const u16* xb = x + (size_t)rb * 128 * 576;
  const u32 wo = (lane / 14 + 16 * w) * 3456 + 16 * (lane % 14);
  const u16* wb = wqkv + nl0;
  auto issue = [&](auto C) {
    constexpr int c = C;
    asm volatile("global_load_dwordx4 %0, %1, %2 offset:%3" : "=v"(xf[0][2 * c]) : "v"(xo0), "s"(xb), "i"(c * 128));
    asm volatile("global_load_dwordx4 %0, %1, %2 offset:%3" : "=v"(xf[0][2 * c + 1]) : "v"(xo0), "s"(xb), "i"(c * 128 + 64));
    asm volatile("global_load_dwordx4 %0, %1, %2 offset:%3" : "=v"(xf[1][2 * c]) : "v"(xo1), "s"(xb), "i"(c * 128));
    asm volatile("global_load_dwordx4 %0, %1, %2 offset:%3" : "=v"(xf[1][2 * c + 1]) : "v"(xo1), "s"(xb), "i"(c * 128 + 64));
    if (lane < 56) {
      sfor<4>([&](auto I) {
        constexpr int i = I, sw = ((i & 1) << 1) | (i >> 1);
        dma16(wb + (64 * c + 4 * i) * 1728, wo, sb + K1_W + (16 * c + 4 * w + sw) * 896);
      });
    }
  };
  auto waitc = [&](auto C, auto N) {
    constexpr int c = C, n = N;
    asm volatile("s_waitcnt vmcnt(%4)" : "+v"(xf[0][2 * c]), "+v"(xf[0][2 * c + 1]), "+v"(xf[1][2 * c]), "+v"(xf[1][2 * c + 1]) : "i"(n > 63 ? 63 : n) : "memory");
    bar();
  };
  sfor<PD>([&](auto C) { issue(C); });
  wait_vm<8 * PD>();
  bar();
  {
    auto task = [&](int cp, int rg) {
      const u32x4 sh = lds128(sb + K1_SH + 16 * cp);
      const float a0 = lo2f(sh.x), a1 = hi2f(sh.x), a2 = lo2f(sh.y), a3 = hi2f(sh.y);
      const float b0 = lo2f(sh.z), b1 = hi2f(sh.z), b2 = lo2f(sh.w), b3 = hi2f(sh.w);
      u32 kk[11], vv[8];
      const u32 ka = sb + K1_KT + rg * 8 * 144 + 4 * cp, va = sb + K1_VT + rg * 8 * 144 + 4 * cp;
#pragma unroll
      for (int i = 0; i < 11; i++) kk[i] = lds32(ka + 144 * i);
#pragma unroll
      for (int i = 0; i < 8; i++) vv[i] = lds32(va + 144 * i);
      if (rt == 0 && rg == 0) { kk[0] = 0; kk[1] = 0; kk[2] = 0; }
      float ra[8], rc[8];
#pragma unroll
      for (int i = 0; i < 8; i++) {
        float sa = lo2f(kk[i]) * a3, sc = hi2f(kk[i]) * b3;
        sa = fmaf(lo2f(kk[i + 1]), a2, sa); sc = fmaf(hi2f(kk[i + 1]), b2, sc);
        sa = fmaf(lo2f(kk[i + 2]), a1, sa); sc = fmaf(hi2f(kk[i + 2]), b1, sc);
        sa = fmaf(lo2f(kk[i + 3]), a0, sa); sc = fmaf(hi2f(kk[i + 3]), b0, sc);
        ra[i] = sa * lo2f(vv[i]); rc[i] = sc * hi2f(vv[i]);
      }
      u32x4 oa = {pk2(ra[7], ra[6]), pk2(ra[5], ra[4]), pk2(ra[3], ra[2]), pk2(ra[1], ra[0])};
      u32x4 oc = {pk2(rc[7], rc[6]), pk2(rc[5], rc[4]), pk2(rc[3], rc[2]), pk2(rc[1], rc[0])};
      char* dst = ws + WS_UR + (size_t)(72 * cg + 2 * cp) * 4096 + 2 * (2040 - 64 * rt - 8 * rg);
      *(GAS u32x4*)dst = oa;
      *(GAS u32x4*)(dst + 4096) = oc;
    };
    task(8 * w + (lane & 7), lane >> 3);
    if (w == 0 && lane < 32) task(32 + (lane & 3), lane >> 2);
  }
  f32x4 acc[2][7];
#pragma unroll
  for (int i = 0; i < 2; i++)
#pragma unroll
    for (int j = 0; j < 7; j++) acc[i][j] = f32x4{0.f, 0.f, 0.f, 0.f};
  const u32 wra = sb + K1_W + (16 * (g >> 1) + 4 * (g & 1) + (l15 >> 2)) * 224 + 8 * (lane & 3);
  const u32 gra = sb + K1_GW + 16 * g;
  const bool gsel = (cb == 0) && (l15 >= 12);
  u32x4 wf[2][7];
  auto rd = [&](auto KS) {
    constexpr int ks = KS;
    sfor<7>([&](auto NT) {
      constexpr int nt = NT;
      const u32x2 t0 = tr_read(wra + ks * 7168 + nt * 32), t1 = tr_read(wra + ks * 7168 + 1792 + nt * 32);
      wf[ks & 1][nt] = u32x4{t0.x, t0.y, t1.x, t1.y};
    });
    const u32x4 gv = lds128(gra + ks * 64);
    if (gsel) wf[ks & 1][6] = gv;
  };
  auto mm = [&](auto KS) {
    constexpr int ks = KS;
    sfor<7>([&](auto NT) {
      constexpr int nt = NT;
      acc[0][nt] = mfma16(wf[ks & 1][nt], xf[0][ks], acc[0][nt]);
      acc[1][nt] = mfma16(wf[ks & 1][nt], xf[1][ks], acc[1][nt]);
    });
  };
  using std::integral_constant;
  waitc(integral_constant<int, 0>{}, integral_constant<int, 8 * (PD - 1) + 2>{});
  rd(integral_constant<int, 0>{});
  sfor<9>([&](auto C) {
    constexpr int c = C;
    if constexpr (c + PD <= 8) issue(integral_constant<int, c + PD>{});
    rd(integral_constant<int, 2 * c + 1>{});
    mm(integral_constant<int, 2 * c>{});
    if constexpr (c < 8) {
      constexpr int last = (c + PD <= 8) ? c + PD : 8;
      constexpr int n = 8 * (last - (c + 1)) + ((c + 1 <= PD - 1) ? 2 : 0);
      __builtin_amdgcn_sched_barrier(0);
      waitc(integral_constant<int, c + 1>{}, integral_constant<int, n>{});
      rd(integral_constant<int, 2 * c + 2>{});
      __builtin_amdgcn_sched_barrier(0);
    }
    mm(integral_constant<int, 2 * c + 1>{});
  });
  const u32 stg = sb + K1_P + w * 7680;
  sfor<7>([&](auto NT) {
    constexpr int nt = NT;
#pragma unroll
    for (int pr = 0; pr < 2; pr++) {
      const int nl = 16 * nt + 4 * g + 2 * pr - shift;
      const bool ok = nl >= 0 && nl < 108;
      const int hs = (nl * 57) >> 10, d = nl - 18 * hs;
      const u32 a = stg + hs * 1280 + l15 * 40 + 2 * d;
      if (ok) {
        st32(a, pk2(acc[0][nt][2 * pr], acc[0][nt][2 * pr + 1]));
        st32(a + 640, pk2(acc[1][nt][2 * pr], acc[1][nt][2 * pr + 1]));
      }
    }
  });
#pragma unroll
  for (int i = 0; i < 3; i++) st32(stg + (i * 64 + lane) * 40 + 36, 0x00003f80u);
  if (cb == 0 && g == 3) {
    float* G = (float*)(ws + WS_G) + rb * 128 + 32 * w + l15;
#pragma unroll
    for (int mt = 0; mt < 2; mt++) { const float p = rbf(acc[mt][6][0]); G[16 * mt] = rbf(1.f / (1.f + __expf(-p))); }
  }
  wait_lgkm();
  char* qo = ws + WS_QKV + ((size_t)(6 * cb) * 2048 + rb * 128 + 32 * w) * 40;
#pragma unroll
  for (int it = 0; it < 8; it++) {
    const int id = it * 64 + lane;
    if (id < 480) {
      const int hs = (id * 205) >> 14, off = id - 80 * hs;
      *(GAS u32x4*)(qo + (size_t)hs * 81920 + off * 16) = lds128(stg + id * 16);
    }
  }
}

constexpr int K2_K = 0, K2_V = 20480, K2_C = 40960;
constexpr int KC_STR = 4160, CS_U = 4 * KC_STR, CS_SZ = CS_U + 4096 + 480 + 32;
constexpr int K2_R = K2_C + 3 * CS_SZ;
constexpr int K2_F = K2_R + 16384 + 32768, K2_LDS = K2_F + 16;

template <int NP>
DEVI void conv_channel(u32 cs, u32 red, u32 flag, int pi, int lane, float Dv, char* ydst) {
  constexpr int NR = 8 / NP, NT = 8 / NP;
  const int g = lane >> 4, l15 = lane & 15;
  const int r = (l15 + 1) & 3;
  const u32 aaddr = cs + r * KC_STR + (r ? 40 : 32) + 2 * (l15 - 15 - r + 8 * g) + 64 * NR * pi;
  const u32 baddr = cs + CS_U + 2 * (2032 - 16 * l15 + 8 * g) + 64 * NR * pi;
  f32x4 acc[8];
#pragma unroll
  for (int c = 0; c < 8; c++) acc[c] = f32x4{0.f, 0.f, 0.f, 0.f};
#pragma unroll
  for (int rho = 0; rho < NR; rho++) {
    u32x4 A[8], G[8];
#pragma unroll
    for (int p = 0; p < 8; p++) {
      const u32x2 a0 = lds64(aaddr + rho * 64 + p * 512), a1 = lds64(aaddr + rho * 64 + p * 512 + 8);
      A[p] = u32x4{a0.x, a0.y, a1.x, a1.y};
      G[p] = lds128(baddr + rho * 64 - p * 512);
    }
#pragma unroll
    for (int c = 0; c < 8; c++)
#pragma unroll
      for (int p = 0; p <= c; p++) acc[c] = mfma16(A[p], G[c - p], acc[c]);
  }
#pragma unroll
  for (int c = 0; c < 8; c++)
    if (c / NT != pi) {
      const int slot = (NP == 2) ? c : (c * 4 + pi);
      st128(red + slot * 1024 + lane * 16, __builtin_bit_cast(u32x4, acc[c]));
    }
  wait_lgkm();
  if (lane == 0) __hip_atomic_fetch_add((LAS u32*)flag, 1u, __ATOMIC_RELAXED, __HIP_MEMORY_SCOPE_WORKGROUP);
  while (rfl(__hip_atomic_load((LAS u32*)flag, __ATOMIC_RELAXED, __HIP_MEMORY_SCOPE_WORKGROUP)) < (u32)NP) __builtin_amdgcn_s_sleep(1);
  asm volatile("" ::: "memory");
#pragma unroll
  for (int c = 0; c < 8; c++)
    if (c / NT == pi) {
      f32x4 y;
      if (NP == 2) y = acc[c] + __builtin_bit_cast(f32x4, lds128(red + c * 1024 + lane * 16));
      else {
        y = acc[c];
#pragma unroll
        for (int q = 1; q < 4; q++) y += __builtin_bit_cast(f32x4, lds128(red + (c * 4 + ((pi + q) & 3)) * 1024 + lane * 16));
      }
      const int s = 256 * c + 16 * l15 + 4 * g;
      const u32x2 u = lds64(cs + CS_U + 2 * (2044 - s));
      const float y0 = rbf(y[0]) + rbf(hi2f(u.y) * Dv), y1 = rbf(y[1]) + rbf(lo2f(u.y) * Dv);
      const float y2 = rbf(y[2]) + rbf(hi2f(u.x) * Dv), y3 = rbf(y[3]) + rbf(lo2f(u.x) * Dv);
      *(GAS u32x2*)(ydst + 2 * s) = u32x2{pk2(y0, y1), pk2(y2, y3)};
    }
}

__global__ __launch_bounds__(768, 3) void k2(const u16* __restrict__ ssmk, const u16* __restrict__ ssmD, char* __restrict__ ws) {
  extern __shared__ __attribute__((aligned(16))) char smem[];
  const u32 sb = (u32)(size_t)smem;
  const int b = blockIdx.x, qb = b & 7, h = b >> 3;
  const int lane = threadIdx.x & 63;
  const u32 w = rfl(threadIdx.x >> 6);
  const int nch = qb < 2 ? 3 : 2;
  if (w < 8) {
    const char* Kg = ws + WS_QKV + ((size_t)(32 + h) * 2048 + 256 * qb - 256) * 40;
    const char* Vg = ws + WS_QKV + ((size_t)(64 + h) * 2048 + 256 * qb - 256) * 40;
    sfor<5>([&](auto I) {
      const int j = (int)w * 5 + I;
      const int jj = j >= 20 ? j - 20 : j;
      if (qb > 0 || jj >= 10) dma16((j >= 20 ? Vg : Kg) + jj * 1024, lane * 16, sb + j * 1024);
    });
    const int c = lane & 31, hh = lane >> 5;
    const char* Qg = ws + WS_QKV + ((size_t)h * 2048 + 256 * qb + 32 * w + c) * 40;
    const u32x2 qa = *(const GAS u32x2*)(Qg + 16 * hh), qc = *(const GAS u32x2*)(Qg + 16 * hh + 8);
    const u32 qd = *(const GAS u32*)(Qg + 32);
    const u32x4 q0 = {qa.x, qa.y, qc.x, qc.y};
    const u32x2 q1 = {hh ? 0u : qd, 0u};
    wait_vm<0>();
    bar();
    const u32 ka = sb + K2_K + (32 * w + c) * 40;
    const int i0 = qb > 0 ? 0 : 8 - (int)w;
    f32x16 S[9];
    const float NEG = -__builtin_inff();
    auto qk = [&](auto I) {
      constexpr int i = I;
      if (i >= i0) {
        const u32x2 k0 = lds64(ka + i * 1280 + 16 * hh), k1 = lds64(ka + i * 1280 + 16 * hh + 8), k2 = lds64(ka + i * 1280 + 32);
        f32x16 z = {};
        z = mfma32(u32x4{k0.x, k0.y, k1.x, k1.y}, q0, z);
        S[i] = mfma32k8(k2, q1, z);
      } else {
#pragma unroll
        for (int e = 0; e < 16; e++) S[i][e] = NEG;
      }
    };
    using std::integral_constant;
    qk(integral_constant<int, 0>{});
    qk(integral_constant<int, 8>{});
    const int thr = c - 4 * hh;
#pragma unroll
    for (int e = 0; e < 16; e++) S[8][e] = ((e & 3) + 8 * (e >> 2) <= thr) ? S[8][e] : S[0][e];
    sfor<7>([&](auto I) { qk(integral_constant<int, I + 1>{}); });
    float m = S[8][0];
#pragma unroll
    for (int i = 1; i < 9; i++)
#pragma unroll
      for (int e = 0; e < 16; e += 2) m = fmaxf(fmaxf(m, S[i][e]), S[i][e + 1]);
    {
      const u32 mu = __builtin_bit_cast(u32, m);
      auto r = __builtin_amdgcn_permlane32_swap(mu, mu, false, false);
      m = fmaxf(u2f(r[0]), u2f(r[1]));
    }
    const float c2 = 0.23570226039551584f * 1.4426950408889634f;
    const float mc = -m * c2;
    const int blk = (lane >> 4) & 1;
    const u32 va = sb + K2_V + (32 * w + 4 * hh + ((lane & 15) >> 2)) * 40 + (blk ? 32 : 8 * (lane & 3));
    f32x16 O = {};
    auto pv = [&](auto I, const f32x16& p) {
      constexpr int i = I;
#pragma unroll
      for (int s2 = 0; s2 < 2; s2++) {
        const u32x2 v0 = tr_read(va + i * 1280 + s2 * 640), v1 = tr_read(va + i * 1280 + s2 * 640 + 320);
        const u32x4 P = {pk2(p[8 * s2], p[8 * s2 + 1]), pk2(p[8 * s2 + 2], p[8 * s2 + 3]), pk2(p[8 * s2 + 4], p[8 * s2 + 5]), pk2(p[8 * s2 + 6], p[8 * s2 + 7])};
        O = mfma32(u32x4{v0.x, v0.y, v1.x, v1.y}, P, O);
      }
    };
    sfor<7>([&](auto I) {
      constexpr int i = I + 1;
      if (i >= i0) {
        f32x16 p;
#pragma unroll
        for (int e = 0; e < 16; e++) p[e] = __builtin_amdgcn_exp2f(fmaf(S[i][e], c2, mc));
        pv(integral_constant<int, i>{}, p);
      }
    });
    {
      f32x16 p, p0, p8;
#pragma unroll
      for (int e = 0; e < 16; e++) {
        p[e] = __builtin_amdgcn_exp2f(fmaf(S[8][e], c2, mc));
        const bool cnd = (e & 3) + 8 * (e >> 2) <= thr;
        p8[e] = cnd ? p[e] : 0.f; p0[e] = cnd ? 0.f : p[e];
      }
      pv(integral_constant<int, 8>{}, p8);
      if (i0 == 0) pv(integral_constant<int, 0>{}, p0);
    }
    float l;
    {
      const u32 lu = f2u(O[10]);
      auto r = __builtin_amdgcn_permlane32_swap(lu, lu, false, false);
      l = u2f(r[0]);
    }
    const float inv = __builtin_amdgcn_rcpf(l);
    char* Og = ws + WS_O + ((size_t)h * 2048 + 256 * qb + 32 * w + c) * 40;
    *(GAS u32x2*)(Og + 8 * hh) = u32x2{pk2(O[0] * inv, O[1] * inv), pk2(O[2] * inv, O[3] * inv)};
    *(GAS u32x2*)(Og + 16 + 8 * hh) = u32x2{pk2(O[4] * inv, O[5] * inv), pk2(O[6] * inv, O[7] * inv)};
    if (hh == 0) *(GAS u32x2*)(Og + 32) = u32x2{pk2(O[8] * inv, O[9] * inv), 0u};
  } else {
    const int cw = w - 8, tid = threadIdx.x - 512;
    const int ch2 = 512 + 2 * h + qb;
    u32x2 pre[3]; u32x4 cur[3];
#pragma unroll
    for (int j = 0; j < 3; j++)
      if (j < nch) {
        const int ch = j < 2 ? 2 * b + j : ch2;
        dma16(ws + WS_UR + (size_t)ch * 4096 + cw * 1024, lane * 16, sb + K2_C + j * CS_SZ + CS_U + cw * 1024);
        const u16* kp = ssmk + (size_t)ch * 2048 + 8 * tid;
        cur[j] = *(const GAS u32x4*)kp;
        pre[j] = tid ? *(const GAS u32x2*)(kp - 4) : u32x2{0u, 0u};
      }
    if (tid == 0) { st128(sb + K2_F, u32x4{0u, 0u, 0u, 0u}); }
#pragma unroll
    for (int j = 0; j < 3; j++)
      if (j < nch) {
        const u32 cs = sb + K2_C + j * CS_SZ;
        const u32 w0 = pre[j].x, w1 = pre[j].y, w2 = cur[j].x, w3 = cur[j].y, w4 = cur[j].z, w5 = cur[j].w;
        const u32 a01 = __builtin_amdgcn_alignbyte(w1, w0, 2), a12 = __builtin_amdgcn_alignbyte(w2, w1, 2), a23 = __builtin_amdgcn_alignbyte(w3, w2, 2),
                  a34 = __builtin_amdgcn_alignbyte(w4, w3, 2), a45 = __builtin_amdgcn_alignbyte(w5, w4, 2);
        st128(cs + 32 + 16 * tid, cur[j]);
        st128(cs + KC_STR + 32 + 16 * tid, u32x4{a01, a12, a23, a34});
        st128(cs + 2 * KC_STR + 32 + 16 * tid, u32x4{w1, w2, w3, w4});
        st128(cs + 3 * KC_STR + 32 + 16 * tid, u32x4{a12, a23, a34, a45});
        if (tid < 8) st128(cs + (tid >> 1) * KC_STR + 16 * (tid & 1), u32x4{0u, 0u, 0u, 0u});
        else if (tid < 38) st128(cs + CS_U + 4096 + 16 * (tid - 8), u32x4{0u, 0u, 0u, 0u});
      }
    const int chA = 2 * b + (cw >> 1);
    const float DA = lo2f(ssmD[chA]), DC = lo2f(ssmD[qb < 2 ? ch2 : 0]);
    wait_vm<0>();
    wait_lgkm();
    bar();
    conv_channel<2>(sb + K2_C + (cw >> 1) * CS_SZ, sb + K2_R + (cw >> 1) * 8192, sb + K2_F + (cw >> 1) * 4, cw & 1, lane, DA, ws + WS_Y2 + (size_t)chA * 4096);
    if (nch == 3) conv_channel<4>(sb + K2_C + 2 * CS_SZ, sb + K2_R + 16384, sb + K2_F + 8, cw, lane, DC, ws + WS_Y2 + (size_t)ch2 * 4096);
  }
}

constexpr int K3_Y = 0, K3_W = 9216, K3_LDS = K3_W + 83 * 1008 + 64;

__global__ __launch_bounds__(256, 1) void k3(const u16* __restrict__ x, const u16* __restrict__ wo, const char* __restrict__ ws, u16* __restrict__ out) {
  extern __shared__ __attribute__((aligned(16))) char smem[];
  const u32 sb = (u32)(size_t)smem;
  const int b = blockIdx.x, xcd = b & 7, idx = b >> 3;
  const int rt = 4 * xcd + (idx & 3), ct = idx >> 2;
  const int lane = threadIdx.x & 63;
  const u32 w = rfl(threadIdx.x >> 6);
  const int g = lane >> 4, l15 = lane & 15;
  const int row = 64 * rt + 16 * w + l15;
  u32x4 of[18]; u32 oe[8];
  const char* ob = ws + WS_O;
  const u32 oo = ((g >> 1) * 2048 + row) * 40 + 16 * (g & 1);
  const u32 oo2 = (4 * g * 2048 + row) * 40 + 32;
  const int r7 = lane / 9, c9 = lane - 9 * r7;
  const u16* wb = wo + 72 * ct;
  sfor<21>([&](auto I) {
    constexpr int i = I;
    if constexpr (i < 16) {
      asm volatile("global_load_dwordx4 %0, %1, %2" : "=v"(of[i]) : "v"(oo), "s"(ob + (size_t)i * 163840));
    } else if constexpr (i < 20) {
      constexpr int q = i - 16;
      gl32<0>(oe[q], oo2, ob + (size_t)q * 81920);
      gl32<0>(oe[4 + q], oo2, ob + (size_t)(16 + q) * 81920);
    }
    const int j = 4 * i + w;
    int r = 7 * j + r7; r = r > 575 ? 575 : r;
    if (lane < 63 && j < 83) dma16(wb, r * 1152 + 16 * c9, sb + K3_W + j * 1008);
  });
  sfor<3>([&](auto I) {
    const int j = 4 * I + w;
    if (j < 9) dma16(ws + WS_Y2 + (size_t)(72 * ct + 8 * j) * 4096 + 128 * rt, (lane >> 3) * 4096 + 16 * (lane & 7), sb + K3_Y + j * 1024);
  });
  u32x2 xv[5];
  const u32 xoff = row * 1152 + 144 * ct + 8 * g;
  sfor<4>([&](auto NT) { gl64<32 * NT>(xv[NT], xoff, x); });
  gl64<0>(xv[4], g >= 2 ? xoff : xoff + 128, x);
  float gt;
  asm volatile("global_load_dword %0, %1, off" : "=v"(gt) : "v"((const float*)(ws + WS_G) + row));
  asm volatile("s_waitcnt vmcnt(0)"
               : "+v"(of[0]), "+v"(of[1]), "+v"(of[2]), "+v"(of[3]), "+v"(of[4]), "+v"(of[5]), "+v"(of[6]), "+v"(of[7]), "+v"(of[8]), "+v"(of[9]),
                 "+v"(of[10]), "+v"(of[11]), "+v"(of[12]), "+v"(of[13]), "+v"(of[14]), "+v"(of[15]), "+v"(xv[0]), "+v"(xv[1]),
                 "+v"(xv[2]), "+v"(xv[3]), "+v"(xv[4]), "+v"(gt)::"memory");
  asm volatile("" : "+v"(oe[0]), "+v"(oe[1]), "+v"(oe[2]), "+v"(oe[3]), "+v"(oe[4]), "+v"(oe[5]), "+v"(oe[6]), "+v"(oe[7])::"memory");
  of[16] = u32x4{oe[0], oe[1], oe[2], oe[3]}; of[17] = u32x4{oe[4], oe[5], oe[6], oe[7]};
  bar();
  f32x4 acc[5];
#pragma unroll
  for (int j = 0; j < 5; j++) acc[j] = f32x4{0.f, 0.f, 0.f, 0.f};
  const int q = l15 >> 2;
  const u32 wa = sb + K3_W + (18 * (g >> 1) + 8 * (g & 1) + q) * 144 + 8 * (lane & 3);
  const u32 wa2 = sb + K3_W + ((4 * g + (q >> 1)) * 18 + 16 + (q & 1)) * 144 + 8 * (lane & 3);
  sfor<18>([&](auto KS) {
    constexpr int ks = KS;
    sfor<5>([&](auto NT) {
      constexpr int nt = NT;
      u32x2 t0, t1;
      if constexpr (ks < 16) { t0 = tr_read(wa + ks * 5184 + nt * 32); t1 = tr_read(wa + ks * 5184 + 576 + nt * 32); }
      else { t0 = tr_read(wa2 + (ks - 16) * 41472 + nt * 32); t1 = tr_read(wa2 + (ks - 16) * 41472 + 5184 + nt * 32); }
      acc[nt] = mfma16(u32x4{t0.x, t0.y, t1.x, t1.y}, of[ks], acc[nt]);
    });
  });
  const float g1 = rbf(1.f - gt);
  const u32 ya = sb + K3_Y + (4 * g + q) * 128 + 32 * w + 8 * (lane & 3);
  u16* orow = out + (size_t)row * 576 + 72 * ct + 4 * g;
  sfor<5>([&](auto NT) {
    constexpr int nt = NT;
    const u32x2 y = tr_read(ya + nt * 2048);
    const float o0 = rbf(gt * rbf(acc[nt][0])) + rbf(g1 * rbf(lo2f(xv[nt].x) * lo2f(y.x)));
    const float o1 = rbf(gt * rbf(acc[nt][1])) + rbf(g1 * rbf(hi2f(xv[nt].x) * hi2f(y.x)));
    const float o2 = rbf(gt * rbf(acc[nt][2])) + rbf(g1 * rbf(lo2f(xv[nt].y) * lo2f(y.y)));
    const float o3 = rbf(gt * rbf(acc[nt][3])) + rbf(g1 * rbf(hi2f(xv[nt].y) * hi2f(y.y)));
    if (nt < 4 || g < 2) *(GAS u32x2*)(orow + 16 * nt) = u32x2{pk2(o0, o1), pk2(o2, o3)};
  });
}

extern "C" void kernel_launch(void* const* d_in, const int* in_sizes, int n_in, void* d_out, int out_size, void* d_ws, size_t ws_size,
                              hipStream_t stream) {
  const u16* x = (const u16*)d_in[0]; const u16* wqkv = (const u16*)d_in[1]; const u16* wo = (const u16*)d_in[2];
  const u16* Bk = (const u16*)d_in[4]; const u16* Bv = (const u16*)d_in[5]; const u16* shk = (const u16*)d_in[6];
  const u16* ssmk = (const u16*)d_in[7]; const u16* ssmD = (const u16*)d_in[8]; const u16* gw = (const u16*)d_in[9];
  (void)hipFuncSetAttribute((const void*)k1, hipFuncAttributeMaxDynamicSharedMemorySize, K1_LDS);
  (void)hipFuncSetAttribute((const void*)k2, hipFuncAttributeMaxDynamicSharedMemorySize, K2_LDS);
  (void)hipFuncSetAttribute((const void*)k3, hipFuncAttributeMaxDynamicSharedMemorySize, K3_LDS);
  k1<<<256, 256, K1_LDS, stream>>>(x, wqkv, Bk, Bv, shk, gw, (char*)d_ws);
  k2<<<256, 768, K2_LDS, stream>>>(ssmk, ssmD, (char*)d_ws);
  k3<<<256, 256, K3_LDS, stream>>>(x, wo, (const char*)d_ws, (u16*)d_out);
}
```

```cpp
#include <hip/hip_runtime.h>
#include <hip/hip_bf16.h>
#include <utility>

typedef unsigned short u16;
typedef unsigned int u32;
typedef u32 u32x2 __attribute__((ext_vector_type(2)));
typedef u32 u32x4 __attribute__((ext_vector_type(4)));
typedef short s16x4 __attribute__((ext_vector_type(4)));
typedef __bf16 bf16x8 __attribute__((ext_vector_type(8)));
typedef __bf16 bf16x2 __attribute__((ext_vector_type(2)));
typedef float f32x2 __attribute__((ext_vector_type(2)));
typedef float f32x4 __attribute__((ext_vector_type(4)));
typedef float f32x16 __attribute__((ext_vector_type(16)));
#define LAS __attribute__((address_space(3)))
#define GAS __attribute__((address_space(1)))
#define DEVI __device__ __forceinline__
#define IC(n) std::integral_constant<int, (n)>{}

constexpr size_t WS_QKV = 0;
constexpr size_t WS_O   = WS_QKV + 96ull * 2048 * 40;
constexpr size_t WS_UR  = WS_O + 32ull * 2048 * 40;
constexpr size_t WS_Y2  = WS_UR + 576ull * 4096;
constexpr size_t WS_G   = WS_Y2 + 576ull * 4096;

template <typename F, int... I> DEVI void sfor_impl(F&& f, std::integer_sequence<int, I...>) { (f(std::integral_constant<int, I>{}), ...); }
template <int N, typename F> DEVI void sfor(F&& f) { sfor_impl(f, std::make_integer_sequence<int, N>{}); }

DEVI u32 pk2(float a, float b) { f32x2 v = {a, b}; return __builtin_bit_cast(u32, __builtin_convertvector(v, bf16x2)); }
DEVI u32 f2u(float a) { return __builtin_bit_cast(u32, a); }
DEVI float u2f(u32 a) { return __builtin_bit_cast(float, a); }
DEVI float lo2f(u32 w) { return __builtin_bit_cast(float, w << 16); }
DEVI float hi2f(u32 w) { return __builtin_bit_cast(float, w & 0xffff0000u); }
DEVI float rbf(float a) { return lo2f(pk2(a, a)); }
DEVI f32x4 mfma16(u32x4 a, u32x4 b, f32x4 c) { return __builtin_amdgcn_mfma_f32_16x16x32_bf16(__builtin_bit_cast(bf16x8, a), __builtin_bit_cast(bf16x8, b), c, 0, 0, 0); }
DEVI f32x16 mfma32(u32x4 a, u32x4 b, f32x16 c) { return __builtin_amdgcn_mfma_f32_32x32x16_bf16(__builtin_bit_cast(bf16x8, a), __builtin_bit_cast(bf16x8, b), c, 0, 0, 0); }
DEVI f32x16 mfma32k8(u32x2 a, u32x2 b, f32x16 c) { return __builtin_amdgcn_mfma_f32_32x32x8bf16_1k(__builtin_bit_cast(s16x4, a), __builtin_bit_cast(s16x4, b), c, 0, 0, 0); }
DEVI u32x2 tr_read(u32 addr) { return __builtin_bit_cast(u32x2, __builtin_amdgcn_ds_read_tr16_b64_v4i16((LAS s16x4*)addr)); }
DEVI u32x4 lds128(u32 addr) { return *(LAS u32x4*)addr; }
DEVI u32x2 lds64(u32 addr) { return *(LAS u32x2*)addr; }
DEVI u32 lds32(u32 addr) { return *(LAS u32*)addr; }
DEVI void st128(u32 addr, u32x4 v) { *(LAS u32x4*)addr = v; }
DEVI void st64(u32 addr, u32x2 v) { *(LAS u32x2*)addr = v; }
DEVI void st32(u32 addr, u32 v) { *(LAS u32*)addr = v; }
DEVI u32x4 cat(u32x2 a, u32x2 b) { return u32x4{a.x, a.y, b.x, b.y}; }
DEVI u32 opaque(u32 v) { asm volatile("" : "+v"(v)); return v; }
DEVI void dma16(const void* sbase, u32 voff, u32 ldsbase) {
  asm volatile("s_mov_b32 m0, %2\n\ts_nop 0\n\tglobal_load_lds_dwordx4 %0, %1" ::"v"(voff), "s"(sbase), "s"(ldsbase) : "memory");
}
template <int NL> DEVI void dma16n(const void* sbase, u32 voff, u32 ldsbase) {
  asm volatile("s_mov_b32 m0, %2\n\ts_mov_b32 exec_hi, %3\n\tglobal_load_lds_dwordx4 %0, %1\n\ts_mov_b32 exec_hi, -1" ::"v"(voff), "s"(sbase), "s"(ldsbase),
               "i"((1u << (NL - 32)) - 1u) : "memory");
}
template <int OFF> DEVI void gl128(u32x4& d, u32 voff, const void* sbase) { asm volatile("global_load_dwordx4 %0, %1, %2 offset:%3" : "=v"(d) : "v"(voff), "s"(sbase), "i"(OFF)); }
template <int N> DEVI void wait_vm() { asm volatile("s_waitcnt vmcnt(%0)" ::"i"(N > 63 ? 63 : N) : "memory"); }
DEVI void wait_lgkm() { asm volatile("s_waitcnt lgkmcnt(0)" ::: "memory"); }
DEVI void bar() { asm volatile("s_barrier" ::: "memory"); }
DEVI u32 rfl(u32 v) { return __builtin_amdgcn_readfirstlane(v); }
#define SCHED() __builtin_amdgcn_sched_barrier(0)
#define STAMP(k, i)

constexpr float QSCALE = 0.23570226039551584f * 1.4426950408889634f;

constexpr int K1_SLOT = 30720, K1_WOFF = 16384, K1_P = 4 * K1_SLOT;
constexpr int K1_KT = K1_P, K1_VT = K1_KT + 9216, K1_KE = K1_VT + 8192, K1_VE = K1_KE + 2048;
constexpr int K1_SH = K1_P + 30720, K1_GW = K1_SH + 1024, K1_DUMMY = K1_GW + 1152, K1_LDS = K1_DUMMY + 1024;

__global__ __launch_bounds__(512, 2) void k1(const u16* __restrict__ x, const u16* __restrict__ wqkv, const u16* __restrict__ Bk,
                                             const u16* __restrict__ Bv, const u16* __restrict__ shk, const u16* __restrict__ gw, char* __restrict__ ws) {
  extern __shared__ __attribute__((aligned(16))) char smem[];
  const u32 sb = (u32)(size_t)smem;
  const int b = blockIdx.x, xcd = b & 7, idx = b >> 3;
  const int hg = xcd & 1, rb = 4 * (xcd >> 1) + (idx & 3), jt = idx >> 2;
  const int lane = threadIdx.x & 63;
  const u32 w = rfl(threadIdx.x >> 6);
  const int shift = (jt & 1) * 4;
  STAMP(0, 0);
  if (w >= 4) {
    const u32 lw = w - 4;
    const int r8 = lane >> 3, c8 = lane & 7;
    sfor<5>([&](auto I) {
      const int j = (int)lw * 5 + I;
      const bool isv = (j >= 9 && j < 17) || j == 19, ext = j >= 17;
      const int r0 = ext ? 8 * (32 * xcd + idx) + (j == 18 ? 8 : 0) : 64 * idx + 8 * (isv ? j - 9 : j);
      int row = r0 + r8 - (isv ? 0 : 3);
      row = row < 0 ? 0 : (row > 2047 ? 2047 : row);
      dma16((isv ? Bv : Bk) + (ext ? 512 : 64 * xcd), row * 1152 + 16 * c8, sb + K1_KT + 1024 * j);
    });
    if (lw == 0) dma16(shk, (lane < 32 ? 512 * xcd : 4096 - 512) + lane * 16, sb + K1_SH);
    else if (lw == 1) dma16(gw, lane * 16, sb + K1_GW);
    else if (lw == 2) { if (lane < 8) dma16(gw + 512, lane * 16, sb + K1_GW + 1024); }
    const u32 xo = (32 * lw + r8) * 1152 + 16 * (c8 ^ r8);
    const char* xb = (const char*)(x + (size_t)rb * 128 * 576);
    const int lr = lane / 14, lc = 108 * jt - shift + 8 * (lane - 14 * lr), seg = lc / 288;
    const u32 wo = (lr + 16 * lw) * 3456 + 2 * (lc + 288 * (seg + hg));
    const char* wb = (const char*)wqkv;
    auto issue = [&](int c) {
      const u32 sl = sb + (c & 3) * K1_SLOT;
      sfor<4>([&](auto I) { dma16(xb + c * 128 + I * 9216, xo, sl + (4 * lw + I) * 1024); });
      sfor<4>([&](auto I) {
        constexpr int i = I, sw = ((i & 1) << 1) | (i >> 1);
        dma16n<56>(wb + (64 * c + 4 * i) * 3456, wo, sl + K1_WOFF + (4 * lw + sw) * 896);
      });
    };
    issue(0); issue(1); issue(2); issue(3);
    wait_vm<32>(); bar();
    wait_vm<24>(); bar();
#pragma unroll 1
    for (int c = 1; c <= 5; c++) { wait_vm<16>(); bar(); issue(c + 3); }
    wait_vm<16>(); bar();
    wait_vm<8>(); bar();
    wait_vm<0>(); bar();
    return;
  }
  const int g = lane >> 4, l15 = lane & 15;
  const u32 stg = sb + K1_P + w * 7680;
  u32 ea[7][2]; float esc[7][2];
#pragma unroll
  for (int nt = 0; nt < 7; nt++)
#pragma unroll
    for (int pr = 0; pr < 2; pr++) {
      const int nl = 16 * nt + 4 * g + 2 * pr - shift;
      const int hs = (nl * 57) >> 10, d = nl - 18 * hs;
      ea[nt][pr] = (nl >= 0 && nl < 108) ? stg + hs * 1280 + l15 * 40 + 2 * d : sb + K1_DUMMY + l15 * 4;
      esc[nt][pr] = (6 * jt + hs < 16) ? QSCALE : 1.f;
    }
  bar();
  STAMP(0, 1);
  {
    auto task = [&](u32 ka, u32 va, u32 sa, bool first, char* dst) {
      const u32x4 sh = lds128(sa);
      const float a0 = lo2f(sh.x), a1 = hi2f(sh.x), a2 = lo2f(sh.y), a3 = hi2f(sh.y);
      const float b0 = lo2f(sh.z), b1 = hi2f(sh.z), b2 = lo2f(sh.w), b3 = hi2f(sh.w);
      u32 kk[11], vv[8];
#pragma unroll
      for (int i = 0; i < 11; i++) kk[i] = lds32(ka + 128 * i);
#pragma unroll
      for (int i = 0; i < 8; i++) vv[i] = lds32(va + 128 * i);
      if (first) { kk[0] = 0; kk[1] = 0; kk[2] = 0; }
      float ra[8], rc[8];
#pragma unroll
      for (int i = 0; i < 8; i++) {
        float sa_ = lo2f(kk[i]) * a3, sc = hi2f(kk[i]) * b3;
        sa_ = fmaf(lo2f(kk[i + 1]), a2, sa_); sc = fmaf(hi2f(kk[i + 1]), b2, sc);
        sa_ = fmaf(lo2f(kk[i + 2]), a1, sa_); sc = fmaf(hi2f(kk[i + 2]), b1, sc);
        sa_ = fmaf(lo2f(kk[i + 3]), a0, sa_); sc = fmaf(hi2f(kk[i + 3]), b0, sc);
        ra[i] = sa_ * lo2f(vv[i]); rc[i] = sc * hi2f(vv[i]);
      }
      *(GAS u32x4*)dst = u32x4{pk2(ra[7], ra[6]), pk2(ra[5], ra[4]), pk2(ra[3], ra[2]), pk2(ra[1], ra[0])};
      *(GAS u32x4*)(dst + 4096) = u32x4{pk2(rc[7], rc[6]), pk2(rc[5], rc[4]), pk2(rc[3], rc[2]), pk2(rc[1], rc[0])};
    };
    const int cp = lane & 31, rg = 2 * w + (lane >> 5);
#pragma unroll 1
    for (int rnd = 0; rnd < (w == 3 ? 2 : 1); rnd++) {
      const bool e = rnd;
      if (!e || lane < 32)
        task(sb + (e ? K1_KE : K1_KT + rg * 1024) + 4 * cp, sb + (e ? K1_VE : K1_VT + rg * 1024) + 4 * cp, sb + K1_SH + (e ? 512 : 0) + 16 * cp,
             e ? b == 0 : (idx == 0 && rg == 0),
             ws + WS_UR + (size_t)((e ? 512 : 64 * xcd) + 2 * cp) * 4096 + 2 * (2040 - (e ? 8 * (32 * xcd + idx) : 64 * idx + 8 * rg)));
    }
  }
  STAMP(0, 2);
  f32x4 acc[2][7];
#pragma unroll
  for (int i = 0; i < 2; i++)
#pragma unroll
    for (int j = 0; j < 7; j++) acc[i][j] = f32x4{0.f, 0.f, 0.f, 0.f};
  const u32 wra = sb + K1_WOFF + (16 * (g >> 1) + 4 * (g & 1) + (l15 >> 2)) * 224 + 8 * (lane & 3);
  const u32 xra0 = sb + (32 * w + l15) * 128 + 16 * (g ^ (l15 & 7)), xra1 = xra0 ^ 64;
  const u32 gra = sb + K1_GW + 16 * g;
  const bool gsel = (jt == 0) && (hg == 0) && (l15 >= 12);
  u32x4 wf[2][7], xf[2][2];
  auto rd = [&](auto KS, int c) {
    constexpr int ks = KS;
    const u32 so = (c & 3) * K1_SLOT;
    const u32 xa = (ks ? xra1 : xra0) + so, wa = wra + so;
    xf[ks][0] = lds128(xa); xf[ks][1] = lds128(xa + 2048);
    sfor<7>([&](auto NT) { wf[ks][NT] = cat(tr_read(wa + ks * 7168 + NT * 32), tr_read(wa + ks * 7168 + 1792 + NT * 32)); });
    const u32x4 gv = lds128(gra + c * 128 + ks * 64);
    if (gsel) wf[ks][6] = gv;
  };
  auto mm = [&](auto KS) {
    constexpr int ks = KS;
    sfor<7>([&](auto NT) {
      acc[0][NT] = mfma16(wf[ks][NT], xf[ks][0], acc[0][NT]);
      acc[1][NT] = mfma16(wf[ks][NT], xf[ks][1], acc[1][NT]);
    });
  };
  auto ilv = [&]() {
    sfor<8>([&](auto) { __builtin_amdgcn_sched_group_barrier(0x8, 1, 0); __builtin_amdgcn_sched_group_barrier(0x100, 2, 0); });
    __builtin_amdgcn_sched_group_barrier(0x8, 1, 0); __builtin_amdgcn_sched_group_barrier(0x100, 1, 0);
    __builtin_amdgcn_sched_group_barrier(0x8, 5, 0);
  };
  bar();
  rd(IC(0), 0);
  SCHED();
#pragma unroll 1
  for (int c = 0; c < 8; c++) {
    rd(IC(1), c); mm(IC(0)); ilv();
    SCHED(); wait_lgkm(); bar(); SCHED();
    rd(IC(0), c + 1); mm(IC(1)); ilv();
    SCHED();
  }
  rd(IC(1), 8); mm(IC(0)); ilv();
  SCHED();
  mm(IC(1));
  STAMP(0, 3);
#pragma unroll
  for (int i = 0; i < 3; i++) st32(stg + (i * 64 + lane) * 40 + 36, 0x3f803f80u);
#pragma unroll
  for (int nt = 0; nt < 7; nt++)
#pragma unroll
    for (int pr = 0; pr < 2; pr++) {
      st32(ea[nt][pr], pk2(acc[0][nt][2 * pr] * esc[nt][pr], acc[0][nt][2 * pr + 1] * esc[nt][pr]));
      st32(ea[nt][pr] + 640, pk2(acc[1][nt][2 * pr] * esc[nt][pr], acc[1][nt][2 * pr + 1] * esc[nt][pr]));
    }
  if (jt == 0 && hg == 0 && g == 3) {
    float* G = (float*)(ws + WS_G) + rb * 128 + 32 * w + l15;
#pragma unroll
    for (int mt = 0; mt < 2; mt++) { const float p = rbf(acc[mt][6][0]); G[16 * mt] = rbf(1.f / (1.f + __expf(-p))); }
  }
  wait_lgkm();
  char* qo = ws + WS_QKV + (size_t)(rb * 128 + 32 * w) * 40;
#pragma unroll
  for (int it = 0; it < 8; it++) {
    const int id = it * 64 + lane;
    const int hs = (id * 205) >> 14, off = id - 80 * hs;
    const int li = 6 * jt + hs, slot = (li >> 4) * 32 + 16 * hg + (li & 15);
    if (id < 480) *(GAS u32x4*)(qo + (u32)(slot * 81920 + off * 16)) = lds128(stg + id * 16);
  }
  STAMP(0, 4);
}

constexpr int K2_K = 0, K2_V = 20480, K2_Q = 40960, K2_C = 51200;
constexpr int KC_STR = 4640, CS_U = 4 * KC_STR, CS_SZ = CS_U + 4096 + 128;
constexpr int K2_R = K2_C + 3 * CS_SZ;
constexpr int K2_F = K2_R + 16384 + 24576, K2_LDS = K2_F + 16;
static_assert(K2_LDS <= 163840, "lds");

template <int NP>
DEVI void conv_channel(u32 cs, u32 red, u32 flag, int pi, int lane, float Dv, char* ydst) {
  constexpr int NR = 8 / NP, NT = 8 / NP;
  const int g = lane >> 4, l15 = lane & 15;
  const u32 aaddr = cs + (l15 & 3) * KC_STR + 2 * (64 * (l15 >> 2) + 8 * g) + 64 * NR * pi;
  const u32 baddr = cs + CS_U + 2 * (1852 - 1792 - 4 * l15 + 8 * g) + 64 * NR * pi;
  const u32 baddr2 = opaque(baddr + 8);
  f32x4 acc[8];
#pragma unroll
  for (int c = 0; c < 8; c++) acc[c] = f32x4{0.f, 0.f, 0.f, 0.f};
#pragma unroll 1
  for (int rho = 0; rho < NR; rho++) {
    u32x4 A[8], G[8];
#pragma unroll
    for (int p = 0; p < 8; p++) {
      A[p] = lds128(aaddr + rho * 64 + p * 512);
      G[p] = cat(lds64(baddr + rho * 64 + (7 - p) * 512), lds64(baddr2 + rho * 64 + (7 - p) * 512));
    }
    SCHED();
#pragma unroll
    for (int c = 0; c < 8; c++)
#pragma unroll
      for (int p = 0; p <= c; p++) acc[c] = mfma16(A[p], G[c - p], acc[c]);
  }
#pragma unroll
  for (int c = 0; c < 8; c++)
    if (c / NT != pi) {
      const int slot = (NP == 2) ? c : (c * 3 + ((pi - c / NT - 1) & 3));
      st128(red + slot * 1024 + lane * 16, __builtin_bit_cast(u32x4, acc[c]));
    }
  wait_lgkm();
  if (lane == 0) __hip_atomic_fetch_add((LAS u32*)flag, 1u, __ATOMIC_RELAXED, __HIP_MEMORY_SCOPE_WORKGROUP);
  while (rfl(__hip_atomic_load((LAS u32*)flag, __ATOMIC_RELAXED, __HIP_MEMORY_SCOPE_WORKGROUP)) < (u32)NP) __builtin_amdgcn_s_sleep(1);
  asm volatile("" ::: "memory");
#pragma unroll
  for (int c = 0; c < 8; c++)
    if (c / NT == pi) {
      f32x4 y = acc[c];
      if (NP == 2) y += __builtin_bit_cast(f32x4, lds128(red + c * 1024 + lane * 16));
      else {
#pragma unroll
        for (int q = 0; q < 3; q++) y += __builtin_bit_cast(f32x4, lds128(red + (c * 3 + q) * 1024 + lane * 16));
      }
      const int s = 256 * c + 4 * lane;
      const u32x2 u = lds64(cs + CS_U + 2 * (2044 - s));
      const float y0 = rbf(y[0]) + rbf(hi2f(u.y) * Dv), y1 = rbf(y[1]) + rbf(lo2f(u.y) * Dv);
      const float y2 = rbf(y[2]) + rbf(hi2f(u.x) * Dv), y3 = rbf(y[3]) + rbf(lo2f(u.x) * Dv);
      *(GAS u32x2*)(ydst + 2 * s) = u32x2{pk2(y0, y1), pk2(y2, y3)};
    }
}

DEVI float xhalf_max(float m) { const u32 mu = f2u(m); auto r = __builtin_amdgcn_permlane32_swap(mu, mu, false, false); return fmaxf(u2f(r[0]), u2f(r[1])); }

__global__ __launch_bounds__(768, 3) void k2(const u16* __restrict__ ssmk, const u16* __restrict__ ssmD, char* __restrict__ ws) {
  extern __shared__ __attribute__((aligned(16))) char smem[];
  const u32 sb = (u32)(size_t)smem;
  const int b = blockIdx.x, xcd = b & 7, idx = b >> 3;
  const int qb = 2 * (xcd >> 1) + (idx & 1), h = 16 * (xcd & 1) + (idx >> 1);
  const int lane = threadIdx.x & 63;
  const u32 w = rfl(threadIdx.x >> 6);
  const bool three = (qb & 3) == 0;
  STAMP(1, 0);
  if (w < 8) {
    const char* Ag = ws + WS_QKV + ((size_t)h * 2048 + 256 * qb) * 40;
#pragma unroll
    for (int I = 0; I < 7; I++) {
      const int j = (int)w + 8 * I;
      const int jj = j < 10 ? j + 10 : (j < 30 ? j - 10 : j - 30);
      const int so = (j < 10 ? 0 : (j < 30 ? 32 : 64)) * 81920 + (jj - 10) * 1024;
      if (j < 50 && (qb > 0 || jj >= 10)) dma16(Ag + so, lane * 16, sb + (j < 10 ? K2_Q + j * 1024 : (j - 10) * 1024));
    }
    const int c = lane & 31, hh = lane >> 5;
    const u32 ka = sb + K2_K + (32 * w + c) * 40 + 16 * hh, ka1 = opaque(ka + 8), ka2 = sb + K2_K + (32 * w + c) * 40 + 32;
    const u32 va = sb + K2_V + (32 * w + 4 * hh + ((lane & 15) >> 2)) * 40 + ((lane & 16) ? 32 : 8 * (lane & 3));
    const int thr = c - 4 * hh;
    const int i0 = qb > 0 ? 0 : 8 - (int)w;
    wait_vm<0>();
    STAMP(1, 2);
    bar();
    STAMP(1, 1);
    const u32x4 q0 = cat(lds64(ka + K2_Q), lds64(ka1 + K2_Q));
    u32x2 q1 = {hh ? 0u : lds32(ka2 + K2_Q), 0u};
    auto qk = [&](int i) {
      f32x16 z = {};
      z = mfma32(cat(lds64(ka + i * 1280), lds64(ka1 + i * 1280)), q0, z);
      return mfma32k8(lds64(ka2 + i * 1280), q1, z);
    };
    f32x16 O;
    auto pv = [&](int i, const f32x16& p) {
#pragma unroll
      for (int s2 = 0; s2 < 2; s2++) {
        const u32x4 P = {pk2(p[8 * s2], p[8 * s2 + 1]), pk2(p[8 * s2 + 2], p[8 * s2 + 3]), pk2(p[8 * s2 + 4], p[8 * s2 + 5]), pk2(p[8 * s2 + 6], p[8 * s2 + 7])};
        O = mfma32(cat(tr_read(va + i * 1280 + s2 * 640), tr_read(va + i * 1280 + s2 * 640 + 320)), P, O);
      }
    };
    f32x16 Sm = qk(8);
    {
      f32x16 S0;
      if (i0 == 0) S0 = qk(0);
      else
#pragma unroll
        for (int e = 0; e < 16; e++) S0[e] = -__builtin_inff();
#pragma unroll
      for (int e = 0; e < 16; e++) Sm[e] = ((e & 3) + 8 * (e >> 2) <= thr) ? Sm[e] : S0[e];
    }
    float m = fmaxf(Sm[0], Sm[1]);
#pragma unroll
    for (int e = 2; e < 16; e += 2) m = fmaxf(fmaxf(m, Sm[e]), Sm[e + 1]);
    m = xhalf_max(m);
    const int ib = i0 > 1 ? i0 : 1;
    float l;
#pragma unroll 1
    for (int pass = 0; pass < 2; pass++) {
      const float mh = rbf(m), ml = rbf(m - mh), ms = mh + ml;
      q1.y = hh ? 0u : pk2(-mh, -ml);
      {
        f32x16 p0, p8;
#pragma unroll
        for (int e = 0; e < 16; e++) {
          const float p = __builtin_amdgcn_exp2f(Sm[e] - ms);
          const bool cnd = (e & 3) + 8 * (e >> 2) <= thr;
          p8[e] = cnd ? p : 0.f; p0[e] = cnd ? 0.f : p;
        }
        O = f32x16{};
        pv(8, p8);
        if (i0 == 0) pv(0, p0);
      }
      f32x16 Sn = qk(ib);
#pragma unroll 1
      for (int i = ib; i < 8; i++) {
        f32x16 p = Sn;
        Sn = qk(i + 1);
#pragma unroll
        for (int e = 0; e < 16; e++) p[e] = __builtin_amdgcn_exp2f(p[e]);
        pv(i, p);
      }
      { const u32 lu = f2u(O[10]); auto r = __builtin_amdgcn_permlane32_swap(lu, lu, false, false); l = u2f(r[0]); }
      if (pass == 1 || __all(l > 8.67e-19f && l < 1.15e18f)) break;
      q1.y = 0u;
#pragma unroll 1
      for (int i = ib; i < 8; i++) {
        const f32x16 s = qk(i);
#pragma unroll
        for (int e = 0; e < 16; e += 2) m = fmaxf(fmaxf(m, s[e]), s[e + 1]);
      }
      m = xhalf_max(m);
    }
    STAMP(1, 3);
    const float inv = __builtin_amdgcn_rcpf(l);
    char* Og = ws + WS_O + (size_t)(16 * qb + 2 * w + (c >> 4)) * 18432 + (c & 15) * 16;
    *(GAS u32x4*)(Og + (h >> 1) * 1024 + (2 * (h & 1) + hh) * 256) =
        u32x4{pk2(O[0] * inv, O[1] * inv), pk2(O[2] * inv, O[3] * inv), pk2(O[4] * inv, O[5] * inv), pk2(O[6] * inv, O[7] * inv)};
    if (hh == 0) *(GAS u32*)(Og + 16384 + (h >> 2) * 256 + (h & 3) * 4) = pk2(O[8] * inv, O[9] * inv);
    STAMP(1, 4);
  } else {
    const int cw = w - 8, tid = threadIdx.x - 512;
    const int ch0 = 64 * xcd + 2 * idx, ch2 = three ? 512 + h + 8 * qb : ch0;
    u32x2 pre[3]; u32x4 cur[3];
#pragma unroll
    for (int j = 0; j < 3; j++) {
      const int ch = j < 2 ? ch0 + j : ch2;
      const u16* kp = ssmk + (size_t)ch * 2048 + 8 * tid;
      cur[j] = *(const GAS u32x4*)kp;
      pre[j] = *(const GAS u32x2*)(kp - (tid ? 4 : 0));
    }
    const int chA = ch0 + (cw >> 1);
    const u32 dA = ssmD[chA], dC = ssmD[ch2];
#pragma unroll
    for (int j = 0; j < 3; j++)
      if (j < 2 || three) dma16(ws + WS_UR + (size_t)(j < 2 ? ch0 + j : ch2) * 4096 + cw * 1024, lane * 16, sb + K2_C + j * CS_SZ + CS_U + cw * 1024);
    if (tid == 0) st128(sb + K2_F, u32x4{0u, 0u, 0u, 0u});
#pragma unroll
    for (int j = 0; j < 3; j++) {
      const u32 cs = sb + K2_C + j * CS_SZ;
      if (tid < 96) st128(cs + (tid / 24) * KC_STR + 16 * (tid % 24), u32x4{0u, 0u, 0u, 0u});
      else if (tid < 104) st128(cs + CS_U + 4096 + 16 * (tid - 96), u32x4{0u, 0u, 0u, 0u});
    }
#pragma unroll
    for (int j = 0; j < 3; j++) {
      const u32 cs = sb + K2_C + j * CS_SZ + 384 + 16 * tid;
      const u32 w0 = tid ? pre[j].x : 0u, w1 = tid ? pre[j].y : 0u, w2 = cur[j].x, w3 = cur[j].y, w4 = cur[j].z, w5 = cur[j].w;
      const u32 a01 = __builtin_amdgcn_alignbyte(w1, w0, 2), a12 = __builtin_amdgcn_alignbyte(w2, w1, 2), a23 = __builtin_amdgcn_alignbyte(w3, w2, 2),
                a34 = __builtin_amdgcn_alignbyte(w4, w3, 2), a45 = __builtin_amdgcn_alignbyte(w5, w4, 2);
      st128(cs, u32x4{a01, a12, a23, a34});
      st128(cs + KC_STR, u32x4{w1, w2, w3, w4});
      st128(cs + 2 * KC_STR, u32x4{a12, a23, a34, a45});
      st128(cs + 3 * KC_STR, cur[j]);
    }
    wait_vm<0>();
    wait_lgkm();
    STAMP(1, 2);
    bar();
    STAMP(1, 1);
    __builtin_amdgcn_s_setprio(2);
    conv_channel<2>(sb + K2_C + (cw >> 1) * CS_SZ, sb + K2_R + (cw >> 1) * 8192, sb + K2_F + (cw >> 1) * 4, cw & 1, lane, lo2f(dA), ws + WS_Y2 + (size_t)chA * 4096);
    STAMP(1, 3);
    if (three) conv_channel<4>(sb + K2_C + 2 * CS_SZ, sb + K2_R + 16384, sb + K2_F + 8, cw, lane, lo2f(dC), ws + WS_Y2 + (size_t)ch2 * 4096);
    STAMP(1, 4);
  }
}

constexpr int K3_Y = 0, K3_X = 9216, K3_W = K3_X + 10080, K3_LDS = K3_W + 84 * 1008 + 64;

__global__ __launch_bounds__(256, 1) void k3(const u16* __restrict__ x, const u16* __restrict__ wo, const char* __restrict__ ws, u16* __restrict__ out) {
  extern __shared__ __attribute__((aligned(16))) char smem[];
  const u32 sb = (u32)(size_t)smem;
  const int b = blockIdx.x, xcd = b & 7, idx = b >> 3;
  const int rt = 8 * (xcd >> 1) + (idx & 7), ct = 4 * (xcd & 1) + (idx >> 3);
  const int lane = threadIdx.x & 63;
  const u32 w = rfl(threadIdx.x >> 6);
  const int g = lane >> 4, l15 = lane & 15;
  STAMP(2, 0);
  u32x4 of[18];
  const char* ob = ws + WS_O + (size_t)(4 * rt + w) * 18432;
  const int r7 = lane / 9, c9 = lane - 9 * r7;
  const char* wb = (const char*)(wo + 72 * ct);
  const u32 wv = (7 * w + r7) * 1152 + 16 * c9;
  sfor<21>([&](auto I) {
    constexpr int i = I;
    if constexpr (i < 20) dma16n<63>(wb + i * 28 * 1152, wv, sb + K3_W + (4 * i + w) * 1008);
    else { int r = 560 + 7 * w + r7; r = r > 575 ? 575 : r; dma16n<63>(wb, r * 1152 + 16 * c9, sb + K3_W + (80 + w) * 1008); }
    if constexpr (i % 7 < 6) { constexpr int f = (i / 7) * 6 + i % 7; gl128<(f & 3) * 1024>(of[f], lane * 16, ob + (f >> 2) * 4096); }
  });
  sfor<3>([&](auto I) {
    const int j = 4 * I + w;
    if (j < 9) dma16(ws + WS_Y2 + (size_t)(72 * ct + 8 * j) * 4096 + 128 * rt, (lane >> 3) * 4096 + 16 * (lane & 7), sb + K3_Y + j * 1024);
    int r = 7 * j + r7; r = r > 63 ? 63 : r;
    if (j < 10) dma16n<63>(x + (size_t)64 * rt * 576 + 72 * ct, r * 1152 + 16 * c9, sb + K3_X + j * 1008);
  });
  float gt;
  asm volatile("global_load_dword %0, %1, %2" : "=v"(gt) : "v"((u32)(64 * rt + 16 * w + l15) * 4), "s"(ws + WS_G));
  f32x4 acc[5];
#pragma unroll
  for (int j = 0; j < 5; j++) acc[j] = f32x4{0.f, 0.f, 0.f, 0.f};
  const int q = l15 >> 2;
  const u32 wa = sb + K3_W + (18 * (g >> 1) + 4 * (g & 1) + q) * 144 + 8 * (lane & 3);
  const u32 wa2 = sb + K3_W + ((4 * g + (q >> 1)) * 18 + 16 + (q & 1)) * 144 + 8 * (lane & 3);
  u32x4 wf[2][5];
  auto rd = [&](auto KS) {
    constexpr int ks = KS;
    sfor<5>([&](auto NT) {
      if constexpr (ks < 16) wf[ks & 1][NT] = cat(tr_read(wa + ks * 5184 + NT * 32), tr_read(wa + ks * 5184 + 1152 + NT * 32));
      else wf[ks & 1][NT] = cat(tr_read(wa2 + (ks - 16) * 41472 + NT * 32), tr_read(wa2 + (ks - 16) * 41472 + 5184 + NT * 32));
    });
  };
  auto mm = [&](auto KS) { sfor<5>([&](auto NT) { acc[NT] = mfma16(wf[KS & 1][NT], of[KS], acc[NT]); }); };
  auto tie = [&](auto Q, auto N) {
    constexpr int f = 6 * Q;
    asm volatile("s_waitcnt vmcnt(%6)" : "+v"(of[f]), "+v"(of[f + 1]), "+v"(of[f + 2]), "+v"(of[f + 3]), "+v"(of[f + 4]), "+v"(of[f + 5]) : "i"(N()) : "memory");
    bar();
  };
  tie(IC(0), IC(26 + 5));
  STAMP(2, 1);
  rd(IC(0));
  sfor<18>([&](auto KS) {
    constexpr int ks = KS;
    SCHED();
    if constexpr (ks == 4) tie(IC(1), IC(13 + 5));
    if constexpr (ks == 9) tie(IC(2), IC(5));
    if constexpr (ks < 17) rd(IC(ks + 1));
    mm(KS);
    sfor<5>([&](auto) { __builtin_amdgcn_sched_group_barrier(0x100, 2, 0); __builtin_amdgcn_sched_group_barrier(0x8, 1, 0); });
  });
  SCHED();
  asm volatile("s_waitcnt vmcnt(0)" : "+v"(gt)::"memory");
  bar();
  STAMP(2, 3);
  const float g1 = rbf(1.f - gt);
  const u32 ya = sb + K3_Y + (4 * g + q) * 128 + 32 * w + 8 * (lane & 3);
  const u32 xa = sb + K3_X + (16 * w + l15) * 144 + 8 * g;
  sfor<5>([&](auto NT) {
    constexpr int nt = NT;
    const u32x2 y = tr_read(ya + nt * 2048), xv = lds64(xa + nt * 32);
    const float o0 = rbf(gt * rbf(acc[nt][0])) + rbf(g1 * rbf(lo2f(xv.x) * lo2f(y.x)));
    const float o1 = rbf(gt * rbf(acc[nt][1])) + rbf(g1 * rbf(hi2f(xv.x) * hi2f(y.x)));
    const float o2 = rbf(gt * rbf(acc[nt][2])) + rbf(g1 * rbf(lo2f(xv.y) * lo2f(y.y)));
    const float o3 = rbf(gt * rbf(acc[nt][3])) + rbf(g1 * rbf(hi2f(xv.y) * hi2f(y.y)));
    if (nt < 4 || g < 2) st64(xa + nt * 32, u32x2{pk2(o0, o1), pk2(o2, o3)});
  });
  wait_lgkm();
  char* orow = (char*)(out + (size_t)(64 * rt + 16 * w) * 576 + 72 * ct);
#pragma unroll
  for (int it = 0; it < 3; it++) {
    const int r = 7 * it + r7;
    if (lane < 63 && r < 16) *(GAS u32x4*)(orow + (u32)(r * 1152 + 16 * c9)) = lds128(sb + K3_X + (16 * w + r) * 144 + 16 * c9);
  }
  STAMP(2, 4);
}

extern "C" void kernel_launch(void* const* d_in, const int* in_sizes, int n_in, void* d_out, int out_size, void* d_ws, size_t ws_size,
                              hipStream_t stream) {
  const u16* x = (const u16*)d_in[0]; const u16* wqkv = (const u16*)d_in[1]; const u16* wo = (const u16*)d_in[2];
  const u16* Bk = (const u16*)d_in[4]; const u16* Bv = (const u16*)d_in[5]; const u16* shk = (const u16*)d_in[6];
  const u16* ssmk = (const u16*)d_in[7]; const u16* ssmD = (const u16*)d_in[8]; const u16* gw = (const u16*)d_in[9];
  (void)hipFuncSetAttribute((const void*)k1, hipFuncAttributeMaxDynamicSharedMemorySize, K1_LDS);
  (void)hipFuncSetAttribute((const void*)k2, hipFuncAttributeMaxDynamicSharedMemorySize, K2_LDS);
  (void)hipFuncSetAttribute((const void*)k3, hipFuncAttributeMaxDynamicSharedMemorySize, K3_LDS);
  k1<<<256, 512, K1_LDS, stream>>>(x, wqkv, Bk, Bv, shk, gw, (char*)d_ws);
  k2<<<256, 768, K2_LDS, stream>>>(ssmk, ssmD, (char*)d_ws);
  k3<<<256, 256, K3_LDS, stream>>>(x, wo, (const char*)d_ws, (u16*)d_out);
}
```

```cpp
#include <hip/hip_runtime.h>
#include <hip/hip_bf16.h>
#include <utility>

typedef unsigned short u16;
typedef unsigned int u32;
typedef u32 u32x2 __attribute__((ext_vector_type(2)));
typedef u32 u32x4 __attribute__((ext_vector_type(4)));
typedef short s16x4 __attribute__((ext_vector_type(4)));
typedef __bf16 bf16x8 __attribute__((ext_vector_type(8)));
typedef __bf16 bf16x2 __attribute__((ext_vector_type(2)));
typedef float f32x2 __attribute__((ext_vector_type(2)));
typedef float f32x4 __attribute__((ext_vector_type(4)));
typedef float f32x16 __attribute__((ext_vector_type(16)));
#define LAS __attribute__((address_space(3)))
#define GAS __attribute__((address_space(1)))
#define DEVI __device__ __forceinline__
#define IC(n) std::integral_constant<int, (n)>{}

constexpr size_t WS_QKV = 0;
constexpr size_t WS_O   = WS_QKV + 96ull * 2048 * 40;
constexpr size_t WS_UR  = WS_O + 32ull * 2048 * 40;
constexpr size_t WS_Y2  = WS_UR + 576ull * 4096;
constexpr size_t WS_G   = WS_Y2 + 576ull * 4096;

template <typename F, int... I> DEVI void sfor_impl(F&& f, std::integer_sequence<int, I...>) { (f(std::integral_constant<int, I>{}), ...); }
template <int N, typename F> DEVI void sfor(F&& f) { sfor_impl(f, std::make_integer_sequence<int, N>{}); }

DEVI u32 pk2(float a, float b) { f32x2 v = {a, b}; return __builtin_bit_cast(u32, __builtin_convertvector(v, bf16x2)); }
DEVI u32 f2u(float a) { return __builtin_bit_cast(u32, a); }
DEVI float u2f(u32 a) { return __builtin_bit_cast(float, a); }
DEVI float lo2f(u32 w) { return __builtin_bit_cast(float, w << 16); }
DEVI float hi2f(u32 w) { return __builtin_bit_cast(float, w & 0xffff0000u); }
DEVI float rbf(float a) { return lo2f(pk2(a, a)); }
DEVI f32x4 mfma16(u32x4 a, u32x4 b, f32x4 c) { return __builtin_amdgcn_mfma_f32_16x16x32_bf16(__builtin_bit_cast(bf16x8, a), __builtin_bit_cast(bf16x8, b), c, 0, 0, 0); }
DEVI f32x16 mfma32(u32x4 a, u32x4 b, f32x16 c) { return __builtin_amdgcn_mfma_f32_32x32x16_bf16(__builtin_bit_cast(bf16x8, a), __builtin_bit_cast(bf16x8, b), c, 0, 0, 0); }
DEVI f32x16 mfma32k8(u32x2 a, u32x2 b, f32x16 c) { return __builtin_amdgcn_mfma_f32_32x32x8bf16_1k(__builtin_bit_cast(s16x4, a), __builtin_bit_cast(s16x4, b), c, 0, 0, 0); }
DEVI u32x2 tr_read(u32 addr) { return __builtin_bit_cast(u32x2, __builtin_amdgcn_ds_read_tr16_b64_v4i16((LAS s16x4*)addr)); }
DEVI u32x4 lds128(u32 addr) { return *(LAS u32x4*)addr; }
DEVI u32x2 lds64(u32 addr) { return *(LAS u32x2*)addr; }
DEVI u32 lds32(u32 addr) { return *(LAS u32*)addr; }
DEVI void st128(u32 addr, u32x4 v) { *(LAS u32x4*)addr = v; }
DEVI void st64(u32 addr, u32x2 v) { *(LAS u32x2*)addr = v; }
DEVI void st32(u32 addr, u32 v) { *(LAS u32*)addr = v; }
DEVI u32x4 cat(u32x2 a, u32x2 b) { return u32x4{a.x, a.y, b.x, b.y}; }
DEVI u32 opaque(u32 v) { asm volatile("" : "+v"(v)); return v; }
DEVI void dma16(const void* sbase, u32 voff, u32 ldsbase) {
  asm volatile("s_mov_b32 m0, %2\n\ts_nop 0\n\tglobal_load_lds_dwordx4 %0, %1" ::"v"(voff), "s"(sbase), "s"(ldsbase) : "memory");
}
template <int NL> DEVI void dma16n(const void* sbase, u32 voff, u32 ldsbase) {
  asm volatile("s_mov_b32 m0, %2\n\ts_mov_b32 exec_hi, %3\n\tglobal_load_lds_dwordx4 %0, %1\n\ts_mov_b32 exec_hi, -1" ::"v"(voff), "s"(sbase), "s"(ldsbase),
               "i"((1u << (NL - 32)) - 1u) : "memory");
}
template <int OFF> DEVI void gl128(u32x4& d, u32 voff, const void* sbase) { asm volatile("global_load_dwordx4 %0, %1, %2 offset:%3" : "=v"(d) : "v"(voff), "s"(sbase), "i"(OFF)); }
template <int N> DEVI void wait_vm() { asm volatile("s_waitcnt vmcnt(%0)" ::"i"(N > 63 ? 63 : N) : "memory"); }
DEVI void wait_lgkm() { asm volatile("s_waitcnt lgkmcnt(0)" ::: "memory"); }
DEVI void bar() { asm volatile("s_barrier" ::: "memory"); }
DEVI u32 rfl(u32 v) { return __builtin_amdgcn_readfirstlane(v); }
#define SCHED() __builtin_amdgcn_sched_barrier(0)
#define STAMP(k, i)

constexpr float QSCALE = 0.23570226039551584f * 1.4426950408889634f;

constexpr int K1_SLOT = 30720, K1_WOFF = 16384, K1_P = 4 * K1_SLOT;
constexpr int K1_G4 = K1_P + 30720, K1_DUMMY = K1_G4 + 3072, K1_SH = K1_G4 + 4096, K1_LDS = K1_G4 + 10112;
static_assert(K1_LDS <= 163840, "lds");

__global__ __launch_bounds__(768, 3) void k1(const u16* __restrict__ x, const u16* __restrict__ wqkv, const u16* __restrict__ Bk,
                                             const u16* __restrict__ Bv, const u16* __restrict__ shk, const u16* __restrict__ gw,
                                             const u16* __restrict__ ssmk, char* __restrict__ ws) {
  extern __shared__ __attribute__((aligned(16))) char smem[];
  const u32 sb = (u32)(size_t)smem;
  const int b = blockIdx.x, xcd = b & 7, idx = b >> 3;
  const int hg = xcd & 1, rb = 4 * (xcd >> 1) + (idx & 3), jt = idx >> 2;
  const int lane = threadIdx.x & 63;
  const u32 w = rfl(threadIdx.x >> 6);
  const int shift = (jt & 1) * 4;
  const int r8 = lane >> 3, c8 = lane & 7;
  STAMP(0, 0);
  if (w >= 8) {
    const u32 pw = w - 8;
    const u32 pb = sb + K1_P + pw * 5120, eb = sb + K1_P + 20480;
#pragma unroll
    for (int i = 0; i < 3; i++) {
      int row = 64 * idx + 16 * pw + 8 * i + r8 - 3; row = row < 0 ? 0 : (row > 2047 ? 2047 : row);
      dma16(Bk + 64 * xcd, row * 1152 + 16 * c8, pb + 1024 * i);
    }
#pragma unroll
    for (int i = 0; i < 2; i++) dma16(Bv + 64 * xcd, (64 * idx + 16 * pw + 8 * i + r8) * 1152 + 16 * c8, pb + 3072 + 1024 * i);
    const int cp = lane & 31, re = 8 * (32 * xcd + idx);
    u32x4 sh, she;
    gl128<0>(sh, 512 * xcd + 16 * cp, shk);
    if (pw == 0) {
#pragma unroll
      for (int i = 0; i < 2; i++) {
        int row = re + 8 * i + r8 - 3; row = row < 0 ? 0 : (row > 2047 ? 2047 : row);
        dma16(Bk + 512, row * 1152 + 16 * c8, eb + 1024 * i);
      }
      dma16(Bv + 512, (re + r8) * 1152 + 16 * c8, eb + 2048);
    }
    gl128<0>(she, 4096 + 16 * cp, shk);
    bar();
    asm volatile("s_waitcnt vmcnt(0)" : "+v"(sh), "+v"(she)::"memory");
#pragma unroll 1
    for (int rnd = 0; rnd < (pw == 0 ? 2 : 1); rnd++) {
      const bool e = rnd;
      const int rg = e ? 0 : lane >> 5;
      const u32 ka = (e ? eb : pb + rg * 1024) + 4 * cp, va = (e ? eb + 2048 : pb + 3072 + rg * 1024) + 4 * cp;
      const int row0 = e ? re : 64 * idx + 16 * pw + 8 * rg;
      u32 kk[11], vv[8];
#pragma unroll
      for (int i = 0; i < 11; i++) kk[i] = lds32(ka + 128 * i);
#pragma unroll
      for (int i = 0; i < 8; i++) vv[i] = lds32(va + 128 * i);
      if (row0 == 0) { kk[0] = 0; kk[1] = 0; kk[2] = 0; }
      char* dst = ws + WS_UR + (size_t)((e ? 512 : 64 * xcd) + 2 * cp) * 4096 + 2 * (2040 - row0);
      float r[8];
      {
        const float a0 = lo2f(sh.x), a1 = hi2f(sh.x), a2 = lo2f(sh.y), a3 = hi2f(sh.y);
#pragma unroll
        for (int i = 0; i < 8; i++) r[i] = fmaf(lo2f(kk[i + 3]), a0, fmaf(lo2f(kk[i + 2]), a1, fmaf(lo2f(kk[i + 1]), a2, lo2f(kk[i]) * a3))) * lo2f(vv[i]);
        if (!e || lane < 32) *(GAS u32x4*)dst = u32x4{pk2(r[7], r[6]), pk2(r[5], r[4]), pk2(r[3], r[2]), pk2(r[1], r[0])};
      }
      SCHED(); bar(); SCHED();
      {
        const float a0 = lo2f(sh.z), a1 = hi2f(sh.z), a2 = lo2f(sh.w), a3 = hi2f(sh.w);
#pragma unroll
        for (int i = 0; i < 8; i++) r[i] = fmaf(hi2f(kk[i + 3]), a0, fmaf(hi2f(kk[i + 2]), a1, fmaf(hi2f(kk[i + 1]), a2, hi2f(kk[i]) * a3))) * hi2f(vv[i]);
        if (!e || lane < 32) *(GAS u32x4*)(dst + 4096) = u32x4{pk2(r[7], r[6]), pk2(r[5], r[4]), pk2(r[3], r[2]), pk2(r[1], r[0])};
      }
      sh = she;
      if (pw == 0 && !e) { SCHED(); bar(); SCHED(); }
    }
    return;
  }
  if (w >= 4) {
    const u32 lw = w - 4;
    const u32 xo = (32 * lw + r8) * 1152 + 16 * (c8 ^ r8);
    const char* xb = (const char*)(x + (size_t)rb * 128 * 576);
    const int lr = lane / 14, lc = 108 * jt - shift + 8 * (lane - 14 * lr), seg = lc / 288;
    const u32 wo = (lr + 16 * lw) * 3456 + 2 * (lc + 288 * (seg + hg));
    const char* wb = (const char*)wqkv;
    auto issue = [&](int c) {
      const u32 sl = sb + (c & 3) * K1_SLOT;
      sfor<4>([&](auto I) { dma16(xb + c * 128 + I * 9216, xo, sl + (4 * lw + I) * 1024); });
      sfor<4>([&](auto I) {
        constexpr int i = I, sw = ((i & 1) << 1) | (i >> 1);
        dma16n<56>(wb + (64 * c + 4 * i) * 3456, wo, sl + K1_WOFF + (4 * lw + sw) * 896);
      });
    };
    issue(0); issue(1);
    wait_vm<8>(); bar();
    issue(2); issue(3);
#pragma unroll 1
    for (int c = 1; c <= 5; c++) { wait_vm<16>(); bar(); issue(c + 3); }
    {
      const int qb = 2 * (xcd >> 1) + (idx & 1), h = 16 * (xcd & 1) + (idx >> 1);
      u32x4 d0, d1, d2;
      const u16* kp = ssmk + (size_t)(64 * xcd + 2 * idx) * 2048;
      gl128<0>(d0, lw * 2048 + lane * 16, kp); gl128<1024>(d1, lw * 2048 + lane * 16, kp);
      gl128<0>(d2, lw * 1024 + lane * 16, ssmk + (size_t)((qb & 3) == 0 ? 512 + h + 8 * qb : 0) * 2048);
      wait_vm<19>(); bar();
      wait_vm<11>(); bar();
      wait_vm<3>(); bar();
      asm volatile("s_waitcnt vmcnt(0)" : "+v"(d0), "+v"(d1), "+v"(d2)::"memory");
    }
    return;
  }
  const int g = lane >> 4, l15 = lane & 15;
  const bool gblk = (jt == 0) && (hg == 0);
  if (gblk) {
#pragma unroll
    for (int i = 0; i < 3; i++) {
      const int k = i * 256 + threadIdx.x;
      if (k < 576) *(LAS u16*)(sb + K1_G4 + ((k >> 5) & 1) * 7168 + ((k >> 2) & 1) * 1792 + (k >> 6) * 128 + ((k >> 3) & 3) * 32 + (k & 3) * 8) = gw[k];
    }
    wait_lgkm();
  }
  const u32 stg = sb + K1_P + w * 7680;
  u32 ea[7][2]; float esc[7][2];
#pragma unroll
  for (int nt = 0; nt < 7; nt++)
#pragma unroll
    for (int pr = 0; pr < 2; pr++) {
      const int nl = 16 * nt + 4 * g + 2 * pr - shift;
      const int hs = (nl * 57) >> 10, d = nl - 18 * hs;
      ea[nt][pr] = (nl >= 0 && nl < 108) ? stg + hs * 1280 + l15 * 40 + 2 * d : sb + K1_DUMMY + l15 * 4;
      esc[nt][pr] = (6 * jt + hs < 16) ? QSCALE : 1.f;
    }
  f32x4 acc[2][7];
#pragma unroll
  for (int i = 0; i < 2; i++)
#pragma unroll
    for (int j = 0; j < 7; j++) acc[i][j] = f32x4{0.f, 0.f, 0.f, 0.f};
  const u32 wra = sb + K1_WOFF + (16 * (g >> 1) + 4 * (g & 1) + (l15 >> 2)) * 224 + 8 * (lane & 3);
  const u32 xra0 = sb + (32 * w + l15) * 128 + 16 * (g ^ (l15 & 7)), xra1 = xra0 ^ 64;
  const bool glane = gblk && (lane & 3) == 3;
  const u32 gra = sb + K1_G4 + 32 * g + 8 * (l15 >> 2);
  u32x4 wf[2][7], xf[2][2];
  auto rd = [&](auto KS, int c) {
    constexpr int ks = KS;
    const u32 so = (c & 3) * K1_SLOT;
    const u32 xa = (ks ? xra1 : xra0) + so, wa = wra + so, wa6 = glane ? gra + c * 128 : wa + 192;
    xf[ks][0] = lds128(xa); xf[ks][1] = lds128(xa + 2048);
    sfor<6>([&](auto NT) { wf[ks][NT] = cat(tr_read(wa + ks * 7168 + NT * 32), tr_read(wa + ks * 7168 + 1792 + NT * 32)); });
    wf[ks][6] = cat(tr_read(wa6 + ks * 7168), tr_read(wa6 + ks * 7168 + 1792));
  };
  auto mm = [&](auto KS) {
    constexpr int ks = KS;
    sfor<7>([&](auto NT) {
      acc[0][NT] = mfma16(wf[ks][NT], xf[ks][0], acc[0][NT]);
      acc[1][NT] = mfma16(wf[ks][NT], xf[ks][1], acc[1][NT]);
    });
  };
  auto ilv = [&]() {
    __builtin_amdgcn_sched_group_barrier(0x2, 6, 0);
    sfor<8>([&](auto) { __builtin_amdgcn_sched_group_barrier(0x8, 1, 0); __builtin_amdgcn_sched_group_barrier(0x100, 2, 0); });
    __builtin_amdgcn_sched_group_barrier(0x8, 6, 0);
  };
  SCHED();
  bar();
  STAMP(0, 1);
  rd(IC(0), 0);
  SCHED();
#pragma unroll 1
  for (int c = 0; c < 8; c++) {
    rd(IC(1), c); mm(IC(0)); ilv();
    SCHED(); wait_lgkm(); bar(); SCHED();
    rd(IC(0), c + 1); mm(IC(1)); ilv();
    SCHED();
  }
  STAMP(0, 2);
  rd(IC(1), 8); mm(IC(0)); ilv();
  SCHED();
  mm(IC(1));
  STAMP(0, 3);
#pragma unroll
  for (int i = 0; i < 3; i++) st32(stg + (i * 64 + lane) * 40 + 36, 0x3f803f80u);
#pragma unroll
  for (int nt = 0; nt < 7; nt++)
#pragma unroll
    for (int pr = 0; pr < 2; pr++) {
      st32(ea[nt][pr], pk2(acc[0][nt][2 * pr] * esc[nt][pr], acc[0][nt][2 * pr + 1] * esc[nt][pr]));
      st32(ea[nt][pr] + 640, pk2(acc[1][nt][2 * pr] * esc[nt][pr], acc[1][nt][2 * pr + 1] * esc[nt][pr]));
    }
  wait_lgkm();
  char* qo = ws + WS_QKV + (size_t)(rb * 128 + 32 * w) * 40;
#pragma unroll
  for (int it = 0; it < 8; it++) {
    const int id = it * 64 + lane;
    const int hs = (id * 205) >> 14, off = id - 80 * hs;
    const int li = 6 * jt + hs, slot = (li >> 4) * 32 + 16 * hg + (li & 15);
    if (id < 480) *(GAS u32x4*)(qo + (u32)(slot * 81920 + off * 16)) = lds128(stg + id * 16);
  }
  if (gblk && g == 3) {
    float* G = (float*)(ws + WS_G) + rb * 128 + 32 * w + l15;
#pragma unroll
    for (int mt = 0; mt < 2; mt++) { const float p = rbf(acc[mt][6][0]); G[16 * mt] = rbf(1.f / (1.f + __expf(-p))); }
  }
  STAMP(0, 4);
}

constexpr int K2_K = 0, K2_V = 20480, K2_Q = 40960, K2_C = 51200;
constexpr int KC_STR = 4640, CS_U = 4 * KC_STR, CS_SZ = CS_U + 4096 + 128;
constexpr int K2_R = K2_C + 3 * CS_SZ;
constexpr int K2_F = K2_R + 16384 + 24576, K2_DUMMY = K2_F + 16, K2_LDS = K2_DUMMY + 1024;
static_assert(K2_LDS <= 163840, "lds");

template <int NP>
DEVI void conv_channel(u32 cs, u32 red, u32 flag, int pi, int lane, float Dv, char* ydst) {
  constexpr int NR = 8 / NP, NT = 8 / NP;
  const int g = lane >> 4, l15 = lane & 15;
  const u32 aaddr = cs + (l15 & 3) * KC_STR + 2 * (64 * (l15 >> 2) + 8 * g) + 64 * NR * pi;
  const u32 baddr = cs + CS_U + 2 * (1852 - 1792 - 4 * l15 + 8 * g) + 64 * NR * pi;
  const u32 baddr2 = opaque(baddr + 8);
  f32x4 acc[8];
#pragma unroll
  for (int c = 0; c < 8; c++) acc[c] = f32x4{0.f, 0.f, 0.f, 0.f};
#pragma unroll 1
  for (int rho = 0; rho < NR; rho++) {
    u32x4 A[8], G[8];
#pragma unroll
    for (int p = 0; p < 8; p++) {
      A[p] = lds128(aaddr + rho * 64 + p * 512);
      G[p] = cat(lds64(baddr + rho * 64 + (7 - p) * 512), lds64(baddr2 + rho * 64 + (7 - p) * 512));
    }
    SCHED();
#pragma unroll
    for (int c = 0; c < 8; c++)
#pragma unroll
      for (int p = 0; p <= c; p++) acc[c] = mfma16(A[p], G[c - p], acc[c]);
  }
#pragma unroll
  for (int c = 0; c < 8; c++)
    if (c / NT != pi) {
      const int slot = (NP == 2) ? c : (c * 3 + ((pi - c / NT - 1) & 3));
      st128(red + slot * 1024 + lane * 16, __builtin_bit_cast(u32x4, acc[c]));
    }
  wait_lgkm();
  if (lane == 0) __hip_atomic_fetch_add((LAS u32*)flag, 1u, __ATOMIC_RELAXED, __HIP_MEMORY_SCOPE_WORKGROUP);
  while (rfl(__hip_atomic_load((LAS u32*)flag, __ATOMIC_RELAXED, __HIP_MEMORY_SCOPE_WORKGROUP)) < (u32)NP) __builtin_amdgcn_s_sleep(1);
  asm volatile("" ::: "memory");
#pragma unroll
  for (int c = 0; c < 8; c++)
    if (c / NT == pi) {
      f32x4 y = acc[c];
      if (NP == 2) y += __builtin_bit_cast(f32x4, lds128(red + c * 1024 + lane * 16));
      else {
#pragma unroll
        for (int q = 0; q < 3; q++) y += __builtin_bit_cast(f32x4, lds128(red + (c * 3 + q) * 1024 + lane * 16));
      }
      const int s = 256 * c + 4 * lane;
      const u32x2 u = lds64(cs + CS_U + 2 * (2044 - s));
      const float y0 = fmaf(hi2f(u.y), Dv, y[0]), y1 = fmaf(lo2f(u.y), Dv, y[1]), y2 = fmaf(hi2f(u.x), Dv, y[2]), y3 = fmaf(lo2f(u.x), Dv, y[3]);
      *(GAS u32x2*)(ydst + 2 * s) = u32x2{pk2(y0, y1), pk2(y2, y3)};
    }
}

DEVI float xhalf_max(float m) { const u32 mu = f2u(m); auto r = __builtin_amdgcn_permlane32_swap(mu, mu, false, false); return fmaxf(u2f(r[0]), u2f(r[1])); }

__global__ __launch_bounds__(768, 3) void k2(const u16* __restrict__ ssmk, const u16* __restrict__ ssmD, const u16* __restrict__ wo, char* __restrict__ ws) {
  extern __shared__ __attribute__((aligned(16))) char smem[];
  const u32 sb = (u32)(size_t)smem;
  const int b = blockIdx.x, xcd = b & 7, idx = b >> 3;
  const int qb = 2 * (xcd >> 1) + (idx & 1), h = 16 * (xcd & 1) + (idx >> 1);
  const int lane = threadIdx.x & 63;
  const u32 wv = rfl(threadIdx.x >> 6);
  const bool three = (qb & 3) == 0;
  STAMP(1, 0);
  if (wv >= 4) {
    const u32 w = wv - 4;
    {
      const char* Ag = ws + WS_QKV + ((size_t)h * 2048 + 256 * qb) * 40 + w * 1280;
      const u32 lo = lane * 16;
      dma16(Ag, lo, sb + K2_Q + w * 1280);
      dma16(Ag + 32 * 81920, lo, sb + K2_K + 10240 + w * 1280);
      dma16(Ag + 64 * 81920, lo, sb + K2_V + 10240 + w * 1280);
      if (lane < 16) {
        dma16(Ag + 1024, lo, sb + K2_Q + w * 1280 + 1024);
        dma16(Ag + 32 * 81920 + 1024, lo, sb + K2_K + 10240 + w * 1280 + 1024);
        dma16(Ag + 64 * 81920 + 1024, lo, sb + K2_V + 10240 + w * 1280 + 1024);
      }
      if (qb > 0) {
        dma16(Ag + 32 * 81920 - 10240, lo, sb + K2_K + w * 1280);
        dma16(Ag + 64 * 81920 - 10240, lo, sb + K2_V + w * 1280);
        if (lane < 16) {
          dma16(Ag + 32 * 81920 - 10240 + 1024, lo, sb + K2_K + w * 1280 + 1024);
          dma16(Ag + 64 * 81920 - 10240 + 1024, lo, sb + K2_V + w * 1280 + 1024);
        }
      }
    }
    const int c = lane & 31, hh = lane >> 5;
    const u32 ka = sb + K2_K + (32 * w + c) * 40 + 16 * hh, ka1 = opaque(ka + 8), ka2 = sb + K2_K + (32 * w + c) * 40 + 32;
    const u32 va = sb + K2_V + (32 * w + 4 * hh + ((lane & 15) >> 2)) * 40 + ((lane & 16) ? 32 : 8 * (lane & 3));
    const int thr = c - 4 * hh;
    const int i0 = qb > 0 ? 0 : 8 - (int)w;
    wait_vm<0>();
    STAMP(1, 2);
    bar();
    STAMP(1, 1);
    {
      const int r = 2 * w + (lane >> 5);
      const char* p = (const char*)wo + (size_t)(18 * idx) * 1152 + (xcd & 1) * 576;
      dma16(p, r * 1152 + (lane & 31) * 16, sb + K2_DUMMY);
      dma16(p, (w < 2 ? (16 + w) * 1152 + (lane < 36 ? lane : 0) * 16 : ((lane >> 2) * 1152 + 512 + (lane & 3) * 16)), sb + K2_DUMMY);
    }
    const u32x4 q0 = cat(lds64(ka + K2_Q), lds64(ka1 + K2_Q));
    u32x2 q1 = {hh ? 0u : lds32(ka2 + K2_Q), 0u};
    struct KF { u32x4 a; u32x2 b; };
    struct VF { u32x4 a, b; };
    auto ldk = [&](int i) { KF k; k.a = cat(lds64(ka + i * 1280), lds64(ka1 + i * 1280)); k.b = lds64(ka2 + i * 1280); return k; };
    auto ldv = [&](int i) { VF v; v.a = cat(tr_read(va + i * 1280), tr_read(va + i * 1280 + 320)); v.b = cat(tr_read(va + i * 1280 + 640), tr_read(va + i * 1280 + 960)); return v; };
    auto qk = [&](const KF& k) { f32x16 z = {}; z = mfma32(k.a, q0, z); return mfma32k8(k.b, q1, z); };
    auto pk8 = [&](const f32x16& p, int o) { return u32x4{pk2(p[o], p[o + 1]), pk2(p[o + 2], p[o + 3]), pk2(p[o + 4], p[o + 5]), pk2(p[o + 6], p[o + 7])}; };
    f32x16 Sm;
    {
      const KF k8 = ldk(8), k0 = ldk(0);
      Sm = qk(k8);
      f32x16 S0 = qk(k0);
#pragma unroll
      for (int e = 0; e < 16; e++) Sm[e] = ((e & 3) + 8 * (e >> 2) <= thr) ? Sm[e] : (i0 == 0 ? S0[e] : -__builtin_inff());
    }
    float m = fmaxf(Sm[0], Sm[1]);
#pragma unroll
    for (int e = 2; e < 16; e += 2) m = fmaxf(fmaxf(m, Sm[e]), Sm[e + 1]);
    m = xhalf_max(m);
    const int ib = i0 > 1 ? i0 : 1;
    f32x16 O;
    float l;
#pragma unroll 1
    for (int pass = 0; pass < 2; pass++) {
      const float mh = rbf(m), ml = rbf(m - mh), ms = mh + ml;
      q1.y = hh ? 0u : pk2(-mh, -ml);
      KF kn = ldk(7);
      VF v8 = ldv(8), v0 = ldv(0);
      f32x16 S[2];
      S[1] = qk(kn);
      kn = ldk(6);
      VF vn = ldv(7);
      {
        f32x16 p0, p8;
#pragma unroll
        for (int e = 0; e < 16; e++) {
          const float p = __builtin_amdgcn_exp2f(Sm[e] - ms);
          const bool cnd = (e & 3) + 8 * (e >> 2) <= thr;
          p8[e] = cnd ? p : 0.f; p0[e] = cnd ? 0.f : p;
        }
        O = f32x16{};
        O = mfma32(v8.a, pk8(p8, 0), O); O = mfma32(v8.b, pk8(p8, 8), O);
        if (i0 == 0) { O = mfma32(v0.a, pk8(p0, 0), O); O = mfma32(v0.b, pk8(p0, 8), O); }
      }
      bool go = true;
      sfor<7>([&](auto J) {
        constexpr int i = 7 - J;
        if (go && i >= ib) {
          SCHED();
          const KF kc = kn; const VF vc = vn;
          f32x16& Sc = S[i & 1];
          f32x16 z = {};
          if constexpr (i > 1) z = mfma32(kc.a, q0, z);
          if constexpr (i > 2) kn = ldk(i - 2);
          if constexpr (i > 1) vn = ldv(i - 1);
          SCHED();
#pragma unroll
          for (int e = 0; e < 8; e++) Sc[e] = __builtin_amdgcn_exp2f(Sc[e]);
          SCHED();
          if constexpr (i > 1) S[(i - 1) & 1] = mfma32k8(kc.b, q1, z);
          const u32x4 pa = pk8(Sc, 0);
          SCHED();
#pragma unroll
          for (int e = 8; e < 12; e++) Sc[e] = __builtin_amdgcn_exp2f(Sc[e]);
          SCHED();
          O = mfma32(vc.a, pa, O);
#pragma unroll
          for (int e = 12; e < 16; e++) Sc[e] = __builtin_amdgcn_exp2f(Sc[e]);
          const u32x4 pb = pk8(Sc, 8);
          SCHED();
          O = mfma32(vc.b, pb, O);
        } else go = false;
      });
      SCHED();
      { const u32 lu = f2u(O[10]); auto r = __builtin_amdgcn_permlane32_swap(lu, lu, false, false); l = u2f(r[0]); }
      if (pass == 1 || __all(l > 8.67e-19f && l < 1.15e18f)) break;
      q1.y = 0u;
#pragma unroll 1
      for (int i = ib; i < 8; i++) {
        const f32x16 s = qk(ldk(i));
#pragma unroll
        for (int e = 0; e < 16; e += 2) m = fmaxf(fmaxf(m, s[e]), s[e + 1]);
      }
      m = xhalf_max(m);
    }
    STAMP(1, 3);
    const float inv = __builtin_amdgcn_rcpf(l);
    char* Og = ws + WS_O + (size_t)(16 * qb + 2 * w + (c >> 4)) * 18432 + (c & 15) * 16;
    *(GAS u32x4*)(Og + (h >> 1) * 1024 + (2 * (h & 1) + hh) * 256) =
        u32x4{pk2(O[0] * inv, O[1] * inv), pk2(O[2] * inv, O[3] * inv), pk2(O[4] * inv, O[5] * inv), pk2(O[6] * inv, O[7] * inv)};
    if (hh == 0) *(GAS u32*)(Og + 16384 + (h >> 2) * 256 + (h & 3) * 4) = pk2(O[8] * inv, O[9] * inv);
    STAMP(1, 4);
  } else {
    const int cw = wv, tid = threadIdx.x;
    const int ch0 = 64 * xcd + 2 * idx, ch2 = three ? 512 + h + 8 * qb : ch0;
    u32x2 pre[3]; u32x4 cur[3];
#pragma unroll
    for (int j = 0; j < 3; j++) {
      const int ch = j < 2 ? ch0 + j : ch2;
      const u16* kp = ssmk + (size_t)ch * 2048 + 8 * tid;
      cur[j] = *(const GAS u32x4*)kp;
      pre[j] = *(const GAS u32x2*)(kp - (tid ? 4 : 0));
    }
#pragma unroll
    for (int j = 0; j < 3; j++)
      dma16(ws + WS_UR + (size_t)(j < 2 ? ch0 + j : ch2) * 4096 + cw * 1024, lane * 16, sb + K2_C + j * CS_SZ + CS_U + cw * 1024);
    const int chA = ch0 + (cw >> 1);
    const u32 dA = ssmD[chA], dC = ssmD[ch2];
    if (tid == 0) st128(sb + K2_F, u32x4{0u, 0u, 0u, 0u});
    {
      const u32 za = lane < 24 ? cw * KC_STR + 16 * lane : CS_U + 4096 + 32 * cw + 16 * (lane & 1);
#pragma unroll
      for (int j = 0; j < 3; j++)
        if (lane < 26) st128(sb + K2_C + j * CS_SZ + za, u32x4{0u, 0u, 0u, 0u});
    }
#pragma unroll
    for (int j = 0; j < 3; j++) {
      const u32 cs = sb + K2_C + j * CS_SZ + 384 + 16 * tid;
      const u32 w0 = tid ? pre[j].x : 0u, w1 = tid ? pre[j].y : 0u, w2 = cur[j].x, w3 = cur[j].y, w4 = cur[j].z, w5 = cur[j].w;
      const u32 a01 = __builtin_amdgcn_alignbyte(w1, w0, 2), a12 = __builtin_amdgcn_alignbyte(w2, w1, 2), a23 = __builtin_amdgcn_alignbyte(w3, w2, 2),
                a34 = __builtin_amdgcn_alignbyte(w4, w3, 2), a45 = __builtin_amdgcn_alignbyte(w5, w4, 2);
      st128(cs, u32x4{a01, a12, a23, a34});
      st128(cs + KC_STR, u32x4{w1, w2, w3, w4});
      st128(cs + 2 * KC_STR, u32x4{a12, a23, a34, a45});
      st128(cs + 3 * KC_STR, cur[j]);
    }
    wait_vm<0>();
    wait_lgkm();
    STAMP(1, 2);
    bar();
    STAMP(1, 1);
    conv_channel<2>(sb + K2_C + (cw >> 1) * CS_SZ, sb + K2_R + (cw >> 1) * 8192, sb + K2_F + (cw >> 1) * 4, cw & 1, lane, lo2f(dA), ws + WS_Y2 + (size_t)chA * 4096);
    STAMP(1, 3);
    if (three) conv_channel<4>(sb + K2_C + 2 * CS_SZ, sb + K2_R + 16384, sb + K2_F + 8, cw, lane, lo2f(dC), ws + WS_Y2 + (size_t)ch2 * 4096);
    STAMP(1, 4);
  }
}

constexpr int K3_Y = 0, K3_X = 9216, K3_W = K3_X + 4 * 3024, K3_LDS = K3_W + 576 * 160;

__global__ __launch_bounds__(512, 2) void k3(const u16* __restrict__ x, const u16* __restrict__ wo, const char* __restrict__ ws, u16* __restrict__ out) {
  extern __shared__ __attribute__((aligned(16))) char smem[];
  const u32 sb = (u32)(size_t)smem;
  const int b = blockIdx.x, xcd = b & 7, idx = b >> 3;
  const int rt = 8 * (xcd >> 1) + (idx & 7), ct = 4 * (xcd & 1) + (idx >> 3);
  const int lane = threadIdx.x & 63;
  const u32 w = rfl(threadIdx.x >> 6);
  STAMP(2, 0);
  if (w >= 4) {
    const u32 lw = w - 4;
    const int r6 = lane / 10, c10 = lane - 10 * r6;
    const u32 vo = (6 * lw + r6) * 1152 + 16 * (c10 > 8 ? 8 : c10);
    const char* wb = (const char*)(wo + 72 * ct);
    sfor<3>([&](auto I) {
      const int j = 4 * I + lw;
      if (j < 9) dma16(ws + WS_Y2 + (size_t)(72 * ct + 8 * j) * 4096 + 128 * rt, (lane >> 3) * 4096 + 16 * (lane & 7), sb + K3_Y + j * 1024);
    });
    sfor<12>([&](auto I) { dma16n<60>(wb + I * 24 * 1152, vo, sb + K3_W + (4 * I + lw) * 960); });
    wait_vm<12>(); bar();
    wait_vm<6>(); bar();
    sfor<12>([&](auto I) { dma16n<60>(wb + (12 + I) * 24 * 1152, vo, sb + K3_W + (4 * (12 + I) + lw) * 960); });
    wait_vm<12>(); bar();
    wait_vm<6>(); bar();
    wait_vm<0>(); bar();
    return;
  }
  const int g = lane >> 4, l15 = lane & 15;
  const int r7 = lane / 9, c9 = lane - 9 * r7;
  const u32 xw = sb + K3_X + w * 3024;
  sfor<3>([&](auto I) { int r = 7 * I + r7; r = r > 15 ? 15 : r; dma16n<63>(x + (size_t)(64 * rt + 16 * w) * 576 + 72 * ct, r * 1152 + 16 * c9, xw + I * 1008); });
  float gt;
  asm volatile("global_load_dword %0, %1, %2" : "=v"(gt) : "v"((u32)(64 * rt + 16 * w + l15) * 4), "s"(ws + WS_G));
  u32x4 of[18];
  const char* ob = ws + WS_O + (size_t)(4 * rt + w) * 18432;
  sfor<18>([&](auto F) { gl128<(F & 3) * 1024>(of[F], lane * 16, ob + (F >> 2) * 4096); });
  const int q = l15 >> 2;
  asm volatile("s_waitcnt vmcnt(18)" : "+v"(gt)::"memory");
  bar();
  STAMP(2, 1);
  f32x4 acc[5], tb[5];
  const u32 xa = xw + l15 * 144 + 8 * g;
  {
    const float g1 = rbf(1.f - gt);
    const u32 ya = sb + K3_Y + (4 * g + q) * 128 + 32 * w + 8 * (lane & 3);
    sfor<5>([&](auto NT) {
      const u32x2 y = tr_read(ya + NT * 2048), xv = lds64(xa + NT * 32);
      tb[NT] = f32x4{g1 * lo2f(xv.x) * lo2f(y.x), g1 * hi2f(xv.x) * hi2f(y.x), g1 * lo2f(xv.y) * lo2f(y.y), g1 * hi2f(xv.y) * hi2f(y.y)};
      acc[NT] = f32x4{0.f, 0.f, 0.f, 0.f};
    });
  }
  const u32 wa = sb + K3_W + (18 * (g >> 1) + 4 * (g & 1) + q) * 160 + 8 * (lane & 3);
  const u32 wa2 = sb + K3_W + ((4 * g + (q >> 1)) * 18 + 16 + (q & 1)) * 160 + 8 * (lane & 3);
  u32x4 wf[2][5];
  auto rd = [&](auto KS) {
    constexpr int ks = KS;
    sfor<5>([&](auto NT) {
      if constexpr (ks < 16) wf[ks & 1][NT] = cat(tr_read(wa + ks * 5760 + NT * 32), tr_read(wa + ks * 5760 + 1280 + NT * 32));
      else wf[ks & 1][NT] = cat(tr_read(wa2 + (ks - 16) * 46080 + NT * 32), tr_read(wa2 + (ks - 16) * 46080 + 5760 + NT * 32));
    });
  };
  auto mm = [&](auto KS) { sfor<5>([&](auto NT) { acc[NT] = mfma16(wf[KS & 1][NT], of[KS], acc[NT]); }); };
  auto tie = [&](auto Q) {
    constexpr int f = 4 * Q;
    asm volatile("s_waitcnt vmcnt(%4)" : "+v"(of[f]), "+v"(of[f + 1]), "+v"(of[f + 2]), "+v"(of[f + 3]) : "i"(f == 12 ? 0 : 14 - f) : "memory");
    if constexpr (f == 12) asm volatile("" : "+v"(of[16]), "+v"(of[17]));
    bar();
  };
  SCHED();
  tie(IC(0));
  STAMP(2, 2);
  rd(IC(0));
  sfor<18>([&](auto KS) {
    constexpr int ks = KS;
    SCHED();
    if constexpr (ks % 4 == 3 && ks < 12) tie(IC(ks / 4 + 1));
    if constexpr (ks < 17) rd(IC(ks + 1));
    mm(KS);
    sfor<5>([&](auto) { __builtin_amdgcn_sched_group_barrier(0x8, 1, 0); __builtin_amdgcn_sched_group_barrier(0x100, 2, 0); });
  });
  SCHED();
  STAMP(2, 3);
  sfor<5>([&](auto NT) {
    constexpr int nt = NT;
    if (nt < 4 || g < 2)
      st64(xa + nt * 32, u32x2{pk2(fmaf(gt, acc[nt][0], tb[nt][0]), fmaf(gt, acc[nt][1], tb[nt][1])), pk2(fmaf(gt, acc[nt][2], tb[nt][2]), fmaf(gt, acc[nt][3], tb[nt][3]))});
  });
  wait_lgkm();
  char* orow = (char*)(out + (size_t)(64 * rt + 16 * w) * 576 + 72 * ct);
#pragma unroll
  for (int it = 0; it < 3; it++) {
    const int r = 7 * it + r7;
    if (lane < 63 && r < 16) *(GAS u32x4*)(orow + (u32)(r * 1152 + 16 * c9)) = lds128(xw + r * 144 + 16 * c9);
  }
  STAMP(2, 4);
}

extern "C" void kernel_launch(void* const* d_in, const int* in_sizes, int n_in, void* d_out, int out_size, void* d_ws, size_t ws_size,
                              hipStream_t stream) {
  const u16* x = (const u16*)d_in[0]; const u16* wqkv = (const u16*)d_in[1]; const u16* wo = (const u16*)d_in[2];
  const u16* Bk = (const u16*)d_in[4]; const u16* Bv = (const u16*)d_in[5]; const u16* shk = (const u16*)d_in[6];
  const u16* ssmk = (const u16*)d_in[7]; const u16* ssmD = (const u16*)d_in[8]; const u16* gw = (const u16*)d_in[9];
  (void)hipFuncSetAttribute((const void*)k1, hipFuncAttributeMaxDynamicSharedMemorySize, K1_LDS);
  (void)hipFuncSetAttribute((const void*)k2, hipFuncAttributeMaxDynamicSharedMemorySize, K2_LDS);
  (void)hipFuncSetAttribute((const void*)k3, hipFuncAttributeMaxDynamicSharedMemorySize, K3_LDS);
  k1<<<256, 768, K1_LDS, stream>>>(x, wqkv, Bk, Bv, shk, gw, ssmk, ((char*)d_ws + ws_size - (16u << 20)));
  k2<<<256, 768, K2_LDS, stream>>>(ssmk, ssmD, wo, ((char*)d_ws + ws_size - (16u << 20)));
  k3<<<256, 512, K3_LDS, stream>>>(x, wo, ((const char*)d_ws + ws_size - (16u << 20)), (u16*)d_out);
}
```

```cpp
#include <hip/hip_runtime.h>
#include <hip/hip_bf16.h>
#include <utility>
#define LEAKA 12
#define LEAKB 16

#define LEAKBLK 100
#define STAMP_RD(i) do { __builtin_amdgcn_sched_barrier(0); asm volatile("s_memrealtime %0\n\ts_waitcnt lgkmcnt(0)" : "=s"(ts_[i])::"memory"); __builtin_amdgcn_sched_barrier(0); } while (0)
#define STAMP_0(k) unsigned long long ts_[5] = {}; STAMP_RD(0)
#define STAMP_1(k) STAMP_RD(1)
#define STAMP_2(k) STAMP_RD(2)
#define STAMP_3(k) STAMP_RD(3)
#define STAMP_4(k) do { STAMP_RD(4); if (blockIdx.x == LEAKBLK && threadIdx.x == LEAKT##k) { unsigned long long* p_ = (unsigned long long*)((char*)ws + (128ull << 20)); \
   for (int i_ = 0; i_ < 5; i_++) p_[(k) * 8 + i_] = ts_[i_]; \
   if ((k) == 2) { __threadfence(); volatile unsigned long long* q_ = p_; const unsigned long long d_ = q_[LEAKB] - q_[LEAKA]; const unsigned long long e_ = ts_[4] + 10ull * (d_ > 5000ull ? 5000ull : d_); \
     while (__builtin_amdgcn_s_memrealtime() < e_) __builtin_amdgcn_s_sleep(2); } } } while (0)
#define STAMP(k, i) STAMP_##i(k)
#define LEAKT0 0
#define LEAKT1 512
#define LEAKT2 0

typedef unsigned short u16;
typedef unsigned int u32;
typedef u32 u32x2 __attribute__((ext_vector_type(2)));
typedef u32 u32x4 __attribute__((ext_vector_type(4)));
typedef short s16x4 __attribute__((ext_vector_type(4)));
typedef __bf16 bf16x8 __attribute__((ext_vector_type(8)));
typedef __bf16 bf16x2 __attribute__((ext_vector_type(2)));
typedef float f32x2 __attribute__((ext_vector_type(2)));
typedef float f32x4 __attribute__((ext_vector_type(4)));
typedef float f32x16 __attribute__((ext_vector_type(16)));
#define LAS __attribute__((address_space(3)))
#define GAS __attribute__((address_space(1)))
#define DEVI __device__ __forceinline__
#define IC(n) std::integral_constant<int, (n)>{}

constexpr size_t WS_QKV = 0;
constexpr size_t WS_O   = WS_QKV + 96ull * 2048 * 40;
constexpr size_t WS_UR  = WS_O + 32ull * 2048 * 40;
constexpr size_t WS_Y2  = WS_UR + 576ull * 4096;
constexpr size_t WS_G   = WS_Y2 + 576ull * 4096;

template <typename F, int... I> DEVI void sfor_impl(F&& f, std::integer_sequence<int, I...>) { (f(std::integral_constant<int, I>{}), ...); }
template <int N, typename F> DEVI void sfor(F&& f) { sfor_impl(f, std::make_integer_sequence<int, N>{}); }

DEVI u32 pk2(float a, float b) { f32x2 v = {a, b}; return __builtin_bit_cast(u32, __builtin_convertvector(v, bf16x2)); }
DEVI u32 f2u(float a) { return __builtin_bit_cast(u32, a); }
DEVI float u2f(u32 a) { return __builtin_bit_cast(float, a); }
DEVI float lo2f(u32 w) { return __builtin_bit_cast(float, w << 16); }
DEVI float hi2f(u32 w) { return __builtin_bit_cast(float, w & 0xffff0000u); }
DEVI float rbf(float a) { return lo2f(pk2(a, a)); }
DEVI f32x4 mfma16(u32x4 a, u32x4 b, f32x4 c) { return __builtin_amdgcn_mfma_f32_16x16x32_bf16(__builtin_bit_cast(bf16x8, a), __builtin_bit_cast(bf16x8, b), c, 0, 0, 0); }
DEVI f32x16 mfma32(u32x4 a, u32x4 b, f32x16 c) { return __builtin_amdgcn_mfma_f32_32x32x16_bf16(__builtin_bit_cast(bf16x8, a), __builtin_bit_cast(bf16x8, b), c, 0, 0, 0); }
DEVI f32x16 mfma32k8(u32x2 a, u32x2 b, f32x16 c) { return __builtin_amdgcn_mfma_f32_32x32x8bf16_1k(__builtin_bit_cast(s16x4, a), __builtin_bit_cast(s16x4, b), c, 0, 0, 0); }
DEVI u32x2 tr_read(u32 addr) { return __builtin_bit_cast(u32x2, __builtin_amdgcn_ds_read_tr16_b64_v4i16((LAS s16x4*)addr)); }
DEVI u32x4 lds128(u32 addr) { return *(LAS u32x4*)addr; }
DEVI u32x2 lds64(u32 addr) { return *(LAS u32x2*)addr; }
DEVI u32 lds32(u32 addr) { return *(LAS u32*)addr; }
DEVI void st128(u32 addr, u32x4 v) { *(LAS u32x4*)addr = v; }
DEVI void st64(u32 addr, u32x2 v) { *(LAS u32x2*)addr = v; }
DEVI void st32(u32 addr, u32 v) { *(LAS u32*)addr = v; }
DEVI u32x4 cat(u32x2 a, u32x2 b) { return u32x4{a.x, a.y, b.x, b.y}; }
DEVI u32 opaque(u32 v) { asm volatile("" : "+v"(v)); return v; }
DEVI void dma16(const void* sbase, u32 voff, u32 ldsbase) {
  asm volatile("s_mov_b32 m0, %2\n\ts_nop 0\n\tglobal_load_lds_dwordx4 %0, %1" ::"v"(voff), "s"(sbase), "s"(ldsbase) : "memory");
}
template <int NL> DEVI void dma16n(const void* sbase, u32 voff, u32 ldsbase) {
  asm volatile("s_mov_b32 m0, %2\n\ts_mov_b32 exec_hi, %3\n\tglobal_load_lds_dwordx4 %0, %1\n\ts_mov_b32 exec_hi, -1" ::"v"(voff), "s"(sbase), "s"(ldsbase),
               "i"((1u << (NL - 32)) - 1u) : "memory");
}
template <int OFF> DEVI void gl128(u32x4& d, u32 voff, const void* sbase) { asm volatile("global_load_dwordx4 %0, %1, %2 offset:%3" : "=v"(d) : "v"(voff), "s"(sbase), "i"(OFF)); }
template <int N> DEVI void wait_vm() { asm volatile("s_waitcnt vmcnt(%0)" ::"i"(N > 63 ? 63 : N) : "memory"); }
DEVI void wait_lgkm() { asm volatile("s_waitcnt lgkmcnt(0)" ::: "memory"); }
DEVI void bar() { asm volatile("s_barrier" ::: "memory"); }
DEVI u32 rfl(u32 v) { return __builtin_amdgcn_readfirstlane(v); }
#define SCHED() __builtin_amdgcn_sched_barrier(0)

constexpr float QSCALE = 0.23570226039551584f * 1.4426950408889634f;

constexpr int K1_SLOT = 30720, K1_WOFF = 16384, K1_P = 4 * K1_SLOT;
constexpr int K1_G4 = K1_P + 30720, K1_DUMMY = K1_G4 + 3072, K1_SH = K1_G4 + 4096, K1_LDS = K1_G4 + 10112;
static_assert(K1_LDS <= 163840, "lds");

__global__ __launch_bounds__(768, 3) void k1(const u16* __restrict__ x, const u16* __restrict__ wqkv, const u16* __restrict__ Bk,
                                             const u16* __restrict__ Bv, const u16* __restrict__ shk, const u16* __restrict__ gw,
                                             const u16* __restrict__ ssmk, char* __restrict__ ws) {
  extern __shared__ __attribute__((aligned(16))) char smem[];
  const u32 sb = (u32)(size_t)smem;
  const int b = blockIdx.x, xcd = b & 7, idx = b >> 3;
  const int hg = xcd & 1, rb = 4 * (xcd >> 1) + (idx & 3), jt = idx >> 2;
  const int lane = threadIdx.x & 63;
  const u32 w = rfl(threadIdx.x >> 6);
  const int shift = (jt & 1) * 4;
  const int r8 = lane >> 3, c8 = lane & 7;
  STAMP(0, 0);
  if (w >= 8) {
    const u32 pw = w - 8;
    const u32 pb = sb + K1_P + pw * 5120, eb = sb + K1_P + 20480;
#pragma unroll
    for (int i = 0; i < 3; i++) {
      int row = 64 * idx + 16 * pw + 8 * i + r8 - 3; row = row < 0 ? 0 : (row > 2047 ? 2047 : row);
      dma16(Bk + 64 * xcd, row * 1152 + 16 * c8, pb + 1024 * i);
    }
#pragma unroll
    for (int i = 0; i < 2; i++) dma16(Bv + 64 * xcd, (64 * idx + 16 * pw + 8 * i + r8) * 1152 + 16 * c8, pb + 3072 + 1024 * i);
    const int cp = lane & 31, re = 8 * (32 * xcd + idx);
    u32x4 sh, she;
    gl128<0>(sh, 512 * xcd + 16 * cp, shk);
    if (pw == 0) {
#pragma unroll
      for (int i = 0; i < 2; i++) {
        int row = re + 8 * i + r8 - 3; row = row < 0 ? 0 : (row > 2047 ? 2047 : row);
        dma16(Bk + 512, row * 1152 + 16 * c8, eb + 1024 * i);
      }
      dma16(Bv + 512, (re + r8) * 1152 + 16 * c8, eb + 2048);
    }
    gl128<0>(she, 4096 + 16 * cp, shk);
    bar();
    asm volatile("s_waitcnt vmcnt(0)" : "+v"(sh), "+v"(she)::"memory");
#pragma unroll 1
    for (int rnd = 0; rnd < (pw == 0 ? 2 : 1); rnd++) {
      const bool e = rnd;
      const int rg = e ? 0 : lane >> 5;
      const u32 ka = (e ? eb : pb + rg * 1024) + 4 * cp, va = (e ? eb + 2048 : pb + 3072 + rg * 1024) + 4 * cp;
      const int row0 = e ? re : 64 * idx + 16 * pw + 8 * rg;
      u32 kk[11], vv[8];
#pragma unroll
      for (int i = 0; i < 11; i++) kk[i] = lds32(ka + 128 * i);
#pragma unroll
      for (int i = 0; i < 8; i++) vv[i] = lds32(va + 128 * i);
      if (row0 == 0) { kk[0] = 0; kk[1] = 0; kk[2] = 0; }
      char* dst = ws + WS_UR + (size_t)((e ? 512 : 64 * xcd) + 2 * cp) * 4096 + 2 * (2040 - row0);
      float r[8];
      {
        const float a0 = lo2f(sh.x), a1 = hi2f(sh.x), a2 = lo2f(sh.y), a3 = hi2f(sh.y);
#pragma unroll
        for (int i = 0; i < 8; i++) r[i] = fmaf(lo2f(kk[i + 3]), a0, fmaf(lo2f(kk[i + 2]), a1, fmaf(lo2f(kk[i + 1]), a2, lo2f(kk[i]) * a3))) * lo2f(vv[i]);
        if (!e || lane < 32) *(GAS u32x4*)dst = u32x4{pk2(r[7], r[6]), pk2(r[5], r[4]), pk2(r[3], r[2]), pk2(r[1], r[0])};
      }
      SCHED(); bar(); SCHED();
      {
        const float a0 = lo2f(sh.z), a1 = hi2f(sh.z), a2 = lo2f(sh.w), a3 = hi2f(sh.w);
#pragma unroll
        for (int i = 0; i < 8; i++) r[i] = fmaf(hi2f(kk[i + 3]), a0, fmaf(hi2f(kk[i + 2]), a1, fmaf(hi2f(kk[i + 1]), a2, hi2f(kk[i]) * a3))) * hi2f(vv[i]);
        if (!e || lane < 32) *(GAS u32x4*)(dst + 4096) = u32x4{pk2(r[7], r[6]), pk2(r[5], r[4]), pk2(r[3], r[2]), pk2(r[1], r[0])};
      }
      sh = she;
      if (pw == 0 && !e) { SCHED(); bar(); SCHED(); }
    }
    return;
  }
  if (w >= 4) {
    const u32 lw = w - 4;
    const u32 xo = (32 * lw + r8) * 1152 + 16 * (c8 ^ r8);
    const char* xb = (const char*)(x + (size_t)rb * 128 * 576);
    const int lr = lane / 14, lc = 108 * jt - shift + 8 * (lane - 14 * lr), seg = lc / 288;
    const u32 wo = (lr + 16 * lw) * 3456 + 2 * (lc + 288 * (seg + hg));
    const char* wb = (const char*)wqkv;
    auto issue = [&](int c) {
      const u32 sl = sb + (c & 3) * K1_SLOT;
      sfor<4>([&](auto I) { dma16(xb + c * 128 + I * 9216, xo, sl + (4 * lw + I) * 1024); });
      sfor<4>([&](auto I) {
        constexpr int i = I, sw = ((i & 1) << 1) | (i >> 1);
        dma16n<56>(wb + (64 * c + 4 * i) * 3456, wo, sl + K1_WOFF + (4 * lw + sw) * 896);
      });
    };
    issue(0); issue(1);
    wait_vm<8>(); bar();
    issue(2); issue(3);
#pragma unroll 1
    for (int c = 1; c <= 5; c++) { wait_vm<16>(); bar(); issue(c + 3); }
    {
      const int qb = 2 * (xcd >> 1) + (idx & 1), h = 16 * (xcd & 1) + (idx >> 1);
      u32x4 d0, d1, d2;
      const u16* kp = ssmk + (size_t)(64 * xcd + 2 * idx) * 2048;
      gl128<0>(d0, lw * 2048 + lane * 16, kp); gl128<1024>(d1, lw * 2048 + lane * 16, kp);
      gl128<0>(d2, lw * 1024 + lane * 16, ssmk + (size_t)((qb & 3) == 0 ? 512 + h + 8 * qb : 0) * 2048);
      wait_vm<19>(); bar();
      wait_vm<11>(); bar();
      wait_vm<3>(); bar();
      asm volatile("s_waitcnt vmcnt(0)" : "+v"(d0), "+v"(d1), "+v"(d2)::"memory");
    }
    return;
  }
  const int g = lane >> 4, l15 = lane & 15;
  const bool gblk = (jt == 0) && (hg == 0);
  if (gblk) {
#pragma unroll
    for (int i = 0; i < 3; i++) {
      const int k = i * 256 + threadIdx.x;
      if (k < 576) *(LAS u16*)(sb + K1_G4 + ((k >> 5) & 1) * 7168 + ((k >> 2) & 1) * 1792 + (k >> 6) * 128 + ((k >> 3) & 3) * 32 + (k & 3) * 8) = gw[k];
    }
    wait_lgkm();
  }
  const u32 stg = sb + K1_P + w * 7680;
  u32 ea[7][2]; float esc[7][2];
#pragma unroll
  for (int nt = 0; nt < 7; nt++)
#pragma unroll
    for (int pr = 0; pr < 2; pr++) {
      const int nl = 16 * nt + 4 * g + 2 * pr - shift;
      const int hs = (nl * 57) >> 10, d = nl - 18 * hs;
      ea[nt][pr] = (nl >= 0 && nl < 108) ? stg + hs * 1280 + l15 * 40 + 2 * d : sb + K1_DUMMY + l15 * 4;
      esc[nt][pr] = (6 * jt + hs < 16) ? QSCALE : 1.f;
    }
  f32x4 acc[2][7];
#pragma unroll
  for (int i = 0; i < 2; i++)
#pragma unroll
    for (int j = 0; j < 7; j++) acc[i][j] = f32x4{0.f, 0.f, 0.f, 0.f};
  const u32 wra = sb + K1_WOFF + (16 * (g >> 1) + 4 * (g & 1) + (l15 >> 2)) * 224 + 8 * (lane & 3);
  const u32 xra0 = sb + (32 * w + l15) * 128 + 16 * (g ^ (l15 & 7)), xra1 = xra0 ^ 64;
  const bool glane = gblk && (lane & 3) == 3;
  const u32 gra = sb + K1_G4 + 32 * g + 8 * (l15 >> 2);
  u32x4 wf[2][7], xf[2][2];
  auto rd = [&](auto KS, int c) {
    constexpr int ks = KS;
    const u32 so = (c & 3) * K1_SLOT;
    const u32 xa = (ks ? xra1 : xra0) + so, wa = wra + so, wa6 = glane ? gra + c * 128 : wa + 192;
    xf[ks][0] = lds128(xa); xf[ks][1] = lds128(xa + 2048);
    sfor<6>([&](auto NT) { wf[ks][NT] = cat(tr_read(wa + ks * 7168 + NT * 32), tr_read(wa + ks * 7168 + 1792 + NT * 32)); });
    wf[ks][6] = cat(tr_read(wa6 + ks * 7168), tr_read(wa6 + ks * 7168 + 1792));
  };
  auto mm = [&](auto KS) {
    constexpr int ks = KS;
    sfor<7>([&](auto NT) {
      acc[0][NT] = mfma16(wf[ks][NT], xf[ks][0], acc[0][NT]);
      acc[1][NT] = mfma16(wf[ks][NT], xf[ks][1], acc[1][NT]);
    });
  };
  auto ilv = [&]() {
    __builtin_amdgcn_sched_group_barrier(0x2, 6, 0);
    sfor<8>([&](auto) { __builtin_amdgcn_sched_group_barrier(0x8, 1, 0); __builtin_amdgcn_sched_group_barrier(0x100, 2, 0); });
    __builtin_amdgcn_sched_group_barrier(0x8, 6, 0);
  };
  SCHED();
  bar();
  STAMP(0, 1);
  rd(IC(0), 0);
  SCHED();
#pragma unroll 1
  for (int c = 0; c < 8; c++) {
    rd(IC(1), c); mm(IC(0)); ilv();
    SCHED(); wait_lgkm(); bar(); SCHED();
    rd(IC(0), c + 1); mm(IC(1)); ilv();
    SCHED();
  }
  STAMP(0, 2);
  rd(IC(1), 8); mm(IC(0)); ilv();
  SCHED();
  mm(IC(1));
  STAMP(0, 3);
#pragma unroll
  for (int i = 0; i < 3; i++) st32(stg + (i * 64 + lane) * 40 + 36, 0x3f803f80u);
#pragma unroll
  for (int nt = 0; nt < 7; nt++)
#pragma unroll
    for (int pr = 0; pr < 2; pr++) {
      st32(ea[nt][pr], pk2(acc[0][nt][2 * pr] * esc[nt][pr], acc[0][nt][2 * pr + 1] * esc[nt][pr]));
      st32(ea[nt][pr] + 640, pk2(acc[1][nt][2 * pr] * esc[nt][pr], acc[1][nt][2 * pr + 1] * esc[nt][pr]));
    }
  wait_lgkm();
  char* qo = ws + WS_QKV + (size_t)(rb * 128 + 32 * w) * 40;
#pragma unroll
  for (int it = 0; it < 8; it++) {
    const int id = it * 64 + lane;
    const int hs = (id * 205) >> 14, off = id - 80 * hs;
    const int li = 6 * jt + hs, slot = (li >> 4) * 32 + 16 * hg + (li & 15);
    if (id < 480) *(GAS u32x4*)(qo + (u32)(slot * 81920 + off * 16)) = lds128(stg + id * 16);
  }
  if (gblk && g == 3) {
    float* G = (float*)(ws + WS_G) + rb * 128 + 32 * w + l15;
#pragma unroll
    for (int mt = 0; mt < 2; mt++) { const float p = rbf(acc[mt][6][0]); G[16 * mt] = rbf(1.f / (1.f + __expf(-p))); }
  }
  STAMP(0, 4);
}

constexpr int K2_K = 0, K2_V = 20480, K2_Q = 40960, K2_C = 51200;
constexpr int KC_STR = 4640, CS_U = 4 * KC_STR, CS_SZ = CS_U + 4096 + 128;
constexpr int K2_R = K2_C + 3 * CS_SZ;
constexpr int K2_F = K2_R + 16384 + 24576, K2_DUMMY = K2_F + 16, K2_LDS = K2_DUMMY + 1024;
static_assert(K2_LDS <= 163840, "lds");

template <int NP>
DEVI void conv_channel(u32 cs, u32 red, u32 flag, int pi, int lane, float Dv, char* ydst) {
  constexpr int NR = 8 / NP, NT = 8 / NP;
  const int g = lane >> 4, l15 = lane & 15;
  const u32 aaddr = cs + (l15 & 3) * KC_STR + 2 * (64 * (l15 >> 2) + 8 * g) + 64 * NR * pi;
  const u32 baddr = cs + CS_U + 2 * (1852 - 1792 - 4 * l15 + 8 * g) + 64 * NR * pi;
  const u32 baddr2 = opaque(baddr + 8);
  f32x4 acc[8];
#pragma unroll
  for (int c = 0; c < 8; c++) acc[c] = f32x4{0.f, 0.f, 0.f, 0.f};
#pragma unroll 1
  for (int rho = 0; rho < NR; rho++) {
    u32x4 A[8], G[8];
#pragma unroll
    for (int p = 0; p < 8; p++) {
      A[p] = lds128(aaddr + rho * 64 + p * 512);
      G[p] = cat(lds64(baddr + rho * 64 + (7 - p) * 512), lds64(baddr2 + rho * 64 + (7 - p) * 512));
    }
    SCHED();
#pragma unroll
    for (int c = 0; c < 8; c++)
#pragma unroll
      for (int p = 0; p <= c; p++) acc[c] = mfma16(A[p], G[c - p], acc[c]);
  }
#pragma unroll
  for (int c = 0; c < 8; c++)
    if (c / NT != pi) {
      const int slot = (NP == 2) ? c : (c * 3 + ((pi - c / NT - 1) & 3));
      st128(red + slot * 1024 + lane * 16, __builtin_bit_cast(u32x4, acc[c]));
    }
  wait_lgkm();
  if (lane == 0) __hip_atomic_fetch_add((LAS u32*)flag, 1u, __ATOMIC_RELAXED, __HIP_MEMORY_SCOPE_WORKGROUP);
  while (rfl(__hip_atomic_load((LAS u32*)flag, __ATOMIC_RELAXED, __HIP_MEMORY_SCOPE_WORKGROUP)) < (u32)NP) __builtin_amdgcn_s_sleep(1);
  asm volatile("" ::: "memory");
#pragma unroll
  for (int c = 0; c < 8; c++)
    if (c / NT == pi) {
      f32x4 y = acc[c];
      if (NP == 2) y += __builtin_bit_cast(f32x4, lds128(red + c * 1024 + lane * 16));
      else {
#pragma unroll
        for (int q = 0; q < 3; q++) y += __builtin_bit_cast(f32x4, lds128(red + (c * 3 + q) * 1024 + lane * 16));
      }
      const int s = 256 * c + 4 * lane;
      const u32x2 u = lds64(cs + CS_U + 2 * (2044 - s));
      const float y0 = fmaf(hi2f(u.y), Dv, y[0]), y1 = fmaf(lo2f(u.y), Dv, y[1]), y2 = fmaf(hi2f(u.x), Dv, y[2]), y3 = fmaf(lo2f(u.x), Dv, y[3]);
      *(GAS u32x2*)(ydst + 2 * s) = u32x2{pk2(y0, y1), pk2(y2, y3)};
    }
}

DEVI float xhalf_max(float m) { const u32 mu = f2u(m); auto r = __builtin_amdgcn_permlane32_swap(mu, mu, false, false); return fmaxf(u2f(r[0]), u2f(r[1])); }

__global__ __launch_bounds__(768, 3) void k2(const u16* __restrict__ ssmk, const u16* __restrict__ ssmD, const u16* __restrict__ wo, char* __restrict__ ws) {
  extern __shared__ __attribute__((aligned(16))) char smem[];
  const u32 sb = (u32)(size_t)smem;
  const int b = blockIdx.x, xcd = b & 7, idx = b >> 3;
  const int qb = 2 * (xcd >> 1) + (idx & 1), h = 16 * (xcd & 1) + (idx >> 1);
  const int lane = threadIdx.x & 63;
  const u32 wv = rfl(threadIdx.x >> 6);
  const bool three = (qb & 3) == 0;
  STAMP(1, 0);
  if (wv >= 4) {
    const u32 w = wv - 4;
    {
      const char* Ag = ws + WS_QKV + ((size_t)h * 2048 + 256 * qb) * 40 + w * 1280;
      const u32 lo = lane * 16;
      dma16(Ag, lo, sb + K2_Q + w * 1280);
      dma16(Ag + 32 * 81920, lo, sb + K2_K + 10240 + w * 1280);
      dma16(Ag + 64 * 81920, lo, sb + K2_V + 10240 + w * 1280);
      if (lane < 16) {
        dma16(Ag + 1024, lo, sb + K2_Q + w * 1280 + 1024);
        dma16(Ag + 32 * 81920 + 1024, lo, sb + K2_K + 10240 + w * 1280 + 1024);
        dma16(Ag + 64 * 81920 + 1024, lo, sb + K2_V + 10240 + w * 1280 + 1024);
      }
      if (qb > 0) {
        dma16(Ag + 32 * 81920 - 10240, lo, sb + K2_K + w * 1280);
        dma16(Ag + 64 * 81920 - 10240, lo, sb + K2_V + w * 1280);
        if (lane < 16) {
          dma16(Ag + 32 * 81920 - 10240 + 1024, lo, sb + K2_K + w * 1280 + 1024);
          dma16(Ag + 64 * 81920 - 10240 + 1024, lo, sb + K2_V + w * 1280 + 1024);
        }
      }
    }
    const int c = lane & 31, hh = lane >> 5;
    const u32 ka = sb + K2_K + (32 * w + c) * 40 + 16 * hh, ka1 = opaque(ka + 8), ka2 = sb + K2_K + (32 * w + c) * 40 + 32;
    const u32 va = sb + K2_V + (32 * w + 4 * hh + ((lane & 15) >> 2)) * 40 + ((lane & 16) ? 32 : 8 * (lane & 3));
    const int thr = c - 4 * hh;
    const int i0 = qb > 0 ? 0 : 8 - (int)w;
    wait_vm<0>();
    STAMP(1, 2);
    bar();
    STAMP(1, 1);
    {
      const int r = 2 * w + (lane >> 5);
      const char* p = (const char*)wo + (size_t)(18 * idx) * 1152 + (xcd & 1) * 576;
      dma16(p, r * 1152 + (lane & 31) * 16, sb + K2_DUMMY);
      dma16(p, (w < 2 ? (16 + w) * 1152 + (lane < 36 ? lane : 0) * 16 : ((lane >> 2) * 1152 + 512 + (lane & 3) * 16)), sb + K2_DUMMY);
    }
    const u32x4 q0 = cat(lds64(ka + K2_Q), lds64(ka1 + K2_Q));
    u32x2 q1 = {hh ? 0u : lds32(ka2 + K2_Q), 0u};
    struct KF { u32x4 a; u32x2 b; };
    struct VF { u32x4 a, b; };
    auto ldk = [&](int i) { KF k; k.a = cat(lds64(ka + i * 1280), lds64(ka1 + i * 1280)); k.b = lds64(ka2 + i * 1280); return k; };
    auto ldv = [&](int i) { VF v; v.a = cat(tr_read(va + i * 1280), tr_read(va + i * 1280 + 320)); v.b = cat(tr_read(va + i * 1280 + 640), tr_read(va + i * 1280 + 960)); return v; };
    auto qk = [&](const KF& k) { f32x16 z = {}; z = mfma32(k.a, q0, z); return mfma32k8(k.b, q1, z); };
    auto pk8 = [&](const f32x16& p, int o) { return u32x4{pk2(p[o], p[o + 1]), pk2(p[o + 2], p[o + 3]), pk2(p[o + 4], p[o + 5]), pk2(p[o + 6], p[o + 7])}; };
    f32x16 Sm;
    {
      const KF k8 = ldk(8), k0 = ldk(0);
      Sm = qk(k8);
      f32x16 S0 = qk(k0);
#pragma unroll
      for (int e = 0; e < 16; e++) Sm[e] = ((e & 3) + 8 * (e >> 2) <= thr) ? Sm[e] : (i0 == 0 ? S0[e] : -__builtin_inff());
    }
    float m = fmaxf(Sm[0], Sm[1]);
#pragma unroll
    for (int e = 2; e < 16; e += 2) m = fmaxf(fmaxf(m, Sm[e]), Sm[e + 1]);
    m = xhalf_max(m);
    const int ib = i0 > 1 ? i0 : 1;
    f32x16 O;
    float l;
#pragma unroll 1
    for (int pass = 0; pass < 2; pass++) {
      const float mh = rbf(m), ml = rbf(m - mh), ms = mh + ml;
      q1.y = hh ? 0u : pk2(-mh, -ml);
      KF kn = ldk(7);
      VF v8 = ldv(8), v0 = ldv(0);
      f32x16 S[2];
      S[1] = qk(kn);
      kn = ldk(6);
      VF vn = ldv(7);
      {
        f32x16 p0, p8;
#pragma unroll
        for (int e = 0; e < 16; e++) {
          const float p = __builtin_amdgcn_exp2f(Sm[e] - ms);
          const bool cnd = (e & 3) + 8 * (e >> 2) <= thr;
          p8[e] = cnd ? p : 0.f; p0[e] = cnd ? 0.f : p;
        }
        O = f32x16{};
        O = mfma32(v8.a, pk8(p8, 0), O); O = mfma32(v8.b, pk8(p8, 8), O);
        if (i0 == 0) { O = mfma32(v0.a, pk8(p0, 0), O); O = mfma32(v0.b, pk8(p0, 8), O); }
      }
      bool go = true;
      sfor<7>([&](auto J) {
        constexpr int i = 7 - J;
        if (go && i >= ib) {
          SCHED();
          const KF kc = kn; const VF vc = vn;
          f32x16& Sc = S[i & 1];
          f32x16 z = {};
          if constexpr (i > 1) z = mfma32(kc.a, q0, z);
          if constexpr (i > 2) kn = ldk(i - 2);
          if constexpr (i > 1) vn = ldv(i - 1);
          SCHED();
#pragma unroll
          for (int e = 0; e < 8; e++) Sc[e] = __builtin_amdgcn_exp2f(Sc[e]);
          SCHED();
          if constexpr (i > 1) S[(i - 1) & 1] = mfma32k8(kc.b, q1, z);
          const u32x4 pa = pk8(Sc, 0);
          SCHED();
#pragma unroll
          for (int e = 8; e < 12; e++) Sc[e] = __builtin_amdgcn_exp2f(Sc[e]);
          SCHED();
          O = mfma32(vc.a, pa, O);
#pragma unroll
          for (int e = 12; e < 16; e++) Sc[e] = __builtin_amdgcn_exp2f(Sc[e]);
          const u32x4 pb = pk8(Sc, 8);
          SCHED();
          O = mfma32(vc.b, pb, O);
        } else go = false;
      });
      SCHED();
      { const u32 lu = f2u(O[10]); auto r = __builtin_amdgcn_permlane32_swap(lu, lu, false, false); l = u2f(r[0]); }
      if (pass == 1 || __all(l > 8.67e-19f && l < 1.15e18f)) break;
      q1.y = 0u;
#pragma unroll 1
      for (int i = ib; i < 8; i++) {
        const f32x16 s = qk(ldk(i));
#pragma unroll
        for (int e = 0; e < 16; e += 2) m = fmaxf(fmaxf(m, s[e]), s[e + 1]);
      }
      m = xhalf_max(m);
    }
    STAMP(1, 3);
    const float inv = __builtin_amdgcn_rcpf(l);
    char* Og = ws + WS_O + (size_t)(16 * qb + 2 * w + (c >> 4)) * 18432 + (c & 15) * 16;
    *(GAS u32x4*)(Og + (h >> 1) * 1024 + (2 * (h & 1) + hh) * 256) =
        u32x4{pk2(O[0] * inv, O[1] * inv), pk2(O[2] * inv, O[3] * inv), pk2(O[4] * inv, O[5] * inv), pk2(O[6] * inv, O[7] * inv)};
    if (hh == 0) *(GAS u32*)(Og + 16384 + (h >> 2) * 256 + (h & 3) * 4) = pk2(O[8] * inv, O[9] * inv);
    STAMP(1, 4);
  } else {
    const int cw = wv, tid = threadIdx.x;
    const int ch0 = 64 * xcd + 2 * idx, ch2 = three ? 512 + h + 8 * qb : ch0;
    u32x2 pre[3]; u32x4 cur[3];
#pragma unroll
    for (int j = 0; j < 3; j++) {
      const int ch = j < 2 ? ch0 + j : ch2;
      const u16* kp = ssmk + (size_t)ch * 2048 + 8 * tid;
      cur[j] = *(const GAS u32x4*)kp;
      pre[j] = *(const GAS u32x2*)(kp - (tid ? 4 : 0));
    }
#pragma unroll
    for (int j = 0; j < 3; j++)
      dma16(ws + WS_UR + (size_t)(j < 2 ? ch0 + j : ch2) * 4096 + cw * 1024, lane * 16, sb + K2_C + j * CS_SZ + CS_U + cw * 1024);
    const int chA = ch0 + (cw >> 1);
    const u32 dA = ssmD[chA], dC = ssmD[ch2];
    if (tid == 0) st128(sb + K2_F, u32x4{0u, 0u, 0u, 0u});
    {
      const u32 za = lane < 24 ? cw * KC_STR + 16 * lane : CS_U + 4096 + 32 * cw + 16 * (lane & 1);
#pragma unroll
      for (int j = 0; j < 3; j++)
        if (lane < 26) st128(sb + K2_C + j * CS_SZ + za, u32x4{0u, 0u, 0u, 0u});
    }
#pragma unroll
    for (int j = 0; j < 3; j++) {
      const u32 cs = sb + K2_C + j * CS_SZ + 384 + 16 * tid;
      const u32 w0 = tid ? pre[j].x : 0u, w1 = tid ? pre[j].y : 0u, w2 = cur[j].x, w3 = cur[j].y, w4 = cur[j].z, w5 = cur[j].w;
      const u32 a01 = __builtin_amdgcn_alignbyte(w1, w0, 2), a12 = __builtin_amdgcn_alignbyte(w2, w1, 2), a23 = __builtin_amdgcn_alignbyte(w3, w2, 2),
                a34 = __builtin_amdgcn_alignbyte(w4, w3, 2), a45 = __builtin_amdgcn_alignbyte(w5, w4, 2);
      st128(cs, u32x4{a01, a12, a23, a34});
      st128(cs + KC_STR, u32x4{w1, w2, w3, w4});
      st128(cs + 2 * KC_STR, u32x4{a12, a23, a34, a45});
      st128(cs + 3 * KC_STR, cur[j]);
    }
    wait_vm<0>();
    wait_lgkm();
    STAMP(1, 2);
    bar();
    STAMP(1, 1);
    conv_channel<2>(sb + K2_C + (cw >> 1) * CS_SZ, sb + K2_R + (cw >> 1) * 8192, sb + K2_F + (cw >> 1) * 4, cw & 1, lane, lo2f(dA), ws + WS_Y2 + (size_t)chA * 4096);
    STAMP(1, 3);
    if (three) conv_channel<4>(sb + K2_C + 2 * CS_SZ, sb + K2_R + 16384, sb + K2_F + 8, cw, lane, lo2f(dC), ws + WS_Y2 + (size_t)ch2 * 4096);
    STAMP(1, 4);
  }
}

constexpr int K3_Y = 0, K3_X = 9216, K3_W = K3_X + 4 * 3024, K3_LDS = K3_W + 576 * 160;

__global__ __launch_bounds__(512, 2) void k3(const u16* __restrict__ x, const u16* __restrict__ wo, const char* __restrict__ ws, u16* __restrict__ out) {
  extern __shared__ __attribute__((aligned(16))) char smem[];
  const u32 sb = (u32)(size_t)smem;
  const int b = blockIdx.x, xcd = b & 7, idx = b >> 3;
  const int rt = 8 * (xcd >> 1) + (idx & 7), ct = 4 * (xcd & 1) + (idx >> 3);
  const int lane = threadIdx.x & 63;
  const u32 w = rfl(threadIdx.x >> 6);
  STAMP(2, 0);
  if (w >= 4) {
    const u32 lw = w - 4;
    const int r6 = lane / 10, c10 = lane - 10 * r6;
    const u32 vo = (6 * lw + r6) * 1152 + 16 * (c10 > 8 ? 8 : c10);
    const char* wb = (const char*)(wo + 72 * ct);
    sfor<3>([&](auto I) {
      const int j = 4 * I + lw;
      if (j < 9) dma16(ws + WS_Y2 + (size_t)(72 * ct + 8 * j) * 4096 + 128 * rt, (lane >> 3) * 4096 + 16 * (lane & 7), sb + K3_Y + j * 1024);
    });
    sfor<12>([&](auto I) { dma16n<60>(wb + I * 24 * 1152, vo, sb + K3_W + (4 * I + lw) * 960); });
    wait_vm<12>(); bar();
    wait_vm<6>(); bar();
    sfor<12>([&](auto I) { dma16n<60>(wb + (12 + I) * 24 * 1152, vo, sb + K3_W + (4 * (12 + I) + lw) * 960); });
    wait_vm<12>(); bar();
    wait_vm<6>(); bar();
    wait_vm<0>(); bar();
    return;
  }
  const int g = lane >> 4, l15 = lane & 15;
  const int r7 = lane / 9, c9 = lane - 9 * r7;
  const u32 xw = sb + K3_X + w * 3024;
  sfor<3>([&](auto I) { int r = 7 * I + r7; r = r > 15 ? 15 : r; dma16n<63>(x + (size_t)(64 * rt + 16 * w) * 576 + 72 * ct, r * 1152 + 16 * c9, xw + I * 1008); });
  float gt;
  asm volatile("global_load_dword %0, %1, %2" : "=v"(gt) : "v"((u32)(64 * rt + 16 * w + l15) * 4), "s"(ws + WS_G));
  u32x4 of[18];
  const char* ob = ws + WS_O + (size_t)(4 * rt + w) * 18432;
  sfor<18>([&](auto F) { gl128<(F & 3) * 1024>(of[F], lane * 16, ob + (F >> 2) * 4096); });
  const int q = l15 >> 2;
  asm volatile("s_waitcnt vmcnt(18)" : "+v"(gt)::"memory");
  bar();
  STAMP(2, 1);
  f32x4 acc[5], tb[5];
  const u32 xa = xw + l15 * 144 + 8 * g;
  {
    const float g1 = rbf(1.f - gt);
    const u32 ya = sb + K3_Y + (4 * g + q) * 128 + 32 * w + 8 * (lane & 3);
    sfor<5>([&](auto NT) {
      const u32x2 y = tr_read(ya + NT * 2048), xv = lds64(xa + NT * 32);
      tb[NT] = f32x4{g1 * lo2f(xv.x) * lo2f(y.x), g1 * hi2f(xv.x) * hi2f(y.x), g1 * lo2f(xv.y) * lo2f(y.y), g1 * hi2f(xv.y) * hi2f(y.y)};
      acc[NT] = f32x4{0.f, 0.f, 0.f, 0.f};
    });
  }
  const u32 wa = sb + K3_W + (18 * (g >> 1) + 4 * (g & 1) + q) * 160 + 8 * (lane & 3);
  const u32 wa2 = sb + K3_W + ((4 * g + (q >> 1)) * 18 + 16 + (q & 1)) * 160 + 8 * (lane & 3);
  u32x4 wf[2][5];
  auto rd = [&](auto KS) {
    constexpr int ks = KS;
    sfor<5>([&](auto NT) {
      if constexpr (ks < 16) wf[ks & 1][NT] = cat(tr_read(wa + ks * 5760 + NT * 32), tr_read(wa + ks * 5760 + 1280 + NT * 32));
      else wf[ks & 1][NT] = cat(tr_read(wa2 + (ks - 16) * 46080 + NT * 32), tr_read(wa2 + (ks - 16) * 46080 + 5760 + NT * 32));
    });
  };
  auto mm = [&](auto KS) { sfor<5>([&](auto NT) { acc[NT] = mfma16(wf[KS & 1][NT], of[KS], acc[NT]); }); };
  auto tie = [&](auto Q) {
    constexpr int f = 4 * Q;
    asm volatile("s_waitcnt vmcnt(%4)" : "+v"(of[f]), "+v"(of[f + 1]), "+v"(of[f + 2]), "+v"(of[f + 3]) : "i"(f == 12 ? 0 : 14 - f) : "memory");
    if constexpr (f == 12) asm volatile("" : "+v"(of[16]), "+v"(of[17]));
    bar();
  };
  SCHED();
  tie(IC(0));
  STAMP(2, 2);
  rd(IC(0));
  sfor<18>([&](auto KS) {
    constexpr int ks = KS;
    SCHED();
    if constexpr (ks % 4 == 3 && ks < 12) tie(IC(ks / 4 + 1));
    if constexpr (ks < 17) rd(IC(ks + 1));
    mm(KS);
    sfor<5>([&](auto) { __builtin_amdgcn_sched_group_barrier(0x8, 1, 0); __builtin_amdgcn_sched_group_barrier(0x100, 2, 0); });
  });
  SCHED();
  STAMP(2, 3);
  sfor<5>([&](auto NT) {
    constexpr int nt = NT;
    if (nt < 4 || g < 2)
      st64(xa + nt * 32, u32x2{pk2(fmaf(gt, acc[nt][0], tb[nt][0]), fmaf(gt, acc[nt][1], tb[nt][1])), pk2(fmaf(gt, acc[nt][2], tb[nt][2]), fmaf(gt, acc[nt][3], tb[nt][3]))});
  });
  wait_lgkm();
  char* orow = (char*)(out + (size_t)(64 * rt + 16 * w) * 576 + 72 * ct);
#pragma unroll
  for (int it = 0; it < 3; it++) {
    const int r = 7 * it + r7;
    if (lane < 63 && r < 16) *(GAS u32x4*)(orow + (u32)(r * 1152 + 16 * c9)) = lds128(xw + r * 144 + 16 * c9);
  }
  STAMP(2, 4);
}

extern "C" void kernel_launch(void* const* d_in, const int* in_sizes, int n_in, void* d_out, int out_size, void* d_ws, size_t ws_size,
                              hipStream_t stream) {
  const u16* x = (const u16*)d_in[0]; const u16* wqkv = (const u16*)d_in[1]; const u16* wo = (const u16*)d_in[2];
  const u16* Bk = (const u16*)d_in[4]; const u16* Bv = (const u16*)d_in[5]; const u16* shk = (const u16*)d_in[6];
  const u16* ssmk = (const u16*)d_in[7]; const u16* ssmD = (const u16*)d_in[8]; const u16* gw = (const u16*)d_in[9];
  (void)hipFuncSetAttribute((const void*)k1, hipFuncAttributeMaxDynamicSharedMemorySize, K1_LDS);
  (void)hipFuncSetAttribute((const void*)k2, hipFuncAttributeMaxDynamicSharedMemorySize, K2_LDS);
  (void)hipFuncSetAttribute((const void*)k3, hipFuncAttributeMaxDynamicSharedMemorySize, K3_LDS);
  k1<<<256, 768, K1_LDS, stream>>>(x, wqkv, Bk, Bv, shk, gw, ssmk, (char*)d_ws);
  k2<<<256, 768, K2_LDS, stream>>>(ssmk, ssmD, wo, (char*)d_ws);
  k3<<<256, 512, K3_LDS, stream>>>(x, wo, (const char*)d_ws, (u16*)d_out);
}
```

```cpp
#include <hip/hip_runtime.h>
#include <hip/hip_bf16.h>
#include <utility>

typedef unsigned short u16;
typedef unsigned int u32;
typedef u32 u32x2 __attribute__((ext_vector_type(2)));
typedef u32 u32x4 __attribute__((ext_vector_type(4)));
typedef short s16x4 __attribute__((ext_vector_type(4)));
typedef __bf16 bf16x8 __attribute__((ext_vector_type(8)));
typedef __bf16 bf16x2 __attribute__((ext_vector_type(2)));
typedef float f32x2 __attribute__((ext_vector_type(2)));
typedef float f32x4 __attribute__((ext_vector_type(4)));
typedef float f32x16 __attribute__((ext_vector_type(16)));
#define LAS __attribute__((address_space(3)))
#define GAS __attribute__((address_space(1)))
#define DEVI __device__ __forceinline__
#define IC(n) std::integral_constant<int, (n)>{}

constexpr size_t WS_QKV = 0;
constexpr size_t WS_O   = WS_QKV + 96ull * 2048 * 40;
constexpr size_t WS_UR  = WS_O + 32ull * 2048 * 40;
constexpr size_t WS_Y2  = WS_UR + 576ull * 4096;
constexpr size_t WS_G   = WS_Y2 + 576ull * 4096;

template <typename F, int... I> DEVI void sfor_impl(F&& f, std::integer_sequence<int, I...>) { (f(std::integral_constant<int, I>{}), ...); }
template <int N, typename F> DEVI void sfor(F&& f) { sfor_impl(f, std::make_integer_sequence<int, N>{}); }

DEVI u32 pk2(float a, float b) { f32x2 v = {a, b}; return __builtin_bit_cast(u32, __builtin_convertvector(v, bf16x2)); }
DEVI u32 f2u(float a) { return __builtin_bit_cast(u32, a); }
DEVI float u2f(u32 a) { return __builtin_bit_cast(float, a); }
DEVI float lo2f(u32 w) { return __builtin_bit_cast(float, w << 16); }
DEVI float hi2f(u32 w) { return __builtin_bit_cast(float, w & 0xffff0000u); }
DEVI float rbf(float a) { return lo2f(pk2(a, a)); }
DEVI f32x4 mfma16(u32x4 a, u32x4 b, f32x4 c) { return __builtin_amdgcn_mfma_f32_16x16x32_bf16(__builtin_bit_cast(bf16x8, a), __builtin_bit_cast(bf16x8, b), c, 0, 0, 0); }
DEVI f32x16 mfma32(u32x4 a, u32x4 b, f32x16 c) { return __builtin_amdgcn_mfma_f32_32x32x16_bf16(__builtin_bit_cast(bf16x8, a), __builtin_bit_cast(bf16x8, b), c, 0, 0, 0); }
DEVI f32x16 mfma32k8(u32x2 a, u32x2 b, f32x16 c) { return __builtin_amdgcn_mfma_f32_32x32x8bf16_1k(__builtin_bit_cast(s16x4, a), __builtin_bit_cast(s16x4, b), c, 0, 0, 0); }
DEVI u32x2 tr_read(u32 addr) { return __builtin_bit_cast(u32x2, __builtin_amdgcn_ds_read_tr16_b64_v4i16((LAS s16x4*)addr)); }
DEVI u32x4 lds128(u32 addr) { return *(LAS u32x4*)addr; }
DEVI u32x2 lds64(u32 addr) { return *(LAS u32x2*)addr; }
DEVI u32 lds32(u32 addr) { return *(LAS u32*)addr; }
DEVI void st128(u32 addr, u32x4 v) { *(LAS u32x4*)addr = v; }
DEVI void st64(u32 addr, u32x2 v) { *(LAS u32x2*)addr = v; }
DEVI void st32(u32 addr, u32 v) { *(LAS u32*)addr = v; }
DEVI u32x4 cat(u32x2 a, u32x2 b) { return u32x4{a.x, a.y, b.x, b.y}; }
DEVI float vfma(float a, float b, float c) { float d; asm("v_fma_f32 %0, %1, %2, %3" : "=v"(d) : "v"(a), "v"(b), "v"(c)); return d; }
DEVI float vmul(float a, float b) { float d; asm("v_mul_f32 %0, %1, %2" : "=v"(d) : "v"(a), "v"(b)); return d; }
DEVI float vadd(float a, float b) { float d; asm("v_add_f32 %0, %1, %2" : "=v"(d) : "v"(a), "v"(b)); return d; }
DEVI f32x4 vadd4(f32x4 a, f32x4 b) { return f32x4{vadd(a[0], b[0]), vadd(a[1], b[1]), vadd(a[2], b[2]), vadd(a[3], b[3])}; }
DEVI u32 opaque(u32 v) { asm volatile("" : "+v"(v)); return v; }
DEVI void dma16(const void* sbase, u32 voff, u32 ldsbase) {
  asm volatile("s_mov_b32 m0, %2\n\ts_nop 0\n\tglobal_load_lds_dwordx4 %0, %1" ::"v"(voff), "s"(sbase), "s"(ldsbase) : "memory");
}
template <int NL> DEVI void dma16n(const void* sbase, u32 voff, u32 ldsbase) {
  asm volatile("s_mov_b32 m0, %2\n\ts_mov_b32 exec_hi, %3\n\tglobal_load_lds_dwordx4 %0, %1\n\ts_mov_b32 exec_hi, -1" ::"v"(voff), "s"(sbase), "s"(ldsbase),
               "i"((1u << (NL - 32)) - 1u) : "memory");
}
template <int OFF> DEVI void gl128(u32x4& d, u32 voff, const void* sbase) { asm volatile("global_load_dwordx4 %0, %1, %2 offset:%3" : "=v"(d) : "v"(voff), "s"(sbase), "i"(OFF)); }
template <int OFF> DEVI void gl64(u32x2& d, u32 voff, const void* sbase) { asm volatile("global_load_dwordx2 %0, %1, %2 offset:%3" : "=v"(d) : "v"(voff), "s"(sbase), "i"(OFF)); }
#define STPOL " sc1"
DEVI void gst128(u32x4 v, u32 voff, void* sbase) { asm volatile("global_store_dwordx4 %0, %1, %2" STPOL "\n\ts_nop 1" ::"v"(voff), "v"(v), "s"(sbase) : "memory"); }
DEVI void gst64(u32x2 v, u32 voff, void* sbase) { asm volatile("global_store_dwordx2 %0, %1, %2" STPOL ::"v"(voff), "v"(v), "s"(sbase) : "memory"); }
#define CODE_PF(from, to, ck, step, lds)                                                                                              \
  do {                                                                                                                                \
    u32 lo_, hi_, len_;                                                                                                               \
    asm volatile("s_getpc_b64 s[20:21]\n\ts_add_u32 %0, s20, " from "@rel32@lo+4\n\ts_addc_u32 %1, s21, " from "@rel32@hi+12\n\ts_mov_b32 %2, " to "-" from \
                 : "=s"(lo_), "=s"(hi_), "=s"(len_)::"s20", "s21", "scc");                                                            \
    const char* code_ = (const char*)(((unsigned long long)hi_ << 32) | lo_);                                                         \
    for (u32 o_ = (ck) * 1024; o_ < len_; o_ += (step) * 1024) { const u32 a_ = o_ + (threadIdx.x & 63) * 16; dma16(code_, a_ < len_ ? a_ : 0u, lds); } \
  } while (0)
template <int N> DEVI void wait_vm() { asm volatile("s_waitcnt vmcnt(%0)" ::"i"(N > 63 ? 63 : N) : "memory"); }
DEVI void wait_lgkm() { asm volatile("s_waitcnt lgkmcnt(0)" ::: "memory"); }
DEVI void bar() { asm volatile("s_barrier" ::: "memory"); }
DEVI u32 rfl(u32 v) { return __builtin_amdgcn_readfirstlane(v); }
#define SCHED() __builtin_amdgcn_sched_barrier(0)
#define STAMP(k, i)
#define STAMP_ARG
#define LSTAMP(i)
#define LSTORE()
#define STAMP_PASS

constexpr float QSCALE = 0.23570226039551584f * 1.4426950408889634f;

constexpr int K1_SLOT = 32768, K1_WOFF = 16384, K1_GW = 4 * K1_SLOT, K1_TILE = 17408;
constexpr int K1_PT = K1_GW + 2048, K1_FL = K1_PT + K1_TILE, K1_DUMMY = K1_FL + 256, K1_LDS = K1_DUMMY + 1024;
static_assert(K1_LDS <= 163840, "lds");
DEVI void lds_inc(u32 a, int lane) { if (lane == 0) __hip_atomic_fetch_add((LAS u32*)a, 1u, __ATOMIC_RELAXED, __HIP_MEMORY_SCOPE_WORKGROUP); }
DEVI u32 lds_peek(u32 a) { return __hip_atomic_load((LAS u32*)a, __ATOMIC_RELAXED, __HIP_MEMORY_SCOPE_WORKGROUP); }
DEVI void prog_set(u32 a, u32 n) { __hip_atomic_store((LAS u32*)a, n, __ATOMIC_RELAXED, __HIP_MEMORY_SCOPE_WORKGROUP); }
DEVI void prog_wait(u32 a, u32 n) { while (__builtin_amdgcn_ballot_w64(lds_peek(a) < n)) __builtin_amdgcn_s_sleep(1); asm volatile("" ::: "memory"); }
DEVI void lds_wait(u32 a, u32 n) { while (rfl(lds_peek(a)) < n) __builtin_amdgcn_s_sleep(1); asm volatile("" ::: "memory"); }
DEVI int k1_line(int hg, int e) { return hg == 0 ? (e < 5 ? e : (e < 10 ? e + 4 : e + 8)) : (e < 4 ? e + 5 : (e < 8 ? e + 10 : (e < 13 ? e + 14 : 26))); }

DEVI void prep_issue(const u16* Bk, const u16* Bv, const u16* shk, int row0, int col, u32 tb, u32 pw, int lane, u32x4& sh) {
  const int r8 = lane >> 3, c8 = lane & 7;
  sfor<2>([&](auto I) {
    const int j = 2 * pw + I;
    int row = row0 + 8 * j + r8 - 3; row = row < 0 ? 0 : row;
    dma16(Bk + col, row * 1152 + 16 * (c8 ^ j), tb + 1024 * j);
    dma16(Bv + col, (row0 + 8 * j + r8) * 1152 + 16 * (c8 ^ j), tb + 9216 + 1024 * j);
  });
  dma16(Bk + col, (row0 + 61 + (r8 > 2 ? 2 : r8)) * 1152 + 16 * c8, tb + 8192);
  gl128<0>(sh, 8 * col + 16 * (8 * pw + r8), shk);
}
DEVI void prep_compute(int row0, int col, u32 tb, u32 pw, int lane, u32x4 sh, char* ws) {
  const int c8 = lane & 7, cp = 8 * pw + (lane >> 3);
  const u32 o0 = tb + 1024 * c8 + 16 * ((cp >> 2) ^ c8) + 4 * (cp & 3), o1 = tb + 1024 * c8 + 16 * ((cp >> 2) ^ ((c8 + 1) & 7)) + 4 * (cp & 3);
  row0 += 8 * c8;
  u32 kk[11], vv[8];
#pragma unroll
  for (int i = 0; i < 11; i++) kk[i] = lds32((i < 8 ? o0 : o1) + 128 * i);
#pragma unroll
  for (int i = 0; i < 8; i++) vv[i] = lds32(o0 + 9216 + 128 * i);
  if (row0 == 0) { kk[0] = 0; kk[1] = 0; kk[2] = 0; }
  const u32 go = 2 * cp * 4096 + 2 * (2040 - row0);
  char* dst = ws + WS_UR + (size_t)col * 4096;
  float r[8];
  {
    const float a0 = lo2f(sh.x), a1 = hi2f(sh.x), a2 = lo2f(sh.y), a3 = hi2f(sh.y);
#pragma unroll
    for (int i = 0; i < 8; i++) r[i] = vmul(vfma(lo2f(kk[i + 3]), a0, vfma(lo2f(kk[i + 2]), a1, vfma(lo2f(kk[i + 1]), a2, vmul(lo2f(kk[i]), a3)))), lo2f(vv[i]));
    gst128(u32x4{pk2(r[7], r[6]), pk2(r[5], r[4]), pk2(r[3], r[2]), pk2(r[1], r[0])}, go, dst);
  }
  {
    const float a0 = lo2f(sh.z), a1 = hi2f(sh.z), a2 = lo2f(sh.w), a3 = hi2f(sh.w);
#pragma unroll
    for (int i = 0; i < 8; i++) r[i] = vmul(vfma(hi2f(kk[i + 3]), a0, vfma(hi2f(kk[i + 2]), a1, vfma(hi2f(kk[i + 1]), a2, vmul(hi2f(kk[i]), a3)))), hi2f(vv[i]));
    gst128(u32x4{pk2(r[7], r[6]), pk2(r[5], r[4]), pk2(r[3], r[2]), pk2(r[1], r[0])}, go, dst + 4096);
  }
}

extern "C" __global__ __launch_bounds__(512, 2) void gm_k1(const u16* __restrict__ x, const u16* __restrict__ wqkv, const u16* __restrict__ Bk,
                                             const u16* __restrict__ Bv, const u16* __restrict__ shk, const u16* __restrict__ gw,
                                             const u16* __restrict__ ssmk, char* __restrict__ ws) {
  extern __shared__ __attribute__((aligned(16))) char smem[];
  const u32 sb = (u32)(size_t)(LAS char*)smem;
  const int b = blockIdx.x, xcd = b & 7, idx = b >> 3;
  const int hg = xcd & 1, ri = idx & 3, rb = 4 * (xcd >> 1) + ri, jt = idx >> 2;
  const int lane = threadIdx.x & 63;
  const u32 w = rfl(threadIdx.x >> 6);
  const int r8 = lane >> 3, c8 = lane & 7;
#define PACE
#define ROT 2
  const int rot = ROT == 0 ? 0 : ROT == 1 ? (jt + 2 * ri) % 9 : ROT == 2 ? ((jt + ri) % 3) * 3 : ((jt + ri) & 1) * 4;
  STAMP(0, 0);
  asm volatile("" ::"s"(x), "s"(wqkv), "s"(Bk), "s"(Bv), "s"(shk), "s"(gw), "s"(ssmk), "s"(ws));
  if (__builtin_expect(jt == 7, 0)) {
    const int th = w >> 2, row0 = th ? 256 * xcd + 64 * ri : 64 * idx, col = th ? 512 : 64 * xcd;
    CODE_PF("gm_k1", "gm_k2", 8 * ri + w, 32, sb + K1_DUMMY);
    u32x4 sh;
    prep_issue(Bk, Bv, shk, row0, col, sb + th * K1_TILE, w & 3, lane, sh);
    asm volatile("s_waitcnt vmcnt(0)" : "+v"(sh)::"memory");
    bar();
    prep_compute(row0, col, sb + th * K1_TILE, w & 3, lane, sh, ws);
    const char* kp = (const char*)ssmk + (size_t)(64 * xcd + 16 * ri + 2 * w) * 4096;
    sfor<8>([&](auto I) { dma16(kp + I * 1024, lane * 16, sb + K1_DUMMY); });
    dma16((const char*)ssmk + (size_t)(512 + 8 * xcd + 2 * ri) * 4096 + w * 1024, lane * 16, sb + K1_DUMMY);
    CODE_PF("gm_k2", "gm_end", 8 * ri + w, 32, sb + K1_DUMMY);
    wait_vm<0>();
    STAMP(0, 4);
    return;
  }
  if (w >= 4) {
    const int cw = w - 4;
    if (cw == 0) { st32(sb + K1_FL + 4 * lane, 0u); wait_lgkm(); }
    const int g = lane >> 4, l15 = lane & 15, wm = cw & 1, wn = cw >> 1;
    const u32 xra = sb + (64 * wm + l15) * 128 + 16 * (g ^ (l15 & 7));
    const u32 xb[2][2] = {{opaque(xra), opaque(xra + 2 * K1_SLOT)}, {opaque(xra ^ 64), opaque((xra ^ 64) + 2 * K1_SLOT)}};
    u32x4 wf[2][4], xf[2][4];
    f32x4 acc[4][4];
    if (hg == 1 && jt == 6 && wn == 1) {
#pragma unroll
      for (int i = 0; i < 4; i++) acc[i][0] = f32x4{0.f, 0.f, 0.f, 0.f};
#pragma unroll 1
      for (int c = 0; c < 9; c++) {
        bar();
        const int ca = c + rot >= 9 ? c + rot - 9 : c + rot;
#pragma unroll
        for (int ks = 0; ks < 2; ks++) {
          const u32x4 gv = lds128(sb + K1_GW + ca * 128 + ks * 64 + 16 * g);
#pragma unroll
          for (int i = 0; i < 4; i++) acc[i][0] = mfma16(gv, lds128(xb[ks][0] + (c & 3) * K1_SLOT + 2048 * i), acc[i][0]);
        }
        wait_lgkm();
      }
      if (g == 0) {
        float* G = (float*)(ws + WS_G) + rb * 128 + 64 * wm + l15;
#pragma unroll
        for (int i = 0; i < 4; i++) { const float p = rbf(acc[i][0][0]); G[16 * i] = rbf(1.f / (1.f + __expf(-p))); }
      }
      return;
    }
    u32 wb[4][2];
    {
      const int q = l15 >> 2, p = lane & 3, f = q | ((g & 1) << 2);
#pragma unroll
      for (int nt = 0; nt < 4; nt++) {
        const u32 a = sb + K1_WOFF + (8 * g + q) * 256 + (((8 * wn + 2 * nt + (p >> 1)) ^ (f << 1)) << 4) + 8 * (p & 1);
        wb[nt][0] = opaque(a); wb[nt][1] = opaque(a + 2 * K1_SLOT);
      }
    }
    auto rd = [&](auto KS, auto C) {
      constexpr int ks = KS, c = C, so = (c & 1) * K1_SLOT, hi = (c >> 1) & 1;
      sfor<4>([&](auto I) {
        xf[ks][I] = lds128(xb[ks][hi] + so + 2048 * I);
        wf[ks][I] = cat(tr_read(wb[I][hi] + so + ks * 8192), tr_read(wb[I][hi] + so + ks * 8192 + 1024));
      });
    };
    auto mm = [&](auto KS, auto FIRST) {
      constexpr int ks = KS;
      sfor<16>([&](auto I) {
        constexpr int mo[16] = {0, 1, 0, 1, 2, 2, 0, 1, 2, 3, 3, 3, 0, 1, 2, 3}, no[16] = {0, 0, 1, 1, 0, 1, 2, 2, 2, 0, 1, 2, 3, 3, 3, 3};
        constexpr int mt = mo[I], nt = no[I];
        acc[mt][nt] = mfma16(xf[ks][mt], wf[ks][nt], FIRST ? f32x4{0.f, 0.f, 0.f, 0.f} : acc[mt][nt]);
      });
    };
    auto ilv = [&]() {
      sfor<12>([&](auto) { __builtin_amdgcn_sched_group_barrier(0x8, 1, 0); __builtin_amdgcn_sched_group_barrier(0x100, 1, 0); });
      __builtin_amdgcn_sched_group_barrier(0x8, 4, 0);
    };
    const int ln = k1_line(hg, 2 * jt + wn);
    const float sc = ln < 9 ? QSCALE : 1.f;
    char* qo = ws + WS_QKV + (size_t)((rb >> 1) * 1728 + 64 * ln) * 512 + (rb & 1) * 256 + wm * 128;
    SCHED();
    __builtin_amdgcn_s_setprio(2);
    bar();
    STAMP(0, 1);
    rd(IC(0), IC(0));
    sfor<9>([&](auto C) {
      constexpr int c = C;
      SCHED();
      rd(IC(1), C); mm(IC(0), IC(c == 0)); ilv();
      SCHED();
      if constexpr (c < 8) {
        LSTAMP(2 * c);
        bar();
        LSTAMP(2 * c + 1);
        SCHED();
        rd(IC(0), IC(c + 1)); mm(IC(1), IC(0)); ilv();
      }
    });
    STAMP(0, 3);
    const u32 stg = sb + K1_SLOT + cw * 9216;
    const u32 sa = stg + l15 * 144 + 8 * g, ra = stg + r8 * 144 + 16 * c8, go = r8 * 512 + 16 * c8;
    u32x4 t[4][2];
    sfor<6>([&](auto NT) {
      constexpr int nt = NT;
      if constexpr (nt < 4) sfor<4>([&](auto M) { acc[M][nt] = mfma16(xf[1][M], wf[1][nt], acc[M][nt]); });
      SCHED();
      if constexpr (nt > 0 && nt < 5) {
        sfor<4>([&](auto M) {
          const f32x4 v = acc[M][nt - 1] * sc;
          st64(sa + (nt - 1) * 2304 + 32 * M, u32x2{pk2(v[0], v[1]), pk2(v[2], v[3])});
        });
        t[nt - 1][0] = lds128(ra + (nt - 1) * 2304); t[nt - 1][1] = lds128(ra + (nt - 1) * 2304 + 1152);
      }
      if constexpr (nt > 1) { gst128(t[nt - 2][0], go, qo + (nt - 2) * 8192); gst128(t[nt - 2][1], go, qo + (nt - 2) * 8192 + 4096); }
      SCHED();
    });
    STAMP(0, 4);
    LSTORE();
    return;
  }
  const u32 lw = w;
  {
    const u32 xo = (32 * lw + r8) * 1152 + 16 * (c8 ^ r8);
    const char* xb = (const char*)(x + (size_t)rb * 128 * 576);
    u32 wo[2];
#pragma unroll
    for (int i = 0; i < 2; i++) {
      const int ch = (lane & 15) ^ (((lane >> 4) | (i << 2)) << 1);
      wo[i] = ((lane >> 4) + 16 * lw) * 3456 + 128 * k1_line(hg, 2 * jt + (ch >> 3)) + 16 * (ch & 7);
    }
    const char* wb = (const char*)wqkv;
    auto issue = [&](int c) {
      const u32 sl = sb + (c & 3) * K1_SLOT;
      c += rot; c = c >= 9 ? c - 9 : c;
      sfor<4>([&](auto I) { dma16(xb + c * 128 + I * 9216, xo, sl + (4 * lw + I) * 1024); });
      sfor<4>([&](auto I) { dma16(wb + (64 * c + 4 * I) * 3456, wo[I >> 1], sl + K1_WOFF + (4 * lw + I) * 1024); });
    };
    if (hg == 1 && jt == 6 && lw == 0) { dma16(gw, lane * 16, sb + K1_GW); dma16(gw, 128 + lane * 16, sb + K1_GW + 128); }
    issue(0);
#pragma unroll 1
    for (int n = 1; n < 8; n++) { issue(n); wait_vm<8>(); bar(); }
    issue(8);
    u32x4 sh;
    prep_issue(Bk, Bv, shk, 64 * idx, 64 * xcd, sb + K1_PT, lw, lane, sh);
    wait_vm<14>(); bar();
    wait_vm<6>(); bar();
    asm volatile("s_waitcnt vmcnt(0)" : "+v"(sh)::"memory");
    lds_inc(sb + K1_FL, lane);
    lds_wait(sb + K1_FL, 4);
    prep_compute(64 * idx, 64 * xcd, sb + K1_PT, lw, lane, sh, ws);
    STAMP(0, 4);
  }
}

constexpr int K2_K = 0, K2_V = 21760, K2_Q = 41536, K2_C = 51776;
constexpr int KC_STR = 4640, CS_U = 4 * KC_STR, CS_SZ = CS_U + 4096 + 128;
constexpr int K2_R = K2_C + 4 * CS_SZ;
constexpr int K2_F = K2_R + 16384, K2_DUMMY = K2_F + 128, K2_LDS = K2_DUMMY + 1024;
static_assert(K2_LDS <= 163840, "lds");

template <int NR> DEVI void conv_mfma(u32 cs, int rho0, int lane, f32x4 (&acc)[8]) {
  const int g = lane >> 4, l15 = lane & 15;
  u32 aa = cs + (l15 & 3) * KC_STR + 2 * (64 * (l15 >> 2) + 8 * g) + 64 * rho0;
  u32 ba = opaque(cs + CS_U + 2 * (1852 - 1792 - 4 * l15 + 8 * g) + 64 * rho0), bb = opaque(ba + 8);
  u32x4 A[8], G[8];
  sfor<5>([&](auto J) { constexpr int p = J < 4 ? J + 2 : 1; A[p] = lds128(aa + p * 512); G[p] = cat(lds64(ba + (7 - p) * 512), lds64(bb + (7 - p) * 512)); SCHED(); });
#pragma unroll 1
  for (int rho = 0; rho < NR; rho++) {
    constexpr int pa[36] = {2, 2, 3, 3, 2, 4, 3, 4, 2, 5, 1, 1, 2, 1, 3, 1, 4, 1, 5, 1, 6, 2, 0, 3, 0, 4, 0, 5, 0, 1, 0, 6, 0, 7, 0, 0};
    constexpr int pe[36] = {2, 3, 2, 3, 4, 2, 4, 3, 5, 2, 1, 2, 1, 3, 1, 4, 1, 5, 1, 6, 1, 0, 2, 0, 3, 0, 4, 0, 5, 0, 1, 0, 6, 0, 7, 0};
    constexpr int rdc[36] = {18, 19, 20, 21, 22, 23, 0, 1, 2, -1, -1, -1, -1, -1, -1, -1, -1, -1, -1, -1, -1, 6, 7, 8, 9, 10, 11, 12, 13, 14, 15, 16, 17, 3, 4, 5};
    sfor<36>([&](auto I) {
      constexpr int i = I, r = rdc[i], x = r / 3;
      if constexpr (i == 9) { aa = opaque(aa + 64); ba = opaque(ba + 64); bb = opaque(bb + 64); }
      acc[pa[i] + pe[i]] = mfma16(A[pa[i]], G[pe[i]], acc[pa[i] + pe[i]]);
      if constexpr (r >= 0) {
        if constexpr (r % 3 == 0) A[x] = lds128(aa + x * 512);
        else if constexpr (r % 3 == 1) { const u32x2 t = lds64(ba + (7 - x) * 512); G[x].x = t.x; G[x].y = t.y; }
        else { const u32x2 t = lds64(bb + (7 - x) * 512); G[x].z = t.x; G[x].w = t.y; }
      }
      SCHED();
    });
  }
  SCHED();
}
DEVI void conv_out(f32x4 y, u32x2 u, float Dv, u32 off, char* ydst) {
  const float y0 = vfma(hi2f(u.y), Dv, y[0]), y1 = vfma(lo2f(u.y), Dv, y[1]), y2 = vfma(hi2f(u.x), Dv, y[2]), y3 = vfma(lo2f(u.x), Dv, y[3]);
  gst64(u32x2{pk2(y0, y1), pk2(y2, y3)}, off, ydst);
}

DEVI float xhalf_max(float m) { const u32 mu = f2u(m); auto r = __builtin_amdgcn_permlane32_swap(mu, mu, false, false); return fmaxf(u2f(r[0]), u2f(r[1])); }

#define ATT_PRIO 1
#define CONV_PRIO 0
#define QDELAY 5
extern "C" __global__ __launch_bounds__(768, 3) void gm_k2(const u16* __restrict__ ssmk, const u16* __restrict__ ssmD, const u16* __restrict__ wo, char* __restrict__ ws) {
  extern __shared__ __attribute__((aligned(16))) char smem[];
  const u32 sb = (u32)(size_t)(LAS char*)smem;
  const int b = blockIdx.x, xcd = b & 7, idx = b >> 3;
  const int qb = idx & 7, h = 4 * xcd + (idx >> 3);
  const int lane = threadIdx.x & 63;
  const u32 wv = rfl(threadIdx.x >> 6);
  const bool four = qb == 0;
  STAMP(1, 0);
  if (threadIdx.x < 32) st32(sb + K2_F + 4 * threadIdx.x, 0u);
  asm volatile("" ::"s"(ssmk), "s"(ssmD), "s"(wo), "s"(ws));
  wait_lgkm();
  bar();
  if (__builtin_expect(wv >= (four ? 8u : 4u), 0)) {
    __builtin_amdgcn_s_setprio(ATT_PRIO);
    const int c = lane & 31, hh = lane >> 5, q4 = (lane & 15) >> 2;
    int w = wv - 4;
    __builtin_amdgcn_s_sleep(QDELAY);
    const char* Ag = ws + WS_QKV + (size_t)(qb * 1728 + 18 * h) * 512;
    const char* Kg = Ag + 576 * 512 - (qb ? 884736 : 0);
    const u32 lo = (lane >> 5) * (qb ? 884736 : 0) + (lane & 31) * 16;
#pragma unroll 1
    for (int s = four ? w - 4 : w; s <= w; s += 4) {
      dma16(Ag + s * 1024, lane * 16, sb + K2_Q + s * 1024);
      dma16(Kg + s * 512, lo, sb + K2_K + s * 1088); dma16(Kg + s * 512 + 4096, lo, sb + K2_K + (8 + s) * 1088);
      const char* eg = s < 4 ? Kg + (s < 2 ? 16 + s : 590 + s) * 512 : Ag + 8192;
      dma16(eg, s < 4 ? lo : lane * 16, sb + (s < 2 ? K2_K + (16 + s) * 1088 : (s < 4 ? K2_V + (14 + s) * 1040 : K2_Q + 8192)));
    }
    const u32 kcol = 64 * w + (lane & 16) * 2 + 16 * (lane & 1) + 4 * (lane & 2);
    u32 ka = opaque(sb + K2_K + (8 * hh + q4) * 1088 + kcol), ka2 = opaque(sb + K2_K + (16 + q4) * 1088 + kcol);
    u32 va = opaque(sb + K2_V + c * 1040 + 64 * w + 16 * hh);
    u32 qa = opaque(sb + K2_Q + q4 * 512 + 64 * w + (lane & 16) * 2 + 8 * (lane & 3)), qa0 = opaque(qa + 4096 * hh);
    const int thr = c - 8 * hh;
    u32 msk[8], nmk[8];
    unsigned long long cnd[16];
#pragma unroll
    for (int e = 0; e < 16; e++) cnd[e] = __builtin_amdgcn_ballot_w64(16 * (e >> 3) + (e & 7) <= thr);
#pragma unroll
    for (int j = 0; j < 8; j++) { const int k0 = 16 * (j >> 2) + 2 * (j & 3); msk[j] = opaque(k0 + 1 <= thr ? 0xffffffffu : (k0 <= thr ? 0xffffu : 0u)); nmk[j] = opaque(~msk[j]); }
    f32x16 S0;
#pragma unroll
    for (int e = 0; e < 16; e++) S0[e] = u2f(opaque(four ? 0xff800000u : 0u));
    const u32 oo = opaque((c >> 4) * 18432 + (c & 15) * 16 + hh * 256);
    const u32 fl = sb + K2_F + 32 + 4 * (four ? 4 + (lane & 3) : (lane & 7));
    if (wv >= 9) st128(sb + (wv < 11 ? K2_K + (9 + wv) * 1088 : K2_V + 18 * 1040) + lane * 16, u32x4{0x3f803f80u, 0x3f803f80u, 0x3f803f80u, 0x3f803f80u});
    SCHED();
    if (four) { dma16(Kg + (572 + w) * 512, lo, sb + K2_V + (w - 4) * 1040); dma16(Kg + (580 + w) * 512, lo, sb + K2_V + (4 + w) * 1040); }
    dma16(Kg + (576 + w) * 512, lo, sb + K2_V + w * 1040); dma16(Kg + (584 + w) * 512, lo, sb + K2_V + (8 + w) * 1040);
    STAMP(1, 2);
    wait_vm<2>();
    prog_set(sb + K2_F + 32 + 4 * w, 1);
    prog_wait(fl, 1);
    STAMP(1, 1);
    struct KF { u32x4 a; u32x2 b; };
    struct VF { u32x4 a, b; };
    auto ldk = [&](int i) { KF k; k.a = cat(tr_read(ka + i * 64), tr_read(ka + 4352 + i * 64)); k.b = tr_read(ka2 + i * 64); return k; };
    auto ldv = [&](int i) { VF v; v.a = lds128(va + i * 64); v.b = lds128(va + i * 64 + 32); return v; };
    auto pk8 = [&](const f32x16& p, int o) { return u32x4{pk2(p[o], p[o + 1]), pk2(p[o + 2], p[o + 3]), pk2(p[o + 4], p[o + 5]), pk2(p[o + 6], p[o + 7])}; };
#pragma unroll 1
    for (;;) {
    const int ib = four ? 8 - w : 1;
    const u32x4 q0 = cat(tr_read(qa0), tr_read(qa0 + 2048));
    const KF k8 = ldk(8);
    u32x2 q1 = tr_read(qa + 8192);
    const KF k0 = ldk(0);
    q1.x = hh ? 0u : q1.x; q1.y = 0u;
    auto qk = [&](const KF& k) { f32x16 z = {}; z = mfma32(k.a, q0, z); return mfma32k8(k.b, q1, z); };
    f32x16 Sm = {};
    Sm = mfma32(k8.a, q0, Sm); S0 = mfma32(k0.a, q0, S0);
    Sm = mfma32k8(k8.b, q1, Sm); S0 = mfma32k8(k0.b, q1, S0);
    KF kf[2]; VF vf[2], v8, v0;
    kf[1] = ldk(7); kf[0] = ldk(6);
    wait_vm<0>();
    prog_set(sb + K2_F + 16 + 4 * wv, 2);
    {
      const char* p = (const char*)wo + idx * 20736 + w * 2592;
      dma16(p, lane * 16, sb + K2_DUMMY); dma16(p + 1024, lane * 16, sb + K2_DUMMY); dma16(p + 1568, lane * 16, sb + K2_DUMMY);
    }
    SCHED();
#pragma unroll
    for (int e = 0; e < 16; e++) Sm[e] = __builtin_amdgcn_inverse_ballot_w64(cnd[e]) ? Sm[e] : S0[e];
    float m = __builtin_fmaxf(__builtin_fmaxf(Sm[0], Sm[1]), Sm[2]), m2 = __builtin_fmaxf(__builtin_fmaxf(Sm[3], Sm[4]), Sm[5]);
#pragma unroll
    for (int e = 6; e < 14; e += 4) { m = fmaxf(fmaxf(m, Sm[e]), Sm[e + 1]); m2 = fmaxf(fmaxf(m2, Sm[e + 2]), Sm[e + 3]); }
    m = fmaxf(fmaxf(m, Sm[14]), Sm[15]);
    m = xhalf_max(fmaxf(m, m2));
    prog_wait(fl, 2);
    v8 = ldv(8); v0 = ldv(0); vf[1] = ldv(7);
    f32x16 O;
    float l;
#pragma unroll 1
    for (int pass = 0; pass < 2; pass++) {
      q1.y = hh ? 0u : pk2(-m, lo2f(pk2(m, m)) - m);
      f32x16 S[2];
      {
        f32x16 p = mfma32k8(kf[1].b, u32x2{0u, q1.y}, Sm);
        S[1] = qk(kf[1]);
#pragma unroll
        for (int e = 0; e < 16; e++) p[e] = __builtin_amdgcn_exp2f(p[e]);
        const u32x4 pa = pk8(p, 0), pb = pk8(p, 8);
        O = f32x16{};
        O = mfma32(v8.a, pa & u32x4{msk[0], msk[1], msk[2], msk[3]}, O); O = mfma32(v8.b, pb & u32x4{msk[4], msk[5], msk[6], msk[7]}, O);
        O = mfma32(v0.a, pa & u32x4{nmk[0], nmk[1], nmk[2], nmk[3]}, O); O = mfma32(v0.b, pb & u32x4{nmk[4], nmk[5], nmk[6], nmk[7]}, O);
      }
      auto tile = [&](auto self, auto I) -> void {
        constexpr int i = I;
        if (i < ib) return;
        SCHED();
        f32x16& Sc = S[i & 1];
        f32x16 z = {};
        if constexpr (i > 1) z = mfma32(kf[(i - 1) & 1].a, q0, z);
        if constexpr (i > 2) kf[i & 1] = ldk(i - 2);
        if constexpr (i > 1) vf[(i - 1) & 1] = ldv(i - 1);
        SCHED();
#pragma unroll
        for (int e = 0; e < 8; e++) Sc[e] = __builtin_amdgcn_exp2f(Sc[e]);
        SCHED();
        if constexpr (i > 1) S[(i - 1) & 1] = mfma32k8(kf[(i - 1) & 1].b, q1, z);
        const u32x4 pa = pk8(Sc, 0);
        SCHED();
#pragma unroll
        for (int e = 8; e < 12; e++) Sc[e] = __builtin_amdgcn_exp2f(Sc[e]);
        SCHED();
        O = mfma32(vf[i & 1].a, pa, O);
#pragma unroll
        for (int e = 12; e < 16; e++) Sc[e] = __builtin_amdgcn_exp2f(Sc[e]);
        const u32x4 pb = pk8(Sc, 8);
        SCHED();
        O = mfma32(vf[i & 1].b, pb, O);
        if constexpr (i > 1) self(self, IC(i - 1));
      };
      tile(tile, IC(7));
      SCHED();
      { const u32 lu = f2u(O[10]); auto r = __builtin_amdgcn_permlane32_swap(lu, lu, false, false); l = u2f(r[0]); }
      if (pass == 1 || __all(l > 8.67e-19f && l < 1.15e18f)) break;
      q1.y = 0u;
#pragma unroll 1
      for (int i = ib; i < 8; i++) {
        const f32x16 s = qk(ldk(i));
#pragma unroll
        for (int e = 0; e < 16; e += 2) m = fmaxf(fmaxf(m, s[e]), s[e + 1]);
      }
      m = xhalf_max(m);
      kf[1] = ldk(7); v8 = ldv(8); v0 = ldv(0); kf[0] = ldk(6); vf[1] = ldv(7);
    }
    STAMP(1, 3);
    float inv = __builtin_amdgcn_rcpf(l);
    asm volatile("s_nop 1" : "+v"(inv));
    char* Og = ws + WS_O + (size_t)(16 * qb + 2 * w) * 18432;
    gst128(u32x4{pk2(vmul(O[0], inv), vmul(O[1], inv)), pk2(vmul(O[2], inv), vmul(O[3], inv)), pk2(vmul(O[4], inv), vmul(O[5], inv)), pk2(vmul(O[6], inv), vmul(O[7], inv))},
           oo, Og + (h >> 1) * 1024 + (h & 1) * 512);
    if (hh == 0) *(GAS u32*)(Og + 16384 + (h >> 2) * 256 + (h & 3) * 4 + oo) = pk2(vmul(O[8], inv), vmul(O[9], inv));
    if (!four || w < 4) break;
    { const u32 d = 64 * (2 * w - 7); w = 7 - w; ka -= d; ka2 -= d; va -= d; qa -= d; qa0 -= d; }
    }
    STAMP(1, 4);
    return;
  }
  const int cw = wv & 3, tid = threadIdx.x & 255, chb = wv < 4 ? 64 * xcd + 2 * idx : 512 + 2 * h;
  const u32 cb = sb + K2_C + (wv < 4 ? 0 : 2 * CS_SZ);
  u32x2 pre[2]; u32x4 cur[2];
  {
    const u32 o = tid * 16, op = o ? o - 8 : 0;
    sfor<2>([&](auto J) { const u16* kp = ssmk + (size_t)(chb + J) * 2048; gl128<0>(cur[J], o, kp); gl64<0>(pre[J], op, kp); });
    sfor<2>([&](auto J) { dma16(ws + WS_UR + (size_t)(chb + J) * 4096, o, cb + CS_U + cw * 1024 + J * CS_SZ); });
  }
  const u32 dv = *(const __attribute__((address_space(4))) u32*)(ssmD + chb);
  {
    const u32 z0 = opaque(cw * KC_STR + 16 * lane), z1 = opaque(CS_U + 4096 + 16 * (lane & 7)), za = cb + (lane < 24 ? z0 : z1);
    st128(za, u32x4{0u, 0u, 0u, 0u}); st128(za + CS_SZ, u32x4{0u, 0u, 0u, 0u});
  }
  f32x4 acc[8];
#pragma unroll
  for (int c = 0; c < 8; c++) { acc[c] = f32x4{0.f, 0.f, 0.f, 0.f}; asm volatile("" : "+v"(acc[c])); }
  asm volatile("s_waitcnt vmcnt(2)" : "+v"(cur[0]), "+v"(cur[1]), "+v"(pre[0]), "+v"(pre[1])::"memory");
#pragma unroll
  for (int j = 0; j < 2; j++) {
    const u32 cs = cb + j * CS_SZ + 384 + 16 * tid;
    const u32 w0 = tid ? pre[j].x : 0u, w1 = tid ? pre[j].y : 0u, w2 = cur[j].x, w3 = cur[j].y, w4 = cur[j].z, w5 = cur[j].w;
    const u32 a01 = __builtin_amdgcn_alignbyte(w1, w0, 2), a12 = __builtin_amdgcn_alignbyte(w2, w1, 2), a23 = __builtin_amdgcn_alignbyte(w3, w2, 2),
              a34 = __builtin_amdgcn_alignbyte(w4, w3, 2), a45 = __builtin_amdgcn_alignbyte(w5, w4, 2);
    st128(cs, u32x4{a01, a12, a23, a34});
    st128(cs + KC_STR, u32x4{w1, w2, w3, w4});
    st128(cs + 2 * KC_STR, u32x4{a12, a23, a34, a45});
    st128(cs + 3 * KC_STR, cur[j]);
  }
  wait_vm<0>();
  prog_set(sb + K2_F + 64 + 4 * wv, 1);
  STAMP(1, 2);
  prog_wait(sb + K2_F + 64 + 4 * (wv & 4) + 4 * (lane & 3), 1);
  const int tm = cw >> 1, pi = cw & 1;
  const u32 cs = cb + tm * CS_SZ;
  STAMP(1, 1);
  conv_mfma<4>(cs, 4 * pi, lane, acc);
  STAMP(1, 3);
  __builtin_amdgcn_s_setprio(CONV_PRIO);
  if (wv >= 4) prog_wait(sb + K2_F + 8 * tm + 4 * (lane & 1), 1);
  const u32 rA = (wv >= 4 ? sb + K2_C + tm * CS_SZ : sb + K2_R + tm * 8192) + lane * 16;
  if (pi) { sfor<4>([&](auto J) { st128(rA + 4096 + J * 1024, __builtin_bit_cast(u32x4, acc[J])); acc[J] = acc[4 + J]; }); }
  else sfor<4>([&](auto J) { st128(rA + J * 1024, __builtin_bit_cast(u32x4, acc[4 + J])); });
  u32x2 uu[4];
#pragma unroll
  for (int j = 0; j < 4; j++) uu[j] = lds64(cs + CS_U + 4088 - 8 * lane - 512 * (4 * pi + j));
  asm volatile("" ::: "memory");
  prog_set(sb + K2_F + 4 * wv, 1);
  prog_wait(sb + K2_F + 4 * (wv ^ 1), 1);
  f32x4 t[4];
#pragma unroll
  for (int j = 0; j < 4; j++) t[j] = __builtin_bit_cast(f32x4, lds128(rA + (pi ^ 1) * 4096 + j * 1024));
  SCHED();
  const u32 yo = (lane >> 4) * 73728 + (lane & 15) * 8;
  char* yd = ws + WS_Y2 + (chb + tm) * 128 + pi * 1179648;
#pragma unroll
  for (int j = 0; j < 4; j++) conv_out(vadd4(acc[j], t[j]), uu[j], tm ? hi2f(dv) : lo2f(dv), yo + j * 294912, yd);
  STAMP(1, 4);
}

constexpr int K3_Y = 0, K3_X = 9216, K3_W = K3_X + 4 * 3024, K3_LDS = K3_W + 576 * 160;

extern "C" __global__ __launch_bounds__(512, 2) void gm_k3(const u16* __restrict__ x, const u16* __restrict__ wo, const char* __restrict__ ws, u16* __restrict__ out) {
  extern __shared__ __attribute__((aligned(16))) char smem[];
  const u32 sb = (u32)(size_t)(LAS char*)smem;
  const int b = blockIdx.x, xcd = b & 7, idx = b >> 3;
  const int rt = 4 * xcd + (idx & 3), ct = idx >> 2;
  const int lane = threadIdx.x & 63;
  const u32 w = rfl(threadIdx.x >> 6);
  STAMP(2, 0);
  asm volatile("" ::"s"(x), "s"(wo), "s"(ws), "s"(out));
  if (w >= 4) {
    const u32 lw = w - 4;
    const int r6 = lane / 10, c10 = lane - 10 * r6;
    const u32 vo = (6 * lw + r6) * 1152 + 16 * (c10 > 8 ? 8 : c10);
    const char* wb = (const char*)(wo + 72 * ct);
    sfor<3>([&](auto I) {
      const int j = 4 * I + lw;
      if (j < 9) dma16(ws + WS_Y2 + (size_t)(rt * 576 + 72 * ct) * 128 + j * 1024, lane * 16, sb + K3_Y + j * 1024);
    });
    sfor<12>([&](auto I) { dma16n<60>(wb + I * 24 * 1152, vo, sb + K3_W + (4 * I + lw) * 960); });
    wait_vm<12>(); bar();
    wait_vm<6>(); bar();
    sfor<12>([&](auto I) { dma16n<60>(wb + (12 + I) * 24 * 1152, vo, sb + K3_W + (4 * (12 + I) + lw) * 960); });
    wait_vm<12>(); bar();
    wait_vm<6>(); bar();
    wait_vm<0>(); bar();
    return;
  }
  const int g = lane >> 4, l15 = lane & 15;
  const int r7 = lane / 9, c9 = lane - 9 * r7;
  const u32 xw = sb + K3_X + w * 3024;
  sfor<3>([&](auto I) { int r = 7 * I + r7; r = r > 15 ? 15 : r; dma16n<63>(x + (size_t)(64 * rt + 16 * w) * 576 + 72 * ct, r * 1152 + 16 * c9, xw + I * 1008); });
  float gt;
  asm volatile("global_load_dword %0, %1, %2" : "=v"(gt) : "v"((u32)(64 * rt + 16 * w + l15) * 4), "s"(ws + WS_G));
  u32x4 of[18];
  const char* ob = ws + WS_O + (size_t)(4 * rt + w) * 18432;
  sfor<18>([&](auto F) { gl128<(F & 3) * 1024>(of[F], lane * 16, ob + (F >> 2) * 4096); });
  const int q = l15 >> 2;
  asm volatile("s_waitcnt vmcnt(18)" : "+v"(gt)::"memory");
  bar();
  STAMP(2, 1);
  f32x4 acc[5], tb[5];
  const u32 xa = xw + l15 * 144 + 8 * g;
  {
    const float g1 = rbf(1.f - gt);
    const u32 ya = sb + K3_Y + (4 * g + q) * 128 + 32 * w + 8 * (lane & 3);
    sfor<5>([&](auto NT) {
      const u32x2 y = tr_read(ya + NT * 2048), xv = lds64(xa + NT * 32);
      tb[NT] = f32x4{g1 * lo2f(xv.x) * lo2f(y.x), g1 * hi2f(xv.x) * hi2f(y.x), g1 * lo2f(xv.y) * lo2f(y.y), g1 * hi2f(xv.y) * hi2f(y.y)};
      acc[NT] = f32x4{0.f, 0.f, 0.f, 0.f};
    });
  }
  const u32 wa = sb + K3_W + (18 * (g >> 1) + 4 * (g & 1) + q) * 160 + 8 * (lane & 3);
  const u32 wa2 = sb + K3_W + ((4 * g + (q >> 1)) * 18 + 16 + (q & 1)) * 160 + 8 * (lane & 3);
  u32x4 wf[2][5];
  auto rd = [&](auto KS) {
    constexpr int ks = KS;
    sfor<5>([&](auto NT) {
      if constexpr (ks < 16) wf[ks & 1][NT] = cat(tr_read(wa + ks * 5760 + NT * 32), tr_read(wa + ks * 5760 + 1280 + NT * 32));
      else wf[ks & 1][NT] = cat(tr_read(wa2 + (ks - 16) * 46080 + NT * 32), tr_read(wa2 + (ks - 16) * 46080 + 5760 + NT * 32));
    });
  };
  auto mm = [&](auto KS) { sfor<5>([&](auto NT) { acc[NT] = mfma16(wf[KS & 1][NT], of[KS], acc[NT]); }); };
  auto tie = [&](auto Q) {
    constexpr int f = 4 * Q;
    asm volatile("s_waitcnt vmcnt(%4)" : "+v"(of[f]), "+v"(of[f + 1]), "+v"(of[f + 2]), "+v"(of[f + 3]) : "i"(f == 12 ? 0 : 14 - f) : "memory");
    if constexpr (f == 12) asm volatile("" : "+v"(of[16]), "+v"(of[17]));
    bar();
  };
  SCHED();
  tie(IC(0));
  STAMP(2, 2);
  rd(IC(0));
  sfor<18>([&](auto KS) {
    constexpr int ks = KS;
    SCHED();
    if constexpr (ks % 4 == 3 && ks < 12) tie(IC(ks / 4 + 1));
    if constexpr (ks < 17) rd(IC(ks + 1));
    mm(KS);
    sfor<5>([&](auto) { __builtin_amdgcn_sched_group_barrier(0x8, 1, 0); __builtin_amdgcn_sched_group_barrier(0x100, 2, 0); });
  });
  SCHED();
  STAMP(2, 3);
  sfor<5>([&](auto NT) {
    constexpr int nt = NT;
    if (nt < 4 || g < 2)
      st64(xa + nt * 32, u32x2{pk2(fmaf(gt, acc[nt][0], tb[nt][0]), fmaf(gt, acc[nt][1], tb[nt][1])), pk2(fmaf(gt, acc[nt][2], tb[nt][2]), fmaf(gt, acc[nt][3], tb[nt][3]))});
  });
  wait_lgkm();
  char* orow = (char*)(out + (size_t)(64 * rt + 16 * w) * 576 + 72 * ct);
#pragma unroll
  for (int it = 0; it < 3; it++) {
    const int r = 7 * it + r7;
    if (lane < 63 && r < 16) *(GAS u32x4*)(orow + (u32)(r * 1152 + 16 * c9)) = lds128(xw + r * 144 + 16 * c9);
  }
  STAMP(2, 4);
}

extern "C" __global__ void gm_end() {}

extern "C" void kernel_launch(void* const* d_in, const int* in_sizes, int n_in, void* d_out, int out_size, void* d_ws, size_t ws_size,
                              hipStream_t stream) {
  const u16* x = (const u16*)d_in[0]; const u16* wqkv = (const u16*)d_in[1]; const u16* wo = (const u16*)d_in[2];
  const u16* Bk = (const u16*)d_in[4]; const u16* Bv = (const u16*)d_in[5]; const u16* shk = (const u16*)d_in[6];
  const u16* ssmk = (const u16*)d_in[7]; const u16* ssmD = (const u16*)d_in[8]; const u16* gw = (const u16*)d_in[9];
  (void)hipFuncSetAttribute((const void*)gm_k1, hipFuncAttributeMaxDynamicSharedMemorySize, K1_LDS);
  (void)hipFuncSetAttribute((const void*)gm_k2, hipFuncAttributeMaxDynamicSharedMemorySize, K2_LDS);
  (void)hipFuncSetAttribute((const void*)gm_k3, hipFuncAttributeMaxDynamicSharedMemorySize, K3_LDS);
  gm_k1<<<256, 512, K1_LDS, stream>>>(x, wqkv, Bk, Bv, shk, gw, ssmk, (char*)d_ws);
  gm_k2<<<256, 768, K2_LDS, stream>>>(ssmk, ssmD, wo, (char*)d_ws);
  gm_k3<<<256, 512, K3_LDS, stream>>>(x, wo, (const char*)d_ws, (u16*)d_out);
}
```

```cpp
#include <hip/hip_runtime.h>
#include <hip/hip_bf16.h>
#include <utility>

typedef unsigned short u16;
typedef unsigned int u32;
typedef u32 u32x2 __attribute__((ext_vector_type(2)));
typedef u32 u32x4 __attribute__((ext_vector_type(4)));
typedef short s16x4 __attribute__((ext_vector_type(4)));
typedef __bf16 bf16x8 __attribute__((ext_vector_type(8)));
typedef __bf16 bf16x2 __attribute__((ext_vector_type(2)));
typedef float f32x2 __attribute__((ext_vector_type(2)));
typedef float f32x4 __attribute__((ext_vector_type(4)));
typedef float f32x16 __attribute__((ext_vector_type(16)));
#define LAS __attribute__((address_space(3)))
#define GAS __attribute__((address_space(1)))
#define DEVI __device__ __forceinline__
#define IC(n) std::integral_constant<int, (n)>{}

constexpr size_t WS_QKV = 0;
constexpr size_t WS_O   = WS_QKV + 96ull * 2048 * 40;
constexpr size_t WS_UR  = WS_O + 32ull * 2048 * 40;
constexpr size_t WS_Y2  = WS_UR + 576ull * 4096;
constexpr size_t WS_G   = WS_Y2 + 576ull * 4096;
constexpr size_t WS_F   = WS_G + 8192;
constexpr u32 F_EP = 0, F_1 = 1024, F_U = 8192, F_O = 20480, F_Y = 36864;

template <typename F, int... I> DEVI void sfor_impl(F&& f, std::integer_sequence<int, I...>) { (f(std::integral_constant<int, I>{}), ...); }
template <int N, typename F> DEVI void sfor(F&& f) { sfor_impl(f, std::make_integer_sequence<int, N>{}); }

DEVI u32 pk2(float a, float b) { f32x2 v = {a, b}; return __builtin_bit_cast(u32, __builtin_convertvector(v, bf16x2)); }
DEVI u32 f2u(float a) { return __builtin_bit_cast(u32, a); }
DEVI float u2f(u32 a) { return __builtin_bit_cast(float, a); }
DEVI float lo2f(u32 w) { return __builtin_bit_cast(float, w << 16); }
DEVI float hi2f(u32 w) { return __builtin_bit_cast(float, w & 0xffff0000u); }
DEVI float rbf(float a) { return lo2f(pk2(a, a)); }
DEVI f32x4 mfma16(u32x4 a, u32x4 b, f32x4 c) { return __builtin_amdgcn_mfma_f32_16x16x32_bf16(__builtin_bit_cast(bf16x8, a), __builtin_bit_cast(bf16x8, b), c, 0, 0, 0); }
DEVI f32x16 mfma32(u32x4 a, u32x4 b, f32x16 c) { return __builtin_amdgcn_mfma_f32_32x32x16_bf16(__builtin_bit_cast(bf16x8, a), __builtin_bit_cast(bf16x8, b), c, 0, 0, 0); }
DEVI f32x16 mfma32k8(u32x2 a, u32x2 b, f32x16 c) { return __builtin_amdgcn_mfma_f32_32x32x8bf16_1k(__builtin_bit_cast(s16x4, a), __builtin_bit_cast(s16x4, b), c, 0, 0, 0); }
DEVI u32x2 tr_read(u32 addr) { return __builtin_bit_cast(u32x2, __builtin_amdgcn_ds_read_tr16_b64_v4i16((LAS s16x4*)addr)); }
DEVI u32x4 lds128(u32 addr) { return *(LAS u32x4*)addr; }
DEVI u32x2 lds64(u32 addr) { return *(LAS u32x2*)addr; }
DEVI u32 lds32(u32 addr) { return *(LAS u32*)addr; }
DEVI void st128(u32 addr, u32x4 v) { *(LAS u32x4*)addr = v; }
DEVI void st64(u32 addr, u32x2 v) { *(LAS u32x2*)addr = v; }
DEVI void st32(u32 addr, u32 v) { *(LAS u32*)addr = v; }
DEVI u32x4 cat(u32x2 a, u32x2 b) { return u32x4{a.x, a.y, b.x, b.y}; }
DEVI float vfma(float a, float b, float c) { float d; asm("v_fma_f32 %0, %1, %2, %3" : "=v"(d) : "v"(a), "v"(b), "v"(c)); return d; }
DEVI float vmul(float a, float b) { float d; asm("v_mul_f32 %0, %1, %2" : "=v"(d) : "v"(a), "v"(b)); return d; }
DEVI float vadd(float a, float b) { float d; asm("v_add_f32 %0, %1, %2" : "=v"(d) : "v"(a), "v"(b)); return d; }
DEVI f32x4 vadd4(f32x4 a, f32x4 b) { return f32x4{vadd(a[0], b[0]), vadd(a[1], b[1]), vadd(a[2], b[2]), vadd(a[3], b[3])}; }
DEVI u32 opaque(u32 v) { asm volatile("" : "+v"(v)); return v; }
DEVI void dma16(const void* sbase, u32 voff, u32 ldsbase) {
  asm volatile("s_mov_b32 m0, %2\n\ts_nop 0\n\tglobal_load_lds_dwordx4 %0, %1" ::"v"(voff), "s"(sbase), "s"(ldsbase) : "memory");
}
template <int NL> DEVI void dma16n(const void* sbase, u32 voff, u32 ldsbase) {
  asm volatile("s_mov_b32 m0, %2\n\ts_mov_b32 exec_hi, %3\n\tglobal_load_lds_dwordx4 %0, %1\n\ts_mov_b32 exec_hi, -1" ::"v"(voff), "s"(sbase), "s"(ldsbase),
               "i"((1u << (NL - 32)) - 1u) : "memory");
}
template <int OFF> DEVI void gl128(u32x4& d, u32 voff, const void* sbase) { asm volatile("global_load_dwordx4 %0, %1, %2 offset:%3" : "=v"(d) : "v"(voff), "s"(sbase), "i"(OFF)); }
template <int OFF> DEVI void gl64(u32x2& d, u32 voff, const void* sbase) { asm volatile("global_load_dwordx2 %0, %1, %2 offset:%3" : "=v"(d) : "v"(voff), "s"(sbase), "i"(OFF)); }
DEVI void dma16c(const void* sbase, u32 voff, u32 ldsbase) {
  asm volatile("s_mov_b32 m0, %2\n\ts_nop 0\n\tglobal_load_lds_dwordx4 %0, %1 sc1" ::"v"(voff), "s"(sbase), "s"(ldsbase) : "memory");
}
template <int OFF> DEVI void gl128c(u32x4& d, u32 voff, const void* sbase) { asm volatile("global_load_dwordx4 %0, %1, %2 offset:%3 sc1" : "=v"(d) : "v"(voff), "s"(sbase), "i"(OFF)); }
template <int OFF> DEVI void gl32c(u32& d, u32 voff, const void* sbase) { asm volatile("global_load_dword %0, %1, %2 offset:%3 sc1" : "=v"(d) : "v"(voff), "s"(sbase), "i"(OFF)); }
DEVI void gst128(u32x4 v, u32 voff, void* sbase) { asm volatile("global_store_dwordx4 %0, %1, %2 sc1\n\ts_nop 1" ::"v"(voff), "v"(v), "s"(sbase) : "memory"); }
DEVI void gst64(u32x2 v, u32 voff, void* sbase) { asm volatile("global_store_dwordx2 %0, %1, %2 sc1" ::"v"(voff), "v"(v), "s"(sbase) : "memory"); }
DEVI void gst32(u32 v, u32 voff, void* sbase) { asm volatile("global_store_dword %0, %1, %2 sc1" ::"v"(voff), "v"(v), "s"(sbase) : "memory"); }
#define CODE_PF(from, to, ck, step, lds)                                                                                              \
  do {                                                                                                                                \
    u32 lo_, hi_, len_;                                                                                                               \
    asm volatile("s_getpc_b64 s[20:21]\n\ts_add_u32 %0, s20, " from "@rel32@lo+4\n\ts_addc_u32 %1, s21, " from "@rel32@hi+12\n\ts_mov_b32 %2, " to "-" from \
                 : "=s"(lo_), "=s"(hi_), "=s"(len_)::"s20", "s21", "scc");                                                            \
    const char* code_ = (const char*)(((unsigned long long)hi_ << 32) | lo_);                                                         \
    for (u32 o_ = (ck) * 1024; o_ < len_; o_ += (step) * 1024) { const u32 a_ = o_ + (threadIdx.x & 63) * 16; dma16(code_, a_ < len_ ? a_ : 0u, lds); } \
  } while (0)
template <int N> DEVI void wait_vm() { asm volatile("s_waitcnt vmcnt(%0)" ::"i"(N > 63 ? 63 : N) : "memory"); }
DEVI void wait_lgkm() { asm volatile("s_waitcnt lgkmcnt(0)" ::: "memory"); }
DEVI void bar() { asm volatile("s_barrier" ::: "memory"); }
DEVI u32 rfl(u32 v) { return __builtin_amdgcn_readfirstlane(v); }
#define SCHED() __builtin_amdgcn_sched_barrier(0)
#define STAMP(i)
#define STAMP_END()

constexpr float QSCALE = 0.23570226039551584f * 1.4426950408889634f;

#define POLL_SLEEP 2
#define POLLW 4
#define VDELAY 3

constexpr int L_SLOT = 32768, L_WOFF = 16384, L_STG = 94208, L_GW = 131072, L_PT = 133120, L_TILE = 17408;
constexpr int L_K = 0, L_V = 21760, L_C = 41536, L_R = L_PT, L_FL = 152320, L_DUMMY = L_FL + 256, L_Q = 153600, L_END = 163840;
constexpr int KC_STR = 4640, CS_U = 4 * KC_STR, CS_SZ = CS_U + 4096 + 128;
constexpr int L_W = L_C, L_X = L_W + 576 * 160, L_Y = L_X + 9216;
static_assert(L_Y + 9216 <= L_FL && L_C + 2 * CS_SZ <= L_STG && L_C + 4 * CS_SZ <= L_PT && L_PT + L_TILE <= L_FL, "lds map");
constexpr int W_PROG = L_FL, W_XCH = L_FL + 64, W_GO = L_FL + 128, W_TAG = L_FL + 144, W_PREP = L_FL + 160;

DEVI u32 lds_peek(u32 a) { return __hip_atomic_load((LAS u32*)a, __ATOMIC_RELAXED, __HIP_MEMORY_SCOPE_WORKGROUP); }
DEVI void prog_set(u32 a, u32 n) { asm volatile("" ::: "memory"); __hip_atomic_store((LAS u32*)a, n, __ATOMIC_RELAXED, __HIP_MEMORY_SCOPE_WORKGROUP); }
DEVI void prog_wait(u32 a, u32 n) { while (__builtin_amdgcn_ballot_w64(lds_peek(a) < n)) __builtin_amdgcn_s_sleep(1); asm volatile("" ::: "memory"); }
DEVI void publish(char* fb, u32 off, u32x2 tag, int lane) { wait_vm<0>(); if (lane == 0) gst64(tag, off, fb); }
DEVI void await(const char* fb, u32 off, u32x2 tag) {
  for (int spin = 0; spin < (1 << 20); spin++) {
    u32x2 v;
    asm volatile("global_load_dwordx2 %0, %1, %2 sc1\n\ts_waitcnt vmcnt(0)" : "=&v"(v) : "v"(off), "s"(fb) : "memory");
    if (!__builtin_amdgcn_ballot_w64(v.x != tag.x || v.y != tag.y)) break;
    __builtin_amdgcn_s_sleep(POLL_SLEEP);
  }
}
DEVI void acquire() {
}

DEVI void prep_issue(const u16* Bk, const u16* Bv, const u16* shk, int row0, int col, u32 tb, u32 pw, int lane, u32x4& sh) {
  const int r8 = lane >> 3, c8 = lane & 7;
  sfor<2>([&](auto I) {
    const int j = 2 * pw + I;
    int row = row0 + 8 * j + r8 - 3; row = row < 0 ? 0 : row;
    dma16(Bk + col, row * 1152 + 16 * (c8 ^ j), tb + 1024 * j);
    dma16(Bv + col, (row0 + 8 * j + r8) * 1152 + 16 * (c8 ^ j), tb + 9216 + 1024 * j);
  });
  dma16(Bk + col, (row0 + 61 + (r8 > 2 ? 2 : r8)) * 1152 + 16 * c8, tb + 8192);
  gl128<0>(sh, 8 * col + 16 * (8 * pw + r8), shk);
}
DEVI void prep_compute(int row0, int col, u32 tb, u32 pw, int lane, u32x4 sh, char* ws) {
  const int c8 = lane & 7, cp = 8 * pw + (lane >> 3);
  const u32 o0 = tb + 1024 * c8 + 16 * ((cp >> 2) ^ c8) + 4 * (cp & 3), o1 = tb + 1024 * c8 + 16 * ((cp >> 2) ^ ((c8 + 1) & 7)) + 4 * (cp & 3);
  row0 += 8 * c8;
  u32 kk[11], vv[8];
#pragma unroll
  for (int i = 0; i < 11; i++) kk[i] = lds32((i < 8 ? o0 : o1) + 128 * i);
#pragma unroll
  for (int i = 0; i < 8; i++) vv[i] = lds32(o0 + 9216 + 128 * i);
  if (row0 == 0) { kk[0] = 0; kk[1] = 0; kk[2] = 0; }
  const u32 go = 2 * cp * 4096 + 2 * (2040 - row0);
  char* dst = ws + WS_UR + (size_t)col * 4096;
  float r[8];
  {
    const float a0 = lo2f(sh.x), a1 = hi2f(sh.x), a2 = lo2f(sh.y), a3 = hi2f(sh.y);
#pragma unroll
    for (int i = 0; i < 8; i++) r[i] = vmul(vfma(lo2f(kk[i + 3]), a0, vfma(lo2f(kk[i + 2]), a1, vfma(lo2f(kk[i + 1]), a2, vmul(lo2f(kk[i]), a3)))), lo2f(vv[i]));
    gst128(u32x4{pk2(r[7], r[6]), pk2(r[5], r[4]), pk2(r[3], r[2]), pk2(r[1], r[0])}, go, dst);
  }
  {
    const float a0 = lo2f(sh.z), a1 = hi2f(sh.z), a2 = lo2f(sh.w), a3 = hi2f(sh.w);
#pragma unroll
    for (int i = 0; i < 8; i++) r[i] = vmul(vfma(hi2f(kk[i + 3]), a0, vfma(hi2f(kk[i + 2]), a1, vfma(hi2f(kk[i + 1]), a2, vmul(hi2f(kk[i]), a3)))), hi2f(vv[i]));
    gst128(u32x4{pk2(r[7], r[6]), pk2(r[5], r[4]), pk2(r[3], r[2]), pk2(r[1], r[0])}, go, dst + 4096);
  }
}

template <int NR> DEVI void conv_mfma(u32 cs, int rho0, int lane, f32x4 (&acc)[8]) {
  const int g = lane >> 4, l15 = lane & 15;
  u32 aa = cs + (l15 & 3) * KC_STR + 2 * (64 * (l15 >> 2) + 8 * g) + 64 * rho0;
  u32 ba = opaque(cs + CS_U + 2 * (1852 - 1792 - 4 * l15 + 8 * g) + 64 * rho0), bb = opaque(ba + 8);
  u32x4 A[8], G[8];
  sfor<5>([&](auto J) { constexpr int p = J < 4 ? J + 2 : 1; A[p] = lds128(aa + p * 512); G[p] = cat(lds64(ba + (7 - p) * 512), lds64(bb + (7 - p) * 512)); SCHED(); });
#pragma unroll 1
  for (int rho = 0; rho < NR; rho++) {
    constexpr int pa[36] = {2, 2, 3, 3, 2, 4, 3, 4, 2, 5, 1, 1, 2, 1, 3, 1, 4, 1, 5, 1, 6, 2, 0, 3, 0, 4, 0, 5, 0, 1, 0, 6, 0, 7, 0, 0};
    constexpr int pe[36] = {2, 3, 2, 3, 4, 2, 4, 3, 5, 2, 1, 2, 1, 3, 1, 4, 1, 5, 1, 6, 1, 0, 2, 0, 3, 0, 4, 0, 5, 0, 1, 0, 6, 0, 7, 0};
    constexpr int rdc[36] = {18, 19, 20, 21, 22, 23, 0, 1, 2, -1, -1, -1, -1, -1, -1, -1, -1, -1, -1, -1, -1, 6, 7, 8, 9, 10, 11, 12, 13, 14, 15, 16, 17, 3, 4, 5};
    sfor<36>([&](auto I) {
      constexpr int i = I, r = rdc[i], x = r / 3;
      if constexpr (i == 9) { aa = opaque(aa + 64); ba = opaque(ba + 64); bb = opaque(bb + 64); }
      acc[pa[i] + pe[i]] = mfma16(A[pa[i]], G[pe[i]], acc[pa[i] + pe[i]]);
      if constexpr (r >= 0) {
        if constexpr (r % 3 == 0) A[x] = lds128(aa + x * 512);
        else if constexpr (r % 3 == 1) { const u32x2 t = lds64(ba + (7 - x) * 512); G[x].x = t.x; G[x].y = t.y; }
        else { const u32x2 t = lds64(bb + (7 - x) * 512); G[x].z = t.x; G[x].w = t.y; }
      }
      SCHED();
    });
  }
  SCHED();
}
DEVI void conv_out(f32x4 y, u32x2 u, float Dv, u32 off, char* ydst) {
  const float y0 = vfma(hi2f(u.y), Dv, y[0]), y1 = vfma(lo2f(u.y), Dv, y[1]), y2 = vfma(hi2f(u.x), Dv, y[2]), y3 = vfma(lo2f(u.x), Dv, y[3]);
  gst64(u32x2{pk2(y0, y1), pk2(y2, y3)}, off, ydst);
}

DEVI float xhalf_max(float m) { const u32 mu = f2u(m); auto r = __builtin_amdgcn_permlane32_swap(mu, mu, false, false); return fmaxf(u2f(r[0]), u2f(r[1])); }

extern "C" __global__ __launch_bounds__(768, 3) void gm_main(const u16* __restrict__ x, const u16* __restrict__ wqkv, const u16* __restrict__ wo,
                                                             const u16* __restrict__ Bk, const u16* __restrict__ Bv, const u16* __restrict__ shk,
                                                             const u16* __restrict__ ssmk, const u16* __restrict__ ssmD, const u16* __restrict__ gw,
                                                             char* __restrict__ ws, u16* __restrict__ out) {
  extern __shared__ __attribute__((aligned(16))) char smem[];
  const u32 sb = (u32)(size_t)(LAS char*)smem;
  const int b = blockIdx.x, xcd = b & 7, idx = b >> 3;
  const int lane = threadIdx.x & 63;
  const u32 wv = rfl(threadIdx.x >> 6);
  const bool spare = idx >= 28;
  char* const fb = ws + WS_F;
  STAMP(0);
  {
    const int hg = xcd & 1, ri = idx & 3, rb = 4 * (xcd >> 1) + ri, jt = idx >> 2;
    const int r8 = lane >> 3, c8 = lane & 7;
    const int rot = ((jt + ri) % 3) * 3;
    if (wv < 4 && !spare) {
      const u32 lw = wv;
      const u32 xo = (32 * lw + r8) * 1152 + 16 * (c8 ^ r8);
      const char* xb = (const char*)(x + (size_t)rb * 128 * 576);
      u32 wof[2];
#pragma unroll
      for (int i = 0; i < 2; i++) {
        const int ch = (lane & 15) ^ (((lane >> 4) | (i << 2)) << 1), ln = 14 * hg + 2 * jt + (ch >> 3);
        wof[i] = ((lane >> 4) + 16 * lw) * 3456 + 128 * (ln > 26 ? 26 : ln) + 16 * (ch & 7);
      }
      const char* wb = (const char*)wqkv;
      auto issue = [&](int c) {
        const u32 sl = sb + (c & 3) * L_SLOT;
        c += rot; c = c >= 9 ? c - 9 : c;
        sfor<4>([&](auto I) { dma16(xb + c * 128 + I * 9216, xo, sl + (4 * lw + I) * 1024); });
        sfor<4>([&](auto I) { dma16(wb + (64 * c + 4 * I) * 3456, wof[I >> 1], sl + L_WOFF + (4 * lw + I) * 1024); });
      };
      if (hg == 1 && jt == 6 && lw == 0) { dma16(gw, lane * 16, sb + L_GW); dma16(gw, 128 + lane * 16, sb + L_GW + 128); }
      issue(0);
#pragma unroll 1
      for (int n = 1; n < 8; n++) { issue(n); wait_vm<8>(); bar(); }
      issue(8);
      u32x4 sh;
      prep_issue(Bk, Bv, shk, 64 * idx, 64 * xcd, sb + L_PT, lw, lane, sh);
      wait_vm<14>(); bar();
      wait_vm<6>(); bar();
      bar();
      asm volatile("s_waitcnt vmcnt(0)" : "+v"(sh)::"memory");
      prog_set(sb + W_PREP + 4 * lw, 1);
      prog_wait(sb + W_PREP + 4 * (lane & 3), 1);
      prep_compute(64 * idx, 64 * xcd, sb + L_PT, lw, lane, sh, ws);
      publish(fb, F_U + ((4 * xcd + lw) * 32 + idx) * 8, lds64(sb + W_TAG), lane);
    } else if (wv >= 8) {
      if (wv == 8) {
        u32x2 e;
        asm volatile("global_load_dwordx2 %0, %1, %2 sc1" : "=v"(e) : "v"(F_EP), "s"(fb));
        st32(sb + L_FL + 4 * lane, 0u);
        asm volatile("s_waitcnt vmcnt(0)" : "+v"(e)::"memory");
        e.x += 1u; e.y += e.x == 0u;
        st64(sb + W_TAG, e);
        wait_lgkm();
      }
      if (spare) { bar(); bar(); }
      else
#pragma unroll 1
        for (int n = 0; n < 10; n++) bar();
    } else if (spare) {
      CODE_PF("gm_main", "gm_end", 8 * ri + wv, 32, sb + L_DUMMY);
      const int th = wv >> 2, row0 = th ? 256 * xcd + 64 * ri : 64 * idx, col = th ? 512 : 64 * xcd;
      u32x4 sh;
      prep_issue(Bk, Bv, shk, row0, col, sb + (th ? 0 : L_PT), wv & 3, lane, sh);
      asm volatile("s_waitcnt vmcnt(0)" : "+v"(sh)::"memory");
      bar();
      prep_compute(row0, col, sb + (th ? 0 : L_PT), wv & 3, lane, sh, ws);
      publish(fb, F_U + (((col >> 4) + (wv & 3)) * 32 + (row0 >> 6)) * 8, lds64(sb + W_TAG), lane);
      bar();
      const char* kp = (const char*)ssmk + (size_t)(64 * xcd + 16 * ri + 2 * wv) * 4096;
      sfor<8>([&](auto I) { dma16(kp + I * 1024, lane * 16, sb + L_DUMMY); });
      const char* wp = (const char*)wo + (8 * ri + wv) * 20736;
#pragma unroll 1
      for (int i = 0; i < 20; i++) dma16(wp + i * 1024, lane * 16, sb + L_DUMMY);
      dma16(wp + 19712, lane * 16, sb + L_DUMMY);
    } else {
      const int cw = wv - 4, g = lane >> 4, l15 = lane & 15, wm = cw & 1, wn = cw >> 1;
      const u32 xra = sb + (64 * wm + l15) * 128 + 16 * (g ^ (l15 & 7));
      const u32 xb[2][2] = {{opaque(xra), opaque(xra + 2 * L_SLOT)}, {opaque(xra ^ 64), opaque((xra ^ 64) + 2 * L_SLOT)}};
      u32x4 wf[2][4], xf[2][4];
      f32x4 acc[4][4];
      const int ln = 14 * hg + 2 * jt + wn;
      __builtin_amdgcn_s_setprio(2);
      if (ln == 27) {
#pragma unroll
        for (int i = 0; i < 4; i++) acc[i][0] = f32x4{0.f, 0.f, 0.f, 0.f};
#pragma unroll 1
        for (int c = 0; c < 9; c++) {
          bar();
          const int ca = c + rot >= 9 ? c + rot - 9 : c + rot;
#pragma unroll
          for (int ks = 0; ks < 2; ks++) {
            const u32x4 gv = lds128(sb + L_GW + ca * 128 + ks * 64 + 16 * g);
#pragma unroll
            for (int i = 0; i < 4; i++) acc[i][0] = mfma16(gv, lds128(xb[ks][0] + (c & 3) * L_SLOT + 2048 * i), acc[i][0]);
          }
          wait_lgkm();
        }
        bar();
        const float p = rbf(g == 0 ? acc[0][0][0] : g == 1 ? acc[1][0][0] : g == 2 ? acc[2][0][0] : acc[3][0][0]);
        gst32(f2u(rbf(1.f / (1.f + __expf(-p)))), lane * 4, ws + WS_G + (rb * 128 + 64 * wm) * 4);
      } else {
    u32 wb[4][2];
    {
      const int q = l15 >> 2, p = lane & 3, f = q | ((g & 1) << 2);
#pragma unroll
      for (int nt = 0; nt < 4; nt++) {
        const u32 a = sb + L_WOFF + (8 * g + q) * 256 + (((8 * wn + 2 * nt + (p >> 1)) ^ (f << 1)) << 4) + 8 * (p & 1);
        wb[nt][0] = opaque(a); wb[nt][1] = opaque(a + 2 * L_SLOT);
      }
    }
    auto rd = [&](auto KS, auto C) {
      constexpr int ks = KS, c = C, so = (c & 1) * L_SLOT, hi = (c >> 1) & 1;
      sfor<4>([&](auto I) {
        xf[ks][I] = lds128(xb[ks][hi] + so + 2048 * I);
        wf[ks][I] = cat(tr_read(wb[I][hi] + so + ks * 8192), tr_read(wb[I][hi] + so + ks * 8192 + 1024));
      });
    };
    auto mm = [&](auto KS, auto FIRST) {
      constexpr int ks = KS;
      sfor<16>([&](auto I) {
        constexpr int mo[16] = {0, 1, 0, 1, 2, 2, 0, 1, 2, 3, 3, 3, 0, 1, 2, 3}, no[16] = {0, 0, 1, 1, 0, 1, 2, 2, 2, 0, 1, 2, 3, 3, 3, 3};
        constexpr int mt = mo[I], nt = no[I];
        acc[mt][nt] = mfma16(xf[ks][mt], wf[ks][nt], FIRST ? f32x4{0.f, 0.f, 0.f, 0.f} : acc[mt][nt]);
      });
    };
    auto ilv = [&]() {
      sfor<12>([&](auto) { __builtin_amdgcn_sched_group_barrier(0x8, 1, 0); __builtin_amdgcn_sched_group_barrier(0x100, 1, 0); });
      __builtin_amdgcn_sched_group_barrier(0x8, 4, 0);
    };
    const float sc = ln < 9 ? QSCALE : 1.f;
    char* qo = ws + WS_QKV + (size_t)((rb >> 1) * 1728 + 64 * ln) * 512 + (rb & 1) * 256 + wm * 128;
    SCHED();
    bar();
    STAMP(1);
    rd(IC(0), IC(0));
    sfor<9>([&](auto C) {
      constexpr int c = C;
      SCHED();
      rd(IC(1), C); mm(IC(0), IC(c == 0)); ilv();
      SCHED();
      if constexpr (c == 8) wait_lgkm();
      bar();
      SCHED();
      if constexpr (c < 8) { rd(IC(0), IC(c + 1)); mm(IC(1), IC(0)); ilv(); }
    });
    STAMP(2);
    const u32 stg = sb + L_STG + cw * 9216;
    const u32 sa = stg + l15 * 144 + 8 * g, ra = stg + r8 * 144 + 16 * c8, go = r8 * 512 + 16 * c8;
    u32x4 t[4][2];
    sfor<6>([&](auto NT) {
      constexpr int nt = NT;
      if constexpr (nt < 4) sfor<4>([&](auto M) { acc[M][nt] = mfma16(xf[1][M], wf[1][nt], acc[M][nt]); });
      SCHED();
      if constexpr (nt > 0 && nt < 5) {
        sfor<4>([&](auto M) {
          const f32x4 v = acc[M][nt - 1] * sc;
          st64(sa + (nt - 1) * 2304 + 32 * M, u32x2{pk2(v[0], v[1]), pk2(v[2], v[3])});
        });
        t[nt - 1][0] = lds128(ra + (nt - 1) * 2304); t[nt - 1][1] = lds128(ra + (nt - 1) * 2304 + 1152);
      }
      if constexpr (nt > 1) { gst128(t[nt - 2][0], go, qo + (nt - 2) * 8192); gst128(t[nt - 2][1], go, qo + (nt - 2) * 8192 + 4096); }
      SCHED();
    });
      }
      publish(fb, F_1 + (ln * 32 + 2 * rb + wm) * 8, lds64(sb + W_TAG), lane);
    }
  }
  if (wv < 8) prog_set(sb + W_PROG + 4 * wv, 1);
  STAMP(3);
  const u32x2 tag = lds64(sb + W_TAG);
  const int qb = spare ? 0 : 1 + idx % 7, h = 4 * xcd + (spare ? idx - 28 : idx / 7);
  if (wv < (spare ? 4u : 8u)) {
    __builtin_amdgcn_s_setprio(1);
    const int c = lane & 31, hh = lane >> 5, q4 = (lane & 15) >> 2;
    int w = spare ? wv + 4 : wv;
    const u32 kcol = 64 * w + (lane & 16) * 2 + 16 * (lane & 1) + 4 * (lane & 2);
    u32 ka = opaque(sb + L_K + (8 * hh + q4) * 1088 + kcol), ka2 = opaque(sb + L_K + (16 + q4) * 1088 + kcol);
    u32 va = opaque(sb + L_V + c * 1040 + 64 * w + 16 * hh);
    u32 qa = opaque(sb + L_Q + q4 * 512 + 64 * w + (lane & 16) * 2 + 8 * (lane & 3)), qa0 = opaque(qa + 4096 * hh);
    const int thr = c - 8 * hh;
    u32 msk[8], nmk[8];
    unsigned long long cnd[16];
#pragma unroll
    for (int e = 0; e < 16; e++) cnd[e] = __builtin_amdgcn_ballot_w64(16 * (e >> 3) + (e & 7) <= thr);
#pragma unroll
    for (int j = 0; j < 8; j++) { const int k0 = 16 * (j >> 2) + 2 * (j & 3); msk[j] = opaque(k0 + 1 <= thr ? 0xffffffffu : (k0 <= thr ? 0xffffu : 0u)); nmk[j] = opaque(~msk[j]); }
    f32x16 S0;
#pragma unroll
    for (int e = 0; e < 16; e++) S0[e] = u2f(opaque(spare ? 0xff800000u : 0u));
    const u32 oo = opaque((c >> 4) * 18432 + (c & 15) * 16 + hh * 256);
    const u32 fl = sb + W_PROG + 4 * (spare ? (lane & 3) : (lane & 7));
    if (wv < 3) st128(sb + (wv < 2 ? L_K + (18 + wv) * 1088 : L_V + 18 * 1040) + lane * 16, u32x4{0x3f803f80u, 0x3f803f80u, 0x3f803f80u, 0x3f803f80u});
    if (wv == POLLW || (spare && wv == 0)) {
      const int seg = (lane >> 4) > 2 ? 2 : lane >> 4, r64 = 4 * qb - 4 + ((lane & 7) | (seg ? 0 : 4));
      await(fb, F_1 + ((9 * seg + ((18 * h + 17 * ((lane >> 3) & 1)) >> 6)) * 32 + (r64 < 0 ? 0 : r64)) * 8, tag);
      prog_set(sb + W_GO, 1);
    } else prog_wait(sb + W_GO, 1);
    acquire();
    STAMP(4);
    const char* Ag = ws + WS_QKV + (size_t)(qb * 1728 + 18 * h) * 512;
    const char* Kg = Ag + 576 * 512 - (qb ? 884736 : 0);
    const u32 lo = (lane >> 5) * (qb ? 884736 : 0) + (lane & 31) * 16;
#pragma unroll 1
    for (int s = wv; s <= w; s += 4) {
      dma16c(Ag + s * 1024, lane * 16, sb + L_Q + s * 1024);
      dma16c(Kg + s * 512, lo, sb + L_K + s * 1088); dma16c(Kg + s * 512 + 4096, lo, sb + L_K + (8 + s) * 1088);
      const char* eg = s < 4 ? Kg + (s < 2 ? 16 + s : 590 + s) * 512 : Ag + 8192;
      dma16c(eg, s < 4 ? lo : lane * 16, sb + (s < 2 ? L_K + (16 + s) * 1088 : (s < 4 ? L_V + (14 + s) * 1040 : L_Q + 8192)));
    }
    __builtin_amdgcn_s_sleep(VDELAY);
    if (spare) { dma16c(Kg + (572 + w) * 512, lo, sb + L_V + (w - 4) * 1040); dma16c(Kg + (580 + w) * 512, lo, sb + L_V + (4 + w) * 1040); }
    dma16c(Kg + (576 + w) * 512, lo, sb + L_V + w * 1040); dma16c(Kg + (584 + w) * 512, lo, sb + L_V + (8 + w) * 1040);
    wait_vm<2>();
    prog_set(sb + W_PROG + 4 * wv, 2);
    prog_wait(fl, 2);
    STAMP(5);
    struct KF { u32x4 a; u32x2 b; };
    struct VF { u32x4 a, b; };
    auto ldk = [&](int i) { KF k; k.a = cat(tr_read(ka + i * 64), tr_read(ka + 4352 + i * 64)); k.b = tr_read(ka2 + i * 64); return k; };
    auto ldv = [&](int i) { VF v; v.a = lds128(va + i * 64); v.b = lds128(va + i * 64 + 32); return v; };
    auto pk8 = [&](const f32x16& p, int o) { return u32x4{pk2(p[o], p[o + 1]), pk2(p[o + 2], p[o + 3]), pk2(p[o + 4], p[o + 5]), pk2(p[o + 6], p[o + 7])}; };
#pragma unroll 1
    for (;;) {
    const int ib = spare ? 8 - w : 1;
    const u32x4 q0 = cat(tr_read(qa0), tr_read(qa0 + 2048));
    const KF k8 = ldk(8);
    u32x2 q1 = tr_read(qa + 8192);
    const KF k0 = ldk(0);
    q1.x = hh ? 0u : q1.x; q1.y = 0u;
    auto qk = [&](const KF& k) { f32x16 z = {}; z = mfma32(k.a, q0, z); return mfma32k8(k.b, q1, z); };
    f32x16 Sm = {};
    Sm = mfma32(k8.a, q0, Sm); S0 = mfma32(k0.a, q0, S0);
    Sm = mfma32k8(k8.b, q1, Sm); S0 = mfma32k8(k0.b, q1, S0);
    KF kf[2]; VF vf[2], v8, v0;
    kf[1] = ldk(7); kf[0] = ldk(6);
    wait_vm<0>();
    prog_set(sb + W_PROG + 4 * wv, 3);
    SCHED();
#pragma unroll
    for (int e = 0; e < 16; e++) Sm[e] = __builtin_amdgcn_inverse_ballot_w64(cnd[e]) ? Sm[e] : S0[e];
    float m = __builtin_fmaxf(__builtin_fmaxf(Sm[0], Sm[1]), Sm[2]), m2 = __builtin_fmaxf(__builtin_fmaxf(Sm[3], Sm[4]), Sm[5]);
#pragma unroll
    for (int e = 6; e < 14; e += 4) { m = fmaxf(fmaxf(m, Sm[e]), Sm[e + 1]); m2 = fmaxf(fmaxf(m2, Sm[e + 2]), Sm[e + 3]); }
    m = fmaxf(fmaxf(m, Sm[14]), Sm[15]);
    m = xhalf_max(fmaxf(m, m2));
    prog_wait(fl, 3);
    v8 = ldv(8); v0 = ldv(0); vf[1] = ldv(7);
    f32x16 O;
    float l;
#pragma unroll 1
    for (int pass = 0; pass < 2; pass++) {
      q1.y = hh ? 0u : pk2(-m, lo2f(pk2(m, m)) - m);
      f32x16 S[2];
      {
        f32x16 p = mfma32k8(kf[1].b, u32x2{0u, q1.y}, Sm);
        S[1] = qk(kf[1]);
#pragma unroll
        for (int e = 0; e < 16; e++) p[e] = __builtin_amdgcn_exp2f(p[e]);
        const u32x4 pa = pk8(p, 0), pb = pk8(p, 8);
        O = f32x16{};
        O = mfma32(v8.a, pa & u32x4{msk[0], msk[1], msk[2], msk[3]}, O); O = mfma32(v8.b, pb & u32x4{msk[4], msk[5], msk[6], msk[7]}, O);
        O = mfma32(v0.a, pa & u32x4{nmk[0], nmk[1], nmk[2], nmk[3]}, O); O = mfma32(v0.b, pb & u32x4{nmk[4], nmk[5], nmk[6], nmk[7]}, O);
      }
      auto tile = [&](auto self, auto I) -> void {
        constexpr int i = I;
        if (i < ib) return;
        SCHED();
        f32x16& Sc = S[i & 1];
        f32x16 z = {};
        if constexpr (i > 1) z = mfma32(kf[(i - 1) & 1].a, q0, z);
        if constexpr (i > 2) kf[i & 1] = ldk(i - 2);
        if constexpr (i > 1) vf[(i - 1) & 1] = ldv(i - 1);
        SCHED();
#pragma unroll
        for (int e = 0; e < 8; e++) Sc[e] = __builtin_amdgcn_exp2f(Sc[e]);
        SCHED();
        if constexpr (i > 1) S[(i - 1) & 1] = mfma32k8(kf[(i - 1) & 1].b, q1, z);
        const u32x4 pa = pk8(Sc, 0);
        SCHED();
#pragma unroll
        for (int e = 8; e < 12; e++) Sc[e] = __builtin_amdgcn_exp2f(Sc[e]);
        SCHED();
        O = mfma32(vf[i & 1].a, pa, O);
#pragma unroll
        for (int e = 12; e < 16; e++) Sc[e] = __builtin_amdgcn_exp2f(Sc[e]);
        const u32x4 pb = pk8(Sc, 8);
        SCHED();
        O = mfma32(vf[i & 1].b, pb, O);
        if constexpr (i > 1) self(self, IC(i - 1));
      };
      tile(tile, IC(7));
      SCHED();
      { const u32 lu = f2u(O[10]); auto r = __builtin_amdgcn_permlane32_swap(lu, lu, false, false); l = u2f(r[0]); }
      if (pass == 1 || __all(l > 8.67e-19f && l < 1.15e18f)) break;
      q1.y = 0u;
#pragma unroll 1
      for (int i = ib; i < 8; i++) {
        const f32x16 s = qk(ldk(i));
#pragma unroll
        for (int e = 0; e < 16; e += 2) m = fmaxf(fmaxf(m, s[e]), s[e + 1]);
      }
      m = xhalf_max(m);
      kf[1] = ldk(7); v8 = ldv(8); v0 = ldv(0); kf[0] = ldk(6); vf[1] = ldv(7);
    }
    STAMP(6);
    float inv = __builtin_amdgcn_rcpf(l);
    asm volatile("s_nop 1" : "+v"(inv));
    char* Og = ws + WS_O + (size_t)(16 * qb + 2 * w) * 18432;
    gst128(u32x4{pk2(vmul(O[0], inv), vmul(O[1], inv)), pk2(vmul(O[2], inv), vmul(O[3], inv)), pk2(vmul(O[4], inv), vmul(O[5], inv)), pk2(vmul(O[6], inv), vmul(O[7], inv))},
           oo, Og + (h >> 1) * 1024 + (h & 1) * 512);
    if (hh == 0) gst32(pk2(vmul(O[8], inv), vmul(O[9], inv)), (c >> 4) * 18432 + (c & 15) * 4, Og + 16384 + h * 64);
    publish(fb, F_O + ((8 * qb + w) * 32 + h) * 8, tag, lane);
    if (!spare || w < 4) break;
    { const u32 d = 64 * (2 * w - 7); w = 7 - w; ka -= d; ka2 -= d; va -= d; qa -= d; qa0 -= d; }
    }
    if (b == 255 && wv == 0) {
#pragma unroll 1
      for (int i = 0; i < 4; i++) await(fb, F_U + ((8 * i + 4 * (lane >> 5)) * 32 + (lane & 31)) * 8, tag);
      if (lane == 0) gst64(tag, F_EP, fb);
    }
    STAMP_END();
    return;
  }
  const int cw = wv & 3;
  {
    const int tid = threadIdx.x & 255, chb = wv >= 8 ? 64 * xcd + 2 * idx : 512 + 2 * h;
    const u32 cb = sb + L_C + (wv >= 8 ? 0 : 2 * CS_SZ);
    u32x2 pre[2]; u32x4 cur[2];
    const u32 o = tid * 16, op = o ? o - 8 : 0;
    sfor<2>([&](auto J) { const u16* kp = ssmk + (size_t)(chb + J) * 2048; gl128<0>(cur[J], o, kp); gl64<0>(pre[J], op, kp); });
    const u32 dv = *(const __attribute__((address_space(4))) u32*)(ssmD + chb);
    {
      const u32 z0 = opaque(cw * KC_STR + 16 * lane), z1 = opaque(CS_U + 4096 + 16 * (lane & 7)), za = cb + (lane < 24 ? z0 : z1);
      st128(za, u32x4{0u, 0u, 0u, 0u}); st128(za + CS_SZ, u32x4{0u, 0u, 0u, 0u});
    }
    f32x4 acc[8];
#pragma unroll
    for (int c = 0; c < 8; c++) { acc[c] = f32x4{0.f, 0.f, 0.f, 0.f}; asm volatile("" : "+v"(acc[c])); }
    asm volatile("s_waitcnt vmcnt(0)" : "+v"(cur[0]), "+v"(cur[1]), "+v"(pre[0]), "+v"(pre[1])::"memory");
#pragma unroll
    for (int j = 0; j < 2; j++) {
      const u32 cs = cb + j * CS_SZ + 384 + 16 * tid;
      const u32 w0 = tid ? pre[j].x : 0u, w1 = tid ? pre[j].y : 0u, w2 = cur[j].x, w3 = cur[j].y, w4 = cur[j].z, w5 = cur[j].w;
      const u32 a01 = __builtin_amdgcn_alignbyte(w1, w0, 2), a12 = __builtin_amdgcn_alignbyte(w2, w1, 2), a23 = __builtin_amdgcn_alignbyte(w3, w2, 2),
                a34 = __builtin_amdgcn_alignbyte(w4, w3, 2), a45 = __builtin_amdgcn_alignbyte(w5, w4, 2);
      st128(cs, u32x4{a01, a12, a23, a34});
      st128(cs + KC_STR, u32x4{w1, w2, w3, w4});
      st128(cs + 2 * KC_STR, u32x4{a12, a23, a34, a45});
      st128(cs + 3 * KC_STR, cur[j]);
    }
    await(fb, F_U + ((chb >> 4) * 32 + 24 - 8 * cw + (lane & 7)) * 8, tag);
    acquire();
    sfor<2>([&](auto J) { dma16c(ws + WS_UR + (size_t)(chb + J) * 4096, o, cb + CS_U + cw * 1024 + J * CS_SZ); });
    wait_vm<0>();
    prog_set(sb + W_XCH + 4 * wv, 1);
    const int tm = cw >> 1, pi = cw & 1;
    const u32 cs = cb + tm * CS_SZ;
    prog_wait(sb + W_XCH + 4 * (wv & 12) + 4 * (lane & 3), 1);
    STAMP(4);
    conv_mfma<4>(cs, 4 * pi, lane, acc);
    STAMP(5);
    if (wv < 8) prog_wait(sb + W_XCH + 32 + 8 * tm + 4 * (lane & 1), 2);
    else prog_wait(sb + W_PROG + 4 * (lane & 7), 1);
    const u32 rA = (wv < 8 ? sb + L_C + tm * CS_SZ : sb + L_R + tm * 8192) + lane * 16;
    if (pi) { sfor<4>([&](auto J) { st128(rA + 4096 + J * 1024, __builtin_bit_cast(u32x4, acc[J])); acc[J] = acc[4 + J]; }); }
    else sfor<4>([&](auto J) { st128(rA + J * 1024, __builtin_bit_cast(u32x4, acc[4 + J])); });
    u32x2 uu[4];
#pragma unroll
    for (int j = 0; j < 4; j++) uu[j] = lds64(cs + CS_U + 4088 - 8 * lane - 512 * (4 * pi + j));
    prog_set(sb + W_XCH + 4 * wv, 2);
    prog_wait(sb + W_XCH + 4 * (wv ^ 1), 2);
    f32x4 t[4];
#pragma unroll
    for (int j = 0; j < 4; j++) t[j] = __builtin_bit_cast(f32x4, lds128(rA + (pi ^ 1) * 4096 + j * 1024));
    wait_lgkm();
    prog_set(sb + W_XCH + 4 * wv, 3);
    const u32 yo = (lane >> 4) * 73728 + (lane & 15) * 8;
    char* yd = ws + WS_Y2 + (chb + tm) * 128 + pi * 1179648;
#pragma unroll
    for (int j = 0; j < 4; j++) conv_out(vadd4(acc[j], t[j]), uu[j], tm ? hi2f(dv) : lo2f(dv), yo + j * 294912, yd);
    publish(fb, F_Y + (pi * 576 + chb + tm) * 8, tag, lane);
    if (wv < 8) { STAMP_END(); return; }
  }
  {
    const int rt = 4 * xcd + (idx & 3), ct = idx >> 2, g = lane >> 4, l15 = lane & 15, q = l15 >> 2;
    prog_wait(sb + W_XCH + (spare ? 16 + 4 * (lane & 7) : 32 + 4 * (lane & 3)), 3);
    const int r7 = lane / 9, c9 = lane - 9 * r7;
    const u32 xw = sb + L_X + cw * 2304;
    {
      const int r6 = lane / 10, c10 = lane - 10 * r6;
      const u32 vo = (6 * cw + r6) * 1152 + 16 * (c10 > 8 ? 8 : c10);
      const char* wb = (const char*)(wo + 72 * ct);
#pragma unroll 1
      for (int i = 0; i < 24; i++) dma16n<60>(wb + i * 24 * 1152, vo, sb + L_W + (4 * i + cw) * 960);
      const u16* xp = x + (size_t)(64 * rt + 16 * cw) * 576 + 72 * ct;
      dma16n<63>(xp, r7 * 1152 + 16 * c9, xw); dma16n<63>(xp + 7 * 576, r7 * 1152 + 16 * c9, xw + 1008); dma16n<63>(xp + 9 * 576, r7 * 1152 + 16 * c9, xw + 1296);
    }
    await(fb, lane < 18 ? F_Y + ((rt >> 4) * 576 + 72 * ct + 18 * cw + lane) * 8 : F_1 + (27 * 32 + rt) * 8, tag);
    acquire();
    {
      const char* yp = ws + WS_Y2 + (size_t)(rt * 576 + 72 * ct + 18 * cw) * 128;
      dma16c(yp, lane * 16, sb + L_Y + cw * 2304); dma16c(yp + 1024, lane * 16, sb + L_Y + cw * 2304 + 1024); dma16c(yp + 1280, lane * 16, sb + L_Y + cw * 2304 + 1280);
    }
    u32 gtu;
    gl32c<0>(gtu, (64 * rt + 16 * cw + l15) * 4, ws + WS_G);
    asm volatile("s_waitcnt vmcnt(0)" : "+v"(gtu)::"memory");
    prog_set(sb + W_XCH + 4 * wv, 4);
    prog_wait(sb + W_XCH + 32 + 4 * (lane & 3), 4);
    const float gt = u2f(gtu);
    f32x4 acc[5], tb[5];
    const u32 xa = xw + l15 * 144 + 8 * g;
    {
      const float g1 = rbf(1.f - gt);
      const u32 ya = sb + L_Y + (4 * g + q) * 128 + 32 * cw + 8 * (lane & 3);
      sfor<5>([&](auto NT) {
        const u32x2 y = tr_read(ya + NT * 2048), xv = lds64(xa + NT * 32);
        tb[NT] = f32x4{vmul(vmul(g1, lo2f(xv.x)), lo2f(y.x)), vmul(vmul(g1, hi2f(xv.x)), hi2f(y.x)), vmul(vmul(g1, lo2f(xv.y)), lo2f(y.y)), vmul(vmul(g1, hi2f(xv.y)), hi2f(y.y))};
      });
    }
    const u32 wa = opaque(sb + L_W + (18 * (g >> 1) + 4 * (g & 1) + q) * 160 + 8 * (lane & 3)), wah = opaque(wa + 46080);
    const u32 wa2 = opaque(sb + L_W + ((4 * g + (q >> 1)) * 18 + 16 + (q & 1)) * 160 + 8 * (lane & 3));
    u32x4 wf[2][5];
    auto rd = [&](auto KS) {
      constexpr int ks = KS;
      sfor<5>([&](auto NT) {
        if constexpr (ks < 16) { const u32 a = (ks < 8 ? wa : wah) + (ks & 7) * 5760 + NT * 32; wf[ks & 1][NT] = cat(tr_read(a), tr_read(a + 1280)); }
        else wf[ks & 1][NT] = cat(tr_read(wa2 + (ks - 16) * 46080 + NT * 32), tr_read(wa2 + (ks - 16) * 46080 + 5760 + NT * 32));
      });
    };
    rd(IC(0));
    STAMP(6);
    await(fb, F_O + ((2 * rt + (cw >> 1)) * 32 + (lane & 31)) * 8, tag);
    acquire();
    __builtin_amdgcn_s_setprio(3);
    u32x4 of[18]; u32 oe[8];
    const char* ob = ws + WS_O + (size_t)(4 * rt + cw) * 18432;
    sfor<16>([&](auto F) { gl128c<(F & 3) * 1024>(of[F], lane * 16, ob + (F >> 2) * 4096); });
    sfor<8>([&](auto F) { gl32c<(F >> 2) * 1024 + (F & 3) * 64>(oe[F], 256 * g + 4 * l15, ob + 16384); });
    STAMP(7);
    sfor<18>([&](auto KS) {
      constexpr int ks = KS;
      SCHED();
      if constexpr (ks < 16 && ks % 2 == 0) asm volatile("s_waitcnt vmcnt(%2)" : "+v"(of[ks]), "+v"(of[ks + 1]) : "i"(22 - ks) : "memory");
      if constexpr (ks == 16) {
        asm volatile("s_waitcnt vmcnt(0)" : "+v"(oe[0]), "+v"(oe[1]), "+v"(oe[2]), "+v"(oe[3]), "+v"(oe[4]), "+v"(oe[5]), "+v"(oe[6]), "+v"(oe[7])::"memory");
        of[16] = u32x4{oe[0], oe[1], oe[2], oe[3]}; of[17] = u32x4{oe[4], oe[5], oe[6], oe[7]};
      }
      if constexpr (ks < 17) rd(IC(ks + 1));
      sfor<5>([&](auto NT) { acc[NT] = mfma16(wf[ks & 1][NT], of[ks], ks == 0 ? f32x4{0.f, 0.f, 0.f, 0.f} : acc[NT]); });
      sfor<5>([&](auto) { __builtin_amdgcn_sched_group_barrier(0x8, 1, 0); __builtin_amdgcn_sched_group_barrier(0x100, 2, 0); });
    });
    SCHED();
    sfor<5>([&](auto NT) {
      constexpr int nt = NT;
      if (nt < 4 || g < 2)
        st64(xa + nt * 32, u32x2{pk2(fmaf(gt, acc[nt][0], tb[nt][0]), fmaf(gt, acc[nt][1], tb[nt][1])), pk2(fmaf(gt, acc[nt][2], tb[nt][2]), fmaf(gt, acc[nt][3], tb[nt][3]))});
    });
    char* orow = (char*)(out + (size_t)(64 * rt + 16 * cw) * 576 + 72 * ct);
    u32x4 t[3];
#pragma unroll
    for (int it = 0; it < 3; it++) t[it] = lds128(xw + it * 1008 + lane * 16);
#pragma unroll
    for (int it = 0; it < 3; it++)
      if (lane < (it < 2 ? 63 : 18)) *(GAS u32x4*)(orow + (u32)((7 * it + r7) * 1152 + 16 * c9)) = t[it];
  }
  STAMP_END();
}
extern "C" __global__ void gm_end() {}

extern "C" void kernel_launch(void* const* d_in, const int* in_sizes, int n_in, void* d_out, int out_size, void* d_ws, size_t ws_size,
                              hipStream_t stream) {
  (void)hipFuncSetAttribute((const void*)gm_main, hipFuncAttributeMaxDynamicSharedMemorySize, L_END);
  gm_main<<<256, 768, L_END, stream>>>((const u16*)d_in[0], (const u16*)d_in[1], (const u16*)d_in[2], (const u16*)d_in[4], (const u16*)d_in[5],
                                       (const u16*)d_in[6], (const u16*)d_in[7], (const u16*)d_in[8], (const u16*)d_in[9], (char*)d_ws, (u16*)d_out);
}
```

```cpp
#include <hip/hip_runtime.h>
#include <hip/hip_bf16.h>
#include <utility>

typedef unsigned short u16;
typedef unsigned int u32;
typedef u32 u32x2 __attribute__((ext_vector_type(2)));
typedef u32 u32x4 __attribute__((ext_vector_type(4)));
typedef short s16x4 __attribute__((ext_vector_type(4)));
typedef __bf16 bf16x8 __attribute__((ext_vector_type(8)));
typedef __bf16 bf16x2 __attribute__((ext_vector_type(2)));
typedef float f32x2 __attribute__((ext_vector_type(2)));
typedef float f32x4 __attribute__((ext_vector_type(4)));
typedef float f32x16 __attribute__((ext_vector_type(16)));
#define LAS __attribute__((address_space(3)))
#define GAS __attribute__((address_space(1)))
#define DEVI __device__ __forceinline__
#define IC(n) std::integral_constant<int, (n)>{}

constexpr size_t WS_QKV = 0;
constexpr size_t WS_O   = WS_QKV + 96ull * 2048 * 40;
constexpr size_t WS_UR  = WS_O + 32ull * 2048 * 40;
constexpr size_t WS_Y2  = WS_UR + 576ull * 4096;
constexpr size_t WS_G   = WS_Y2 + 576ull * 4096;
constexpr size_t WS_F   = WS_G + 8192;
constexpr u32 F_EP = 0, F_1 = 1024, F_U = F_1 + 256 * 512, F_O = F_U + 288 * 256, F_Y = F_O + 256 * 512;

template <typename F, int... I> DEVI void sfor_impl(F&& f, std::integer_sequence<int, I...>) { (f(std::integral_constant<int, I>{}), ...); }
template <int N, typename F> DEVI void sfor(F&& f) { sfor_impl(f, std::make_integer_sequence<int, N>{}); }

DEVI u32 pk2(float a, float b) { f32x2 v = {a, b}; return __builtin_bit_cast(u32, __builtin_convertvector(v, bf16x2)); }
DEVI u32 f2u(float a) { return __builtin_bit_cast(u32, a); }
DEVI float u2f(u32 a) { return __builtin_bit_cast(float, a); }
DEVI float lo2f(u32 w) { return __builtin_bit_cast(float, w << 16); }
DEVI float hi2f(u32 w) { return __builtin_bit_cast(float, w & 0xffff0000u); }
DEVI float rbf(float a) { return lo2f(pk2(a, a)); }
DEVI f32x4 mfma16(u32x4 a, u32x4 b, f32x4 c) { return __builtin_amdgcn_mfma_f32_16x16x32_bf16(__builtin_bit_cast(bf16x8, a), __builtin_bit_cast(bf16x8, b), c, 0, 0, 0); }
DEVI f32x16 mfma32(u32x4 a, u32x4 b, f32x16 c) { return __builtin_amdgcn_mfma_f32_32x32x16_bf16(__builtin_bit_cast(bf16x8, a), __builtin_bit_cast(bf16x8, b), c, 0, 0, 0); }
DEVI f32x16 mfma32k8(u32x2 a, u32x2 b, f32x16 c) { return __builtin_amdgcn_mfma_f32_32x32x8bf16_1k(__builtin_bit_cast(s16x4, a), __builtin_bit_cast(s16x4, b), c, 0, 0, 0); }
DEVI u32x2 tr_read(u32 addr) { return __builtin_bit_cast(u32x2, __builtin_amdgcn_ds_read_tr16_b64_v4i16((LAS s16x4*)addr)); }
DEVI u32x4 lds128(u32 addr) { return *(LAS u32x4*)addr; }
DEVI u32x2 lds64(u32 addr) { return *(LAS u32x2*)addr; }
DEVI u32 lds32(u32 addr) { return *(LAS u32*)addr; }
DEVI void st128(u32 addr, u32x4 v) { *(LAS u32x4*)addr = v; }
DEVI void st64(u32 addr, u32x2 v) { *(LAS u32x2*)addr = v; }
DEVI void st32(u32 addr, u32 v) { *(LAS u32*)addr = v; }
DEVI u32x4 cat(u32x2 a, u32x2 b) { return u32x4{a.x, a.y, b.x, b.y}; }
DEVI float vfma(float a, float b, float c) { float d; asm("v_fma_f32 %0, %1, %2, %3" : "=v"(d) : "v"(a), "v"(b), "v"(c)); return d; }
DEVI float vmul(float a, float b) { float d; asm("v_mul_f32 %0, %1, %2" : "=v"(d) : "v"(a), "v"(b)); return d; }
DEVI float vadd(float a, float b) { float d; asm("v_add_f32 %0, %1, %2" : "=v"(d) : "v"(a), "v"(b)); return d; }
DEVI f32x4 vadd4(f32x4 a, f32x4 b) { return f32x4{vadd(a[0], b[0]), vadd(a[1], b[1]), vadd(a[2], b[2]), vadd(a[3], b[3])}; }
DEVI u32 opaque(u32 v) { asm volatile("" : "+v"(v)); return v; }
DEVI void dma16(const void* sbase, u32 voff, u32 ldsbase) {
  asm volatile("s_mov_b32 m0, %2\n\ts_nop 0\n\tglobal_load_lds_dwordx4 %0, %1" ::"v"(voff), "s"(sbase), "s"(ldsbase) : "memory");
}
template <int NL> DEVI void dma16n(const void* sbase, u32 voff, u32 ldsbase) {
  asm volatile("s_mov_b32 m0, %2\n\ts_mov_b32 exec_hi, %3\n\tglobal_load_lds_dwordx4 %0, %1\n\ts_mov_b32 exec_hi, -1" ::"v"(voff), "s"(sbase), "s"(ldsbase),
               "i"((1u << (NL - 32)) - 1u) : "memory");
}
template <int OFF> DEVI void gl128(u32x4& d, u32 voff, const void* sbase) { asm volatile("global_load_dwordx4 %0, %1, %2 offset:%3" : "=v"(d) : "v"(voff), "s"(sbase), "i"(OFF)); }
template <int OFF> DEVI void gl64(u32x2& d, u32 voff, const void* sbase) { asm volatile("global_load_dwordx2 %0, %1, %2 offset:%3" : "=v"(d) : "v"(voff), "s"(sbase), "i"(OFF)); }
DEVI void dma16c(const void* sbase, u32 voff, u32 ldsbase) {
  asm volatile("s_mov_b32 m0, %2\n\ts_nop 0\n\tglobal_load_lds_dwordx4 %0, %1 sc1" ::"v"(voff), "s"(sbase), "s"(ldsbase) : "memory");
}
template <int OFF> DEVI void gl128c(u32x4& d, u32 voff, const void* sbase) { asm volatile("global_load_dwordx4 %0, %1, %2 offset:%3 sc1" : "=v"(d) : "v"(voff), "s"(sbase), "i"(OFF)); }
template <int OFF> DEVI void gl32c(u32& d, u32 voff, const void* sbase) { asm volatile("global_load_dword %0, %1, %2 offset:%3 sc1" : "=v"(d) : "v"(voff), "s"(sbase), "i"(OFF)); }
DEVI void gst128(u32x4 v, u32 voff, void* sbase) { asm volatile("global_store_dwordx4 %0, %1, %2 sc1\n\ts_nop 1" ::"v"(voff), "v"(v), "s"(sbase) : "memory"); }
DEVI void gst64(u32x2 v, u32 voff, void* sbase) { asm volatile("global_store_dwordx2 %0, %1, %2 sc1" ::"v"(voff), "v"(v), "s"(sbase) : "memory"); }
DEVI void gst32(u32 v, u32 voff, void* sbase) { asm volatile("global_store_dword %0, %1, %2 sc1" ::"v"(voff), "v"(v), "s"(sbase) : "memory"); }
#define CODE_PF(from, to, ck, step, lds)                                                                                              \
  do {                                                                                                                                \
    u32 lo_, hi_, len_;                                                                                                               \
    asm volatile("s_getpc_b64 s[20:21]\n\ts_add_u32 %0, s20, " from "@rel32@lo+4\n\ts_addc_u32 %1, s21, " from "@rel32@hi+12\n\ts_mov_b32 %2, " to "-" from \
                 : "=s"(lo_), "=s"(hi_), "=s"(len_)::"s20", "s21", "scc");                                                            \
    const char* code_ = (const char*)(((unsigned long long)hi_ << 32) | lo_);                                                         \
    for (u32 o_ = (ck) * 1024; o_ < len_; o_ += (step) * 1024) { const u32 a_ = o_ + (threadIdx.x & 63) * 16; dma16(code_, a_ < len_ ? a_ : 0u, lds); } \
  } while (0)
template <int N> DEVI void wait_vm() { asm volatile("s_waitcnt vmcnt(%0)" ::"i"(N > 63 ? 63 : N) : "memory"); }
DEVI void wait_lgkm() { asm volatile("s_waitcnt lgkmcnt(0)" ::: "memory"); }
DEVI void bar() { asm volatile("s_barrier" ::: "memory"); }
DEVI u32 rfl(u32 v) { return __builtin_amdgcn_readfirstlane(v); }
#define SCHED() __builtin_amdgcn_sched_barrier(0)
#define STAMP(i)
#define STAMP_END()

constexpr float QSCALE = 0.23570226039551584f * 1.4426950408889634f;

#define POLL_SLEEP 8
#define POLL_FAST 4
#define WODELAY 16
#define SSM_EARLY 1
#define WO_EARLY 1
#define SPDELAY 30
#define LDPOL "sc1"
#define FSPOL "sc1"
#define POLLW 4
#define VDELAY 3

constexpr int L_SLOT = 32768, L_WOFF = 16384, L_STG = 94208, L_GW = 131072, L_PT = 133120, L_TILE = 17408;
constexpr int L_K = 0, L_V = 21760, L_C = 41536, L_R = L_PT, L_FL = 152320, L_DUMMY = L_FL + 256, L_Q = 153600, L_END = 163840;
constexpr int KC_STR = 4640, CS_U = 4 * KC_STR, CS_SZ = CS_U + 4096 + 128;
constexpr int L_W = L_C, L_X = L_W + 576 * 160, L_Y = L_X + 9216;
static_assert(L_Y + 9216 <= L_FL && L_C + 2 * CS_SZ <= L_STG && L_C + 4 * CS_SZ <= L_PT && L_PT + L_TILE <= L_FL, "lds map");
constexpr int W_PROG = L_FL, W_XCH = L_FL + 64, W_GO = L_FL + 128, W_TAG = L_FL + 144;

DEVI u32 lds_peek(u32 a) { return __hip_atomic_load((LAS u32*)a, __ATOMIC_RELAXED, __HIP_MEMORY_SCOPE_WORKGROUP); }
DEVI void prog_set(u32 a, u32 n) { asm volatile("" ::: "memory"); __hip_atomic_store((LAS u32*)a, n, __ATOMIC_RELAXED, __HIP_MEMORY_SCOPE_WORKGROUP); }
DEVI void prog_wait(u32 a, u32 n) { while (__builtin_amdgcn_ballot_w64(lds_peek(a) < n)) __builtin_amdgcn_s_sleep(1); asm volatile("" ::: "memory"); }
DEVI void publish(char* fb, u32 off, u32x2 tag, bool on) { wait_vm<0>(); if (on) asm volatile("global_store_dwordx2 %0, %1, %2 " FSPOL ::"v"(off), "v"(tag), "s"(fb) : "memory"); }
template <int SLP> DEVI void await(const char* fb, u32 off, u32x2 tag) {
  for (int spin = 0; spin < (1 << 20); spin++) {
    u32x2 v;
    asm volatile("global_load_dwordx2 %0, %1, %2 " LDPOL "\n\ts_waitcnt vmcnt(0)" : "=&v"(v) : "v"(off), "s"(fb) : "memory");
    if (!__builtin_amdgcn_ballot_w64(v.x != tag.x || v.y != tag.y)) break;
    __builtin_amdgcn_s_sleep(SLP);
  }
}

DEVI void prep_issue(const u16* Bk, const u16* Bv, const u16* shk, int row0, int col, u32 tb, u32 pw, int lane, u32x4& sh) {
  const int r8 = lane >> 3, c8 = lane & 7;
  sfor<2>([&](auto I) {
    const int j = 2 * pw + I;
    int row = row0 + 8 * j + r8 - 3; row = row < 0 ? 0 : row;
    dma16(Bk + col, row * 1152 + 16 * (c8 ^ j), tb + 1024 * j);
    dma16(Bv + col, (row0 + 8 * j + r8) * 1152 + 16 * (c8 ^ j), tb + 9216 + 1024 * j);
  });
  dma16(Bk + col, (row0 + 61 + (r8 > 2 ? 2 : r8)) * 1152 + 16 * c8, tb + 8192);
  gl128<0>(sh, 8 * col + 16 * (8 * pw + r8), shk);
}
template <typename F> DEVI void prep_compute(int row0, int col, u32 tb, u32 pw, int lane, u32x4 sh, char* ws, F&& mid) {
  const int c8 = lane & 7, cp = 8 * pw + (lane >> 3);
  const u32 o0 = tb + 1024 * c8 + 16 * ((cp >> 2) ^ c8) + 4 * (cp & 3), o1 = tb + 1024 * c8 + 16 * ((cp >> 2) ^ ((c8 + 1) & 7)) + 4 * (cp & 3);
  row0 += 8 * c8;
  u32 kk[11], vv[8];
#pragma unroll
  for (int i = 0; i < 11; i++) kk[i] = lds32((i < 8 ? o0 : o1) + 128 * i);
#pragma unroll
  for (int i = 0; i < 8; i++) vv[i] = lds32(o0 + 9216 + 128 * i);
  if (row0 == 0) { kk[0] = 0; kk[1] = 0; kk[2] = 0; }
  const u32 go = 2 * cp * 4096 + 2 * (2040 - row0);
  char* dst = ws + WS_UR + (size_t)col * 4096;
  float r[8];
  {
    const float a0 = lo2f(sh.x), a1 = hi2f(sh.x), a2 = lo2f(sh.y), a3 = hi2f(sh.y);
#pragma unroll
    for (int i = 0; i < 8; i++) r[i] = vmul(vfma(lo2f(kk[i + 3]), a0, vfma(lo2f(kk[i + 2]), a1, vfma(lo2f(kk[i + 1]), a2, vmul(lo2f(kk[i]), a3)))), lo2f(vv[i]));
    gst128(u32x4{pk2(r[7], r[6]), pk2(r[5], r[4]), pk2(r[3], r[2]), pk2(r[1], r[0])}, go, dst);
  }
  mid();
  {
    const float a0 = lo2f(sh.z), a1 = hi2f(sh.z), a2 = lo2f(sh.w), a3 = hi2f(sh.w);
#pragma unroll
    for (int i = 0; i < 8; i++) r[i] = vmul(vfma(hi2f(kk[i + 3]), a0, vfma(hi2f(kk[i + 2]), a1, vfma(hi2f(kk[i + 1]), a2, vmul(hi2f(kk[i]), a3)))), hi2f(vv[i]));
    gst128(u32x4{pk2(r[7], r[6]), pk2(r[5], r[4]), pk2(r[3], r[2]), pk2(r[1], r[0])}, go, dst + 4096);
  }
}

template <int NR> DEVI void conv_mfma(u32 cs, int rho0, int lane, f32x4 (&acc)[8]) {
  const int g = lane >> 4, l15 = lane & 15;
  u32 aa = cs + (l15 & 3) * KC_STR + 2 * (64 * (l15 >> 2) + 8 * g) + 64 * rho0;
  u32 ba = opaque(cs + CS_U + 2 * (1852 - 1792 - 4 * l15 + 8 * g) + 64 * rho0), bb = opaque(ba + 8);
  u32x4 A[8], G[8];
  sfor<5>([&](auto J) { constexpr int p = J < 4 ? J + 2 : 1; A[p] = lds128(aa + p * 512); G[p] = cat(lds64(ba + (7 - p) * 512), lds64(bb + (7 - p) * 512)); SCHED(); });
#pragma unroll 1
  for (int rho = 0; rho < NR; rho++) {
    constexpr int pa[36] = {2, 2, 3, 3, 2, 4, 3, 4, 2, 5, 1, 1, 2, 1, 3, 1, 4, 1, 5, 1, 6, 2, 0, 3, 0, 4, 0, 5, 0, 1, 0, 6, 0, 7, 0, 0};
    constexpr int pe[36] = {2, 3, 2, 3, 4, 2, 4, 3, 5, 2, 1, 2, 1, 3, 1, 4, 1, 5, 1, 6, 1, 0, 2, 0, 3, 0, 4, 0, 5, 0, 1, 0, 6, 0, 7, 0};
    constexpr int rdc[36] = {18, 19, 20, 21, 22, 23, 0, 1, 2, -1, -1, -1, -1, -1, -1, -1, -1, -1, -1, -1, -1, 6, 7, 8, 9, 10, 11, 12, 13, 14, 15, 16, 17, 3, 4, 5};
    sfor<36>([&](auto I) {
      constexpr int i = I, r = rdc[i], x = r / 3;
      if constexpr (i == 9) { aa = opaque(aa + 64); ba = opaque(ba + 64); bb = opaque(bb + 64); }
      acc[pa[i] + pe[i]] = mfma16(A[pa[i]], G[pe[i]], acc[pa[i] + pe[i]]);
      if constexpr (r >= 0) {
        if constexpr (r % 3 == 0) A[x] = lds128(aa + x * 512);
        else if constexpr (r % 3 == 1) { const u32x2 t = lds64(ba + (7 - x) * 512); G[x].x = t.x; G[x].y = t.y; }
        else { const u32x2 t = lds64(bb + (7 - x) * 512); G[x].z = t.x; G[x].w = t.y; }
      }
      SCHED();
    });
  }
  SCHED();
}
DEVI void conv_out(f32x4 y, u32x2 u, float Dv, u32 off, char* ydst) {
  const float y0 = vfma(hi2f(u.y), Dv, y[0]), y1 = vfma(lo2f(u.y), Dv, y[1]), y2 = vfma(hi2f(u.x), Dv, y[2]), y3 = vfma(lo2f(u.x), Dv, y[3]);
  gst64(u32x2{pk2(y0, y1), pk2(y2, y3)}, off, ydst);
}

DEVI float xhalf_max(float m) { const u32 mu = f2u(m); auto r = __builtin_amdgcn_permlane32_swap(mu, mu, false, false); return fmaxf(u2f(r[0]), u2f(r[1])); }

extern "C" __global__ __launch_bounds__(768, 3) void gm_main(const u16* __restrict__ x, const u16* __restrict__ wqkv, const u16* __restrict__ wo,
                                                             const u16* __restrict__ Bk, const u16* __restrict__ Bv, const u16* __restrict__ shk,
                                                             const u16* __restrict__ ssmk, const u16* __restrict__ ssmD, const u16* __restrict__ gw,
                                                             char* __restrict__ ws, u16* __restrict__ out) {
  extern __shared__ __attribute__((aligned(16))) char smem[];
  const u32 sb = (u32)(size_t)(LAS char*)smem;
  const int b = blockIdx.x, xcd = b & 7, idx = b >> 3;
  const int lane = threadIdx.x & 63;
  const u32 wv = rfl(threadIdx.x >> 6);
  const bool spare = idx >= 28;
  char* const fb = ws + WS_F;
  const int qb = spare ? 0 : 1 + idx % 7, h = 4 * xcd + (spare ? idx - 28 : idx / 7);
  const int chb = wv >= 8 ? 64 * xcd + 2 * idx : 512 + 2 * h;
  u32x2 pre[2]; u32x4 cur[2];
  STAMP(0);
  {
    const int hg = xcd & 1, ri = idx & 3, rb = 4 * (xcd >> 1) + ri, jt = idx >> 2;
    const int r8 = lane >> 3, c8 = lane & 7;
    const int rot = ((jt + ri) % 3) * 3;
    if (wv < 4 && !spare) {
      const u32 lw = wv;
      const u32 xo = (32 * lw + r8) * 1152 + 16 * (c8 ^ r8);
      const char* xb = (const char*)(x + (size_t)rb * 128 * 576);
      u32 wof[2];
#pragma unroll
      for (int i = 0; i < 2; i++) {
        const int ch = (lane & 15) ^ (((lane >> 4) | (i << 2)) << 1), ln = 14 * hg + 2 * jt + (ch >> 3);
        wof[i] = ((lane >> 4) + 16 * lw) * 3456 + 128 * (ln > 26 ? 26 : ln) + 16 * (ch & 7);
      }
      const char* wb = (const char*)wqkv;
      auto issue = [&](int c) {
        const u32 sl = sb + (c & 3) * L_SLOT;
        c += rot; c = c >= 9 ? c - 9 : c;
        sfor<4>([&](auto I) { dma16(xb + c * 128 + I * 9216, xo, sl + (4 * lw + I) * 1024); });
        sfor<4>([&](auto I) { dma16(wb + (64 * c + 4 * I) * 3456, wof[I >> 1], sl + L_WOFF + (4 * lw + I) * 1024); });
      };
      if (hg == 1 && jt == 6 && lw == 0) { dma16(gw, lane * 16, sb + L_GW); dma16(gw, 128 + lane * 16, sb + L_GW + 128); }
      issue(0);
      auto wopf = [&]() {
        const char* wp = (const char*)wo + (idx == 27 ? 663552 - 24576 : idx * 23712) + lw * 6144;
        sfor<6>([&](auto I) { dma16(wp + I * 1024, lane * 16, sb + L_DUMMY); });
      };
#pragma unroll 1
      for (int n = 1; n < 8; n++) { issue(n); wait_vm<8>(); bar(); }
      issue(8);
      wopf();
      wait_vm<14>(); bar();
      wait_vm<6>(); bar();
      bar();
    } else if (wv >= 8 || (spare && wv >= 4)) {
      const bool ex = wv < 8;
      const int row0 = ex ? 256 * xcd + 64 * ri : 64 * idx, col = ex ? 512 : 64 * xcd;
      const u32 tb = sb + (ex ? 0 : L_PT);
      u32x2 e;
      asm volatile("global_load_dwordx2 %0, %1, %2 sc1" : "=v"(e) : "v"(F_EP), "s"(fb));
      u32x4 sh;
      prep_issue(Bk, Bv, shk, row0, col, tb, wv & 3, lane, sh);
      {
        const u32 o = (threadIdx.x & 255) * 16, op = o ? o - 8 : 0;
        sfor<2>([&](auto J) { const u16* kp = ssmk + (size_t)(chb + J) * 2048; gl128<0>(cur[J], o, kp); gl64<0>(pre[J], op, kp); });
      }
      if (wv == 8) st32(sb + L_FL + 4 * lane, 0u);
      if (!spare) bar();
      asm volatile("s_waitcnt vmcnt(0)" : "+v"(e), "+v"(sh)::"memory");
      e.x += 1u; e.y += e.x == 0u;
      if (wv == 8) st64(sb + W_TAG, e);
      wait_lgkm();
      bar();
      prep_compute(row0, col, tb, wv & 3, lane, sh, ws, [&]() { if (!spare) { SCHED(); bar(); SCHED(); } });
      bar();
      if (!spare) { bar(); bar(); }
      publish(fb, F_U + (((col >> 1) + 8 * (wv & 3) + lane) * 32 + (row0 >> 6)) * 8, e, lane < 8);
      if (!spare)
#pragma unroll 1
        for (int n = 0; n < 4; n++) bar();
    } else if (spare) {
      CODE_PF("gm_main", "gm_end", 4 * ri + wv, 16, sb + L_DUMMY);
      bar(); bar();
    } else {
      const int cw = wv - 4, g = lane >> 4, l15 = lane & 15, wm = cw & 1, wn = cw >> 1;
      const u32 xra = sb + (64 * wm + l15) * 128 + 16 * (g ^ (l15 & 7));
      const u32 xb[2][2] = {{opaque(xra), opaque(xra + 2 * L_SLOT)}, {opaque(xra ^ 64), opaque((xra ^ 64) + 2 * L_SLOT)}};
      u32x4 wf[2][4], xf[2][4];
      f32x4 acc[4][4];
      const int ln = 14 * hg + 2 * jt + wn;
      u32 pof; bool pon;
      {
        const int r64 = 2 * rb + wm, seg = ln / 9, lc = ln - 9 * seg, hc = (64 * lc) / 18 + (lane >> 2), li = lane & 1, nx = (lane >> 1) & 1;
        pon = ln == 27 ? lane < 8 : (lane < 20 && hc < 32 && ((18 * hc + 17 * li) >> 6) == lc && (!nx || (seg > 0 && r64 < 28)));
        pof = opaque(ln == 27 ? F_Y + ((r64 * 8 + lane) * 80 + 72) * 8 : F_1 + ((hc * 8 + (r64 >> 2) + nx) * 64 + seg * 16 + li * 8 + (r64 & 3) + (nx ? 0 : 4)) * 8);
      }
      __builtin_amdgcn_s_setprio(2);
      if (ln == 27) {
#pragma unroll
        for (int i = 0; i < 4; i++) acc[i][0] = f32x4{0.f, 0.f, 0.f, 0.f};
#pragma unroll 1
        for (int c = 0; c < 9; c++) {
          bar();
          const int ca = c + rot >= 9 ? c + rot - 9 : c + rot;
#pragma unroll
          for (int ks = 0; ks < 2; ks++) {
            const u32x4 gv = lds128(sb + L_GW + ca * 128 + ks * 64 + 16 * g);
#pragma unroll
            for (int i = 0; i < 4; i++) acc[i][0] = mfma16(gv, lds128(xb[ks][0] + (c & 3) * L_SLOT + 2048 * i), acc[i][0]);
          }
          wait_lgkm();
        }
        bar();
        const float p = rbf(g == 0 ? acc[0][0][0] : g == 1 ? acc[1][0][0] : g == 2 ? acc[2][0][0] : acc[3][0][0]);
        gst32(f2u(rbf(1.f / (1.f + __expf(-p)))), lane * 4, ws + WS_G + (rb * 128 + 64 * wm) * 4);
      } else {
    u32 wb[4][2];
    {
      const int q = l15 >> 2, p = lane & 3, f = q | ((g & 1) << 2);
#pragma unroll
      for (int nt = 0; nt < 4; nt++) {
        const u32 a = sb + L_WOFF + (8 * g + q) * 256 + (((8 * wn + 2 * nt + (p >> 1)) ^ (f << 1)) << 4) + 8 * (p & 1);
        wb[nt][0] = opaque(a); wb[nt][1] = opaque(a + 2 * L_SLOT);
      }
    }
    auto rd = [&](auto KS, auto C) {
      constexpr int ks = KS, c = C, so = (c & 1) * L_SLOT, hi = (c >> 1) & 1;
      sfor<4>([&](auto I) {
        xf[ks][I] = lds128(xb[ks][hi] + so + 2048 * I);
        wf[ks][I] = cat(tr_read(wb[I][hi] + so + ks * 8192), tr_read(wb[I][hi] + so + ks * 8192 + 1024));
      });
    };
    auto mm = [&](auto KS, auto FIRST) {
      constexpr int ks = KS;
      sfor<16>([&](auto I) {
        constexpr int mo[16] = {0, 1, 0, 1, 2, 2, 0, 1, 2, 3, 3, 3, 0, 1, 2, 3}, no[16] = {0, 0, 1, 1, 0, 1, 2, 2, 2, 0, 1, 2, 3, 3, 3, 3};
        constexpr int mt = mo[I], nt = no[I];
        acc[mt][nt] = mfma16(xf[ks][mt], wf[ks][nt], FIRST ? f32x4{0.f, 0.f, 0.f, 0.f} : acc[mt][nt]);
      });
    };
    auto ilv = [&]() {
      sfor<12>([&](auto) { __builtin_amdgcn_sched_group_barrier(0x8, 1, 0); __builtin_amdgcn_sched_group_barrier(0x100, 1, 0); });
      __builtin_amdgcn_sched_group_barrier(0x8, 4, 0);
    };
    const float sc = ln < 9 ? QSCALE : 1.f;
    char* qo = ws + WS_QKV + (size_t)((2 * rb + wm) * 1728 + 64 * ln) * 128;
    SCHED();
    bar();
    STAMP(1);
    rd(IC(0), IC(0));
    sfor<9>([&](auto C) {
      constexpr int c = C;
      SCHED();
      rd(IC(1), C); mm(IC(0), IC(c == 0)); ilv();
      SCHED();
      if constexpr (c == 8) wait_lgkm();
      bar();
      SCHED();
      if constexpr (c < 8) { rd(IC(0), IC(c + 1)); mm(IC(1), IC(0)); ilv(); }
    });
    STAMP(2);
    const u32 stg = sb + L_STG + cw * 9216;
    const u32 sa = stg + l15 * 144 + 8 * g, ra = stg + r8 * 144 + 16 * c8, go = lane * 16;
    u32x4 t[4][2];
    sfor<6>([&](auto NT) {
      constexpr int nt = NT;
      if constexpr (nt < 4) sfor<4>([&](auto M) { acc[M][nt] = mfma16(xf[1][M], wf[1][nt], acc[M][nt]); });
      SCHED();
      if constexpr (nt > 0 && nt < 5) {
        sfor<4>([&](auto M) {
          const f32x4 v = acc[M][nt - 1] * sc;
          st64(sa + (nt - 1) * 2304 + 32 * M, u32x2{pk2(v[0], v[1]), pk2(v[2], v[3])});
        });
        t[nt - 1][0] = lds128(ra + (nt - 1) * 2304); t[nt - 1][1] = lds128(ra + (nt - 1) * 2304 + 1152);
      }
      if constexpr (nt > 1) { gst128(t[nt - 2][0], go, qo + (nt - 2) * 2048); gst128(t[nt - 2][1], go, qo + (nt - 2) * 2048 + 1024); }
      SCHED();
    });
      }
      STAMP(8);
      publish(fb, pof, lds64(sb + W_TAG), pon);
    }
  }
  if (wv < 8) prog_set(sb + W_PROG + 4 * wv, 1);
  STAMP(3);
  const u32x2 tag = lds64(sb + W_TAG);
  const int cw = wv & 3;
  if (wv >= (spare ? 4u : 8u)) {
    const int tid = threadIdx.x & 255;
    const u32 cb = sb + L_C + (wv >= 8 ? 0 : 2 * CS_SZ);
    const u32 fo = F_U + ((chb >> 1) * 32 + 24 - 8 * cw + (lane & 7)) * 8;
    if (spare) __builtin_amdgcn_s_sleep(SPDELAY);
    u32x2 fv;
    asm volatile("global_load_dwordx2 %0, %1, %2 " LDPOL : "=v"(fv) : "v"(fo), "s"(fb));
    const u32 dv = *(const __attribute__((address_space(4))) u32*)(ssmD + chb);
    {
      const u32 z0 = opaque(cw * KC_STR + 16 * lane), z1 = opaque(CS_U + 4096 + 16 * (lane & 7)), za = cb + (lane < 24 ? z0 : z1);
      st128(za, u32x4{0u, 0u, 0u, 0u}); st128(za + CS_SZ, u32x4{0u, 0u, 0u, 0u});
    }
    f32x4 acc[8];
#pragma unroll
    for (int c = 0; c < 8; c++) { acc[c] = f32x4{0.f, 0.f, 0.f, 0.f}; asm volatile("" : "+v"(acc[c])); }
    asm volatile("s_waitcnt vmcnt(1)" : "+v"(cur[0]), "+v"(cur[1]), "+v"(pre[0]), "+v"(pre[1])::"memory");
#pragma unroll
    for (int j = 0; j < 2; j++) {
      const u32 cs = cb + j * CS_SZ + 384 + 16 * tid;
      const u32 w0 = tid ? pre[j].x : 0u, w1 = tid ? pre[j].y : 0u, w2 = cur[j].x, w3 = cur[j].y, w4 = cur[j].z, w5 = cur[j].w;
      const u32 a01 = __builtin_amdgcn_alignbyte(w1, w0, 2), a12 = __builtin_amdgcn_alignbyte(w2, w1, 2), a23 = __builtin_amdgcn_alignbyte(w3, w2, 2),
                a34 = __builtin_amdgcn_alignbyte(w4, w3, 2), a45 = __builtin_amdgcn_alignbyte(w5, w4, 2);
      st128(cs, u32x4{a01, a12, a23, a34});
      st128(cs + KC_STR, u32x4{w1, w2, w3, w4});
      st128(cs + 2 * KC_STR, u32x4{a12, a23, a34, a45});
      st128(cs + 3 * KC_STR, cur[j]);
    }
    STAMP(1);
    asm volatile("s_waitcnt vmcnt(0)" : "+v"(fv)::"memory");
    if (__builtin_amdgcn_ballot_w64(fv.x != tag.x || fv.y != tag.y)) await<POLL_SLEEP>(fb, fo, tag);
    STAMP(2);
    sfor<2>([&](auto J) { dma16c(ws + WS_UR + (size_t)(chb + J) * 4096, tid * 16, cb + CS_U + cw * 1024 + J * CS_SZ); });
    wait_vm<0>();
    prog_set(sb + W_XCH + 4 * wv, 1);
    const int tm = cw >> 1, pi = cw & 1;
    const u32 cs = cb + tm * CS_SZ;
    prog_wait(sb + W_XCH + 4 * (wv & 12) + 4 * (lane & 3), 1);
    STAMP(4);
    conv_mfma<4>(cs, 4 * pi, lane, acc);
    STAMP(5);
    __builtin_amdgcn_s_setprio(2);
    if (wv < 8) prog_wait(sb + W_XCH + 32 + 8 * tm + 4 * (lane & 1), 2);
    else prog_wait(sb + W_PROG + 4 * (lane & 7), 1);
    const u32 rA = (wv < 8 ? sb + L_C + tm * CS_SZ : sb + L_R + tm * 8192) + lane * 16;
    if (pi) { sfor<4>([&](auto J) { st128(rA + 4096 + J * 1024, __builtin_bit_cast(u32x4, acc[J])); acc[J] = acc[4 + J]; }); }
    else sfor<4>([&](auto J) { st128(rA + J * 1024, __builtin_bit_cast(u32x4, acc[4 + J])); });
    u32x2 uu[4];
#pragma unroll
    for (int j = 0; j < 4; j++) uu[j] = lds64(cs + CS_U + 4088 - 8 * lane - 512 * (4 * pi + j));
    prog_set(sb + W_XCH + 4 * wv, 2);
    prog_wait(sb + W_XCH + 4 * (wv ^ 1), 2);
    f32x4 t[4];
#pragma unroll
    for (int j = 0; j < 4; j++) t[j] = __builtin_bit_cast(f32x4, lds128(rA + (pi ^ 1) * 4096 + j * 1024));
    wait_lgkm();
    prog_set(sb + W_XCH + 4 * wv, 3);
    const u32 yo = (lane >> 4) * 73728 + (lane & 15) * 8;
    char* yd = ws + WS_Y2 + (chb + tm) * 128 + pi * 1179648;
#pragma unroll
    for (int j = 0; j < 4; j++) conv_out(vadd4(acc[j], t[j]), uu[j], tm ? hi2f(dv) : lo2f(dv), yo + j * 294912, yd);
    STAMP(8);
    publish(fb, F_Y + (((16 * pi + lane) * 8 + (chb + tm) / 72) * 80 + (chb + tm) % 72) * 8, tag, lane < 16);
  }
  if (wv < 8) {
    __builtin_amdgcn_s_setprio(1);
    const int c = lane & 31, hh = lane >> 5, q4 = (lane & 15) >> 2;
    const int w = spare ? wv ^ 4 : wv;
    const u32 kcol = 64 * w + (lane & 16) * 2 + 16 * (lane & 1) + 4 * (lane & 2);
    const u32 ka = opaque(sb + L_K + (8 * hh + q4) * 1088 + kcol), ka2 = opaque(sb + L_K + (16 + q4) * 1088 + kcol);
    const u32 va = opaque(sb + L_V + c * 1040 + 64 * w + 16 * hh);
    const u32 qa = opaque(sb + L_Q + q4 * 512 + 64 * w + (lane & 16) * 2 + 8 * (lane & 3)), qa0 = opaque(qa + 4096 * hh);
    const int thr = c - 8 * hh;
    u32 msk[8], nmk[8];
    unsigned long long cnd[16];
#pragma unroll
    for (int e = 0; e < 16; e++) cnd[e] = __builtin_amdgcn_ballot_w64(16 * (e >> 3) + (e & 7) <= thr);
#pragma unroll
    for (int j = 0; j < 8; j++) { const int k0 = 16 * (j >> 2) + 2 * (j & 3); msk[j] = opaque(k0 + 1 <= thr ? 0xffffffffu : (k0 <= thr ? 0xffffu : 0u)); nmk[j] = opaque(~msk[j]); }
    f32x16 S0;
#pragma unroll
    for (int e = 0; e < 16; e++) S0[e] = u2f(opaque(spare ? 0xff800000u : 0u));
    const u32 oo = opaque((c >> 4) * 18432 + (c & 15) * 16 + hh * 256);
    const u32 fl = sb + W_PROG + 4 * (spare ? (lane & 3) : (lane & 7));
    if (wv < 3) st128(sb + (wv < 2 ? L_K + (18 + wv) * 1088 : L_V + 18 * 1040) + lane * 16, u32x4{0x3f803f80u, 0x3f803f80u, 0x3f803f80u, 0x3f803f80u});
    if (wv == (spare ? 0u : (u32)POLLW)) {
      await<POLL_FAST>(fb, F_1 + ((h * 8 + qb) * 64 + ((lane < 48 ? lane : lane - 16) | (lane < 16 || spare ? 4 : 0))) * 8, tag);
      prog_set(sb + W_GO, 1);
    } else prog_wait(sb + W_GO, 1);
    STAMP(4);
    if (!spare || wv < 4) {
    const char* Ag = ws + WS_QKV + (size_t)(4 * qb * 1728 + 18 * h) * 128;
    const char* Kg = Ag + 576 * 128 - (qb ? 884736 : 0);
    const u32 lo = ((lane >> 3) & (qb ? 7 : 3)) * 221184 + (lane & 7) * 16, lq = ((lane >> 3) & 3) * 221184 + (lane >> 5) * 128 + (lane & 7) * 16;
#pragma unroll 1
    for (int s = wv; s <= w; s += 4) {
      dma16c(Ag + s * 256, lq, sb + L_Q + s * 1024);
      dma16c(Kg + s * 128, lo, sb + L_K + s * 1088); dma16c(Kg + s * 128 + 1024, lo, sb + L_K + (8 + s) * 1088);
      const char* eg = s < 4 ? Kg + (s < 2 ? 16 + s : 590 + s) * 128 : Ag + 2048;
      dma16c(eg, s < 4 ? lo : lq, sb + (s < 2 ? L_K + (16 + s) * 1088 : (s < 4 ? L_V + (14 + s) * 1040 : L_Q + 8192)));
    }
    __builtin_amdgcn_s_sleep(VDELAY);
    if (spare) { dma16c(Kg + (572 + w) * 128, lo, sb + L_V + (w - 4) * 1040); dma16c(Kg + (580 + w) * 128, lo, sb + L_V + (4 + w) * 1040); }
    dma16c(Kg + (576 + w) * 128, lo, sb + L_V + w * 1040); dma16c(Kg + (584 + w) * 128, lo, sb + L_V + (8 + w) * 1040);
    wait_vm<2>();
    prog_set(sb + W_PROG + 4 * wv, 2);
    }
    prog_wait(fl, 2);
    STAMP(5);
    struct KF { u32x4 a; u32x2 b; };
    struct VF { u32x4 a, b; };
    auto ldk = [&](int i) { KF k; k.a = cat(tr_read(ka + i * 64), tr_read(ka + 4352 + i * 64)); k.b = tr_read(ka2 + i * 64); return k; };
    auto ldv = [&](int i) { VF v; v.a = lds128(va + i * 64); v.b = lds128(va + i * 64 + 32); return v; };
    auto pk8 = [&](const f32x16& p, int o) { return u32x4{pk2(p[o], p[o + 1]), pk2(p[o + 2], p[o + 3]), pk2(p[o + 4], p[o + 5]), pk2(p[o + 6], p[o + 7])}; };
    const int ib = spare ? 8 - w : 1;
    const u32x4 q0 = cat(tr_read(qa0), tr_read(qa0 + 2048));
    const KF k8 = ldk(8);
    u32x2 q1 = tr_read(qa + 8192);
    const KF k0 = ldk(0);
    q1.x = hh ? 0u : q1.x; q1.y = 0u;
    auto qk = [&](const KF& k) { f32x16 z = {}; z = mfma32(k.a, q0, z); return mfma32k8(k.b, q1, z); };
    f32x16 Sm = {};
    Sm = mfma32(k8.a, q0, Sm); S0 = mfma32(k0.a, q0, S0);
    Sm = mfma32k8(k8.b, q1, Sm); S0 = mfma32k8(k0.b, q1, S0);
    KF kf[2]; VF vf[2], v8, v0;
    kf[1] = ldk(7); kf[0] = ldk(6);
    wait_vm<0>();
    prog_set(sb + W_PROG + 4 * wv, 3);
    SCHED();
#pragma unroll
    for (int e = 0; e < 16; e++) Sm[e] = __builtin_amdgcn_inverse_ballot_w64(cnd[e]) ? Sm[e] : S0[e];
    float m = __builtin_fmaxf(__builtin_fmaxf(Sm[0], Sm[1]), Sm[2]), m2 = __builtin_fmaxf(__builtin_fmaxf(Sm[3], Sm[4]), Sm[5]);
#pragma unroll
    for (int e = 6; e < 14; e += 4) { m = fmaxf(fmaxf(m, Sm[e]), Sm[e + 1]); m2 = fmaxf(fmaxf(m2, Sm[e + 2]), Sm[e + 3]); }
    m = fmaxf(fmaxf(m, Sm[14]), Sm[15]);
    m = xhalf_max(fmaxf(m, m2));
    prog_wait(fl, 3);
    v8 = ldv(8); v0 = ldv(0); vf[1] = ldv(7);
    f32x16 O;
    float l;
#pragma unroll 1
    for (int pass = 0; pass < 2; pass++) {
      q1.y = hh ? 0u : pk2(-m, lo2f(pk2(m, m)) - m);
      f32x16 S[2];
      {
        f32x16 p = mfma32k8(kf[1].b, u32x2{0u, q1.y}, Sm);
        S[1] = qk(kf[1]);
#pragma unroll
        for (int e = 0; e < 16; e++) p[e] = __builtin_amdgcn_exp2f(p[e]);
        const u32x4 pa = pk8(p, 0), pb = pk8(p, 8);
        O = f32x16{};
        O = mfma32(v8.a, pa & u32x4{msk[0], msk[1], msk[2], msk[3]}, O); O = mfma32(v8.b, pb & u32x4{msk[4], msk[5], msk[6], msk[7]}, O);
        O = mfma32(v0.a, pa & u32x4{nmk[0], nmk[1], nmk[2], nmk[3]}, O); O = mfma32(v0.b, pb & u32x4{nmk[4], nmk[5], nmk[6], nmk[7]}, O);
      }
      auto tile = [&](auto self, auto I) -> void {
        constexpr int i = I;
        if (i < ib) return;
        SCHED();
        f32x16& Sc = S[i & 1];
        f32x16 z = {};
        if constexpr (i > 1) z = mfma32(kf[(i - 1) & 1].a, q0, z);
        if constexpr (i > 2) kf[i & 1] = ldk(i - 2);
        if constexpr (i > 1) vf[(i - 1) & 1] = ldv(i - 1);
        SCHED();
#pragma unroll
        for (int e = 0; e < 8; e++) Sc[e] = __builtin_amdgcn_exp2f(Sc[e]);
        SCHED();
        if constexpr (i > 1) S[(i - 1) & 1] = mfma32k8(kf[(i - 1) & 1].b, q1, z);
        const u32x4 pa = pk8(Sc, 0);
        SCHED();
#pragma unroll
        for (int e = 8; e < 12; e++) Sc[e] = __builtin_amdgcn_exp2f(Sc[e]);
        SCHED();
        O = mfma32(vf[i & 1].a, pa, O);
#pragma unroll
        for (int e = 12; e < 16; e++) Sc[e] = __builtin_amdgcn_exp2f(Sc[e]);
        const u32x4 pb = pk8(Sc, 8);
        SCHED();
        O = mfma32(vf[i & 1].b, pb, O);
        if constexpr (i > 1) self(self, IC(i - 1));
      };
      tile(tile, IC(7));
      SCHED();
      { const u32 lu = f2u(O[10]); auto r = __builtin_amdgcn_permlane32_swap(lu, lu, false, false); l = u2f(r[0]); }
      if (pass == 1 || __all(l > 8.67e-19f && l < 1.15e18f)) break;
      q1.y = 0u;
#pragma unroll 1
      for (int i = ib; i < 8; i++) {
        const f32x16 s = qk(ldk(i));
#pragma unroll
        for (int e = 0; e < 16; e += 2) m = fmaxf(fmaxf(m, s[e]), s[e + 1]);
      }
      m = xhalf_max(m);
      kf[1] = ldk(7); v8 = ldv(8); v0 = ldv(0); kf[0] = ldk(6); vf[1] = ldv(7);
    }
    STAMP(6);
    float inv = __builtin_amdgcn_rcpf(l);
    asm volatile("s_nop 1" : "+v"(inv));
    char* Og = ws + WS_O + (size_t)(16 * qb + 2 * w) * 18432;
    gst128(u32x4{pk2(vmul(O[0], inv), vmul(O[1], inv)), pk2(vmul(O[2], inv), vmul(O[3], inv)), pk2(vmul(O[4], inv), vmul(O[5], inv)), pk2(vmul(O[6], inv), vmul(O[7], inv))},
           oo, Og + (h >> 1) * 1024 + (h & 1) * 512);
    if (hh == 0) gst32(pk2(vmul(O[8], inv), vmul(O[9], inv)), (c >> 4) * 18432 + (c & 15) * 4, Og + 16384 + h * 64);
    publish(fb, F_O + (((4 * qb + (w >> 1)) * 8 + lane) * 64 + (w & 1) * 32 + h) * 8, tag, lane < 8);
      if (b == 255 && wv == 4) {
#pragma unroll 1
        for (int spin = 0; spin < (1 << 18); spin++) {
          u32x4 v[9];
          sfor<9>([&](auto I) { gl128c<0>(v[I], (lane >> 4) * 2048 + (lane & 15) * 16, fb + F_U + I * 8192); });
          asm volatile("s_waitcnt vmcnt(0)" : "+v"(v[0]), "+v"(v[1]), "+v"(v[2]), "+v"(v[3]), "+v"(v[4]), "+v"(v[5]), "+v"(v[6]), "+v"(v[7]), "+v"(v[8])::"memory");
          u32 bad = 0;
#pragma unroll
          for (int i = 0; i < 9; i++) bad |= (v[i].x ^ tag.x) | (v[i].y ^ tag.y) | (v[i].z ^ tag.x) | (v[i].w ^ tag.y);
          if (!__builtin_amdgcn_ballot_w64(bad != 0)) break;
          __builtin_amdgcn_s_sleep(16);
        }
        if (lane == 0) gst64(tag, F_EP, fb);
      }
    STAMP_END();
    return;
  }
  {
    const int rt = 4 * xcd + (idx & 3), ct = idx >> 2, g = lane >> 4, l15 = lane & 15, q = l15 >> 2;
    prog_wait(sb + W_XCH + (spare ? 16 + 4 * (lane & 7) : 32 + 4 * (lane & 3)), 3);
    prog_wait(sb + W_PROG + 4 * (spare ? (lane & 3) : (lane & 7)), 3);
    STAMP(9);
    const int r7 = lane / 9, c9 = lane - 9 * r7;
    const u32 xw = sb + L_X + cw * 2304;
    {
      const int r6 = lane / 10, c10 = lane - 10 * r6;
      const u32 vo = (6 * cw + r6) * 1152 + 16 * (c10 > 8 ? 8 : c10);
      const char* wb = (const char*)(wo + 72 * ct);
#pragma unroll 4
      for (int i = 0; i < 24; i++) dma16n<60>(wb + i * 24 * 1152, vo, sb + L_W + (4 * i + cw) * 960);
      const u16* xp = x + (size_t)(64 * rt + 16 * cw) * 576 + 72 * ct;
      dma16n<63>(xp, r7 * 1152 + 16 * c9, xw); dma16n<63>(xp + 7 * 576, r7 * 1152 + 16 * c9, xw + 1008); dma16n<63>(xp + 9 * 576, r7 * 1152 + 16 * c9, xw + 1296);
    }
    STAMP(10);
    await<POLL_SLEEP>(fb, F_Y + ((rt * 8 + ct) * 80 + (lane < 18 ? 18 * cw + lane : 72)) * 8, tag);
    STAMP(11);
    {
      const char* yp = ws + WS_Y2 + (size_t)(rt * 576 + 72 * ct + 18 * cw) * 128;
      dma16c(yp, lane * 16, sb + L_Y + cw * 2304); dma16c(yp + 1024, lane * 16, sb + L_Y + cw * 2304 + 1024); dma16c(yp + 1280, lane * 16, sb + L_Y + cw * 2304 + 1280);
    }
    u32 gtu;
    gl32c<0>(gtu, (64 * rt + 16 * cw + l15) * 4, ws + WS_G);
    asm volatile("s_waitcnt vmcnt(0)" : "+v"(gtu)::"memory");
    prog_set(sb + W_XCH + 4 * wv, 4);
    prog_wait(sb + W_XCH + 32 + 4 * (lane & 3), 4);
    const float gt = u2f(gtu);
    f32x4 acc[5], tb[5];
    const u32 xa = xw + l15 * 144 + 8 * g;
    {
      const float g1 = rbf(1.f - gt);
      const u32 ya = sb + L_Y + (4 * g + q) * 128 + 32 * cw + 8 * (lane & 3);
      sfor<5>([&](auto NT) {
        const u32x2 y = tr_read(ya + NT * 2048), xv = lds64(xa + NT * 32);
        tb[NT] = f32x4{vmul(vmul(g1, lo2f(xv.x)), lo2f(y.x)), vmul(vmul(g1, hi2f(xv.x)), hi2f(y.x)), vmul(vmul(g1, lo2f(xv.y)), lo2f(y.y)), vmul(vmul(g1, hi2f(xv.y)), hi2f(y.y))};
      });
    }
    const u32 wa = opaque(sb + L_W + (18 * (g >> 1) + 4 * (g & 1) + q) * 160 + 8 * (lane & 3)), wah = opaque(wa + 46080);
    const u32 wa2 = opaque(sb + L_W + ((4 * g + (q >> 1)) * 18 + 16 + (q & 1)) * 160 + 8 * (lane & 3));
    u32x4 wf[2][5];
    auto rd = [&](auto KS) {
      constexpr int ks = KS;
      sfor<5>([&](auto NT) {
        if constexpr (ks < 16) { const u32 a = (ks < 8 ? wa : wah) + (ks & 7) * 5760 + NT * 32; wf[ks & 1][NT] = cat(tr_read(a), tr_read(a + 1280)); }
        else wf[ks & 1][NT] = cat(tr_read(wa2 + (ks - 16) * 46080 + NT * 32), tr_read(wa2 + (ks - 16) * 46080 + 5760 + NT * 32));
      });
    };
    rd(IC(0));
    STAMP(6);
    await<POLL_FAST>(fb, F_O + ((rt * 8 + ct) * 64 + (cw >> 1) * 32 + (lane & 31)) * 8, tag);
    STAMP(12);
    __builtin_amdgcn_s_setprio(3);
    u32x4 of[18]; u32 oe[8];
    const char* ob = ws + WS_O + (size_t)(4 * rt + cw) * 18432;
    sfor<16>([&](auto F) { gl128c<(F & 3) * 1024>(of[F], lane * 16, ob + (F >> 2) * 4096); });
    sfor<8>([&](auto F) { gl32c<(F >> 2) * 1024 + (F & 3) * 64>(oe[F], 256 * g + 4 * l15, ob + 16384); });
    STAMP(7);
    sfor<18>([&](auto KS) {
      constexpr int ks = KS;
      SCHED();
      if constexpr (ks < 16 && ks % 2 == 0) asm volatile("s_waitcnt vmcnt(%2)" : "+v"(of[ks]), "+v"(of[ks + 1]) : "i"(22 - ks) : "memory");
      if constexpr (ks == 16) {
        asm volatile("s_waitcnt vmcnt(0)" : "+v"(oe[0]), "+v"(oe[1]), "+v"(oe[2]), "+v"(oe[3]), "+v"(oe[4]), "+v"(oe[5]), "+v"(oe[6]), "+v"(oe[7])::"memory");
        of[16] = u32x4{oe[0], oe[1], oe[2], oe[3]}; of[17] = u32x4{oe[4], oe[5], oe[6], oe[7]};
      }
      if constexpr (ks < 17) rd(IC(ks + 1));
      sfor<5>([&](auto NT) { acc[NT] = mfma16(wf[ks & 1][NT], of[ks], ks == 0 ? f32x4{0.f, 0.f, 0.f, 0.f} : acc[NT]); });
      sfor<5>([&](auto) { __builtin_amdgcn_sched_group_barrier(0x8, 1, 0); __builtin_amdgcn_sched_group_barrier(0x100, 2, 0); });
    });
    SCHED();
    STAMP(13);
    sfor<5>([&](auto NT) {
      constexpr int nt = NT;
      if (nt < 4 || g < 2)
        st64(xa + nt * 32, u32x2{pk2(fmaf(gt, acc[nt][0], tb[nt][0]), fmaf(gt, acc[nt][1], tb[nt][1])), pk2(fmaf(gt, acc[nt][2], tb[nt][2]), fmaf(gt, acc[nt][3], tb[nt][3]))});
    });
    char* orow = (char*)(out + (size_t)(64 * rt + 16 * cw) * 576 + 72 * ct);
    u32x4 t[3];
#pragma unroll
    for (int it = 0; it < 3; it++) t[it] = lds128(xw + it * 1008 + lane * 16);
#pragma unroll
    for (int it = 0; it < 3; it++)
      if (lane < (it < 2 ? 63 : 18)) *(GAS u32x4*)(orow + (u32)((7 * it + r7) * 1152 + 16 * c9)) = t[it];
  }
  STAMP_END();
}
extern "C" __global__ void gm_end() {}

extern "C" void kernel_launch(void* const* d_in, const int* in_sizes, int n_in, void* d_out, int out_size, void* d_ws, size_t ws_size,
                              hipStream_t stream) {
  (void)hipFuncSetAttribute((const void*)gm_main, hipFuncAttributeMaxDynamicSharedMemorySize, L_END);
  gm_main<<<256, 768, L_END, stream>>>((const u16*)d_in[0], (const u16*)d_in[1], (const u16*)d_in[2], (const u16*)d_in[4], (const u16*)d_in[5],
                                       (const u16*)d_in[6], (const u16*)d_in[7], (const u16*)d_in[8], (const u16*)d_in[9], (char*)d_ws, (u16*)d_out);
}
```

```cpp
#include <hip/hip_runtime.h>
#include <hip/hip_bf16.h>
#include <utility>

typedef unsigned short u16;
typedef unsigned int u32;
typedef u32 u32x2 __attribute__((ext_vector_type(2)));
typedef u32 u32x4 __attribute__((ext_vector_type(4)));
typedef short s16x4 __attribute__((ext_vector_type(4)));
typedef __bf16 bf16x8 __attribute__((ext_vector_type(8)));
typedef __bf16 bf16x2 __attribute__((ext_vector_type(2)));
typedef float f32x2 __attribute__((ext_vector_type(2)));
typedef float f32x4 __attribute__((ext_vector_type(4)));
typedef float f32x16 __attribute__((ext_vector_type(16)));
#define LAS __attribute__((address_space(3)))
#define GAS __attribute__((address_space(1)))
#define DEVI __device__ __forceinline__
#define IC(n) std::integral_constant<int, (n)>{}

constexpr size_t WS_QKV = 0;
constexpr size_t WS_O   = WS_QKV + 96ull * 2048 * 40;
constexpr size_t WS_UR  = WS_O + 32ull * 2048 * 40;
constexpr size_t WS_Y2  = WS_UR + 576ull * 4096;
constexpr size_t WS_G   = WS_Y2 + 576ull * 4096;
constexpr size_t WS_F   = WS_G + 8192;
constexpr u32 F_EP = 0, F_1 = 1024, F_U = F_1 + 256 * 512, F_O = F_U + 288 * 256, F_Y = F_O + 256 * 512;

template <typename F, int... I> DEVI void sfor_impl(F&& f, std::integer_sequence<int, I...>) { (f(std::integral_constant<int, I>{}), ...); }
template <int N, typename F> DEVI void sfor(F&& f) { sfor_impl(f, std::make_integer_sequence<int, N>{}); }

DEVI u32 pk2(float a, float b) { f32x2 v = {a, b}; return __builtin_bit_cast(u32, __builtin_convertvector(v, bf16x2)); }
DEVI u32 f2u(float a) { return __builtin_bit_cast(u32, a); }
DEVI float u2f(u32 a) { return __builtin_bit_cast(float, a); }
DEVI float lo2f(u32 w) { return __builtin_bit_cast(float, w << 16); }
DEVI float hi2f(u32 w) { return __builtin_bit_cast(float, w & 0xffff0000u); }
DEVI float rbf(float a) { return lo2f(pk2(a, a)); }
DEVI f32x4 mfma16(u32x4 a, u32x4 b, f32x4 c) { return __builtin_amdgcn_mfma_f32_16x16x32_bf16(__builtin_bit_cast(bf16x8, a), __builtin_bit_cast(bf16x8, b), c, 0, 0, 0); }
DEVI f32x16 mfma32(u32x4 a, u32x4 b, f32x16 c) { return __builtin_amdgcn_mfma_f32_32x32x16_bf16(__builtin_bit_cast(bf16x8, a), __builtin_bit_cast(bf16x8, b), c, 0, 0, 0); }
DEVI f32x16 mfma32k8(u32x2 a, u32x2 b, f32x16 c) { return __builtin_amdgcn_mfma_f32_32x32x8bf16_1k(__builtin_bit_cast(s16x4, a), __builtin_bit_cast(s16x4, b), c, 0, 0, 0); }
DEVI u32x2 tr_read(u32 addr) { return __builtin_bit_cast(u32x2, __builtin_amdgcn_ds_read_tr16_b64_v4i16((LAS s16x4*)addr)); }
DEVI u32x4 lds128(u32 addr) { return *(LAS u32x4*)addr; }
DEVI u32x2 lds64(u32 addr) { return *(LAS u32x2*)addr; }
DEVI u32 lds32(u32 addr) { return *(LAS u32*)addr; }
DEVI void st128(u32 addr, u32x4 v) { *(LAS u32x4*)addr = v; }
DEVI void st64(u32 addr, u32x2 v) { *(LAS u32x2*)addr = v; }
DEVI void st32(u32 addr, u32 v) { *(LAS u32*)addr = v; }
DEVI u32x4 cat(u32x2 a, u32x2 b) { return u32x4{a.x, a.y, b.x, b.y}; }
DEVI float vfma(float a, float b, float c) { float d; asm("v_fma_f32 %0, %1, %2, %3" : "=v"(d) : "v"(a), "v"(b), "v"(c)); return d; }
DEVI float vmul(float a, float b) { float d; asm("v_mul_f32 %0, %1, %2" : "=v"(d) : "v"(a), "v"(b)); return d; }
DEVI float vadd(float a, float b) { float d; asm("v_add_f32 %0, %1, %2" : "=v"(d) : "v"(a), "v"(b)); return d; }
DEVI f32x4 vadd4(f32x4 a, f32x4 b) { return f32x4{vadd(a[0], b[0]), vadd(a[1], b[1]), vadd(a[2], b[2]), vadd(a[3], b[3])}; }
DEVI u32 opaque(u32 v) { asm volatile("" : "+v"(v)); return v; }
DEVI void dma16(const void* sbase, u32 voff, u32 ldsbase) {
  asm volatile("s_mov_b32 m0, %2\n\ts_nop 0\n\tglobal_load_lds_dwordx4 %0, %1" ::"v"(voff), "s"(sbase), "s"(ldsbase) : "memory");
}
template <int NL> DEVI void dma16n(const void* sbase, u32 voff, u32 ldsbase) {
  asm volatile("s_mov_b32 m0, %2\n\ts_mov_b32 exec_hi, %3\n\tglobal_load_lds_dwordx4 %0, %1\n\ts_mov_b32 exec_hi, -1" ::"v"(voff), "s"(sbase), "s"(ldsbase),
               "i"((1u << (NL - 32)) - 1u) : "memory");
}
template <int OFF> DEVI void gl128(u32x4& d, u32 voff, const void* sbase) { asm volatile("global_load_dwordx4 %0, %1, %2 offset:%3" : "=v"(d) : "v"(voff), "s"(sbase), "i"(OFF)); }
template <int OFF> DEVI void gl64(u32x2& d, u32 voff, const void* sbase) { asm volatile("global_load_dwordx2 %0, %1, %2 offset:%3" : "=v"(d) : "v"(voff), "s"(sbase), "i"(OFF)); }
DEVI void dma16c(const void* sbase, u32 voff, u32 ldsbase) {
  asm volatile("s_mov_b32 m0, %2\n\ts_nop 0\n\tglobal_load_lds_dwordx4 %0, %1 sc1" ::"v"(voff), "s"(sbase), "s"(ldsbase) : "memory");
}
template <int OFF> DEVI void gl128c(u32x4& d, u32 voff, const void* sbase) { asm volatile("global_load_dwordx4 %0, %1, %2 offset:%3 sc1" : "=v"(d) : "v"(voff), "s"(sbase), "i"(OFF)); }
template <int OFF> DEVI void gl32c(u32& d, u32 voff, const void* sbase) { asm volatile("global_load_dword %0, %1, %2 offset:%3 sc1" : "=v"(d) : "v"(voff), "s"(sbase), "i"(OFF)); }
DEVI void gst128(u32x4 v, u32 voff, void* sbase) { asm volatile("global_store_dwordx4 %0, %1, %2 sc1\n\ts_nop 1" ::"v"(voff), "v"(v), "s"(sbase) : "memory"); }
DEVI void gst64(u32x2 v, u32 voff, void* sbase) { asm volatile("global_store_dwordx2 %0, %1, %2 sc1" ::"v"(voff), "v"(v), "s"(sbase) : "memory"); }
DEVI void gst32(u32 v, u32 voff, void* sbase) { asm volatile("global_store_dword %0, %1, %2 sc1" ::"v"(voff), "v"(v), "s"(sbase) : "memory"); }
#define CODE_PF(from, to, ck, step, lds)                                                                                              \
  do {                                                                                                                                \
    u32 lo_, hi_, len_;                                                                                                               \
    asm volatile("s_getpc_b64 s[20:21]\n\ts_add_u32 %0, s20, " from "@rel32@lo+4\n\ts_addc_u32 %1, s21, " from "@rel32@hi+12\n\ts_mov_b32 %2, " to "-" from \
                 : "=s"(lo_), "=s"(hi_), "=s"(len_)::"s20", "s21", "scc");                                                            \
    const char* code_ = (const char*)(((unsigned long long)hi_ << 32) | lo_);                                                         \
    for (u32 o_ = (ck) * 1024; o_ < len_; o_ += (step) * 1024) { const u32 a_ = o_ + (threadIdx.x & 63) * 16; dma16(code_, a_ < len_ ? a_ : 0u, lds); } \
  } while (0)
template <int N> DEVI void wait_vm() { asm volatile("s_waitcnt vmcnt(%0)" ::"i"(N > 63 ? 63 : N) : "memory"); }
DEVI void wait_lgkm() { asm volatile("s_waitcnt lgkmcnt(0)" ::: "memory"); }
DEVI void bar() { asm volatile("s_barrier" ::: "memory"); }
DEVI u32 rfl(u32 v) { return __builtin_amdgcn_readfirstlane(v); }
#define SCHED() __builtin_amdgcn_sched_barrier(0)
#define STAMP(i)
#define STAMP_END()

constexpr float QSCALE = 0.23570226039551584f * 1.4426950408889634f;

#define POLL_SLEEP 8
#define POLL_FAST 4
#define SPDELAY 30
#define LDPOL "sc1"
#define FSPOL "sc1"
#define CVDELAY 0
#define PB 0
#define ROT 2
#define OUTPOL "sc1"
#define POLLW 4
#define VDELAY 3

constexpr int L_SLOT = 32768, L_WOFF = 16384, L_STG = 94208, L_GW = 131072, L_PT = 133120, L_TILE = 17408;
constexpr int L_K = 0, L_V = 21760, L_C = 41536, L_R = L_PT + 1024, L_FL = 152320, L_DUMMY = L_FL + 256, L_Q = 153600, L_END = 163840;
constexpr int KC_STR = 4640, CS_U = 4 * KC_STR, CS_SZ = CS_U + 4096 + 128;
constexpr int L_W = L_C, L_X = L_W + 576 * 160, L_Y = L_X + 9216;
static_assert(L_Y + 9216 <= L_FL && L_C + 2 * CS_SZ <= L_STG && L_C + 4 * CS_SZ <= L_PT && L_PT + L_TILE <= L_FL, "lds map");
constexpr int W_PROG = L_FL, W_XCH = L_FL + 64, W_GO = L_FL + 128, W_TAG = L_FL + 144;

DEVI u32 lds_peek(u32 a) { return __hip_atomic_load((LAS u32*)a, __ATOMIC_RELAXED, __HIP_MEMORY_SCOPE_WORKGROUP); }
DEVI void prog_set(u32 a, u32 n) { asm volatile("" ::: "memory"); __hip_atomic_store((LAS u32*)a, n, __ATOMIC_RELAXED, __HIP_MEMORY_SCOPE_WORKGROUP); }
DEVI void prog_wait(u32 a, u32 n) { while (__builtin_amdgcn_ballot_w64(lds_peek(a) < n)) __builtin_amdgcn_s_sleep(1); asm volatile("" ::: "memory"); }
DEVI void publish(char* fb, u32 off, u32x2 tag, bool on) { wait_vm<0>(); if (on) asm volatile("global_store_dwordx2 %0, %1, %2 " FSPOL ::"v"(off), "v"(tag), "s"(fb) : "memory"); }
template <int SLP> DEVI void await(const char* fb, u32 off, u32x2 tag) {
  for (int spin = 0; spin < (1 << 20); spin++) {
    u32x2 v;
    asm volatile("global_load_dwordx2 %0, %1, %2 " LDPOL "\n\ts_waitcnt vmcnt(0)" : "=&v"(v) : "v"(off), "s"(fb) : "memory");
    if (!__builtin_amdgcn_ballot_w64(v.x != tag.x || v.y != tag.y)) break;
    __builtin_amdgcn_s_sleep(SLP);
  }
}

DEVI void prep_issue(const u16* Bk, const u16* Bv, const u16* shk, int row0, int col, u32 tb, u32 pw, int lane, u32x4& sh) {
  const int r8 = lane >> 3, c8 = lane & 7;
  sfor<2>([&](auto I) {
    const int j = 2 * pw + I;
    int row = row0 + 8 * j + r8 - 3; row = row < 0 ? 0 : row;
    dma16(Bk + col, row * 1152 + 16 * (c8 ^ j), tb + 1024 * j);
    dma16(Bv + col, (row0 + 8 * j + r8) * 1152 + 16 * (c8 ^ j), tb + 9216 + 1024 * j);
  });
  dma16(Bk + col, (row0 + 61 + (r8 > 2 ? 2 : r8)) * 1152 + 16 * c8, tb + 8192);
  gl128<0>(sh, 8 * col + 16 * (8 * pw + r8), shk);
}
template <typename F> DEVI void prep_compute(int row0, int col, u32 tb, u32 pw, int lane, u32x4 sh, char* ws, F&& mid) {
  const int c8 = lane & 7, cp = 8 * pw + (lane >> 3);
  const u32 o0 = tb + 1024 * c8 + 16 * ((cp >> 2) ^ c8) + 4 * (cp & 3), o1 = tb + 1024 * c8 + 16 * ((cp >> 2) ^ ((c8 + 1) & 7)) + 4 * (cp & 3);
  row0 += 8 * c8;
  u32 kk[11], vv[8];
#pragma unroll
  for (int i = 0; i < 11; i++) kk[i] = lds32((i < 8 ? o0 : o1) + 128 * i);
#pragma unroll
  for (int i = 0; i < 8; i++) vv[i] = lds32(o0 + 9216 + 128 * i);
  if (row0 == 0) { kk[0] = 0; kk[1] = 0; kk[2] = 0; }
  const u32 go = 2 * cp * 4096 + 2 * (2040 - row0);
  char* dst = ws + WS_UR + (size_t)col * 4096;
  float r[8];
  {
    const float a0 = lo2f(sh.x), a1 = hi2f(sh.x), a2 = lo2f(sh.y), a3 = hi2f(sh.y);
#pragma unroll
    for (int i = 0; i < 8; i++) r[i] = vmul(vfma(lo2f(kk[i + 3]), a0, vfma(lo2f(kk[i + 2]), a1, vfma(lo2f(kk[i + 1]), a2, vmul(lo2f(kk[i]), a3)))), lo2f(vv[i]));
    gst128(u32x4{pk2(r[7], r[6]), pk2(r[5], r[4]), pk2(r[3], r[2]), pk2(r[1], r[0])}, go, dst);
  }
  mid();
  {
    const float a0 = lo2f(sh.z), a1 = hi2f(sh.z), a2 = lo2f(sh.w), a3 = hi2f(sh.w);
#pragma unroll
    for (int i = 0; i < 8; i++) r[i] = vmul(vfma(hi2f(kk[i + 3]), a0, vfma(hi2f(kk[i + 2]), a1, vfma(hi2f(kk[i + 1]), a2, vmul(hi2f(kk[i]), a3)))), hi2f(vv[i]));
    gst128(u32x4{pk2(r[7], r[6]), pk2(r[5], r[4]), pk2(r[3], r[2]), pk2(r[1], r[0])}, go, dst + 4096);
  }
}

template <int NR> DEVI void conv_mfma(u32 cs, int rho0, int lane, f32x4 (&acc)[8]) {
  const int g = lane >> 4, l15 = lane & 15;
  u32 aa = cs + (l15 & 3) * KC_STR + 2 * (64 * (l15 >> 2) + 8 * g) + 64 * rho0;
  u32 ba = opaque(cs + CS_U + 2 * (1852 - 1792 - 4 * l15 + 8 * g) + 64 * rho0), bb = opaque(ba + 8);
  u32x4 A[8], G[8];
  sfor<5>([&](auto J) { constexpr int p = J < 4 ? J + 2 : 1; A[p] = lds128(aa + p * 512); G[p] = cat(lds64(ba + (7 - p) * 512), lds64(bb + (7 - p) * 512)); SCHED(); });
#pragma unroll 1
  for (int rho = 0; rho < NR; rho++) {
    constexpr int pa[36] = {2, 2, 3, 3, 2, 4, 3, 4, 2, 5, 1, 1, 2, 1, 3, 1, 4, 1, 5, 1, 6, 2, 0, 3, 0, 4, 0, 5, 0, 1, 0, 6, 0, 7, 0, 0};
    constexpr int pe[36] = {2, 3, 2, 3, 4, 2, 4, 3, 5, 2, 1, 2, 1, 3, 1, 4, 1, 5, 1, 6, 1, 0, 2, 0, 3, 0, 4, 0, 5, 0, 1, 0, 6, 0, 7, 0};
    constexpr int rdc[36] = {18, 19, 20, 21, 22, 23, 0, 1, 2, -1, -1, -1, -1, -1, -1, -1, -1, -1, -1, -1, -1, 6, 7, 8, 9, 10, 11, 12, 13, 14, 15, 16, 17, 3, 4, 5};
    sfor<36>([&](auto I) {
      constexpr int i = I, r = rdc[i], x = r / 3;
      if constexpr (i == 9) { aa = opaque(aa + 64); ba = opaque(ba + 64); bb = opaque(bb + 64); }
      acc[pa[i] + pe[i]] = mfma16(A[pa[i]], G[pe[i]], acc[pa[i] + pe[i]]);
      if constexpr (r >= 0) {
        if constexpr (r % 3 == 0) A[x] = lds128(aa + x * 512);
        else if constexpr (r % 3 == 1) { const u32x2 t = lds64(ba + (7 - x) * 512); G[x].x = t.x; G[x].y = t.y; }
        else { const u32x2 t = lds64(bb + (7 - x) * 512); G[x].z = t.x; G[x].w = t.y; }
      }
      SCHED();
    });
  }
  SCHED();
}
DEVI void conv_out(f32x4 y, u32x2 u, float Dv, u32 off, char* ydst) {
  const float y0 = vfma(hi2f(u.y), Dv, y[0]), y1 = vfma(lo2f(u.y), Dv, y[1]), y2 = vfma(hi2f(u.x), Dv, y[2]), y3 = vfma(lo2f(u.x), Dv, y[3]);
  gst64(u32x2{pk2(y0, y1), pk2(y2, y3)}, off, ydst);
}

DEVI float xhalf_max(float m) { const u32 mu = f2u(m); auto r = __builtin_amdgcn_permlane32_swap(mu, mu, false, false); return fmaxf(u2f(r[0]), u2f(r[1])); }

extern "C" __global__ __launch_bounds__(768, 3) void gm_main(const u16* __restrict__ x, const u16* __restrict__ wqkv, const u16* __restrict__ wo,
                                                             const u16* __restrict__ Bk, const u16* __restrict__ Bv, const u16* __restrict__ shk,
                                                             const u16* __restrict__ ssmk, const u16* __restrict__ ssmD, const u16* __restrict__ gw,
                                                             char* __restrict__ ws, u16* __restrict__ out) {
  extern __shared__ __attribute__((aligned(16))) char smem[];
  const u32 sb = (u32)(size_t)(LAS char*)smem;
  const int b = blockIdx.x, xcd = b & 7, idx = b >> 3;
  const int lane = threadIdx.x & 63;
  const u32 wv = rfl(threadIdx.x >> 6);
  const bool spare = idx >= 28;
  char* const fb = ws + WS_F;
  const int qb = spare ? 0 : 1 + idx % 7, h = 4 * xcd + (spare ? idx - 28 : idx / 7);
  const int chb = wv >= 8 ? 64 * xcd + 2 * idx : 512 + 2 * h;
  u32x2 pre[2], fv; u32x4 cur[2];
  const u32 fo = F_U + ((chb >> 1) * 32 + 24 - 8 * (wv & 3) + (lane & 7)) * 8;
  STAMP(0);
  {
    const int hg = xcd & 1, ri = idx & 3, rb = 4 * (xcd >> 1) + ri, jt = idx >> 2;
    const int r8 = lane >> 3, c8 = lane & 7;
    const int rot = ROT == 1 ? (jt + 2 * ri) % 9 : ROT == 2 ? ((jt + ri) % 3) * 3 : ((jt + ri) & 1) * 4;
    if (wv < 4 && !spare) {
      const u32 lw = wv;
      const u32 xo = (32 * lw + r8) * 1152 + 16 * (c8 ^ r8);
      const char* xb = (const char*)(x + (size_t)rb * 128 * 576);
      u32 wof[2];
#pragma unroll
      for (int i = 0; i < 2; i++) {
        const int ch = (lane & 15) ^ (((lane >> 4) | (i << 2)) << 1), ln = 14 * hg + 2 * jt + (ch >> 3);
        wof[i] = ((lane >> 4) + 16 * lw) * 3456 + 128 * (ln > 26 ? 26 : ln) + 16 * (ch & 7);
      }
      const char* wb = (const char*)wqkv;
      auto issue = [&](int c) {
        const u32 sl = sb + (c & 3) * L_SLOT;
        c += rot; c = c >= 9 ? c - 9 : c;
        sfor<4>([&](auto I) { dma16(xb + c * 128 + I * 9216, xo, sl + (4 * lw + I) * 1024); });
        sfor<4>([&](auto I) { dma16(wb + (64 * c + 4 * I) * 3456, wof[I >> 1], sl + L_WOFF + (4 * lw + I) * 1024); });
      };
      if (hg == 1 && jt == 6 && lw == 0) { dma16(gw, lane * 16, sb + L_GW); dma16(gw, 128 + lane * 16, sb + L_GW + 128); }
      issue(0);
#pragma unroll 1
      for (int n = 1; n < 8; n++) { issue(n); wait_vm<8>(); bar(); }
      issue(8);
      {
        const char* wp = (const char*)wo + (idx == 27 ? 663552 - 24576 : idx * 23712) + lw * 6144;
        sfor<6>([&](auto I) { dma16(wp + I * 1024, lane * 16, sb + L_DUMMY); });
      }
      wait_vm<14>(); bar();
      wait_vm<6>(); bar();
      bar();
    } else if (wv >= 8 || (spare && wv >= 4)) {
      const bool ex = wv < 8;
      const int row0 = ex ? 256 * xcd + 64 * ri : 64 * idx, col = ex ? 512 : 64 * xcd;
      const u32 tb = sb + (ex ? 0 : L_PT);
      if (wv == 8) st32(sb + L_FL + 4 * lane, 0u);
      if (!spare) sfor<PB>([&](auto) { bar(); });
      u32x2 e;
      asm volatile("global_load_dwordx2 %0, %1, %2 sc1" : "=v"(e) : "v"(F_EP), "s"(fb));
      {
        const int t3 = (4 * xcd + (idx & 3)) * 8 + (idx >> 2), k = wv & 3;
        const u32 mo = k == 0 ? F_1 + (h * 8 + qb) * 512 : k == 1 ? F_O + t3 * 512 : k == 2 ? F_Y + t3 * 640 : F_U + (chb >> 1) * 256;
        asm volatile("s_mov_b32 m0, %2\n\ts_nop 0\n\tglobal_load_lds_dword %0, %1 sc1" ::"v"(mo + (lane < (k == 3 ? 4 : k == 2 ? 10 : 8) ? lane : 0) * 64), "s"(fb), "s"(sb + L_DUMMY) : "memory");
      }
      u32x4 sh;
      prep_issue(Bk, Bv, shk, row0, col, tb, wv & 3, lane, sh);
      {
        const u32 o = (threadIdx.x & 255) * 16, op = o ? o - 8 : 0;
        sfor<2>([&](auto J) { const u16* kp = ssmk + (size_t)(chb + J) * 2048; gl128<0>(cur[J], o, kp); gl64<0>(pre[J], op, kp); });
      }
      if (!spare) bar();
      asm volatile("s_waitcnt vmcnt(0)" : "+v"(e), "+v"(sh), "+v"(cur[0]), "+v"(cur[1]), "+v"(pre[0]), "+v"(pre[1])::"memory");
      e.x += 1u; e.y += e.x == 0u;
      if (wv == 8) st64(sb + W_TAG, e);
      wait_lgkm();
      bar();
      prep_compute(row0, col, tb, wv & 3, lane, sh, ws, [&]() { if (!spare) { SCHED(); bar(); SCHED(); } });
      bar();
      if (!spare) { bar(); bar(); }
      publish(fb, F_U + (((col >> 1) + 8 * (wv & 3) + lane) * 32 + (row0 >> 6)) * 8, e, lane < 8);
      if (!spare) sfor<3 - PB>([&](auto) { bar(); });
      else __builtin_amdgcn_s_sleep(SPDELAY);
      asm volatile("global_load_dwordx2 %0, %1, %2 sc1" : "=v"(fv) : "v"(fo), "s"(fb));
      if (!spare) bar();
    } else if (spare) {
      CODE_PF("gm_main", "gm_end", 4 * ri + wv, 16, sb + L_DUMMY);
      bar(); bar();
    } else {
      const int cw = wv - 4, g = lane >> 4, l15 = lane & 15, wm = cw & 1, wn = cw >> 1;
      const u32 xra = sb + (64 * wm + l15) * 128 + 16 * (g ^ (l15 & 7));
      const u32 xb[2][2] = {{opaque(xra), opaque(xra + 2 * L_SLOT)}, {opaque(xra ^ 64), opaque((xra ^ 64) + 2 * L_SLOT)}};
      u32x4 wf[2][4], xf[2][4];
      f32x4 acc[4][4];
      const int ln = 14 * hg + 2 * jt + wn;
      u32 pof; bool pon;
      {
        const int r64 = 2 * rb + wm, seg = ln / 9, lc = ln - 9 * seg, hc = (64 * lc) / 18 + (lane >> 2), li = lane & 1, nx = (lane >> 1) & 1;
        pon = ln == 27 ? lane < 16 : (lane < 20 && hc < 32 && ((18 * hc + 17 * li) >> 6) == lc && (!nx || (seg > 0 && r64 < 28)));
        pof = opaque(ln == 27 ? F_Y + (((4 * (r64 >> 2) + 2 * (r64 & 1)) * 8 + lane) * 80 + 72 + ((r64 >> 1) & 1)) * 8 : F_1 + ((hc * 8 + (r64 >> 2) + nx) * 64 + seg * 16 + li * 8 + (r64 & 3) + (nx ? 0 : 4)) * 8);
      }
      __builtin_amdgcn_s_setprio(2);
      if (ln == 27) {
#pragma unroll
        for (int i = 0; i < 4; i++) acc[i][0] = f32x4{0.f, 0.f, 0.f, 0.f};
#pragma unroll 1
        for (int c = 0; c < 9; c++) {
          bar();
          const int ca = c + rot >= 9 ? c + rot - 9 : c + rot;
#pragma unroll
          for (int ks = 0; ks < 2; ks++) {
            const u32x4 gv = lds128(sb + L_GW + ca * 128 + ks * 64 + 16 * g);
#pragma unroll
            for (int i = 0; i < 4; i++) acc[i][0] = mfma16(gv, lds128(xb[ks][0] + (c & 3) * L_SLOT + 2048 * i), acc[i][0]);
          }
          wait_lgkm();
        }
        bar();
        const float p = rbf(g == 0 ? acc[0][0][0] : g == 1 ? acc[1][0][0] : g == 2 ? acc[2][0][0] : acc[3][0][0]);
        gst32(f2u(rbf(1.f / (1.f + __expf(-p)))), lane * 4, ws + WS_G + (rb * 128 + 64 * wm) * 4);
      } else {
    u32 wb[4][2];
    {
      const int q = l15 >> 2, p = lane & 3, f = q | ((g & 1) << 2);
#pragma unroll
      for (int nt = 0; nt < 4; nt++) {
        const u32 a = sb + L_WOFF + (8 * g + q) * 256 + (((8 * wn + 2 * nt + (p >> 1)) ^ (f << 1)) << 4) + 8 * (p & 1);
        wb[nt][0] = opaque(a); wb[nt][1] = opaque(a + 2 * L_SLOT);
      }
    }
    auto rd = [&](auto KS, auto C) {
      constexpr int ks = KS, c = C, so = (c & 1) * L_SLOT, hi = (c >> 1) & 1;
      sfor<4>([&](auto I) {
        xf[ks][I] = lds128(xb[ks][hi] + so + 2048 * I);
        wf[ks][I] = cat(tr_read(wb[I][hi] + so + ks * 8192), tr_read(wb[I][hi] + so + ks * 8192 + 1024));
      });
    };
    auto mm = [&](auto KS, auto FIRST) {
      constexpr int ks = KS;
      sfor<16>([&](auto I) {
        constexpr int mo[16] = {0, 1, 0, 1, 2, 2, 0, 1, 2, 3, 3, 3, 0, 1, 2, 3}, no[16] = {0, 0, 1, 1, 0, 1, 2, 2, 2, 0, 1, 2, 3, 3, 3, 3};
        constexpr int mt = mo[I], nt = no[I];
        acc[mt][nt] = mfma16(xf[ks][mt], wf[ks][nt], FIRST ? f32x4{0.f, 0.f, 0.f, 0.f} : acc[mt][nt]);
      });
    };
    auto ilv = [&]() {
      sfor<12>([&](auto) { __builtin_amdgcn_sched_group_barrier(0x8, 1, 0); __builtin_amdgcn_sched_group_barrier(0x100, 1, 0); });
      __builtin_amdgcn_sched_group_barrier(0x8, 4, 0);
    };
    const float sc = ln < 9 ? QSCALE : 1.f;
    char* qo = ws + WS_QKV + (size_t)((2 * rb + wm) * 1728 + 64 * ln) * 128;
    SCHED();
    bar();
    STAMP(1);
    rd(IC(0), IC(0));
    sfor<9>([&](auto C) {
      constexpr int c = C;
      SCHED();
      rd(IC(1), C); mm(IC(0), IC(c == 0)); ilv();
      SCHED();
      if constexpr (c == 8) wait_lgkm();
      bar();
      SCHED();
      if constexpr (c < 8) { rd(IC(0), IC(c + 1)); mm(IC(1), IC(0)); ilv(); }
    });
    STAMP(2);
    const u32 stg = sb + L_STG + cw * 9216;
    const u32 sa = stg + l15 * 144 + 8 * g, ra = stg + r8 * 144 + 16 * c8, go = lane * 16;
    u32x4 t[4][2];
    sfor<6>([&](auto NT) {
      constexpr int nt = NT;
      if constexpr (nt < 4) sfor<4>([&](auto M) { acc[M][nt] = mfma16(xf[1][M], wf[1][nt], acc[M][nt]); });
      SCHED();
      if constexpr (nt > 0 && nt < 5) {
        sfor<4>([&](auto M) {
          const f32x4 v = acc[M][nt - 1] * sc;
          st64(sa + (nt - 1) * 2304 + 32 * M, u32x2{pk2(v[0], v[1]), pk2(v[2], v[3])});
        });
        t[nt - 1][0] = lds128(ra + (nt - 1) * 2304); t[nt - 1][1] = lds128(ra + (nt - 1) * 2304 + 1152);
      }
      if constexpr (nt > 1) { gst128(t[nt - 2][0], go, qo + (nt - 2) * 2048); gst128(t[nt - 2][1], go, qo + (nt - 2) * 2048 + 1024); }
      SCHED();
    });
      }
      STAMP(8);
      publish(fb, pof, lds64(sb + W_TAG), pon);
    }
  }
  const int rt = 4 * xcd + (idx & 3), ct = idx >> 2;
  auto wo_load = [&](int r0, int r1) {
    const int r6 = lane / 10, c10 = lane - 10 * r6;
    const u32 vo = (6 * (wv & 3) + r6) * 1152 + 16 * (c10 > 8 ? 8 : c10);
    const char* wb = (const char*)(wo + 72 * ct);
#pragma unroll 4
    for (int i = r0; i < r1; i++) dma16n<60>(wb + i * 24 * 1152, vo, sb + L_W + (4 * i + (wv & 3)) * 960);
  };
  STAMP(3);
  const u32x2 tag = lds64(sb + W_TAG);
  const int cw = wv & 3;
  u32 ypub = 0;
  if (wv >= (spare ? 4u : 8u)) {
    const int tid = threadIdx.x & 255;
    const u32 cb = sb + L_C + (wv >= 8 ? 0 : 2 * CS_SZ);
    const u32 dv = *(const __attribute__((address_space(4))) u32*)(ssmD + chb);
    {
      const u32 z0 = opaque(cw * KC_STR + 16 * lane), z1 = opaque(CS_U + 4096 + 16 * (lane & 7)), za = cb + (lane < 24 ? z0 : z1);
      st128(za, u32x4{0u, 0u, 0u, 0u}); st128(za + CS_SZ, u32x4{0u, 0u, 0u, 0u});
    }
    f32x4 acc[8];
#pragma unroll
    for (int c = 0; c < 8; c++) { acc[c] = f32x4{0.f, 0.f, 0.f, 0.f}; asm volatile("" : "+v"(acc[c])); }
    __builtin_amdgcn_s_sleep(CVDELAY);
    asm volatile("s_waitcnt vmcnt(0)" : "+v"(fv)::"memory");
    if (__builtin_amdgcn_ballot_w64(fv.x != tag.x || fv.y != tag.y)) await<POLL_SLEEP>(fb, fo, tag);
    STAMP(2);
    sfor<2>([&](auto J) { dma16c(ws + WS_UR + (size_t)(chb + J) * 4096, tid * 16, cb + CS_U + cw * 1024 + J * CS_SZ); });
#pragma unroll
    for (int j = 0; j < 2; j++) {
      const u32 cs = cb + j * CS_SZ + 384 + 16 * tid;
      const u32 w0 = tid ? pre[j].x : 0u, w1 = tid ? pre[j].y : 0u, w2 = cur[j].x, w3 = cur[j].y, w4 = cur[j].z, w5 = cur[j].w;
      const u32 a01 = __builtin_amdgcn_alignbyte(w1, w0, 2), a12 = __builtin_amdgcn_alignbyte(w2, w1, 2), a23 = __builtin_amdgcn_alignbyte(w3, w2, 2),
                a34 = __builtin_amdgcn_alignbyte(w4, w3, 2), a45 = __builtin_amdgcn_alignbyte(w5, w4, 2);
      st128(cs, u32x4{a01, a12, a23, a34});
      st128(cs + KC_STR, u32x4{w1, w2, w3, w4});
      st128(cs + 2 * KC_STR, u32x4{a12, a23, a34, a45});
      st128(cs + 3 * KC_STR, cur[j]);
    }
    STAMP(1);
    wait_vm<0>();
    prog_set(sb + W_XCH + 4 * wv, 1);
    const int tm = cw >> 1, pi = cw & 1;
    const u32 cs = cb + tm * CS_SZ;
    prog_wait(sb + W_XCH + 4 * (wv & 12) + 4 * (lane & 3), 1);
    STAMP(4);
    conv_mfma<4>(cs, 4 * pi, lane, acc);
    STAMP(5);
    __builtin_amdgcn_s_setprio(2);
    if (wv < 8) prog_wait(sb + W_XCH + 32 + 8 * tm + 4 * (lane & 1), 2);
    const u32 rA = (wv < 8 ? sb + L_C + tm * CS_SZ : sb + L_R + tm * 8192) + lane * 16;
    if (pi) { sfor<4>([&](auto J) { st128(rA + 4096 + J * 1024, __builtin_bit_cast(u32x4, acc[J])); acc[J] = acc[4 + J]; }); }
    else sfor<4>([&](auto J) { st128(rA + J * 1024, __builtin_bit_cast(u32x4, acc[4 + J])); });
    u32x2 uu[4];
#pragma unroll
    for (int j = 0; j < 4; j++) uu[j] = lds64(cs + CS_U + 4088 - 8 * lane - 512 * (4 * pi + j));
    prog_set(sb + W_XCH + 4 * wv, 2);
    prog_wait(sb + W_XCH + 4 * (wv ^ 1), 2);
    f32x4 t[4];
#pragma unroll
    for (int j = 0; j < 4; j++) t[j] = __builtin_bit_cast(f32x4, lds128(rA + (pi ^ 1) * 4096 + j * 1024));
    wait_lgkm();
    prog_set(sb + W_XCH + 4 * wv, 3);
    const u32 yo = ((lane >> 3) & 3) * 73728 + (lane >> 5) * 64 + (lane & 7) * 8;
    char* yd = ws + WS_Y2 + (chb + tm) * 128 + pi * 1179648;
#pragma unroll
    for (int j = 0; j < 4; j++) conv_out(vadd4(acc[j], t[j]), uu[j], tm ? hi2f(dv) : lo2f(dv), yo + j * 294912, yd);
    STAMP(8);
    ypub = F_Y + (((16 * pi + lane) * 8 + (chb + tm) / 72) * 80 + (chb + tm) % 72) * 8;
    if (wv < 8) publish(fb, ypub, tag, lane < 16);
  }
  if (wv < 8) {
    __builtin_amdgcn_s_setprio(1);
    const int c = lane & 31, hh = lane >> 5, q4 = (lane & 15) >> 2;
    const int w = spare ? wv ^ 4 : wv;
    const u32 kcol = 64 * w + (lane & 16) * 2 + 16 * (lane & 1) + 4 * (lane & 2);
    const u32 ka = opaque(sb + L_K + (8 * hh + q4) * 1088 + kcol), ka2 = opaque(sb + L_K + (16 + q4) * 1088 + kcol);
    const u32 va = opaque(sb + L_V + c * 1040 + 64 * w + 16 * hh);
    const u32 qa = opaque(sb + L_Q + q4 * 512 + 64 * w + (lane & 16) * 2 + 8 * (lane & 3)), qa0 = opaque(qa + 4096 * hh);
    const int thr = c - 8 * hh;
    u32 msk[8], nmk[8];
    unsigned long long cnd[16];
#pragma unroll
    for (int e = 0; e < 16; e++) cnd[e] = __builtin_amdgcn_ballot_w64(16 * (e >> 3) + (e & 7) <= thr);
#pragma unroll
    for (int j = 0; j < 8; j++) { const int k0 = 16 * (j >> 2) + 2 * (j & 3); msk[j] = opaque(k0 + 1 <= thr ? 0xffffffffu : (k0 <= thr ? 0xffffu : 0u)); nmk[j] = opaque(~msk[j]); }
    f32x16 S0;
#pragma unroll
    for (int e = 0; e < 16; e++) S0[e] = u2f(opaque(spare ? 0xff800000u : 0u));
    const u32 oo = opaque((c >> 4) * 18432 + (c & 15) * 16 + hh * 256);
    const u32 fl = sb + W_PROG + 4 * (spare ? (lane & 3) : (lane & 7));
    if (wv < 3) st128(sb + (wv < 2 ? L_K + (18 + wv) * 1088 : L_V + 18 * 1040) + lane * 16, u32x4{0x3f803f80u, 0x3f803f80u, 0x3f803f80u, 0x3f803f80u});
    if (wv == (spare ? 0u : (u32)POLLW)) {
      await<POLL_FAST>(fb, F_1 + ((h * 8 + qb) * 64 + ((lane < 48 ? lane : lane - 16) | (lane < 16 || spare ? 4 : 0))) * 8, tag);
      prog_set(sb + W_GO, 1);
    } else prog_wait(sb + W_GO, 1);
    STAMP(4);
    if (!spare || wv < 4) {
    const char* Ag = ws + WS_QKV + (size_t)(4 * qb * 1728 + 18 * h) * 128;
    const char* Kg = Ag + 576 * 128 - (qb ? 884736 : 0);
    const u32 lo = ((lane >> 3) & (qb ? 7 : 3)) * 221184 + (lane & 7) * 16, lq = ((lane >> 3) & 3) * 221184 + (lane >> 5) * 128 + (lane & 7) * 16;
#pragma unroll 1
    for (int s = wv; s <= w; s += 4) {
      dma16c(Ag + s * 256, lq, sb + L_Q + s * 1024);
      dma16c(Kg + s * 128, lo, sb + L_K + s * 1088); dma16c(Kg + s * 128 + 1024, lo, sb + L_K + (8 + s) * 1088);
      const char* eg = s < 4 ? Kg + (s < 2 ? 16 + s : 590 + s) * 128 : Ag + 2048;
      dma16c(eg, s < 4 ? lo : lq, sb + (s < 2 ? L_K + (16 + s) * 1088 : (s < 4 ? L_V + (14 + s) * 1040 : L_Q + 8192)));
    }
    __builtin_amdgcn_s_sleep(VDELAY);
    if (spare) { dma16c(Kg + (572 + w) * 128, lo, sb + L_V + (w - 4) * 1040); dma16c(Kg + (580 + w) * 128, lo, sb + L_V + (4 + w) * 1040); }
    dma16c(Kg + (576 + w) * 128, lo, sb + L_V + w * 1040); dma16c(Kg + (584 + w) * 128, lo, sb + L_V + (8 + w) * 1040);
    wait_vm<2>();
    prog_set(sb + W_PROG + 4 * wv, 2);
    }
    prog_wait(fl, 2);
    STAMP(5);
    struct KF { u32x4 a; u32x2 b; };
    struct VF { u32x4 a, b; };
    auto ldk = [&](int i) { KF k; k.a = cat(tr_read(ka + i * 64), tr_read(ka + 4352 + i * 64)); k.b = tr_read(ka2 + i * 64); return k; };
    auto ldv = [&](int i) { VF v; v.a = lds128(va + i * 64); v.b = lds128(va + i * 64 + 32); return v; };
    auto pk8 = [&](const f32x16& p, int o) { return u32x4{pk2(p[o], p[o + 1]), pk2(p[o + 2], p[o + 3]), pk2(p[o + 4], p[o + 5]), pk2(p[o + 6], p[o + 7])}; };
    const int ib = spare ? 8 - w : 1;
    const u32x4 q0 = cat(tr_read(qa0), tr_read(qa0 + 2048));
    const KF k8 = ldk(8);
    u32x2 q1 = tr_read(qa + 8192);
    const KF k0 = ldk(0);
    q1.x = hh ? 0u : q1.x; q1.y = 0u;
    auto qk = [&](const KF& k) { f32x16 z = {}; z = mfma32(k.a, q0, z); return mfma32k8(k.b, q1, z); };
    f32x16 Sm = {};
    Sm = mfma32(k8.a, q0, Sm); S0 = mfma32(k0.a, q0, S0);
    Sm = mfma32k8(k8.b, q1, Sm); S0 = mfma32k8(k0.b, q1, S0);
    KF kf[2]; VF vf[2], v8, v0;
    kf[1] = ldk(7); kf[0] = ldk(6);
    wait_vm<0>();
    prog_set(sb + W_PROG + 4 * wv, 3);
    SCHED();
#pragma unroll
    for (int e = 0; e < 16; e++) Sm[e] = __builtin_amdgcn_inverse_ballot_w64(cnd[e]) ? Sm[e] : S0[e];
    float m = __builtin_fmaxf(__builtin_fmaxf(Sm[0], Sm[1]), Sm[2]), m2 = __builtin_fmaxf(__builtin_fmaxf(Sm[3], Sm[4]), Sm[5]);
#pragma unroll
    for (int e = 6; e < 14; e += 4) { m = fmaxf(fmaxf(m, Sm[e]), Sm[e + 1]); m2 = fmaxf(fmaxf(m2, Sm[e + 2]), Sm[e + 3]); }
    m = fmaxf(fmaxf(m, Sm[14]), Sm[15]);
    m = xhalf_max(fmaxf(m, m2));
    prog_wait(fl, 3);
    v8 = ldv(8); v0 = ldv(0); vf[1] = ldv(7);
    f32x16 O;
    float l;
#pragma unroll 1
    for (int pass = 0; pass < 2; pass++) {
      q1.y = hh ? 0u : pk2(-m, lo2f(pk2(m, m)) - m);
      f32x16 S[2];
      {
        f32x16 p = mfma32k8(kf[1].b, u32x2{0u, q1.y}, Sm);
        S[1] = qk(kf[1]);
#pragma unroll
        for (int e = 0; e < 16; e++) p[e] = __builtin_amdgcn_exp2f(p[e]);
        const u32x4 pa = pk8(p, 0), pb = pk8(p, 8);
        O = f32x16{};
        O = mfma32(v8.a, pa & u32x4{msk[0], msk[1], msk[2], msk[3]}, O); O = mfma32(v8.b, pb & u32x4{msk[4], msk[5], msk[6], msk[7]}, O);
        O = mfma32(v0.a, pa & u32x4{nmk[0], nmk[1], nmk[2], nmk[3]}, O); O = mfma32(v0.b, pb & u32x4{nmk[4], nmk[5], nmk[6], nmk[7]}, O);
      }
      auto tile = [&](auto self, auto I) -> void {
        constexpr int i = I;
        if (i < ib) return;
        SCHED();
        f32x16& Sc = S[i & 1];
        f32x16 z = {};
        if constexpr (i > 1) z = mfma32(kf[(i - 1) & 1].a, q0, z);
        if constexpr (i > 2) kf[i & 1] = ldk(i - 2);
        if constexpr (i > 1) vf[(i - 1) & 1] = ldv(i - 1);
        SCHED();
#pragma unroll
        for (int e = 0; e < 8; e++) Sc[e] = __builtin_amdgcn_exp2f(Sc[e]);
        SCHED();
        if constexpr (i > 1) S[(i - 1) & 1] = mfma32k8(kf[(i - 1) & 1].b, q1, z);
        const u32x4 pa = pk8(Sc, 0);
        SCHED();
#pragma unroll
        for (int e = 8; e < 12; e++) Sc[e] = __builtin_amdgcn_exp2f(Sc[e]);
        SCHED();
        O = mfma32(vf[i & 1].a, pa, O);
#pragma unroll
        for (int e = 12; e < 16; e++) Sc[e] = __builtin_amdgcn_exp2f(Sc[e]);
        const u32x4 pb = pk8(Sc, 8);
        SCHED();
        O = mfma32(vf[i & 1].b, pb, O);
        if constexpr (i > 1) self(self, IC(i - 1));
      };
      tile(tile, IC(7));
      SCHED();
      { const u32 lu = f2u(O[10]); auto r = __builtin_amdgcn_permlane32_swap(lu, lu, false, false); l = u2f(r[0]); }
      if (pass == 1 || __all(l > 8.67e-19f && l < 1.15e18f)) break;
      q1.y = 0u;
#pragma unroll 1
      for (int i = ib; i < 8; i++) {
        const f32x16 s = qk(ldk(i));
#pragma unroll
        for (int e = 0; e < 16; e += 2) m = fmaxf(fmaxf(m, s[e]), s[e + 1]);
      }
      m = xhalf_max(m);
      kf[1] = ldk(7); v8 = ldv(8); v0 = ldv(0); kf[0] = ldk(6); vf[1] = ldv(7);
    }
    STAMP(6);
    float inv = __builtin_amdgcn_rcpf(l);
    asm volatile("s_nop 1" : "+v"(inv));
    char* Og = ws + WS_O + (size_t)(16 * qb + 2 * w) * 18432;
    gst128(u32x4{pk2(vmul(O[0], inv), vmul(O[1], inv)), pk2(vmul(O[2], inv), vmul(O[3], inv)), pk2(vmul(O[4], inv), vmul(O[5], inv)), pk2(vmul(O[6], inv), vmul(O[7], inv))},
           oo, Og + (h >> 1) * 1024 + (h & 1) * 512);
    if (hh == 0) gst32(pk2(vmul(O[8], inv), vmul(O[9], inv)), (c >> 4) * 18432 + (c & 15) * 4, Og + 16384 + h * 64);
    publish(fb, F_O + (((4 * qb + (w & 3)) * 8 + lane) * 64 + (w >> 2) * 32 + h) * 8, tag, lane < 8);
      if (b == 255 && wv == 4) {
#pragma unroll 1
        for (int spin = 0; spin < (1 << 18); spin++) {
          u32x4 v[9];
          sfor<9>([&](auto I) { gl128c<0>(v[I], (lane >> 4) * 2048 + (lane & 15) * 16, fb + F_U + I * 8192); });
          asm volatile("s_waitcnt vmcnt(0)" : "+v"(v[0]), "+v"(v[1]), "+v"(v[2]), "+v"(v[3]), "+v"(v[4]), "+v"(v[5]), "+v"(v[6]), "+v"(v[7]), "+v"(v[8])::"memory");
          u32 bad = 0;
#pragma unroll
          for (int i = 0; i < 9; i++) bad |= (v[i].x ^ tag.x) | (v[i].y ^ tag.y) | (v[i].z ^ tag.x) | (v[i].w ^ tag.y);
          if (!__builtin_amdgcn_ballot_w64(bad != 0)) break;
          __builtin_amdgcn_s_sleep(16);
        }
        if (lane == 0) gst64(tag, F_EP, fb);
      }
    STAMP_END();
    return;
  }
  {
    const int g = lane >> 4, l15 = lane & 15, q = l15 >> 2;
    const int r7 = lane / 9, c9 = lane - 9 * r7;
    const u32 xw = sb + L_X + cw * 2304;
    const int m16 = 16 * (rt >> 2) + 2 * (rt & 3) + 8 * (cw >> 1) + (cw & 1);
    if (__builtin_amdgcn_ballot_w64(lds_peek(sb + W_PROG + 4 * (spare ? (lane & 3) : (lane & 7))) < 3u)) { publish(fb, ypub, tag, lane < 16); ypub = 0; }
    prog_wait(sb + W_XCH + (spare ? 16 + 4 * (lane & 7) : 32 + 4 * (lane & 3)), 3);
    prog_wait(sb + W_PROG + 4 * (spare ? (lane & 3) : (lane & 7)), 3);
    STAMP(9);
    {
      const u16* xp = x + (size_t)(16 * m16) * 576 + 72 * ct;
      dma16n<63>(xp, r7 * 1152 + 16 * c9, xw); dma16n<63>(xp + 7 * 576, r7 * 1152 + 16 * c9, xw + 1008); dma16n<63>(xp + 9 * 576, r7 * 1152 + 16 * c9, xw + 1296);
      wo_load(0, 4);
      wait_vm<7>();
      if (ypub && lane < 16) gst64(tag, ypub, fb);
      wo_load(4, 24);
    }
    STAMP(10);
    await<POLL_SLEEP>(fb, F_Y + ((rt * 8 + ct) * 80 + (lane < 18 ? 18 * cw + lane : 72 + (cw >> 1))) * 8, tag);
    STAMP(11);
    {
      const char* yp = ws + WS_Y2 + (size_t)(rt * 576 + 72 * ct + 18 * cw) * 128;
      dma16c(yp, lane * 16, sb + L_Y + cw * 2304); dma16c(yp + 1024, lane * 16, sb + L_Y + cw * 2304 + 1024); dma16c(yp + 1280, lane * 16, sb + L_Y + cw * 2304 + 1280);
    }
    u32 gtu;
    gl32c<0>(gtu, (16 * m16 + l15) * 4, ws + WS_G);
    asm volatile("s_waitcnt vmcnt(0)" : "+v"(gtu)::"memory");
    prog_set(sb + W_XCH + 4 * wv, 4);
    prog_wait(sb + W_XCH + 32 + 4 * (lane & 3), 4);
    const float gt = u2f(gtu);
    f32x4 acc[5], tb[5];
    const u32 xa = xw + l15 * 144 + 8 * g;
    {
      const float g1 = rbf(1.f - gt);
      const u32 ya = sb + L_Y + (4 * g + q) * 128 + 32 * cw + 8 * (lane & 3);
      sfor<5>([&](auto NT) {
        const u32x2 y = tr_read(ya + NT * 2048), xv = lds64(xa + NT * 32);
        tb[NT] = f32x4{vmul(vmul(g1, lo2f(xv.x)), lo2f(y.x)), vmul(vmul(g1, hi2f(xv.x)), hi2f(y.x)), vmul(vmul(g1, lo2f(xv.y)), lo2f(y.y)), vmul(vmul(g1, hi2f(xv.y)), hi2f(y.y))};
      });
    }
    const u32 wa = opaque(sb + L_W + (18 * (g >> 1) + 4 * (g & 1) + q) * 160 + 8 * (lane & 3)), wah = opaque(wa + 46080);
    const u32 wa2 = opaque(sb + L_W + ((4 * g + (q >> 1)) * 18 + 16 + (q & 1)) * 160 + 8 * (lane & 3));
    u32x4 wf[2][5];
    auto rd = [&](auto KS) {
      constexpr int ks = KS;
      sfor<5>([&](auto NT) {
        if constexpr (ks < 16) { const u32 a = (ks < 8 ? wa : wah) + (ks & 7) * 5760 + NT * 32; wf[ks & 1][NT] = cat(tr_read(a), tr_read(a + 1280)); }
        else wf[ks & 1][NT] = cat(tr_read(wa2 + (ks - 16) * 46080 + NT * 32), tr_read(wa2 + (ks - 16) * 46080 + 5760 + NT * 32));
      });
    };
    rd(IC(0));
    STAMP(6);
    u32x4 of[16]; u32 oe[8];
    const char* ob = ws + WS_O + (size_t)m16 * 18432;
    {
      const u32 oo = F_O + ((rt * 8 + ct) * 64 + (cw >> 1) * 32 + (lane & 31)) * 8;
      u32 miss = ~0u;
      sfor<4>([&](auto G) {
#pragma unroll 1
        for (int spin = 0; (miss & (0xffu << (8 * G))) && spin < (1 << 20); spin++) {
          u32x2 v;
          asm volatile("global_load_dwordx2 %0, %1, %2 sc1\n\ts_waitcnt vmcnt(0)" : "=&v"(v) : "v"(oo), "s"(fb) : "memory");
          miss = (u32)__builtin_amdgcn_ballot_w64(v.x != tag.x || v.y != tag.y);
          if (miss & (0xffu << (8 * G))) __builtin_amdgcn_s_sleep(POLL_FAST);
        }
        sfor<4>([&](auto F) { gl128c<F * 1024>(of[4 * G + F], lane * 16, ob + G * 4096); });
      });
    }
    STAMP(12);
    __builtin_amdgcn_s_setprio(3);
    sfor<8>([&](auto F) { gl32c<(F >> 2) * 1024 + (F & 3) * 64>(oe[F], 256 * g + 4 * l15, ob + 16384); });
    sfor<16>([&](auto KS) {
      constexpr int ks = KS;
      SCHED();
      if constexpr (ks % 2 == 0) asm volatile("s_waitcnt vmcnt(%2)" : "+v"(of[ks]), "+v"(of[ks + 1]) : "i"(22 - ks) : "memory");
      rd(IC(ks + 1));
      sfor<5>([&](auto NT) { acc[NT] = mfma16(wf[ks & 1][NT], of[ks], ks == 0 ? f32x4{0.f, 0.f, 0.f, 0.f} : acc[NT]); });
      sfor<5>([&](auto) { __builtin_amdgcn_sched_group_barrier(0x8, 1, 0); __builtin_amdgcn_sched_group_barrier(0x100, 2, 0); });
    });
    SCHED();
    asm volatile("s_waitcnt vmcnt(0)" : "+v"(oe[0]), "+v"(oe[1]), "+v"(oe[2]), "+v"(oe[3]), "+v"(oe[4]), "+v"(oe[5]), "+v"(oe[6]), "+v"(oe[7])::"memory");
    rd(IC(17));
    sfor<5>([&](auto NT) { acc[NT] = mfma16(wf[0][NT], u32x4{oe[0], oe[1], oe[2], oe[3]}, acc[NT]); });
    sfor<5>([&](auto NT) { acc[NT] = mfma16(wf[1][NT], u32x4{oe[4], oe[5], oe[6], oe[7]}, acc[NT]); });
    SCHED();
    STAMP(13);
    sfor<5>([&](auto NT) {
      constexpr int nt = NT;
      if (nt < 4 || g < 2)
        st64(xa + nt * 32, u32x2{pk2(fmaf(gt, acc[nt][0], tb[nt][0]), fmaf(gt, acc[nt][1], tb[nt][1])), pk2(fmaf(gt, acc[nt][2], tb[nt][2]), fmaf(gt, acc[nt][3], tb[nt][3]))});
    });
    char* orow = (char*)(out + (size_t)(16 * m16) * 576 + 72 * ct);
    u32x4 t[3];
#pragma unroll
    for (int it = 0; it < 3; it++) t[it] = lds128(xw + it * 1008 + lane * 16);
#pragma unroll
    for (int it = 0; it < 3; it++)
      if (lane < (it < 2 ? 63 : 18)) asm volatile("global_store_dwordx4 %0, %1, %2 " OUTPOL "\n\ts_nop 1" ::"v"((7 * it + r7) * 1152 + 16 * c9), "v"(t[it]), "s"(orow) : "memory");
  }
  STAMP_END();
}
extern "C" __global__ void gm_end() {}

extern "C" void kernel_launch(void* const* d_in, const int* in_sizes, int n_in, void* d_out, int out_size, void* d_ws, size_t ws_size,
                              hipStream_t stream) {
  (void)hipFuncSetAttribute((const void*)gm_main, hipFuncAttributeMaxDynamicSharedMemorySize, L_END);
  gm_main<<<256, 768, L_END, stream>>>((const u16*)d_in[0], (const u16*)d_in[1], (const u16*)d_in[2], (const u16*)d_in[4], (const u16*)d_in[5],
                                       (const u16*)d_in[6], (const u16*)d_in[7], (const u16*)d_in[8], (const u16*)d_in[9], (char*)d_ws, (u16*)d_out);
}
```

```cpp
#include <hip/hip_runtime.h>
#include <hip/hip_bf16.h>
#include <utility>

typedef unsigned short u16;
typedef unsigned int u32;
typedef u32 u32x2 __attribute__((ext_vector_type(2)));
typedef u32 u32x4 __attribute__((ext_vector_type(4)));
typedef short s16x4 __attribute__((ext_vector_type(4)));
typedef __bf16 bf16x8 __attribute__((ext_vector_type(8)));
typedef __bf16 bf16x2 __attribute__((ext_vector_type(2)));
typedef float f32x2 __attribute__((ext_vector_type(2)));
typedef float f32x4 __attribute__((ext_vector_type(4)));
typedef float f32x16 __attribute__((ext_vector_type(16)));
#define LAS __attribute__((address_space(3)))
#define GAS __attribute__((address_space(1)))
#define DEVI __device__ __forceinline__
#define IC(n) std::integral_constant<int, (n)>{}

constexpr size_t WS_QKV = 0;
constexpr size_t WS_O   = WS_QKV + 96ull * 2048 * 40;
constexpr size_t WS_UR  = WS_O + 32ull * 2048 * 40;
constexpr size_t WS_Y2  = WS_UR + 576ull * 4096;
constexpr size_t WS_G   = WS_Y2 + 576ull * 4096;
constexpr size_t WS_F   = WS_G + 8192;
constexpr u32 F_EP = 0, F_1 = 1024, F_U = F_1 + 256 * 512, F_O = F_U + 288 * 256, F_Y = F_O + 256 * 512;

template <typename F, int... I> DEVI void sfor_impl(F&& f, std::integer_sequence<int, I...>) { (f(std::integral_constant<int, I>{}), ...); }
template <int N, typename F> DEVI void sfor(F&& f) { sfor_impl(f, std::make_integer_sequence<int, N>{}); }

DEVI u32 pk2(float a, float b) { f32x2 v = {a, b}; return __builtin_bit_cast(u32, __builtin_convertvector(v, bf16x2)); }
DEVI u32 f2u(float a) { return __builtin_bit_cast(u32, a); }
DEVI float u2f(u32 a) { return __builtin_bit_cast(float, a); }
DEVI float lo2f(u32 w) { return __builtin_bit_cast(float, w << 16); }
DEVI float hi2f(u32 w) { return __builtin_bit_cast(float, w & 0xffff0000u); }
DEVI float rbf(float a) { return lo2f(pk2(a, a)); }
DEVI f32x4 mfma16(u32x4 a, u32x4 b, f32x4 c) { return __builtin_amdgcn_mfma_f32_16x16x32_bf16(__builtin_bit_cast(bf16x8, a), __builtin_bit_cast(bf16x8, b), c, 0, 0, 0); }
DEVI f32x16 mfma32(u32x4 a, u32x4 b, f32x16 c) { return __builtin_amdgcn_mfma_f32_32x32x16_bf16(__builtin_bit_cast(bf16x8, a), __builtin_bit_cast(bf16x8, b), c, 0, 0, 0); }
DEVI f32x16 mfma32k8(u32x2 a, u32x2 b, f32x16 c) { return __builtin_amdgcn_mfma_f32_32x32x8bf16_1k(__builtin_bit_cast(s16x4, a), __builtin_bit_cast(s16x4, b), c, 0, 0, 0); }
DEVI u32x2 tr_read(u32 addr) { return __builtin_bit_cast(u32x2, __builtin_amdgcn_ds_read_tr16_b64_v4i16((LAS s16x4*)addr)); }
DEVI u32x4 lds128(u32 addr) { return *(LAS u32x4*)addr; }
DEVI u32x2 lds64(u32 addr) { return *(LAS u32x2*)addr; }
DEVI u32 lds32(u32 addr) { return *(LAS u32*)addr; }
DEVI void st128(u32 addr, u32x4 v) { *(LAS u32x4*)addr = v; }
DEVI void st64(u32 addr, u32x2 v) { *(LAS u32x2*)addr = v; }
DEVI void st32(u32 addr, u32 v) { *(LAS u32*)addr = v; }
DEVI u32x4 cat(u32x2 a, u32x2 b) { return u32x4{a.x, a.y, b.x, b.y}; }
DEVI float vfma(float a, float b, float c) { float d; asm("v_fma_f32 %0, %1, %2, %3" : "=v"(d) : "v"(a), "v"(b), "v"(c)); return d; }
DEVI float vmul(float a, float b) { float d; asm("v_mul_f32 %0, %1, %2" : "=v"(d) : "v"(a), "v"(b)); return d; }
DEVI float vadd(float a, float b) { float d; asm("v_add_f32 %0, %1, %2" : "=v"(d) : "v"(a), "v"(b)); return d; }
DEVI f32x4 vadd4(f32x4 a, f32x4 b) { return f32x4{vadd(a[0], b[0]), vadd(a[1], b[1]), vadd(a[2], b[2]), vadd(a[3], b[3])}; }
DEVI u32 opaque(u32 v) { asm volatile("" : "+v"(v)); return v; }
DEVI void dma16(const void* sbase, u32 voff, u32 ldsbase) {
  asm volatile("s_mov_b32 m0, %2\n\ts_nop 0\n\tglobal_load_lds_dwordx4 %0, %1" ::"v"(voff), "s"(sbase), "s"(ldsbase) : "memory");
}
template <int NL> DEVI void dma16n(const void* sbase, u32 voff, u32 ldsbase) {
  asm volatile("s_mov_b32 m0, %2\n\ts_mov_b32 exec_hi, %3\n\tglobal_load_lds_dwordx4 %0, %1\n\ts_mov_b32 exec_hi, -1" ::"v"(voff), "s"(sbase), "s"(ldsbase),
               "i"((1u << (NL - 32)) - 1u) : "memory");
}
template <int OFF> DEVI void gl128(u32x4& d, u32 voff, const void* sbase) { asm volatile("global_load_dwordx4 %0, %1, %2 offset:%3" : "=v"(d) : "v"(voff), "s"(sbase), "i"(OFF)); }
template <int OFF> DEVI void gl64(u32x2& d, u32 voff, const void* sbase) { asm volatile("global_load_dwordx2 %0, %1, %2 offset:%3" : "=v"(d) : "v"(voff), "s"(sbase), "i"(OFF)); }
DEVI void dma16c(const void* sbase, u32 voff, u32 ldsbase) {
  asm volatile("s_mov_b32 m0, %2\n\ts_nop 0\n\tglobal_load_lds_dwordx4 %0, %1 sc1" ::"v"(voff), "s"(sbase), "s"(ldsbase) : "memory");
}
template <int OFF> DEVI void gl128c(u32x4& d, u32 voff, const void* sbase) { asm volatile("global_load_dwordx4 %0, %1, %2 offset:%3 sc1" : "=v"(d) : "v"(voff), "s"(sbase), "i"(OFF)); }
template <int OFF> DEVI void gl32c(u32& d, u32 voff, const void* sbase) { asm volatile("global_load_dword %0, %1, %2 offset:%3 sc1" : "=v"(d) : "v"(voff), "s"(sbase), "i"(OFF)); }
DEVI void gst128(u32x4 v, u32 voff, void* sbase) { asm volatile("global_store_dwordx4 %0, %1, %2 sc1\n\ts_nop 1" ::"v"(voff), "v"(v), "s"(sbase) : "memory"); }
DEVI void gst64(u32x2 v, u32 voff, void* sbase) { asm volatile("global_store_dwordx2 %0, %1, %2 sc1" ::"v"(voff), "v"(v), "s"(sbase) : "memory"); }
DEVI void gst32(u32 v, u32 voff, void* sbase) { asm volatile("global_store_dword %0, %1, %2 sc1" ::"v"(voff), "v"(v), "s"(sbase) : "memory"); }
#define CODE_PF(from, to, ck, step, lds)                                                                                              \
  do {                                                                                                                                \
    u32 lo_, hi_, len_;                                                                                                               \
    asm volatile("s_getpc_b64 s[20:21]\n\ts_add_u32 %0, s20, " from "@rel32@lo+4\n\ts_addc_u32 %1, s21, " from "@rel32@hi+12\n\ts_mov_b32 %2, " to "-" from \
                 : "=s"(lo_), "=s"(hi_), "=s"(len_)::"s20", "s21", "scc");                                                            \
    const char* code_ = (const char*)(((unsigned long long)hi_ << 32) | lo_);                                                         \
    for (u32 o_ = (ck) * 1024; o_ < len_; o_ += (step) * 1024) { const u32 a_ = o_ + (threadIdx.x & 63) * 16; dma16(code_, a_ < len_ ? a_ : 0u, lds); } \
  } while (0)
template <int N> DEVI void wait_vm() { asm volatile("s_waitcnt vmcnt(%0)" ::"i"(N > 63 ? 63 : N) : "memory"); }
DEVI void wait_lgkm() { asm volatile("s_waitcnt lgkmcnt(0)" ::: "memory"); }
DEVI void bar() { asm volatile("s_barrier" ::: "memory"); }
DEVI u32 rfl(u32 v) { return __builtin_amdgcn_readfirstlane(v); }
#define SCHED() __builtin_amdgcn_sched_barrier(0)
#define STAMP(i)
#define STAMP_END()

constexpr float QSCALE = 0.23570226039551584f * 1.4426950408889634f;

#define POLL_SLEEP 8
#define POLL_FAST 4
#define SPDELAY 30
#define LDPOL "sc1"
#define FSPOL "sc1"
#define CVDELAY 0
#define PB 0
#define OUTPOL "sc1"
#define POLLW 4
#define VDELAY 3

constexpr int L_SLOT = 32768, L_WOFF = 16384, L_STG = 94208, L_GW = 131072, L_PT = 133120, L_TILE = 17408;
constexpr int L_K = 0, L_V = 21760, L_C = 41536, L_R = L_PT + 1024, L_FL = 152320, L_DUMMY = L_FL + 256, L_Q = 153600, L_END = 163840;
constexpr int KC_STR = 4640, CS_U = 4 * KC_STR, CS_SZ = CS_U + 4096 + 128;
constexpr int L_W = L_C, L_X = L_W + 576 * 160, L_Y = L_X + 9216;
static_assert(L_Y + 9216 <= L_FL && L_C + 2 * CS_SZ <= L_STG && L_C + 4 * CS_SZ <= L_PT && L_PT + L_TILE <= L_FL, "lds map");
constexpr int W_PROG = L_FL, W_XCH = L_FL + 64, W_GO = L_FL + 128, W_TAG = L_FL + 144;

DEVI u32 lds_peek(u32 a) { return __hip_atomic_load((LAS u32*)a, __ATOMIC_RELAXED, __HIP_MEMORY_SCOPE_WORKGROUP); }
DEVI void prog_set(u32 a, u32 n) { asm volatile("" ::: "memory"); __hip_atomic_store((LAS u32*)a, n, __ATOMIC_RELAXED, __HIP_MEMORY_SCOPE_WORKGROUP); }
DEVI void prog_wait(u32 a, u32 n) { while (__builtin_amdgcn_ballot_w64(lds_peek(a) < n)) __builtin_amdgcn_s_sleep(1); asm volatile("" ::: "memory"); }
DEVI void publish(char* fb, u32 off, u32x2 tag, bool on) { wait_vm<0>(); if (on) asm volatile("global_store_dwordx2 %0, %1, %2 " FSPOL ::"v"(off), "v"(tag), "s"(fb) : "memory"); }
template <int SLP> DEVI void await(const char* fb, u32 off, u32x2 tag) {
  for (int spin = 0; spin < (1 << 20); spin++) {
    u32x2 v;
    asm volatile("global_load_dwordx2 %0, %1, %2 " LDPOL "\n\ts_waitcnt vmcnt(0)" : "=&v"(v) : "v"(off), "s"(fb) : "memory");
    if (!__builtin_amdgcn_ballot_w64(v.x != tag.x || v.y != tag.y)) break;
    __builtin_amdgcn_s_sleep(SLP);
  }
}

DEVI void prep_issue(const u16* Bk, const u16* Bv, const u16* shk, int row0, int col, u32 tb, u32 pw, int lane, u32x4& sh) {
  const int r8 = lane >> 3, c8 = lane & 7;
  sfor<2>([&](auto I) {
    const int j = 2 * pw + I;
    int row = row0 + 8 * j + r8 - 3; row = row < 0 ? 0 : row;
    dma16(Bk + col, row * 1152 + 16 * (c8 ^ j), tb + 1024 * j);
    dma16(Bv + col, (row0 + 8 * j + r8) * 1152 + 16 * (c8 ^ j), tb + 9216 + 1024 * j);
  });
  dma16(Bk + col, (row0 + 61 + (r8 > 2 ? 2 : r8)) * 1152 + 16 * c8, tb + 8192);
  gl128<0>(sh, 8 * col + 16 * (8 * pw + r8), shk);
}
template <typename F> DEVI void prep_compute(int row0, int col, u32 tb, u32 pw, int lane, u32x4 sh, char* ws, F&& mid) {
  const int c8 = lane & 7, cp = 8 * pw + (lane >> 3);
  const u32 o0 = tb + 1024 * c8 + 16 * ((cp >> 2) ^ c8) + 4 * (cp & 3), o1 = tb + 1024 * c8 + 16 * ((cp >> 2) ^ ((c8 + 1) & 7)) + 4 * (cp & 3);
  row0 += 8 * c8;
  u32 kk[11], vv[8];
#pragma unroll
  for (int i = 0; i < 11; i++) kk[i] = lds32((i < 8 ? o0 : o1) + 128 * i);
#pragma unroll
  for (int i = 0; i < 8; i++) vv[i] = lds32(o0 + 9216 + 128 * i);
  if (row0 == 0) { kk[0] = 0; kk[1] = 0; kk[2] = 0; }
  const u32 go = 2 * cp * 4096 + 2 * (2040 - row0);
  char* dst = ws + WS_UR + (size_t)col * 4096;
  float r[8];
  {
    const float a0 = lo2f(sh.x), a1 = hi2f(sh.x), a2 = lo2f(sh.y), a3 = hi2f(sh.y);
#pragma unroll
    for (int i = 0; i < 8; i++) r[i] = vmul(vfma(lo2f(kk[i + 3]), a0, vfma(lo2f(kk[i + 2]), a1, vfma(lo2f(kk[i + 1]), a2, vmul(lo2f(kk[i]), a3)))), lo2f(vv[i]));
    gst128(u32x4{pk2(r[7], r[6]), pk2(r[5], r[4]), pk2(r[3], r[2]), pk2(r[1], r[0])}, go, dst);
  }
  mid();
  {
    const float a0 = lo2f(sh.z), a1 = hi2f(sh.z), a2 = lo2f(sh.w), a3 = hi2f(sh.w);
#pragma unroll
    for (int i = 0; i < 8; i++) r[i] = vmul(vfma(hi2f(kk[i + 3]), a0, vfma(hi2f(kk[i + 2]), a1, vfma(hi2f(kk[i + 1]), a2, vmul(hi2f(kk[i]), a3)))), hi2f(vv[i]));
    gst128(u32x4{pk2(r[7], r[6]), pk2(r[5], r[4]), pk2(r[3], r[2]), pk2(r[1], r[0])}, go, dst + 4096);
  }
}

template <int NR> DEVI void conv_mfma(u32 cs, int rho0, int lane, f32x4 (&acc)[8]) {
  const int g = lane >> 4, l15 = lane & 15;
  u32 aa = cs + (l15 & 3) * KC_STR + 2 * (64 * (l15 >> 2) + 8 * g) + 64 * rho0;
  u32 ba = opaque(cs + CS_U + 2 * (1852 - 1792 - 4 * l15 + 8 * g) + 64 * rho0), bb = opaque(ba + 8);
  u32x4 A[8], G[8];
  sfor<5>([&](auto J) { constexpr int p = J < 4 ? J + 2 : 1; A[p] = lds128(aa + p * 512); G[p] = cat(lds64(ba + (7 - p) * 512), lds64(bb + (7 - p) * 512)); SCHED(); });
#pragma unroll 1
  for (int rho = 0; rho < NR; rho++) {
    constexpr int pa[36] = {2, 2, 3, 3, 2, 4, 3, 4, 2, 5, 1, 1, 2, 1, 3, 1, 4, 1, 5, 1, 6, 2, 0, 3, 0, 4, 0, 5, 0, 1, 0, 6, 0, 7, 0, 0};
    constexpr int pe[36] = {2, 3, 2, 3, 4, 2, 4, 3, 5, 2, 1, 2, 1, 3, 1, 4, 1, 5, 1, 6, 1, 0, 2, 0, 3, 0, 4, 0, 5, 0, 1, 0, 6, 0, 7, 0};
    constexpr int rdc[36] = {18, 19, 20, 21, 22, 23, 0, 1, 2, -1, -1, -1, -1, -1, -1, -1, -1, -1, -1, -1, -1, 6, 7, 8, 9, 10, 11, 12, 13, 14, 15, 16, 17, 3, 4, 5};
    sfor<36>([&](auto I) {
      constexpr int i = I, r = rdc[i], x = r / 3;
      if constexpr (i == 9) { aa = opaque(aa + 64); ba = opaque(ba + 64); bb = opaque(bb + 64); }
      acc[pa[i] + pe[i]] = mfma16(A[pa[i]], G[pe[i]], acc[pa[i] + pe[i]]);
      if constexpr (r >= 0) {
        if constexpr (r % 3 == 0) A[x] = lds128(aa + x * 512);
        else if constexpr (r % 3 == 1) { const u32x2 t = lds64(ba + (7 - x) * 512); G[x].x = t.x; G[x].y = t.y; }
        else { const u32x2 t = lds64(bb + (7 - x) * 512); G[x].z = t.x; G[x].w = t.y; }
      }
      SCHED();
    });
  }
  SCHED();
}
DEVI void conv_out(f32x4 y, u32x2 u, float Dv, u32 off, char* ydst) {
  const float y0 = vfma(hi2f(u.y), Dv, y[0]), y1 = vfma(lo2f(u.y), Dv, y[1]), y2 = vfma(hi2f(u.x), Dv, y[2]), y3 = vfma(lo2f(u.x), Dv, y[3]);
  gst64(u32x2{pk2(y0, y1), pk2(y2, y3)}, off, ydst);
}

DEVI float xhalf_max(float m) { const u32 mu = f2u(m); auto r = __builtin_amdgcn_permlane32_swap(mu, mu, false, false); return fmaxf(u2f(r[0]), u2f(r[1])); }

extern "C" __global__ __launch_bounds__(768, 3) void gm_main(const u16* __restrict__ x, const u16* __restrict__ wqkv, char* __restrict__ ws,
                                                             const u16* __restrict__ Bk, const u16* __restrict__ Bv, const u16* __restrict__ shk,
                                                             const u16* __restrict__ ssmk, const u16* __restrict__ wo, const u16* __restrict__ ssmD,
                                                             const u16* __restrict__ gw, u16* __restrict__ out) {
  extern __shared__ __attribute__((aligned(16))) char smem[];
  const u32 sb = (u32)(size_t)(LAS char*)smem;
  const int b = blockIdx.x, xcd = b & 7, idx = b >> 3;
  const int lane = threadIdx.x & 63;
  const u32 wv = rfl(threadIdx.x >> 6);
  const bool spare = idx >= 28;
  char* const fb = ws + WS_F;
  const int qb = ((idx >> 2) + 1) & 7, h = 4 * xcd + (idx & 3);
  const int chb = wv >= 8 ? 64 * xcd + 2 * idx : 512 + 2 * h;
  u32x2 pre[2], fv; u32x4 cur[2];
  const u32 fo = F_U + ((chb >> 1) * 32 + 24 - 8 * (wv & 3) + (lane & 7)) * 8;
  STAMP(0);
  {
    const int hg = xcd & 1, ri = idx & 3, rb = 4 * (xcd >> 1) + ri, jt = idx >> 2;
    const int r8 = lane >> 3, c8 = lane & 7;
    const int rot = ((0x124924u >> (2 * (jt + ri))) & 3) * 3;
    const u32 xo = opaque((32 * wv + r8) * 1152 + 16 * (c8 ^ r8));
    asm volatile("" ::"s"(x), "s"(wqkv));
    if (wv < 4 && !spare) {
      const u32 lw = wv;
      const char* xb = (const char*)(x + (size_t)rb * 128 * 576);
      sfor<4>([&](auto I) { dma16(xb + rot * 128 + I * 9216, xo, sb + (4 * lw + I) * 1024); });
      u32 wof[2];
#pragma unroll
      for (int i = 0; i < 2; i++) {
        const int ch = (lane & 15) ^ (((lane >> 4) | (i << 2)) << 1), ln = 14 * hg + 2 * jt + (ch >> 3);
        wof[i] = ((lane >> 4) + 16 * lw) * 3456 + 128 * (ln > 26 ? 26 : ln) + 16 * (ch & 7);
      }
      const char* wb = (const char*)wqkv;
      auto issue = [&](int c) {
        const u32 sl = sb + (c & 3) * L_SLOT;
        c += rot; c = c >= 9 ? c - 9 : c;
        sfor<4>([&](auto I) { dma16(xb + c * 128 + I * 9216, xo, sl + (4 * lw + I) * 1024); });
        sfor<4>([&](auto I) { dma16(wb + (64 * c + 4 * I) * 3456, wof[I >> 1], sl + L_WOFF + (4 * lw + I) * 1024); });
      };
      sfor<4>([&](auto I) { dma16(wb + (64 * rot + 4 * I) * 3456, wof[I >> 1], sb + L_WOFF + (4 * lw + I) * 1024); });
#pragma unroll 1
      for (int n = 1; n < 8; n++) { issue(n); wait_vm<8>(); bar(); }
      issue(8);
      {
        const char* wp = (const char*)wo + (idx == 27 ? 663552 - 24576 : idx * 23712) + lw * 6144;
        sfor<6>([&](auto I) { dma16(wp + I * 1024, lane * 16, sb + L_DUMMY); });
      }
      wait_vm<14>(); bar();
      wait_vm<6>(); bar();
      bar();
    } else if (wv >= 8 || (spare && wv >= 4)) {
      const bool ex = wv < 8;
      const int row0 = ex ? 256 * xcd + 64 * ri : 64 * idx, col = ex ? 512 : 64 * xcd;
      const u32 tb = sb + (ex ? 0 : L_PT);
      if (wv == 8) st32(sb + L_FL + 4 * lane, 0u);
      if (!spare) sfor<PB>([&](auto) { bar(); });
      u32x2 e;
      asm volatile("global_load_dwordx2 %0, %1, %2 sc1" : "=v"(e) : "v"(F_EP), "s"(fb));
      {
        const int t3 = (4 * xcd + (idx & 3)) * 8 + (idx >> 2), k = wv & 3;
        const u32 mo = k == 0 ? F_1 + (h * 8 + qb) * 512 : k == 1 ? F_O + t3 * 512 : k == 2 ? F_Y + t3 * 640 : F_U + (chb >> 1) * 256;
        asm volatile("s_mov_b32 m0, %2\n\ts_nop 0\n\tglobal_load_lds_dword %0, %1 sc1" ::"v"(mo + (lane < (k == 3 ? 4 : k == 2 ? 10 : 8) ? lane : 0) * 64), "s"(fb), "s"(sb + L_DUMMY) : "memory");
      }
      u32x4 sh;
      prep_issue(Bk, Bv, shk, row0, col, tb, wv & 3, lane, sh);
      {
        const u32 o = (threadIdx.x & 255) * 16, op = o ? o - 8 : 0;
        sfor<2>([&](auto J) { const u16* kp = ssmk + (size_t)(chb + J) * 2048; gl128<0>(cur[J], o, kp); gl64<0>(pre[J], op, kp); });
      }
      if (!spare) bar();
      asm volatile("s_waitcnt vmcnt(0)" : "+v"(e), "+v"(sh), "+v"(cur[0]), "+v"(cur[1]), "+v"(pre[0]), "+v"(pre[1])::"memory");
      e.x += 1u; e.y += e.x == 0u;
      if (wv == 8) st64(sb + W_TAG, e);
      wait_lgkm();
      bar();
      prep_compute(row0, col, tb, wv & 3, lane, sh, ws, [&]() { if (!spare) { SCHED(); bar(); SCHED(); } });
      bar();
      if (!spare) { bar(); bar(); }
      publish(fb, F_U + (((col >> 1) + 8 * (wv & 3) + lane) * 32 + (row0 >> 6)) * 8, e, lane < 8);
      if (!spare) sfor<3 - PB>([&](auto) { bar(); });
      else __builtin_amdgcn_s_sleep(SPDELAY);
      asm volatile("global_load_dwordx2 %0, %1, %2 sc1" : "=v"(fv) : "v"(fo), "s"(fb));
      if (!spare) bar();
    } else if (spare) {
      CODE_PF("gm_main", "gm_end", 4 * ri + wv, 16, sb + L_DUMMY);
      bar(); bar();
    } else {
      const int cw = wv - 4, g = lane >> 4, l15 = lane & 15, wm = cw & 1, wn = cw >> 1;
      const u32 xra = sb + (64 * wm + l15) * 128 + 16 * (g ^ (l15 & 7));
      const u32 xb[2][2] = {{opaque(xra), opaque(xra + 2 * L_SLOT)}, {opaque(xra ^ 64), opaque((xra ^ 64) + 2 * L_SLOT)}};
      u32x4 wf[2][4], xf[2][4];
      f32x4 acc[4][4];
      const int ln = 14 * hg + 2 * jt + wn;
      u32 pof; bool pon;
      {
        const int r64 = 2 * rb + wm, seg = ln / 9, lc = ln - 9 * seg, hc = (64 * lc) / 18 + (lane >> 2), li = lane & 1, nx = (lane >> 1) & 1;
        pon = ln == 27 ? lane < 16 : (lane < 20 && hc < 32 && ((18 * hc + 17 * li) >> 6) == lc && (!nx || (seg > 0 && r64 < 28)));
        pof = opaque(ln == 27 ? F_Y + (((4 * (r64 >> 2) + 2 * (r64 & 1)) * 8 + lane) * 80 + 72 + ((r64 >> 1) & 1)) * 8 : F_1 + ((hc * 8 + (r64 >> 2) + nx) * 64 + seg * 16 + li * 8 + (r64 & 3) + (nx ? 0 : 4)) * 8);
      }
      __builtin_amdgcn_s_setprio(2);
      if (ln == 27) {
        dma16(gw, wm * 128 + lane * 16, sb + L_GW + wm * 128);
        wait_vm<0>();
#pragma unroll
        for (int i = 0; i < 4; i++) acc[i][0] = f32x4{0.f, 0.f, 0.f, 0.f};
#pragma unroll 1
        for (int c = 0; c < 9; c++) {
          bar();
          const int ca = c + rot >= 9 ? c + rot - 9 : c + rot;
#pragma unroll
          for (int ks = 0; ks < 2; ks++) {
            const u32x4 gv = lds128(sb + L_GW + ca * 128 + ks * 64 + 16 * g);
#pragma unroll
            for (int i = 0; i < 4; i++) acc[i][0] = mfma16(gv, lds128(xb[ks][0] + (c & 3) * L_SLOT + 2048 * i), acc[i][0]);
          }
          wait_lgkm();
        }
        bar();
        const float p = rbf(g == 0 ? acc[0][0][0] : g == 1 ? acc[1][0][0] : g == 2 ? acc[2][0][0] : acc[3][0][0]);
        gst32(f2u(rbf(1.f / (1.f + __expf(-p)))), lane * 4, ws + WS_G + (rb * 128 + 64 * wm) * 4);
      } else {
    u32 wb[4][2];
    {
      const int q = l15 >> 2, p = lane & 3, f = q | ((g & 1) << 2);
#pragma unroll
      for (int nt = 0; nt < 4; nt++) {
        const u32 a = sb + L_WOFF + (8 * g + q) * 256 + (((8 * wn + 2 * nt + (p >> 1)) ^ (f << 1)) << 4) + 8 * (p & 1);
        wb[nt][0] = opaque(a); wb[nt][1] = opaque(a + 2 * L_SLOT);
      }
    }
    auto rd = [&](auto KS, auto C) {
      constexpr int ks = KS, c = C, so = (c & 1) * L_SLOT, hi = (c >> 1) & 1;
      sfor<4>([&](auto I) {
        xf[ks][I] = lds128(xb[ks][hi] + so + 2048 * I);
        wf[ks][I] = cat(tr_read(wb[I][hi] + so + ks * 8192), tr_read(wb[I][hi] + so + ks * 8192 + 1024));
      });
    };
    auto mm = [&](auto KS, auto FIRST) {
      constexpr int ks = KS;
      sfor<16>([&](auto I) {
        constexpr int mo[16] = {0, 1, 0, 1, 2, 2, 0, 1, 2, 3, 3, 3, 0, 1, 2, 3}, no[16] = {0, 0, 1, 1, 0, 1, 2, 2, 2, 0, 1, 2, 3, 3, 3, 3};
        constexpr int mt = mo[I], nt = no[I];
        acc[mt][nt] = mfma16(xf[ks][mt], wf[ks][nt], FIRST ? f32x4{0.f, 0.f, 0.f, 0.f} : acc[mt][nt]);
      });
    };
    auto ilv = [&]() {
      sfor<12>([&](auto) { __builtin_amdgcn_sched_group_barrier(0x8, 1, 0); __builtin_amdgcn_sched_group_barrier(0x100, 1, 0); });
      __builtin_amdgcn_sched_group_barrier(0x8, 4, 0);
    };
    const float sc = ln < 9 ? QSCALE : 1.f;
    char* qo = ws + WS_QKV + (size_t)((2 * rb + wm) * 1728 + 64 * ln) * 128;
    SCHED();
    bar();
    STAMP(1);
    rd(IC(0), IC(0));
    sfor<9>([&](auto C) {
      constexpr int c = C;
      SCHED();
      rd(IC(1), C); mm(IC(0), IC(c == 0)); ilv();
      SCHED();
      if constexpr (c == 8) wait_lgkm();
      bar();
      SCHED();
      if constexpr (c < 8) { rd(IC(0), IC(c + 1)); mm(IC(1), IC(0)); ilv(); }
    });
    STAMP(2);
    const u32 stg = sb + L_STG + cw * 9216;
    const u32 sa = stg + l15 * 144 + 8 * g, ra = stg + r8 * 144 + 16 * c8, go = lane * 16;
    u32x4 t[4][2];
    sfor<6>([&](auto NT) {
      constexpr int nt = NT;
      if constexpr (nt < 4) sfor<4>([&](auto M) { acc[M][nt] = mfma16(xf[1][M], wf[1][nt], acc[M][nt]); });
      SCHED();
      if constexpr (nt > 0 && nt < 5) {
        sfor<4>([&](auto M) {
          const f32x4 v = acc[M][nt - 1] * sc;
          st64(sa + (nt - 1) * 2304 + 32 * M, u32x2{pk2(v[0], v[1]), pk2(v[2], v[3])});
        });
        t[nt - 1][0] = lds128(ra + (nt - 1) * 2304); t[nt - 1][1] = lds128(ra + (nt - 1) * 2304 + 1152);
      }
      if constexpr (nt > 1) { gst128(t[nt - 2][0], go, qo + (nt - 2) * 2048); gst128(t[nt - 2][1], go, qo + (nt - 2) * 2048 + 1024); }
      SCHED();
    });
      }
      STAMP(8);
      publish(fb, pof, lds64(sb + W_TAG), pon);
    }
  }
  const int rt = 4 * xcd + (idx & 3), ct = idx >> 2;
  auto wo_load = [&](int r0, int r1) {
    const int r6 = lane / 10, c10 = lane - 10 * r6;
    const u32 vo = (6 * (wv & 3) + r6) * 1152 + 16 * (c10 > 8 ? 8 : c10);
    const char* wb = (const char*)(wo + 72 * ct);
#pragma unroll 4
    for (int i = r0; i < r1; i++) dma16n<60>(wb + i * 24 * 1152, vo, sb + L_W + (4 * i + (wv & 3)) * 960);
  };
  STAMP(3);
  const u32x2 tag = lds64(sb + W_TAG);
  const int cw = wv & 3;
  u32 ypub = 0;
  if (wv >= (spare ? 4u : 8u)) {
    const int tid = threadIdx.x & 255;
    const u32 cb = sb + L_C + (wv >= 8 ? 0 : 2 * CS_SZ);
    const u32 dv = *(const __attribute__((address_space(4))) u32*)(ssmD + chb);
    {
      const u32 z0 = opaque(cw * KC_STR + 16 * lane), z1 = opaque(CS_U + 4096 + 16 * (lane & 7)), za = cb + (lane < 24 ? z0 : z1);
      st128(za, u32x4{0u, 0u, 0u, 0u}); st128(za + CS_SZ, u32x4{0u, 0u, 0u, 0u});
    }
    f32x4 acc[8];
#pragma unroll
    for (int c = 0; c < 8; c++) { acc[c] = f32x4{0.f, 0.f, 0.f, 0.f}; asm volatile("" : "+v"(acc[c])); }
    __builtin_amdgcn_s_sleep(CVDELAY);
    asm volatile("s_waitcnt vmcnt(0)" : "+v"(fv)::"memory");
    if (__builtin_amdgcn_ballot_w64(fv.x != tag.x || fv.y != tag.y)) await<POLL_SLEEP>(fb, fo, tag);
    STAMP(2);
    sfor<2>([&](auto J) { dma16c(ws + WS_UR + (size_t)(chb + J) * 4096, tid * 16, cb + CS_U + cw * 1024 + J * CS_SZ); });
#pragma unroll
    for (int j = 0; j < 2; j++) {
      const u32 cs = cb + j * CS_SZ + 384 + 16 * tid;
      const u32 w0 = tid ? pre[j].x : 0u, w1 = tid ? pre[j].y : 0u, w2 = cur[j].x, w3 = cur[j].y, w4 = cur[j].z, w5 = cur[j].w;
      const u32 a01 = __builtin_amdgcn_alignbyte(w1, w0, 2), a12 = __builtin_amdgcn_alignbyte(w2, w1, 2), a23 = __builtin_amdgcn_alignbyte(w3, w2, 2),
                a34 = __builtin_amdgcn_alignbyte(w4, w3, 2), a45 = __builtin_amdgcn_alignbyte(w5, w4, 2);
      st128(cs, u32x4{a01, a12, a23, a34});
      st128(cs + KC_STR, u32x4{w1, w2, w3, w4});
      st128(cs + 2 * KC_STR, u32x4{a12, a23, a34, a45});
      st128(cs + 3 * KC_STR, cur[j]);
    }
    STAMP(1);
    wait_vm<0>();
    prog_set(sb + W_XCH + 4 * wv, 1);
    const int tm = cw >> 1, pi = cw & 1;
    const u32 cs = cb + tm * CS_SZ;
    prog_wait(sb + W_XCH + 4 * (wv & 12) + 4 * (lane & 3), 1);
    STAMP(4);
    conv_mfma<4>(cs, 4 * pi, lane, acc);
    STAMP(5);
    __builtin_amdgcn_s_setprio(2);
    if (wv < 8) prog_wait(sb + W_XCH + 32 + 8 * tm + 4 * (lane & 1), 2);
    const u32 rA = (wv < 8 ? sb + L_C + tm * CS_SZ : sb + L_R + tm * 8192) + lane * 16;
    if (pi) { sfor<4>([&](auto J) { st128(rA + 4096 + J * 1024, __builtin_bit_cast(u32x4, acc[J])); acc[J] = acc[4 + J]; }); }
    else sfor<4>([&](auto J) { st128(rA + J * 1024, __builtin_bit_cast(u32x4, acc[4 + J])); });
    u32x2 uu[4];
#pragma unroll
    for (int j = 0; j < 4; j++) uu[j] = lds64(cs + CS_U + 4088 - 8 * lane - 512 * (4 * pi + j));
    prog_set(sb + W_XCH + 4 * wv, 2);
    prog_wait(sb + W_XCH + 4 * (wv ^ 1), 2);
    f32x4 t[4];
#pragma unroll
    for (int j = 0; j < 4; j++) t[j] = __builtin_bit_cast(f32x4, lds128(rA + (pi ^ 1) * 4096 + j * 1024));
    wait_lgkm();
    prog_set(sb + W_XCH + 4 * wv, 3);
    const u32 yo = ((lane >> 3) & 3) * 73728 + (lane >> 5) * 64 + (lane & 7) * 8;
    char* yd = ws + WS_Y2 + (chb + tm) * 128 + pi * 1179648;
#pragma unroll
    for (int j = 0; j < 4; j++) conv_out(vadd4(acc[j], t[j]), uu[j], tm ? hi2f(dv) : lo2f(dv), yo + j * 294912, yd);
    STAMP(8);
    ypub = F_Y + (((16 * pi + lane) * 8 + (chb + tm) / 72) * 80 + (chb + tm) % 72) * 8;
    if (wv < 8) publish(fb, ypub, tag, lane < 16);
  }
  if (wv < 8) {
    __builtin_amdgcn_s_setprio(1);
    const int c = lane & 31, hh = lane >> 5, q4 = (lane & 15) >> 2;
    const int w = spare ? wv ^ 4 : wv;
    const u32 kcol = 64 * w + (lane & 16) * 2 + 16 * (lane & 1) + 4 * (lane & 2);
    const u32 ka = opaque(sb + L_K + (8 * hh + q4) * 1088 + kcol), ka2 = opaque(sb + L_K + (16 + q4) * 1088 + kcol);
    const u32 va = opaque(sb + L_V + c * 1040 + 64 * w + 16 * hh);
    const u32 qa = opaque(sb + L_Q + q4 * 512 + 64 * w + (lane & 16) * 2 + 8 * (lane & 3)), qa0 = opaque(qa + 4096 * hh);
    const int thr = c - 8 * hh;
    const u32 lom = opaque(hh ? 0u : ~0u);
    u32 msk[8], nmk[8];
    unsigned long long cnd[16];
#pragma unroll
    for (int e = 0; e < 16; e++) cnd[e] = __builtin_amdgcn_ballot_w64(16 * (e >> 3) + (e & 7) <= thr);
#pragma unroll
    for (int j = 0; j < 8; j++) { const int k0 = 16 * (j >> 2) + 2 * (j & 3); msk[j] = opaque(k0 + 1 <= thr ? 0xffffffffu : (k0 <= thr ? 0xffffu : 0u)); nmk[j] = opaque(~msk[j]); }
    f32x16 S0;
#pragma unroll
    for (int e = 0; e < 16; e++) S0[e] = u2f(opaque(spare ? 0xff800000u : 0u));
    const u32 oo = opaque((c >> 4) * 18432 + (c & 15) * 16 + hh * 256);
    const u32 fl = sb + W_PROG + 4 * (spare ? (lane & 3) : (lane & 7));
    if (wv < 3) st128(sb + (wv < 2 ? L_K + (18 + wv) * 1088 : L_V + 18 * 1040) + lane * 16, u32x4{0x3f803f80u, 0x3f803f80u, 0x3f803f80u, 0x3f803f80u});
    if (wv == (spare ? 0u : (u32)POLLW)) {
      await<POLL_FAST>(fb, F_1 + ((h * 8 + qb) * 64 + ((lane < 48 ? lane : lane - 16) | (lane < 16 || spare ? 4 : 0))) * 8, tag);
      prog_set(sb + W_GO, 1);
    } else prog_wait(sb + W_GO, 1);
    STAMP(4);
    if (!spare || wv < 4) {
    const char* Ag = ws + WS_QKV + (size_t)(4 * qb * 1728 + 18 * h) * 128;
    const char* Kg = Ag + 576 * 128 - (qb ? 884736 : 0);
    const u32 lo = ((lane >> 3) & (qb ? 7 : 3)) * 221184 + (lane & 7) * 16, lq = ((lane >> 3) & 3) * 221184 + (lane >> 5) * 128 + (lane & 7) * 16;
#pragma unroll 1
    for (int s = wv; s <= w; s += 4) {
      dma16c(Ag + s * 256, lq, sb + L_Q + s * 1024);
      dma16c(Kg + s * 128, lo, sb + L_K + s * 1088); dma16c(Kg + s * 128 + 1024, lo, sb + L_K + (8 + s) * 1088);
      const char* eg = s < 4 ? Kg + (s < 2 ? 16 + s : 590 + s) * 128 : Ag + 2048;
      dma16c(eg, s < 4 ? lo : lq, sb + (s < 2 ? L_K + (16 + s) * 1088 : (s < 4 ? L_V + (14 + s) * 1040 : L_Q + 8192)));
    }
    __builtin_amdgcn_s_sleep(VDELAY);
    if (spare) { dma16c(Kg + (572 + w) * 128, lo, sb + L_V + (w - 4) * 1040); dma16c(Kg + (580 + w) * 128, lo, sb + L_V + (4 + w) * 1040); }
    dma16c(Kg + (576 + w) * 128, lo, sb + L_V + w * 1040); dma16c(Kg + (584 + w) * 128, lo, sb + L_V + (8 + w) * 1040);
    wait_vm<2>();
    prog_set(sb + W_PROG + 4 * wv, 2);
    }
    prog_wait(fl, 2);
    STAMP(5);
    struct KF { u32x4 a; u32x2 b; };
    struct VF { u32x4 a, b; };
    auto ldk = [&](int i) { KF k; k.a = cat(tr_read(ka + i * 64), tr_read(ka + 4352 + i * 64)); k.b = tr_read(ka2 + i * 64); return k; };
    auto ldv = [&](int i) { VF v; v.a = lds128(va + i * 64); v.b = lds128(va + i * 64 + 32); return v; };
    auto pk8 = [&](const f32x16& p, int o) { return u32x4{pk2(p[o], p[o + 1]), pk2(p[o + 2], p[o + 3]), pk2(p[o + 4], p[o + 5]), pk2(p[o + 6], p[o + 7])}; };
    const int ib = spare ? 8 - w : 1;
    const u32x4 q0 = cat(tr_read(qa0), tr_read(qa0 + 2048));
    const KF k8 = ldk(8);
    u32x2 q1 = tr_read(qa + 8192);
    const KF k0 = ldk(0);
    SCHED();
    q1.x &= lom; q1.y = 0u;
    auto qk = [&](const KF& k) { f32x16 z = {}; z = mfma32(k.a, q0, z); return mfma32k8(k.b, q1, z); };
    f32x16 Sm = {};
    Sm = mfma32(k8.a, q0, Sm); S0 = mfma32(k0.a, q0, S0);
    Sm = mfma32k8(k8.b, q1, Sm); S0 = mfma32k8(k0.b, q1, S0);
    KF kf[2]; VF vf[2], v8, v0;
    kf[1] = ldk(7); kf[0] = ldk(6);
    SCHED();
#pragma unroll
    for (int e = 0; e < 16; e++) Sm[e] = __builtin_amdgcn_inverse_ballot_w64(cnd[e]) ? Sm[e] : S0[e];
    float m = __builtin_fmaxf(__builtin_fmaxf(Sm[0], Sm[1]), Sm[2]), m2 = __builtin_fmaxf(__builtin_fmaxf(Sm[3], Sm[4]), Sm[5]);
#pragma unroll
    for (int e = 6; e < 14; e += 4) { m = fmaxf(fmaxf(m, Sm[e]), Sm[e + 1]); m2 = fmaxf(fmaxf(m2, Sm[e + 2]), Sm[e + 3]); }
    m = fmaxf(fmaxf(m, Sm[14]), Sm[15]);
    m = xhalf_max(fmaxf(m, m2));
    f32x16 O;
    float l;
#pragma unroll 1
    for (int pass = 0; pass < 2; pass++) {
      q1.y = pk2(-m, lo2f(pk2(m, m)) - m) & lom;
      f32x16 S[2];
      {
        f32x16 p = mfma32k8(kf[1].b, u32x2{0u, q1.y}, Sm);
        S[1] = qk(kf[1]);
        SCHED();
        wait_vm<0>();
        prog_set(sb + W_PROG + 4 * wv, 3);
#pragma unroll
        for (int e = 0; e < 8; e++) p[e] = __builtin_amdgcn_exp2f(p[e]);
        SCHED();
        const u32 vfl = lds_peek(fl);
        SCHED();
#pragma unroll
        for (int e = 8; e < 16; e++) p[e] = __builtin_amdgcn_exp2f(p[e]);
        SCHED();
        if (__builtin_amdgcn_ballot_w64(vfl < 3u)) prog_wait(fl, 3);
        asm volatile("" ::: "memory");
        v8 = ldv(8); v0 = ldv(0); vf[1] = ldv(7);
        SCHED();
        const u32x4 pa = pk8(p, 0), pb = pk8(p, 8);
        O = f32x16{};
        O = mfma32(v8.a, pa & u32x4{msk[0], msk[1], msk[2], msk[3]}, O); O = mfma32(v8.b, pb & u32x4{msk[4], msk[5], msk[6], msk[7]}, O);
        O = mfma32(v0.a, pa & u32x4{nmk[0], nmk[1], nmk[2], nmk[3]}, O); O = mfma32(v0.b, pb & u32x4{nmk[4], nmk[5], nmk[6], nmk[7]}, O);
      }
      auto tile = [&](auto self, auto I) -> void {
        constexpr int i = I;
        if (i < ib) return;
        SCHED();
        f32x16& Sc = S[i & 1];
        f32x16 z = {};
        if constexpr (i > 1) z = mfma32(kf[(i - 1) & 1].a, q0, z);
        if constexpr (i > 2) kf[i & 1] = ldk(i - 2);
        if constexpr (i > 1) vf[(i - 1) & 1] = ldv(i - 1);
        SCHED();
#pragma unroll
        for (int e = 0; e < 8; e++) Sc[e] = __builtin_amdgcn_exp2f(Sc[e]);
        SCHED();
        if constexpr (i > 1) S[(i - 1) & 1] = mfma32k8(kf[(i - 1) & 1].b, q1, z);
        const u32x4 pa = pk8(Sc, 0);
        SCHED();
#pragma unroll
        for (int e = 8; e < 12; e++) Sc[e] = __builtin_amdgcn_exp2f(Sc[e]);
        SCHED();
        O = mfma32(vf[i & 1].a, pa, O);
#pragma unroll
        for (int e = 12; e < 16; e++) Sc[e] = __builtin_amdgcn_exp2f(Sc[e]);
        const u32x4 pb = pk8(Sc, 8);
        SCHED();
        O = mfma32(vf[i & 1].b, pb, O);
        if constexpr (i > 1) self(self, IC(i - 1));
      };
      tile(tile, IC(7));
      SCHED();
      { const u32 lu = f2u(O[10]); auto r = __builtin_amdgcn_permlane32_swap(lu, lu, false, false); l = u2f(r[0]); }
      if (pass == 1 || __all(l > 8.67e-19f && l < 1.15e18f)) break;
      q1.y = 0u;
#pragma unroll 1
      for (int i = ib; i < 8; i++) {
        const f32x16 s = qk(ldk(i));
#pragma unroll
        for (int e = 0; e < 16; e += 2) m = fmaxf(fmaxf(m, s[e]), s[e + 1]);
      }
      m = xhalf_max(m);
      kf[1] = ldk(7); kf[0] = ldk(6);
    }
    STAMP(6);
    float inv = __builtin_amdgcn_rcpf(l);
    asm volatile("s_nop 1" : "+v"(inv));
    char* Og = ws + WS_O + (size_t)(16 * qb + 2 * w) * 18432;
    gst128(u32x4{pk2(vmul(O[0], inv), vmul(O[1], inv)), pk2(vmul(O[2], inv), vmul(O[3], inv)), pk2(vmul(O[4], inv), vmul(O[5], inv)), pk2(vmul(O[6], inv), vmul(O[7], inv))},
           oo, Og + (h >> 1) * 1024 + (h & 1) * 512);
    if (hh == 0) gst32(pk2(vmul(O[8], inv), vmul(O[9], inv)), (c >> 4) * 18432 + (c & 15) * 4, Og + 16384 + h * 64);
    publish(fb, F_O + (((4 * qb + (w & 3)) * 8 + lane) * 64 + (w >> 2) * 32 + h) * 8, tag, lane < 8);
      if (b == 255 && wv == 4) {
#pragma unroll 1
        for (int spin = 0; spin < (1 << 18); spin++) {
          u32x4 v[9];
          sfor<9>([&](auto I) { gl128c<0>(v[I], (lane >> 4) * 2048 + (lane & 15) * 16, fb + F_U + I * 8192); });
          asm volatile("s_waitcnt vmcnt(0)" : "+v"(v[0]), "+v"(v[1]), "+v"(v[2]), "+v"(v[3]), "+v"(v[4]), "+v"(v[5]), "+v"(v[6]), "+v"(v[7]), "+v"(v[8])::"memory");
          u32 bad = 0;
#pragma unroll
          for (int i = 0; i < 9; i++) bad |= (v[i].x ^ tag.x) | (v[i].y ^ tag.y) | (v[i].z ^ tag.x) | (v[i].w ^ tag.y);
          if (!__builtin_amdgcn_ballot_w64(bad != 0)) break;
          __builtin_amdgcn_s_sleep(16);
        }
        if (lane == 0) gst64(tag, F_EP, fb);
      }
    STAMP_END();
    return;
  }
  {
    const int g = lane >> 4, l15 = lane & 15, q = l15 >> 2;
    const int r7 = lane / 9, c9 = lane - 9 * r7;
    const u32 xw = sb + L_X + cw * 2304;
    const int m16 = 16 * (rt >> 2) + 2 * (rt & 3) + 8 * (cw >> 1) + (cw & 1);
    if (__builtin_amdgcn_ballot_w64(lds_peek(sb + W_PROG + 4 * (spare ? (lane & 3) : (lane & 7))) < 3u)) { publish(fb, ypub, tag, lane < 16); ypub = 0; }
    prog_wait(sb + W_XCH + (spare ? 16 + 4 * (lane & 7) : 32 + 4 * (lane & 3)), 3);
    prog_wait(sb + W_PROG + 4 * (spare ? (lane & 3) : (lane & 7)), 3);
    STAMP(9);
    {
      const u16* xp = x + (size_t)(16 * m16) * 576 + 72 * ct;
      dma16n<63>(xp, r7 * 1152 + 16 * c9, xw); dma16n<63>(xp + 7 * 576, r7 * 1152 + 16 * c9, xw + 1008); dma16n<63>(xp + 9 * 576, r7 * 1152 + 16 * c9, xw + 1296);
      wo_load(0, 4);
      wait_vm<7>();
      if (ypub && lane < 16) gst64(tag, ypub, fb);
      wo_load(4, 24);
    }
    STAMP(10);
    await<POLL_SLEEP>(fb, F_Y + ((rt * 8 + ct) * 80 + (lane < 18 ? 18 * cw + lane : 72 + (cw >> 1))) * 8, tag);
    STAMP(11);
    {
      const char* yp = ws + WS_Y2 + (size_t)(rt * 576 + 72 * ct + 18 * cw) * 128;
      dma16c(yp, lane * 16, sb + L_Y + cw * 2304); dma16c(yp + 1024, lane * 16, sb + L_Y + cw * 2304 + 1024); dma16c(yp + 1280, lane * 16, sb + L_Y + cw * 2304 + 1280);
    }
    u32 gtu;
    gl32c<0>(gtu, (16 * m16 + l15) * 4, ws + WS_G);
    asm volatile("s_waitcnt vmcnt(0)" : "+v"(gtu)::"memory");
    prog_set(sb + W_XCH + 4 * wv, 4);
    prog_wait(sb + W_XCH + 32 + 4 * (lane & 3), 4);
    const float gt = u2f(gtu);
    f32x4 acc[5], tb[5];
    const u32 xa = xw + l15 * 144 + 8 * g;
    {
      const float g1 = rbf(1.f - gt);
      const u32 ya = sb + L_Y + (4 * g + q) * 128 + 32 * cw + 8 * (lane & 3);
      sfor<5>([&](auto NT) {
        const u32x2 y = tr_read(ya + NT * 2048), xv = lds64(xa + NT * 32);
        tb[NT] = f32x4{vmul(vmul(g1, lo2f(xv.x)), lo2f(y.x)), vmul(vmul(g1, hi2f(xv.x)), hi2f(y.x)), vmul(vmul(g1, lo2f(xv.y)), lo2f(y.y)), vmul(vmul(g1, hi2f(xv.y)), hi2f(y.y))};
      });
    }
    const u32 wa = opaque(sb + L_W + (18 * (g >> 1) + 4 * (g & 1) + q) * 160 + 8 * (lane & 3)), wah = opaque(wa + 46080);
    const u32 wa2 = opaque(sb + L_W + ((4 * g + (q >> 1)) * 18 + 16 + (q & 1)) * 160 + 8 * (lane & 3));
    u32x4 wf[2][5];
    auto rd = [&](auto KS) {
      constexpr int ks = KS;
      sfor<5>([&](auto NT) {
        if constexpr (ks < 16) { const u32 a = (ks < 8 ? wa : wah) + (ks & 7) * 5760 + NT * 32; wf[ks & 1][NT] = cat(tr_read(a), tr_read(a + 1280)); }
        else wf[ks & 1][NT] = cat(tr_read(wa2 + (ks - 16) * 46080 + NT * 32), tr_read(wa2 + (ks - 16) * 46080 + 5760 + NT * 32));
      });
    };
    rd(IC(0));
    STAMP(6);
    u32x4 of[16]; u32 oe[8];
    const char* ob = ws + WS_O + (size_t)m16 * 18432;
    {
      const u32 oo = F_O + ((rt * 8 + ct) * 64 + (cw >> 1) * 32 + (lane & 31)) * 8;
      u32 miss = ~0u;
      sfor<4>([&](auto G) {
#pragma unroll 1
        for (int spin = 0; (miss & (0xffu << (8 * G))) && spin < (1 << 20); spin++) {
          u32x2 v;
          asm volatile("global_load_dwordx2 %0, %1, %2 sc1\n\ts_waitcnt vmcnt(0)" : "=&v"(v) : "v"(oo), "s"(fb) : "memory");
          miss = (u32)__builtin_amdgcn_ballot_w64(v.x != tag.x || v.y != tag.y);
          if (miss & (0xffu << (8 * G))) __builtin_amdgcn_s_sleep(POLL_FAST);
        }
        sfor<4>([&](auto F) { gl128c<F * 1024>(of[4 * G + F], lane * 16, ob + G * 4096); });
      });
    }
    STAMP(12);
    __builtin_amdgcn_s_setprio(3);
    sfor<8>([&](auto F) { gl32c<(F >> 2) * 1024 + (F & 3) * 64>(oe[F], 256 * g + 4 * l15, ob + 16384); });
    sfor<16>([&](auto KS) {
      constexpr int ks = KS;
      SCHED();
      if constexpr (ks % 2 == 0) asm volatile("s_waitcnt vmcnt(%2)" : "+v"(of[ks]), "+v"(of[ks + 1]) : "i"(22 - ks) : "memory");
      rd(IC(ks + 1));
      sfor<5>([&](auto NT) { acc[NT] = mfma16(wf[ks & 1][NT], of[ks], ks == 0 ? f32x4{0.f, 0.f, 0.f, 0.f} : acc[NT]); });
      sfor<5>([&](auto) { __builtin_amdgcn_sched_group_barrier(0x8, 1, 0); __builtin_amdgcn_sched_group_barrier(0x100, 2, 0); });
    });
    SCHED();
    asm volatile("s_waitcnt vmcnt(0)" : "+v"(oe[0]), "+v"(oe[1]), "+v"(oe[2]), "+v"(oe[3]), "+v"(oe[4]), "+v"(oe[5]), "+v"(oe[6]), "+v"(oe[7])::"memory");
    rd(IC(17));
    sfor<5>([&](auto NT) { acc[NT] = mfma16(wf[0][NT], u32x4{oe[0], oe[1], oe[2], oe[3]}, acc[NT]); });
    sfor<5>([&](auto NT) { acc[NT] = mfma16(wf[1][NT], u32x4{oe[4], oe[5], oe[6], oe[7]}, acc[NT]); });
    SCHED();
    STAMP(13);
    sfor<5>([&](auto NT) {
      constexpr int nt = NT;
      if (nt < 4 || g < 2)
        st64(xa + nt * 32, u32x2{pk2(fmaf(gt, acc[nt][0], tb[nt][0]), fmaf(gt, acc[nt][1], tb[nt][1])), pk2(fmaf(gt, acc[nt][2], tb[nt][2]), fmaf(gt, acc[nt][3], tb[nt][3]))});
    });
    char* orow = (char*)(out + (size_t)(16 * m16) * 576 + 72 * ct);
    u32x4 t[3];
#pragma unroll
    for (int it = 0; it < 3; it++) t[it] = lds128(xw + it * 1008 + lane * 16);
#pragma unroll
    for (int it = 0; it < 3; it++)
      if (lane < (it < 2 ? 63 : 18)) asm volatile("global_store_dwordx4 %0, %1, %2 " OUTPOL "\n\ts_nop 1" ::"v"((7 * it + r7) * 1152 + 16 * c9), "v"(t[it]), "s"(orow) : "memory");
  }
  STAMP_END();
}
extern "C" __global__ void gm_end() {}

extern "C" void kernel_launch(void* const* d_in, const int* in_sizes, int n_in, void* d_out, int out_size, void* d_ws, size_t ws_size,
                              hipStream_t stream) {
  (void)hipFuncSetAttribute((const void*)gm_main, hipFuncAttributeMaxDynamicSharedMemorySize, L_END);
  gm_main<<<256, 768, L_END, stream>>>((const u16*)d_in[0], (const u16*)d_in[1], (char*)d_ws, (const u16*)d_in[4], (const u16*)d_in[5], (const u16*)d_in[6],
                                       (const u16*)d_in[7], (const u16*)d_in[2], (const u16*)d_in[8], (const u16*)d_in[9], (u16*)d_out);
}
```

```cpp
#include <hip/hip_runtime.h>
#include <hip/hip_bf16.h>
#include <utility>

#define STAMP_RD(v) asm volatile("s_memrealtime %0\n\ts_waitcnt lgkmcnt(0)" : "=s"(v)::"memory")
#define STAMP(i) do { if ((i) == 0) STAMP_RD(t0_); else if ((i) == 1) STAMP_RD(t1_); } while (0)
#define STAMP_END() do { unsigned long long te_; STAMP_RD(te_); if (1 == 15) t1_ = te_; if (blockIdx.x == 100 && (threadIdx.x >> 6) == 4) { const unsigned long long d_ = t1_ - t0_, e_ = t0_ + 3000ull + 10ull * (d_ > 5000ull ? 5000ull : d_); \
   while (__builtin_amdgcn_s_memrealtime() < e_) __builtin_amdgcn_s_sleep(2); } } while (0)

typedef unsigned short u16;
typedef unsigned int u32;
typedef u32 u32x2 __attribute__((ext_vector_type(2)));
typedef u32 u32x4 __attribute__((ext_vector_type(4)));
typedef short s16x4 __attribute__((ext_vector_type(4)));
typedef __bf16 bf16x8 __attribute__((ext_vector_type(8)));
typedef __bf16 bf16x2 __attribute__((ext_vector_type(2)));
typedef float f32x2 __attribute__((ext_vector_type(2)));
typedef float f32x4 __attribute__((ext_vector_type(4)));
typedef float f32x16 __attribute__((ext_vector_type(16)));
#define LAS __attribute__((address_space(3)))
#define GAS __attribute__((address_space(1)))
#define DEVI __device__ __forceinline__
#define IC(n) std::integral_constant<int, (n)>{}

constexpr size_t WS_QKV = 0;
constexpr size_t WS_O   = WS_QKV + 96ull * 2048 * 40;
constexpr size_t WS_UR  = WS_O + 32ull * 2048 * 40;
constexpr size_t WS_Y2  = WS_UR + 576ull * 4096;
constexpr size_t WS_G   = WS_Y2 + 576ull * 4096;
constexpr size_t WS_F   = WS_G + 8192;
constexpr u32 F_EP = 0, F_1 = 1024, F_U = F_1 + 256 * 512, F_O = F_U + 288 * 256, F_Y = F_O + 256 * 512;

template <typename F, int... I> DEVI void sfor_impl(F&& f, std::integer_sequence<int, I...>) { (f(std::integral_constant<int, I>{}), ...); }
template <int N, typename F> DEVI void sfor(F&& f) { sfor_impl(f, std::make_integer_sequence<int, N>{}); }

DEVI u32 pk2(float a, float b) { f32x2 v = {a, b}; return __builtin_bit_cast(u32, __builtin_convertvector(v, bf16x2)); }
DEVI u32 f2u(float a) { return __builtin_bit_cast(u32, a); }
DEVI float u2f(u32 a) { return __builtin_bit_cast(float, a); }
DEVI float lo2f(u32 w) { return __builtin_bit_cast(float, w << 16); }
DEVI float hi2f(u32 w) { return __builtin_bit_cast(float, w & 0xffff0000u); }
DEVI float rbf(float a) { return lo2f(pk2(a, a)); }
DEVI f32x4 mfma16(u32x4 a, u32x4 b, f32x4 c) { return __builtin_amdgcn_mfma_f32_16x16x32_bf16(__builtin_bit_cast(bf16x8, a), __builtin_bit_cast(bf16x8, b), c, 0, 0, 0); }
DEVI f32x16 mfma32(u32x4 a, u32x4 b, f32x16 c) { return __builtin_amdgcn_mfma_f32_32x32x16_bf16(__builtin_bit_cast(bf16x8, a), __builtin_bit_cast(bf16x8, b), c, 0, 0, 0); }
DEVI f32x16 mfma32k8(u32x2 a, u32x2 b, f32x16 c) { return __builtin_amdgcn_mfma_f32_32x32x8bf16_1k(__builtin_bit_cast(s16x4, a), __builtin_bit_cast(s16x4, b), c, 0, 0, 0); }
DEVI u32x2 tr_read(u32 addr) { return __builtin_bit_cast(u32x2, __builtin_amdgcn_ds_read_tr16_b64_v4i16((LAS s16x4*)addr)); }
DEVI u32x4 lds128(u32 addr) { return *(LAS u32x4*)addr; }
DEVI u32x2 lds64(u32 addr) { return *(LAS u32x2*)addr; }
DEVI u32 lds32(u32 addr) { return *(LAS u32*)addr; }
DEVI void st128(u32 addr, u32x4 v) { *(LAS u32x4*)addr = v; }
DEVI void st64(u32 addr, u32x2 v) { *(LAS u32x2*)addr = v; }
DEVI void st32(u32 addr, u32 v) { *(LAS u32*)addr = v; }
DEVI u32x4 cat(u32x2 a, u32x2 b) { return u32x4{a.x, a.y, b.x, b.y}; }
DEVI float vfma(float a, float b, float c) { float d; asm("v_fma_f32 %0, %1, %2, %3" : "=v"(d) : "v"(a), "v"(b), "v"(c)); return d; }
DEVI float vmul(float a, float b) { float d; asm("v_mul_f32 %0, %1, %2" : "=v"(d) : "v"(a), "v"(b)); return d; }
DEVI float vadd(float a, float b) { float d; asm("v_add_f32 %0, %1, %2" : "=v"(d) : "v"(a), "v"(b)); return d; }
DEVI f32x4 vadd4(f32x4 a, f32x4 b) { return f32x4{vadd(a[0], b[0]), vadd(a[1], b[1]), vadd(a[2], b[2]), vadd(a[3], b[3])}; }
DEVI u32 opaque(u32 v) { asm volatile("" : "+v"(v)); return v; }
DEVI void dma16(const void* sbase, u32 voff, u32 ldsbase) {
  asm volatile("s_mov_b32 m0, %2\n\ts_nop 0\n\tglobal_load_lds_dwordx4 %0, %1" ::"v"(voff), "s"(sbase), "s"(ldsbase) : "memory");
}
template <int NL> DEVI void dma16n(const void* sbase, u32 voff, u32 ldsbase) {
  asm volatile("s_mov_b32 m0, %2\n\ts_mov_b32 exec_hi, %3\n\tglobal_load_lds_dwordx4 %0, %1\n\ts_mov_b32 exec_hi, -1" ::"v"(voff), "s"(sbase), "s"(ldsbase),
               "i"((1u << (NL - 32)) - 1u) : "memory");
}
template <int OFF> DEVI void gl128(u32x4& d, u32 voff, const void* sbase) { asm volatile("global_load_dwordx4 %0, %1, %2 offset:%3" : "=v"(d) : "v"(voff), "s"(sbase), "i"(OFF)); }
template <int OFF> DEVI void gl64(u32x2& d, u32 voff, const void* sbase) { asm volatile("global_load_dwordx2 %0, %1, %2 offset:%3" : "=v"(d) : "v"(voff), "s"(sbase), "i"(OFF)); }
DEVI void dma16c(const void* sbase, u32 voff, u32 ldsbase) {
  asm volatile("s_mov_b32 m0, %2\n\ts_nop 0\n\tglobal_load_lds_dwordx4 %0, %1 sc1" ::"v"(voff), "s"(sbase), "s"(ldsbase) : "memory");
}
template <int OFF> DEVI void gl128c(u32x4& d, u32 voff, const void* sbase) { asm volatile("global_load_dwordx4 %0, %1, %2 offset:%3 sc1" : "=v"(d) : "v"(voff), "s"(sbase), "i"(OFF)); }
template <int OFF> DEVI void gl32c(u32& d, u32 voff, const void* sbase) { asm volatile("global_load_dword %0, %1, %2 offset:%3 sc1" : "=v"(d) : "v"(voff), "s"(sbase), "i"(OFF)); }
DEVI void gst128(u32x4 v, u32 voff, void* sbase) { asm volatile("global_store_dwordx4 %0, %1, %2 sc1\n\ts_nop 1" ::"v"(voff), "v"(v), "s"(sbase) : "memory"); }
DEVI void gst64(u32x2 v, u32 voff, void* sbase) { asm volatile("global_store_dwordx2 %0, %1, %2 sc1" ::"v"(voff), "v"(v), "s"(sbase) : "memory"); }
DEVI void gst32(u32 v, u32 voff, void* sbase) { asm volatile("global_store_dword %0, %1, %2 sc1" ::"v"(voff), "v"(v), "s"(sbase) : "memory"); }
#define CODE_PF(from, to, ck, step, lds)                                                                                              \
  do {                                                                                                                                \
    u32 lo_, hi_, len_;                                                                                                               \
    asm volatile("s_getpc_b64 s[20:21]\n\ts_add_u32 %0, s20, " from "@rel32@lo+4\n\ts_addc_u32 %1, s21, " from "@rel32@hi+12\n\ts_mov_b32 %2, " to "-" from \
                 : "=s"(lo_), "=s"(hi_), "=s"(len_)::"s20", "s21", "scc");                                                            \
    const char* code_ = (const char*)(((unsigned long long)hi_ << 32) | lo_);                                                         \
    for (u32 o_ = (ck) * 1024; o_ < len_; o_ += (step) * 1024) { const u32 a_ = o_ + (threadIdx.x & 63) * 16; dma16(code_, a_ < len_ ? a_ : 0u, lds); } \
  } while (0)
template <int N> DEVI void wait_vm() { asm volatile("s_waitcnt vmcnt(%0)" ::"i"(N > 63 ? 63 : N) : "memory"); }
DEVI void wait_lgkm() { asm volatile("s_waitcnt lgkmcnt(0)" ::: "memory"); }
DEVI void bar() { asm volatile("s_barrier" ::: "memory"); }
DEVI u32 rfl(u32 v) { return __builtin_amdgcn_readfirstlane(v); }
#define SCHED() __builtin_amdgcn_sched_barrier(0)

constexpr float QSCALE = 0.23570226039551584f * 1.4426950408889634f;

#define POLL_SLEEP 8
#define POLL_FAST 4
#define SPDELAY 24
#define LDPOL "sc1"
#define FSPOL "sc1"
#define CVDELAY 0
#define PB 0
#define OUTPOL "sc1"
#define BAL 0
#define CPRIO 2
#define WOGATE 3
#define POLLW 4
#define VDELAY 3

constexpr int L_SLOT = 32768, L_WOFF = 16384, L_STG = 94208, L_GW = 131072, L_PT = 133120, L_TILE = 17408;
constexpr int L_K = 0, L_V = 21760, L_C = 41536, L_R = L_PT + 1024, L_FL = 152320, L_DUMMY = L_FL + 256, L_Q = 153600, L_END = 163840;
constexpr int KC_STR = 4640, CS_U = 4 * KC_STR, CS_SZ = CS_U + 4096 + 128;
constexpr int L_W = L_C, L_X = L_W + 576 * 160, L_Y = L_X + 9216;
static_assert(L_Y + 9216 <= L_FL && L_C + 2 * CS_SZ <= L_STG && L_C + 4 * CS_SZ <= L_PT && L_PT + L_TILE <= L_FL, "lds map");
constexpr int W_PROG = L_FL, W_XCH = L_FL + 64, W_GO = L_FL + 128, W_TAG = L_FL + 144;

DEVI u32 lds_peek(u32 a) { return __hip_atomic_load((LAS u32*)a, __ATOMIC_RELAXED, __HIP_MEMORY_SCOPE_WORKGROUP); }
DEVI void prog_set(u32 a, u32 n) { asm volatile("" ::: "memory"); __hip_atomic_store((LAS u32*)a, n, __ATOMIC_RELAXED, __HIP_MEMORY_SCOPE_WORKGROUP); }
DEVI void prog_wait(u32 a, u32 n) { while (__builtin_amdgcn_ballot_w64(lds_peek(a) < n)) __builtin_amdgcn_s_sleep(1); asm volatile("" ::: "memory"); }
DEVI void publish(char* fb, u32 off, u32x2 tag, bool on) { wait_vm<0>(); if (on) asm volatile("global_store_dwordx2 %0, %1, %2 " FSPOL ::"v"(off), "v"(tag), "s"(fb) : "memory"); }
template <int SLP> DEVI void await(const char* fb, u32 off, u32x2 tag) {
  for (int spin = 0; spin < (1 << 20); spin++) {
    u32x2 v;
    asm volatile("global_load_dwordx2 %0, %1, %2 " LDPOL "\n\ts_waitcnt vmcnt(0)" : "=&v"(v) : "v"(off), "s"(fb) : "memory");
    if (!__builtin_amdgcn_ballot_w64(v.x != tag.x || v.y != tag.y)) break;
    __builtin_amdgcn_s_sleep(SLP);
  }
}

DEVI void prep_issue(const u16* Bk, const u16* Bv, const u16* shk, int row0, int col, u32 tb, u32 pw, int lane, u32x4& sh) {
  const int r8 = lane >> 3, c8 = lane & 7;
  sfor<2>([&](auto I) {
    const int j = 2 * pw + I;
    int row = row0 + 8 * j + r8 - 3; row = row < 0 ? 0 : row;
    dma16(Bk + col, row * 1152 + 16 * (c8 ^ j), tb + 1024 * j);
    dma16(Bv + col, (row0 + 8 * j + r8) * 1152 + 16 * (c8 ^ j), tb + 9216 + 1024 * j);
  });
  dma16(Bk + col, (row0 + 61 + (r8 > 2 ? 2 : r8)) * 1152 + 16 * c8, tb + 8192);
  gl128<0>(sh, 8 * col + 16 * (8 * pw + r8), shk);
}
template <typename F> DEVI void prep_compute(int row0, int col, u32 tb, u32 pw, int lane, u32x4 sh, char* ws, F&& mid) {
  const int c8 = lane & 7, cp = 8 * pw + (lane >> 3);
  const u32 o0 = tb + 1024 * c8 + 16 * ((cp >> 2) ^ c8) + 4 * (cp & 3), o1 = tb + 1024 * c8 + 16 * ((cp >> 2) ^ ((c8 + 1) & 7)) + 4 * (cp & 3);
  row0 += 8 * c8;
  u32 kk[11], vv[8];
#pragma unroll
  for (int i = 0; i < 11; i++) kk[i] = lds32((i < 8 ? o0 : o1) + 128 * i);
#pragma unroll
  for (int i = 0; i < 8; i++) vv[i] = lds32(o0 + 9216 + 128 * i);
  if (row0 == 0) { kk[0] = 0; kk[1] = 0; kk[2] = 0; }
  const u32 go = 2 * cp * 4096 + 2 * (2040 - row0);
  char* dst = ws + WS_UR + (size_t)col * 4096;
  float r[8];
  {
    const float a0 = lo2f(sh.x), a1 = hi2f(sh.x), a2 = lo2f(sh.y), a3 = hi2f(sh.y);
#pragma unroll
    for (int i = 0; i < 8; i++) r[i] = vmul(vfma(lo2f(kk[i + 3]), a0, vfma(lo2f(kk[i + 2]), a1, vfma(lo2f(kk[i + 1]), a2, vmul(lo2f(kk[i]), a3)))), lo2f(vv[i]));
    gst128(u32x4{pk2(r[7], r[6]), pk2(r[5], r[4]), pk2(r[3], r[2]), pk2(r[1], r[0])}, go, dst);
  }
  mid();
  {
    const float a0 = lo2f(sh.z), a1 = hi2f(sh.z), a2 = lo2f(sh.w), a3 = hi2f(sh.w);
#pragma unroll
    for (int i = 0; i < 8; i++) r[i] = vmul(vfma(hi2f(kk[i + 3]), a0, vfma(hi2f(kk[i + 2]), a1, vfma(hi2f(kk[i + 1]), a2, vmul(hi2f(kk[i]), a3)))), hi2f(vv[i]));
    gst128(u32x4{pk2(r[7], r[6]), pk2(r[5], r[4]), pk2(r[3], r[2]), pk2(r[1], r[0])}, go, dst + 4096);
  }
}

template <int NR> DEVI void conv_mfma(u32 cs, int rho0, int lane, f32x4 (&acc)[8]) {
  const int g = lane >> 4, l15 = lane & 15;
  u32 aa = cs + (l15 & 3) * KC_STR + 2 * (64 * (l15 >> 2) + 8 * g) + 64 * rho0;
  u32 ba = opaque(cs + CS_U + 2 * (1852 - 1792 - 4 * l15 + 8 * g) + 64 * rho0), bb = opaque(ba + 8);
  u32x4 A[8], G[8];
  sfor<5>([&](auto J) { constexpr int p = J < 4 ? J + 2 : 1; A[p] = lds128(aa + p * 512); G[p] = cat(lds64(ba + (7 - p) * 512), lds64(bb + (7 - p) * 512)); SCHED(); });
#pragma unroll 1
  for (int rho = 0; rho < NR; rho++) {
    constexpr int pa[36] = {2, 2, 3, 3, 2, 4, 3, 4, 2, 5, 1, 1, 2, 1, 3, 1, 4, 1, 5, 1, 6, 2, 0, 3, 0, 4, 0, 5, 0, 1, 0, 6, 0, 7, 0, 0};
    constexpr int pe[36] = {2, 3, 2, 3, 4, 2, 4, 3, 5, 2, 1, 2, 1, 3, 1, 4, 1, 5, 1, 6, 1, 0, 2, 0, 3, 0, 4, 0, 5, 0, 1, 0, 6, 0, 7, 0};
    constexpr int rdc[36] = {18, 19, 20, 21, 22, 23, 0, 1, 2, -1, -1, -1, -1, -1, -1, -1, -1, -1, -1, -1, -1, 6, 7, 8, 9, 10, 11, 12, 13, 14, 15, 16, 17, 3, 4, 5};
    sfor<36>([&](auto I) {
      constexpr int i = I, r = rdc[i], x = r / 3;
      if constexpr (i == 9) { aa = opaque(aa + 64); ba = opaque(ba + 64); bb = opaque(bb + 64); }
      acc[pa[i] + pe[i]] = mfma16(A[pa[i]], G[pe[i]], acc[pa[i] + pe[i]]);
      if constexpr (r >= 0) {
        if constexpr (r % 3 == 0) A[x] = lds128(aa + x * 512);
        else if constexpr (r % 3 == 1) { const u32x2 t = lds64(ba + (7 - x) * 512); G[x].x = t.x; G[x].y = t.y; }
        else { const u32x2 t = lds64(bb + (7 - x) * 512); G[x].z = t.x; G[x].w = t.y; }
      }
      SCHED();
    });
  }
  SCHED();
}
DEVI void conv_out(f32x4 y, u32x2 u, float Dv, u32 off, char* ydst) {
  const float y0 = vfma(hi2f(u.y), Dv, y[0]), y1 = vfma(lo2f(u.y), Dv, y[1]), y2 = vfma(hi2f(u.x), Dv, y[2]), y3 = vfma(lo2f(u.x), Dv, y[3]);
  gst64(u32x2{pk2(y0, y1), pk2(y2, y3)}, off, ydst);
}

DEVI float xhalf_max(float m) { const u32 mu = f2u(m); auto r = __builtin_amdgcn_permlane32_swap(mu, mu, false, false); return fmaxf(u2f(r[0]), u2f(r[1])); }

extern "C" __global__ __launch_bounds__(768, 3) void gm_main(const u16* __restrict__ x, const u16* __restrict__ wqkv, char* __restrict__ ws,
                                                             const u16* __restrict__ Bk, const u16* __restrict__ Bv, const u16* __restrict__ shk,
                                                             const u16* __restrict__ ssmk, const u16* __restrict__ wo, const u16* __restrict__ ssmD,
                                                             const u16* __restrict__ gw, u16* __restrict__ out) {
  extern __shared__ __attribute__((aligned(16))) char smem[];
  const u32 sb = (u32)(size_t)(LAS char*)smem;
  const int b = blockIdx.x, xcd = b & 7, idx = b >> 3;
  const int lane = threadIdx.x & 63;
  const u32 wv = rfl(threadIdx.x >> 6);
  const bool spare = idx >= 28;
  char* const fb = ws + WS_F;
  const int qb = ((idx >> 2) + 1) & 7, h = 4 * xcd + (idx & 3);
  const int chb = wv >= 8 ? 64 * xcd + 2 * idx : 512 + 2 * h;
  u32x2 pre[2], fv; u32x4 cur[2];
  const u32 fo = F_U + ((chb >> 1) * 32 + 24 - 8 * (wv & 3) + (lane & 7)) * 8;
  unsigned long long t0_ = 0, t1_ = 0;
  STAMP(0);
  {
    const int hg = xcd & 1, ri = idx & 3, rb = 4 * (xcd >> 1) + ri, jt = idx >> 2;
    const int r8 = lane >> 3, c8 = lane & 7;
    const int rot = ((0x124924u >> (2 * (jt + ri))) & 3) * 3;
    const u32 xo = opaque((32 * wv + r8) * 1152 + 16 * (c8 ^ r8));
    asm volatile("" ::"s"(x), "s"(wqkv));
    if (wv < 4 && !spare) {
      const u32 lw = wv;
      const char* xb = (const char*)(x + (size_t)rb * 128 * 576);
      sfor<4>([&](auto I) { dma16(xb + rot * 128 + I * 9216, xo, sb + (4 * lw + I) * 1024); });
      u32 wof[2];
#pragma unroll
      for (int i = 0; i < 2; i++) {
        const int ch = (lane & 15) ^ (((lane >> 4) | (i << 2)) << 1), ln = 14 * hg + 2 * jt + (ch >> 3);
        wof[i] = ((lane >> 4) + 16 * lw) * 3456 + 128 * (ln > 26 ? 26 : ln) + 16 * (ch & 7);
      }
      const char* wb = (const char*)wqkv;
      auto issue = [&](int c) {
        const u32 sl = sb + (c & 3) * L_SLOT;
        c += rot; c = c >= 9 ? c - 9 : c;
        sfor<4>([&](auto I) { dma16(xb + c * 128 + I * 9216, xo, sl + (4 * lw + I) * 1024); });
        sfor<4>([&](auto I) { dma16(wb + (64 * c + 4 * I) * 3456, wof[I >> 1], sl + L_WOFF + (4 * lw + I) * 1024); });
      };
      sfor<4>([&](auto I) { dma16(wb + (64 * rot + 4 * I) * 3456, wof[I >> 1], sb + L_WOFF + (4 * lw + I) * 1024); });
#pragma unroll 1
      for (int n = 1; n < 8; n++) { issue(n); wait_vm<8>(); bar(); }
      issue(8);
      wait_vm<8>(); bar();
      __builtin_amdgcn_s_sleep(2);
      {
        const char* wp = (const char*)wo + (idx == 27 ? 663552 - 24576 : idx * 23712) + lw * 6144;
        sfor<6>([&](auto I) { dma16(wp + I * 1024, lane * 16, sb + L_DUMMY); });
      }
      wait_vm<6>(); bar();
      bar();
    } else if (wv >= 8 || (spare && wv >= 4)) {
      const bool ex = wv < 8;
      const int row0 = ex ? 256 * xcd + 64 * ri : 64 * idx, col = ex ? 512 : 64 * xcd;
      const u32 tb = sb + (ex ? 0 : L_PT);
      if (wv == 8) st32(sb + L_FL + 4 * lane, 0u);
      if (!spare) sfor<PB>([&](auto) { bar(); });
      u32x2 e;
      asm volatile("global_load_dwordx2 %0, %1, %2 sc1" : "=v"(e) : "v"(F_EP), "s"(fb));
      {
        const int t3 = (4 * xcd + (idx & 3)) * 8 + (idx >> 2), k = wv & 3;
        const u32 mo = k == 0 ? F_1 + (h * 8 + qb) * 512 : k == 1 ? F_O + t3 * 512 : k == 2 ? F_Y + t3 * 640 : F_U + (chb >> 1) * 256;
        asm volatile("s_mov_b32 m0, %2\n\ts_nop 0\n\tglobal_load_lds_dword %0, %1 sc1" ::"v"(mo + (lane < (k == 3 ? 4 : k == 2 ? 10 : 8) ? lane : 0) * 64), "s"(fb), "s"(sb + L_DUMMY) : "memory");
      }
      u32x4 sh;
      prep_issue(Bk, Bv, shk, row0, col, tb, wv & 3, lane, sh);
      {
        const u32 o = (threadIdx.x & 255) * 16, op = o ? o - 8 : 0;
        sfor<2>([&](auto J) { const u16* kp = ssmk + (size_t)(chb + J) * 2048; gl128<0>(cur[J], o, kp); gl64<0>(pre[J], op, kp); });
      }
      if (!spare) bar();
      asm volatile("s_waitcnt vmcnt(0)" : "+v"(e), "+v"(sh), "+v"(cur[0]), "+v"(cur[1]), "+v"(pre[0]), "+v"(pre[1])::"memory");
      e.x += 1u; e.y += e.x == 0u;
      if (wv == 8) st64(sb + W_TAG, e);
      wait_lgkm();
      bar();
      prep_compute(row0, col, tb, wv & 3, lane, sh, ws, [&]() { if (!spare) { SCHED(); bar(); SCHED(); } });
      bar();
      if (!spare) bar();
      publish(fb, F_U + (((col >> 1) + 8 * (wv & 3) + lane) * 32 + (row0 >> 6)) * 8, e, lane < 8);
      if (!spare) sfor<3 - PB>([&](auto) { bar(); });
      else __builtin_amdgcn_s_sleep(SPDELAY);
      asm volatile("global_load_dwordx2 %0, %1, %2 sc1" : "=v"(fv) : "v"(fo), "s"(fb));
      if (!spare) { bar(); bar(); }
    } else if (spare) {
      CODE_PF("gm_main", "gm_end", 4 * ri + wv, 16, sb + L_DUMMY);
      bar(); bar();
    } else {
      const int cw = wv - 4, g = lane >> 4, l15 = lane & 15, wm = cw & 1, wn = cw >> 1;
      const u32 xra = sb + (64 * wm + l15) * 128 + 16 * (g ^ (l15 & 7));
      const u32 xb[2][2] = {{opaque(xra), opaque(xra + 2 * L_SLOT)}, {opaque(xra ^ 64), opaque((xra ^ 64) + 2 * L_SLOT)}};
      u32x4 wf[2][4], xf[2][4];
      f32x4 acc[4][4];
      const int ln = 14 * hg + 2 * jt + wn;
      u32 pof; bool pon;
      {
        const int r64 = 2 * rb + wm, seg = ln / 9, lc = ln - 9 * seg, hc = (64 * lc) / 18 + (lane >> 2), li = lane & 1, nx = (lane >> 1) & 1;
        pon = ln == 27 ? lane < 16 : (lane < 20 && hc < 32 && ((18 * hc + 17 * li) >> 6) == lc && (!nx || (seg > 0 && r64 < 28)));
        pof = opaque(ln == 27 ? F_Y + (((4 * (r64 >> 2) + 2 * (r64 & 1)) * 8 + lane) * 80 + 72 + ((r64 >> 1) & 1)) * 8 : F_1 + ((hc * 8 + (r64 >> 2) + nx) * 64 + seg * 16 + li * 8 + (r64 & 3) + (nx ? 0 : 4)) * 8);
      }
      __builtin_amdgcn_s_setprio(2);
      if (ln == 27) {
        dma16(gw, wm * 128 + lane * 16, sb + L_GW + wm * 128);
        wait_vm<0>();
#pragma unroll
        for (int i = 0; i < 4; i++) acc[i][0] = f32x4{0.f, 0.f, 0.f, 0.f};
#pragma unroll 1
        for (int c = 0; c < 9; c++) {
          bar();
          const int ca = c + rot >= 9 ? c + rot - 9 : c + rot;
#pragma unroll
          for (int ks = 0; ks < 2; ks++) {
            const u32x4 gv = lds128(sb + L_GW + ca * 128 + ks * 64 + 16 * g);
#pragma unroll
            for (int i = 0; i < 4; i++) acc[i][0] = mfma16(gv, lds128(xb[ks][0] + (c & 3) * L_SLOT + 2048 * i), acc[i][0]);
          }
          wait_lgkm();
        }
        bar();
        const float p = rbf(g == 0 ? acc[0][0][0] : g == 1 ? acc[1][0][0] : g == 2 ? acc[2][0][0] : acc[3][0][0]);
        gst32(f2u(rbf(1.f / (1.f + __expf(-p)))), lane * 4, ws + WS_G + (rb * 128 + 64 * wm) * 4);
      } else {
    u32 wb[4][2];
    {
      const int q = l15 >> 2, p = lane & 3, f = q | ((g & 1) << 2);
#pragma unroll
      for (int nt = 0; nt < 4; nt++) {
        const u32 a = sb + L_WOFF + (8 * g + q) * 256 + (((8 * wn + 2 * nt + (p >> 1)) ^ (f << 1)) << 4) + 8 * (p & 1);
        wb[nt][0] = opaque(a); wb[nt][1] = opaque(a + 2 * L_SLOT);
      }
    }
    auto rd = [&](auto KS, auto C) {
      constexpr int ks = KS, c = C, so = (c & 1) * L_SLOT, hi = (c >> 1) & 1;
      sfor<4>([&](auto I) {
        xf[ks][I] = lds128(xb[ks][hi] + so + 2048 * I);
        wf[ks][I] = cat(tr_read(wb[I][hi] + so + ks * 8192), tr_read(wb[I][hi] + so + ks * 8192 + 1024));
      });
    };
    auto mm = [&](auto KS, auto FIRST) {
      constexpr int ks = KS;
      sfor<16>([&](auto I) {
        constexpr int mo[16] = {0, 1, 0, 1, 2, 2, 0, 1, 2, 3, 3, 3, 0, 1, 2, 3}, no[16] = {0, 0, 1, 1, 0, 1, 2, 2, 2, 0, 1, 2, 3, 3, 3, 3};
        constexpr int mt = mo[I], nt = no[I];
        acc[mt][nt] = mfma16(xf[ks][mt], wf[ks][nt], FIRST ? f32x4{0.f, 0.f, 0.f, 0.f} : acc[mt][nt]);
      });
    };
    auto ilv = [&]() {
      sfor<12>([&](auto) { __builtin_amdgcn_sched_group_barrier(0x8, 1, 0); __builtin_amdgcn_sched_group_barrier(0x100, 1, 0); });
      __builtin_amdgcn_sched_group_barrier(0x8, 4, 0);
    };
    const float sc = ln < 9 ? QSCALE : 1.f;
    char* qo = ws + WS_QKV + (size_t)((2 * rb + wm) * 1728 + 64 * ln) * 128;
    SCHED();
    bar();
    STAMP(1);
    rd(IC(0), IC(0));
    sfor<9>([&](auto C) {
      constexpr int c = C;
      SCHED();
      rd(IC(1), C); mm(IC(0), IC(c == 0)); ilv();
      SCHED();
      if constexpr (c == 8) wait_lgkm();
      bar();
      SCHED();
      if constexpr (c < 8) { rd(IC(0), IC(c + 1)); mm(IC(1), IC(0)); ilv(); }
    });
    STAMP(2);
    const u32 stg = sb + L_STG + cw * 9216;
    const u32 sa = stg + l15 * 144 + 8 * g, ra = stg + r8 * 144 + 16 * c8, go = lane * 16;
    u32x4 t[4][2];
    sfor<6>([&](auto NT) {
      constexpr int nt = NT;
      if constexpr (nt < 4) sfor<4>([&](auto M) { acc[M][nt] = mfma16(xf[1][M], wf[1][nt], acc[M][nt]); });
      SCHED();
      if constexpr (nt > 0 && nt < 5) {
        sfor<4>([&](auto M) {
          const f32x4 v = acc[M][nt - 1] * sc;
          st64(sa + (nt - 1) * 2304 + 32 * M, u32x2{pk2(v[0], v[1]), pk2(v[2], v[3])});
        });
        t[nt - 1][0] = lds128(ra + (nt - 1) * 2304); t[nt - 1][1] = lds128(ra + (nt - 1) * 2304 + 1152);
      }
      if constexpr (nt > 1) { gst128(t[nt - 2][0], go, qo + (nt - 2) * 2048); gst128(t[nt - 2][1], go, qo + (nt - 2) * 2048 + 1024); }
      SCHED();
    });
      }
      STAMP(8);
      publish(fb, pof, lds64(sb + W_TAG), pon);
    }
  }
  const int rt = 4 * xcd + (idx & 3), ct = idx >> 2;
  auto wo_load = [&](int r0, int r1) {
    const int r6 = lane / 10, c10 = lane - 10 * r6;
    const u32 vo = (6 * (wv & 3) + r6) * 1152 + 16 * (c10 > 8 ? 8 : c10);
    const char* wb = (const char*)(wo + 72 * ct);
#pragma unroll 4
    for (int i = r0; i < r1; i++) dma16n<60>(wb + i * 24 * 1152, vo, sb + L_W + (4 * i + (wv & 3)) * 960);
  };
  STAMP(3);
  const u32x2 tag = lds64(sb + W_TAG);
  const int cw = wv & 3;
  u32 ypub = 0;
  if (wv >= (spare ? 4u : 8u)) {
    const int tid = threadIdx.x & 255;
    const u32 cb = sb + L_C + (wv >= 8 ? 0 : 2 * CS_SZ);
    __builtin_amdgcn_s_sleep(CVDELAY);
    asm volatile("s_waitcnt vmcnt(0)" : "+v"(fv)::"memory");
    if (__builtin_amdgcn_ballot_w64(fv.x != tag.x || fv.y != tag.y)) await<POLL_SLEEP>(fb, fo, tag);
    STAMP(2);
    sfor<2>([&](auto J) { dma16c(ws + WS_UR + (size_t)(chb + J) * 4096, tid * 16, cb + CS_U + cw * 1024 + J * CS_SZ); });
    const u32 dv = *(const __attribute__((address_space(4))) u32*)(ssmD + chb);
    {
      const u32 z0 = opaque(cw * KC_STR + 16 * lane), z1 = opaque(CS_U + 4096 + 16 * (lane & 7)), za = cb + (lane < 24 ? z0 : z1);
      st128(za, u32x4{0u, 0u, 0u, 0u}); st128(za + CS_SZ, u32x4{0u, 0u, 0u, 0u});
    }
    f32x4 acc[8];
#pragma unroll
    for (int c = 0; c < 8; c++) { acc[c] = f32x4{0.f, 0.f, 0.f, 0.f}; asm volatile("" : "+v"(acc[c])); }
#pragma unroll
    for (int j = 0; j < 2; j++) {
      const u32 cs = cb + j * CS_SZ + 384 + 16 * tid;
      const u32 w0 = tid ? pre[j].x : 0u, w1 = tid ? pre[j].y : 0u, w2 = cur[j].x, w3 = cur[j].y, w4 = cur[j].z, w5 = cur[j].w;
      const u32 a01 = __builtin_amdgcn_alignbyte(w1, w0, 2), a12 = __builtin_amdgcn_alignbyte(w2, w1, 2), a23 = __builtin_amdgcn_alignbyte(w3, w2, 2),
                a34 = __builtin_amdgcn_alignbyte(w4, w3, 2), a45 = __builtin_amdgcn_alignbyte(w5, w4, 2);
      st128(cs, u32x4{a01, a12, a23, a34});
      st128(cs + KC_STR, u32x4{w1, w2, w3, w4});
      st128(cs + 2 * KC_STR, u32x4{a12, a23, a34, a45});
      st128(cs + 3 * KC_STR, cur[j]);
    }
    STAMP(1);
    wait_vm<0>();
    prog_set(sb + W_XCH + 4 * wv, 1);
    const int tm = cw >> 1, pi = cw & 1;
    const u32 cs = cb + tm * CS_SZ;
    prog_wait(sb + W_XCH + 4 * (wv & 12) + 4 * (lane & 3), 1);
    STAMP(4);
    conv_mfma<4>(cs, 4 * pi, lane, acc);
    STAMP(5);
    __builtin_amdgcn_s_setprio(CPRIO);
    if (wv < 8) prog_wait(sb + W_XCH + 32 + 8 * tm + 4 * (lane & 1), 2);
    const u32 rA = (wv < 8 ? sb + L_C + tm * CS_SZ : sb + L_R + tm * 8192) + lane * 16;
    if (pi) { sfor<4>([&](auto J) { st128(rA + 4096 + J * 1024, __builtin_bit_cast(u32x4, acc[J])); acc[J] = acc[4 + J]; }); }
    else sfor<4>([&](auto J) { st128(rA + J * 1024, __builtin_bit_cast(u32x4, acc[4 + J])); });
    u32x2 uu[4];
#pragma unroll
    for (int j = 0; j < 4; j++) uu[j] = lds64(cs + CS_U + 4088 - 8 * lane - 512 * (4 * pi + j));
    prog_set(sb + W_XCH + 4 * wv, 2);
    prog_wait(sb + W_XCH + 4 * (wv ^ 1), 2);
    f32x4 t[4];
#pragma unroll
    for (int j = 0; j < 4; j++) t[j] = __builtin_bit_cast(f32x4, lds128(rA + (pi ^ 1) * 4096 + j * 1024));
    wait_lgkm();
    prog_set(sb + W_XCH + 4 * wv, 3);
    const u32 yo = ((lane >> 3) & 3) * 73728 + (lane >> 5) * 64 + (lane & 7) * 8;
    char* yd = ws + WS_Y2 + (chb + tm) * 128 + pi * 1179648;
#pragma unroll
    for (int j = 0; j < 4; j++) conv_out(vadd4(acc[j], t[j]), uu[j], tm ? hi2f(dv) : lo2f(dv), yo + j * 294912, yd);
    STAMP(8);
    ypub = F_Y + (((16 * pi + lane) * 8 + (chb + tm) / 72) * 80 + (chb + tm) % 72) * 8;
    if (wv < 8) publish(fb, ypub, tag, lane < 16);
  }
  if (wv < 8) {
    __builtin_amdgcn_s_setprio(1);
    const int c = lane & 31, hh = lane >> 5, q4 = (lane & 15) >> 2;
    const int w = spare ? wv ^ 4 : wv;
    const u32 kcol = 64 * w + (lane & 16) * 2 + 16 * (lane & 1) + 4 * (lane & 2);
    const u32 ka = opaque(sb + L_K + (8 * hh + q4) * 1088 + kcol), ka2 = opaque(sb + L_K + (16 + q4) * 1088 + kcol);
    const u32 va = opaque(sb + L_V + c * 1040 + 64 * w + 16 * hh);
    const u32 qa = opaque(sb + L_Q + q4 * 512 + 64 * w + (lane & 16) * 2 + 8 * (lane & 3)), qa0 = opaque(qa + 4096 * hh);
    const int thr = c - 8 * hh;
    const u32 lom = opaque(hh ? 0u : ~0u);
    u32 msk[8], nmk[8];
    unsigned long long cnd[16];
#pragma unroll
    for (int e = 0; e < 16; e++) cnd[e] = __builtin_amdgcn_ballot_w64(16 * (e >> 3) + (e & 7) <= thr);
#pragma unroll
    for (int j = 0; j < 8; j++) { const int k0 = 16 * (j >> 2) + 2 * (j & 3); msk[j] = opaque(k0 + 1 <= thr ? 0xffffffffu : (k0 <= thr ? 0xffffu : 0u)); nmk[j] = opaque(~msk[j]); }
    f32x16 S0;
#pragma unroll
    for (int e = 0; e < 16; e++) S0[e] = u2f(opaque(spare ? 0xff800000u : 0u));
    const u32 oo = opaque((c >> 4) * 18432 + (c & 15) * 16 + hh * 256);
    const u32 fl = sb + W_PROG + 4 * (spare ? (lane & 3) : (lane & 7));
    if (wv < 3) st128(sb + (wv < 2 ? L_K + (18 + wv) * 1088 : L_V + 18 * 1040) + lane * 16, u32x4{0x3f803f80u, 0x3f803f80u, 0x3f803f80u, 0x3f803f80u});
    if (wv == (spare ? 0u : (u32)POLLW)) {
      await<POLL_FAST>(fb, F_1 + ((h * 8 + qb) * 64 + ((lane < 48 ? lane : lane - 16) | (lane < 16 || spare ? 4 : 0))) * 8, tag);
      prog_set(sb + W_GO, 1);
    } else prog_wait(sb + W_GO, 1);
    STAMP(4);
    __builtin_amdgcn_s_setprio(BAL ? 3 : 1);
    if (!spare || wv < 4) {
    const char* Ag = ws + WS_QKV + (size_t)(4 * qb * 1728 + 18 * h) * 128;
    const char* Kg = Ag + 576 * 128 - (qb ? 884736 : 0);
    const u32 lo = ((lane >> 3) & (qb ? 7 : 3)) * 221184 + (lane & 7) * 16, lq = ((lane >> 3) & 3) * 221184 + (lane >> 5) * 128 + (lane & 7) * 16;
#pragma unroll 1
    for (int s = wv; s <= w; s += 4) {
      dma16c(Ag + s * 256, lq, sb + L_Q + s * 1024);
      dma16c(Kg + s * 128, lo, sb + L_K + s * 1088); dma16c(Kg + s * 128 + 1024, lo, sb + L_K + (8 + s) * 1088);
      const char* eg = s < 4 ? Kg + (s < 2 ? 16 + s : 590 + s) * 128 : Ag + 2048;
      dma16c(eg, s < 4 ? lo : lq, sb + (s < 2 ? L_K + (16 + s) * 1088 : (s < 4 ? L_V + (14 + s) * 1040 : L_Q + 8192)));
    }
    __builtin_amdgcn_s_sleep(VDELAY);
    if (spare) { dma16c(Kg + (572 + w) * 128, lo, sb + L_V + (w - 4) * 1040); dma16c(Kg + (580 + w) * 128, lo, sb + L_V + (4 + w) * 1040); }
    dma16c(Kg + (576 + w) * 128, lo, sb + L_V + w * 1040); dma16c(Kg + (584 + w) * 128, lo, sb + L_V + (8 + w) * 1040);
    prog_set(sb + W_PROG + 4 * wv, 1);
    wait_vm<2>();
    prog_set(sb + W_PROG + 4 * wv, 2);
    }
    prog_wait(fl, 2);
    STAMP(5);
    struct KF { u32x4 a; u32x2 b; };
    struct VF { u32x4 a, b; };
    auto ldk = [&](int i) { KF k; k.a = cat(tr_read(ka + i * 64), tr_read(ka + 4352 + i * 64)); k.b = tr_read(ka2 + i * 64); return k; };
    auto ldv = [&](int i) { VF v; v.a = lds128(va + i * 64); v.b = lds128(va + i * 64 + 32); return v; };
    auto pk8 = [&](const f32x16& p, int o) { return u32x4{pk2(p[o], p[o + 1]), pk2(p[o + 2], p[o + 3]), pk2(p[o + 4], p[o + 5]), pk2(p[o + 6], p[o + 7])}; };
    const int ib = spare ? 8 - w : 1;
    const u32x4 q0 = cat(tr_read(qa0), tr_read(qa0 + 2048));
    const KF k8 = ldk(8);
    u32x2 q1 = tr_read(qa + 8192);
    const KF k0 = ldk(0);
    SCHED();
    q1.x &= lom; q1.y = 0u;
    auto qk = [&](const KF& k) { f32x16 z = {}; z = mfma32(k.a, q0, z); return mfma32k8(k.b, q1, z); };
    f32x16 Sm = {};
    Sm = mfma32(k8.a, q0, Sm); S0 = mfma32(k0.a, q0, S0);
    Sm = mfma32k8(k8.b, q1, Sm); S0 = mfma32k8(k0.b, q1, S0);
    KF kf[2]; VF vf[2], v8, v0;
    kf[1] = ldk(7); kf[0] = ldk(6);
    SCHED();
#pragma unroll
    for (int e = 0; e < 16; e++) Sm[e] = __builtin_amdgcn_inverse_ballot_w64(cnd[e]) ? Sm[e] : S0[e];
    float m = __builtin_fmaxf(__builtin_fmaxf(Sm[0], Sm[1]), Sm[2]), m2 = __builtin_fmaxf(__builtin_fmaxf(Sm[3], Sm[4]), Sm[5]);
#pragma unroll
    for (int e = 6; e < 14; e += 4) { m = fmaxf(fmaxf(m, Sm[e]), Sm[e + 1]); m2 = fmaxf(fmaxf(m2, Sm[e + 2]), Sm[e + 3]); }
    m = fmaxf(fmaxf(m, Sm[14]), Sm[15]);
    m = xhalf_max(fmaxf(m, m2));
    f32x16 O;
    float l;
#pragma unroll 1
    for (int pass = 0; pass < 2; pass++) {
      q1.y = pk2(-m, lo2f(pk2(m, m)) - m) & lom;
      f32x16 S[2];
      {
        f32x16 p = mfma32k8(kf[1].b, u32x2{0u, q1.y}, Sm);
        S[1] = qk(kf[1]);
        SCHED();
        wait_vm<0>();
        prog_set(sb + W_PROG + 4 * wv, 3);
#pragma unroll
        for (int e = 0; e < 8; e++) p[e] = __builtin_amdgcn_exp2f(p[e]);
        SCHED();
        const u32 vfl = lds_peek(fl);
        SCHED();
#pragma unroll
        for (int e = 8; e < 16; e++) p[e] = __builtin_amdgcn_exp2f(p[e]);
        SCHED();
        if (__builtin_amdgcn_ballot_w64(vfl < 3u)) prog_wait(fl, 3);
        asm volatile("" ::: "memory");
        v8 = ldv(8); v0 = ldv(0); vf[1] = ldv(7);
        SCHED();
        const u32x4 pa = pk8(p, 0), pb = pk8(p, 8);
        O = f32x16{};
        O = mfma32(v8.a, pa & u32x4{msk[0], msk[1], msk[2], msk[3]}, O); O = mfma32(v8.b, pb & u32x4{msk[4], msk[5], msk[6], msk[7]}, O);
        O = mfma32(v0.a, pa & u32x4{nmk[0], nmk[1], nmk[2], nmk[3]}, O); O = mfma32(v0.b, pb & u32x4{nmk[4], nmk[5], nmk[6], nmk[7]}, O);
      }
      auto tile = [&](auto self, auto I) -> void {
        constexpr int i = I;
        if (i < ib) return;
        SCHED();
        f32x16& Sc = S[i & 1];
        f32x16 z = {};
        if constexpr (i > 1) z = mfma32(kf[(i - 1) & 1].a, q0, z);
        if constexpr (i > 2) kf[i & 1] = ldk(i - 2);
        if constexpr (i > 1) vf[(i - 1) & 1] = ldv(i - 1);
        SCHED();
#pragma unroll
        for (int e = 0; e < 8; e++) Sc[e] = __builtin_amdgcn_exp2f(Sc[e]);
        SCHED();
        if constexpr (i > 1) S[(i - 1) & 1] = mfma32k8(kf[(i - 1) & 1].b, q1, z);
        const u32x4 pa = pk8(Sc, 0);
        SCHED();
#pragma unroll
        for (int e = 8; e < 12; e++) Sc[e] = __builtin_amdgcn_exp2f(Sc[e]);
        SCHED();
        O = mfma32(vf[i & 1].a, pa, O);
#pragma unroll
        for (int e = 12; e < 16; e++) Sc[e] = __builtin_amdgcn_exp2f(Sc[e]);
        const u32x4 pb = pk8(Sc, 8);
        SCHED();
        O = mfma32(vf[i & 1].b, pb, O);
        if constexpr (i > 1) self(self, IC(i - 1));
      };
      tile(tile, IC(7));
      SCHED();
      { const u32 lu = f2u(O[10]); auto r = __builtin_amdgcn_permlane32_swap(lu, lu, false, false); l = u2f(r[0]); }
      if (pass == 1 || __all(l > 8.67e-19f && l < 1.15e18f)) break;
      q1.y = 0u;
#pragma unroll 1
      for (int i = ib; i < 8; i++) {
        const f32x16 s = qk(ldk(i));
#pragma unroll
        for (int e = 0; e < 16; e += 2) m = fmaxf(fmaxf(m, s[e]), s[e + 1]);
      }
      m = xhalf_max(m);
      kf[1] = ldk(7); kf[0] = ldk(6);
    }
    STAMP(6);
    float inv = __builtin_amdgcn_rcpf(l);
    asm volatile("s_nop 1" : "+v"(inv));
    char* Og = ws + WS_O + (size_t)(16 * qb + 2 * w) * 18432;
    gst128(u32x4{pk2(vmul(O[0], inv), vmul(O[1], inv)), pk2(vmul(O[2], inv), vmul(O[3], inv)), pk2(vmul(O[4], inv), vmul(O[5], inv)), pk2(vmul(O[6], inv), vmul(O[7], inv))},
           oo, Og + (h >> 1) * 1024 + (h & 1) * 512);
    if (hh == 0) gst32(pk2(vmul(O[8], inv), vmul(O[9], inv)), (c >> 4) * 18432 + (c & 15) * 4, Og + 16384 + h * 64);
    publish(fb, F_O + (((4 * qb + (w & 3)) * 8 + lane) * 64 + (w >> 2) * 32 + h) * 8, tag, lane < 8);
      if (b == 255 && wv == 4) {
#pragma unroll 1
        for (int spin = 0; spin < (1 << 18); spin++) {
          u32x4 v[9];
          sfor<9>([&](auto I) { gl128c<0>(v[I], (lane >> 4) * 2048 + (lane & 15) * 16, fb + F_U + I * 8192); });
          asm volatile("s_waitcnt vmcnt(0)" : "+v"(v[0]), "+v"(v[1]), "+v"(v[2]), "+v"(v[3]), "+v"(v[4]), "+v"(v[5]), "+v"(v[6]), "+v"(v[7]), "+v"(v[8])::"memory");
          u32 bad = 0;
#pragma unroll
          for (int i = 0; i < 9; i++) bad |= (v[i].x ^ tag.x) | (v[i].y ^ tag.y) | (v[i].z ^ tag.x) | (v[i].w ^ tag.y);
          if (!__builtin_amdgcn_ballot_w64(bad != 0)) break;
          __builtin_amdgcn_s_sleep(16);
        }
        if (lane == 0) gst64(tag, F_EP, fb);
      }
    STAMP_END();
    return;
  }
  {
    const int g = lane >> 4, l15 = lane & 15, q = l15 >> 2;
    const int r7 = lane / 9, c9 = lane - 9 * r7;
    const u32 xw = sb + L_X + cw * 2304;
    const int m16 = 16 * (rt >> 2) + 2 * (rt & 3) + 8 * (cw >> 1) + (cw & 1);
    if (__builtin_amdgcn_ballot_w64(lds_peek(sb + W_PROG + 4 * (spare ? (lane & 3) : (lane & 7))) < (u32)WOGATE)) { publish(fb, ypub, tag, lane < 16); ypub = 0; }
    prog_wait(sb + W_XCH + (spare ? 16 + 4 * (lane & 7) : 32 + 4 * (lane & 3)), 3);
    prog_wait(sb + W_PROG + 4 * (spare ? (lane & 3) : (lane & 7)), WOGATE);
    STAMP(9);
    {
      const u16* xp = x + (size_t)(16 * m16) * 576 + 72 * ct;
      dma16n<63>(xp, r7 * 1152 + 16 * c9, xw); dma16n<63>(xp + 7 * 576, r7 * 1152 + 16 * c9, xw + 1008); dma16n<63>(xp + 9 * 576, r7 * 1152 + 16 * c9, xw + 1296);
      wo_load(0, 4);
      wait_vm<7>();
      if (ypub && lane < 16) gst64(tag, ypub, fb);
      wo_load(4, 24);
    }
    STAMP(10);
    await<POLL_SLEEP>(fb, F_Y + ((rt * 8 + ct) * 80 + (lane < 18 ? 18 * cw + lane : 72 + (cw >> 1))) * 8, tag);
    STAMP(11);
    {
      const char* yp = ws + WS_Y2 + (size_t)(rt * 576 + 72 * ct + 18 * cw) * 128;
      dma16c(yp, lane * 16, sb + L_Y + cw * 2304); dma16c(yp + 1024, lane * 16, sb + L_Y + cw * 2304 + 1024); dma16c(yp + 1280, lane * 16, sb + L_Y + cw * 2304 + 1280);
    }
    u32 gtu;
    gl32c<0>(gtu, (16 * m16 + l15) * 4, ws + WS_G);
    asm volatile("s_waitcnt vmcnt(0)" : "+v"(gtu)::"memory");
    prog_set(sb + W_XCH + 4 * wv, 4);
    prog_wait(sb + W_XCH + 32 + 4 * (lane & 3), 4);
    const float gt = u2f(gtu);
    f32x4 acc[5], tb[5];
    const u32 xa = xw + l15 * 144 + 8 * g;
    {
      const float g1 = rbf(1.f - gt);
      const u32 ya = sb + L_Y + (4 * g + q) * 128 + 32 * cw + 8 * (lane & 3);
      sfor<5>([&](auto NT) {
        const u32x2 y = tr_read(ya + NT * 2048), xv = lds64(xa + NT * 32);
        tb[NT] = f32x4{vmul(vmul(g1, lo2f(xv.x)), lo2f(y.x)), vmul(vmul(g1, hi2f(xv.x)), hi2f(y.x)), vmul(vmul(g1, lo2f(xv.y)), lo2f(y.y)), vmul(vmul(g1, hi2f(xv.y)), hi2f(y.y))};
      });
    }
    const u32 wa = opaque(sb + L_W + (18 * (g >> 1) + 4 * (g & 1) + q) * 160 + 8 * (lane & 3)), wah = opaque(wa + 46080);
    const u32 wa2 = opaque(sb + L_W + ((4 * g + (q >> 1)) * 18 + 16 + (q & 1)) * 160 + 8 * (lane & 3));
    u32x4 wf[2][5];
    auto rd = [&](auto KS) {
      constexpr int ks = KS;
      sfor<5>([&](auto NT) {
        if constexpr (ks < 16) { const u32 a = (ks < 8 ? wa : wah) + (ks & 7) * 5760 + NT * 32; wf[ks & 1][NT] = cat(tr_read(a), tr_read(a + 1280)); }
        else wf[ks & 1][NT] = cat(tr_read(wa2 + (ks - 16) * 46080 + NT * 32), tr_read(wa2 + (ks - 16) * 46080 + 5760 + NT * 32));
      });
    };
    rd(IC(0));
    STAMP(6);
    u32x4 of[16]; u32 oe[8];
    const char* ob = ws + WS_O + (size_t)m16 * 18432;
    {
      const u32 oo = F_O + ((rt * 8 + ct) * 64 + (cw >> 1) * 32 + (lane & 31)) * 8;
      u32 miss = ~0u;
      sfor<4>([&](auto G) {
#pragma unroll 1
        for (int spin = 0; (miss & (0xffu << (8 * G))) && spin < (1 << 20); spin++) {
          u32x2 v;
          asm volatile("global_load_dwordx2 %0, %1, %2 sc1\n\ts_waitcnt vmcnt(0)" : "=&v"(v) : "v"(oo), "s"(fb) : "memory");
          miss = (u32)__builtin_amdgcn_ballot_w64(v.x != tag.x || v.y != tag.y);
          if (miss & (0xffu << (8 * G))) __builtin_amdgcn_s_sleep(POLL_FAST);
        }
        sfor<4>([&](auto F) { gl128c<F * 1024>(of[4 * G + F], lane * 16, ob + G * 4096); });
      });
    }
    STAMP(12);
    __builtin_amdgcn_s_setprio(3);
    sfor<8>([&](auto F) { gl32c<(F >> 2) * 1024 + (F & 3) * 64>(oe[F], 256 * g + 4 * l15, ob + 16384); });
    sfor<16>([&](auto KS) {
      constexpr int ks = KS;
      SCHED();
      if constexpr (ks % 2 == 0) asm volatile("s_waitcnt vmcnt(%2)" : "+v"(of[ks]), "+v"(of[ks + 1]) : "i"(22 - ks) : "memory");
      rd(IC(ks + 1));
      sfor<5>([&](auto NT) { acc[NT] = mfma16(wf[ks & 1][NT], of[ks], ks == 0 ? f32x4{0.f, 0.f, 0.f, 0.f} : acc[NT]); });
      sfor<5>([&](auto) { __builtin_amdgcn_sched_group_barrier(0x8, 1, 0); __builtin_amdgcn_sched_group_barrier(0x100, 2, 0); });
    });
    SCHED();
    asm volatile("s_waitcnt vmcnt(0)" : "+v"(oe[0]), "+v"(oe[1]), "+v"(oe[2]), "+v"(oe[3]), "+v"(oe[4]), "+v"(oe[5]), "+v"(oe[6]), "+v"(oe[7])::"memory");
    rd(IC(17));
    sfor<5>([&](auto NT) { acc[NT] = mfma16(wf[0][NT], u32x4{oe[0], oe[1], oe[2], oe[3]}, acc[NT]); });
    sfor<5>([&](auto NT) { acc[NT] = mfma16(wf[1][NT], u32x4{oe[4], oe[5], oe[6], oe[7]}, acc[NT]); });
    SCHED();
    STAMP(13);
    sfor<5>([&](auto NT) {
      constexpr int nt = NT;
      if (nt < 4 || g < 2)
        st64(xa + nt * 32, u32x2{pk2(fmaf(gt, acc[nt][0], tb[nt][0]), fmaf(gt, acc[nt][1], tb[nt][1])), pk2(fmaf(gt, acc[nt][2], tb[nt][2]), fmaf(gt, acc[nt][3], tb[nt][3]))});
    });
    char* orow = (char*)(out + (size_t)(16 * m16) * 576 + 72 * ct);
    u32x4 t[3];
#pragma unroll
    for (int it = 0; it < 3; it++) t[it] = lds128(xw + it * 1008 + lane * 16);
#pragma unroll
    for (int it = 0; it < 3; it++)
      if (lane < (it < 2 ? 63 : 18)) asm volatile("global_store_dwordx4 %0, %1, %2 " OUTPOL "\n\ts_nop 1" ::"v"((7 * it + r7) * 1152 + 16 * c9), "v"(t[it]), "s"(orow) : "memory");
  }
  STAMP_END();
}
extern "C" __global__ void gm_end() {}

extern "C" void kernel_launch(void* const* d_in, const int* in_sizes, int n_in, void* d_out, int out_size, void* d_ws, size_t ws_size,
                              hipStream_t stream) {
  (void)hipFuncSetAttribute((const void*)gm_main, hipFuncAttributeMaxDynamicSharedMemorySize, L_END);
  gm_main<<<256, 768, L_END, stream>>>((const u16*)d_in[0], (const u16*)d_in[1], (char*)d_ws, (const u16*)d_in[4], (const u16*)d_in[5], (const u16*)d_in[6],
                                       (const u16*)d_in[7], (const u16*)d_in[2], (const u16*)d_in[8], (const u16*)d_in[9], (u16*)d_out);
}
```

```cpp
#include <hip/hip_runtime.h>
#include <hip/hip_bf16.h>
#include <utility>

typedef unsigned short u16;
typedef unsigned int u32;
typedef u32 u32x2 __attribute__((ext_vector_type(2)));
typedef u32 u32x4 __attribute__((ext_vector_type(4)));
typedef short s16x4 __attribute__((ext_vector_type(4)));
typedef __bf16 bf16x8 __attribute__((ext_vector_type(8)));
typedef __bf16 bf16x2 __attribute__((ext_vector_type(2)));
typedef float f32x2 __attribute__((ext_vector_type(2)));
typedef float f32x4 __attribute__((ext_vector_type(4)));
typedef float f32x16 __attribute__((ext_vector_type(16)));
#define LAS __attribute__((address_space(3)))
#define GAS __attribute__((address_space(1)))
#define DEVI __device__ __forceinline__
#define IC(n) std::integral_constant<int, (n)>{}

constexpr size_t WS_QKV = 0;
constexpr size_t WS_O   = WS_QKV + 96ull * 2048 * 40;
constexpr size_t WS_UR  = WS_O + 32ull * 2048 * 40;
constexpr size_t WS_Y2  = WS_UR + 576ull * 4096;
constexpr size_t WS_G   = WS_Y2 + 576ull * 4096;
constexpr size_t WS_F   = WS_G + 8192;
constexpr u32 F_EP = 0, F_1 = 1024, F_U = F_1 + 256 * 512, F_O = F_U + 288 * 256, F_Y = F_O + 256 * 512;

template <typename F, int... I> DEVI void sfor_impl(F&& f, std::integer_sequence<int, I...>) { (f(std::integral_constant<int, I>{}), ...); }
template <int N, typename F> DEVI void sfor(F&& f) { sfor_impl(f, std::make_integer_sequence<int, N>{}); }

DEVI u32 pk2(float a, float b) { f32x2 v = {a, b}; return __builtin_bit_cast(u32, __builtin_convertvector(v, bf16x2)); }
DEVI u32 f2u(float a) { return __builtin_bit_cast(u32, a); }
DEVI float u2f(u32 a) { return __builtin_bit_cast(float, a); }
DEVI float lo2f(u32 w) { return __builtin_bit_cast(float, w << 16); }
DEVI float hi2f(u32 w) { return __builtin_bit_cast(float, w & 0xffff0000u); }
DEVI float rbf(float a) { return lo2f(pk2(a, a)); }
DEVI f32x4 mfma16(u32x4 a, u32x4 b, f32x4 c) { return __builtin_amdgcn_mfma_f32_16x16x32_bf16(__builtin_bit_cast(bf16x8, a), __builtin_bit_cast(bf16x8, b), c, 0, 0, 0); }
DEVI f32x16 mfma32(u32x4 a, u32x4 b, f32x16 c) { return __builtin_amdgcn_mfma_f32_32x32x16_bf16(__builtin_bit_cast(bf16x8, a), __builtin_bit_cast(bf16x8, b), c, 0, 0, 0); }
DEVI f32x16 mfma32k8(u32x2 a, u32x2 b, f32x16 c) { return __builtin_amdgcn_mfma_f32_32x32x8bf16_1k(__builtin_bit_cast(s16x4, a), __builtin_bit_cast(s16x4, b), c, 0, 0, 0); }
DEVI u32x2 tr_read(u32 addr) { return __builtin_bit_cast(u32x2, __builtin_amdgcn_ds_read_tr16_b64_v4i16((LAS s16x4*)addr)); }
DEVI u32x4 lds128(u32 addr) { return *(LAS u32x4*)addr; }
DEVI u32x2 lds64(u32 addr) { return *(LAS u32x2*)addr; }
DEVI u32 lds32(u32 addr) { return *(LAS u32*)addr; }
DEVI void st128(u32 addr, u32x4 v) { *(LAS u32x4*)addr = v; }
DEVI void st64(u32 addr, u32x2 v) { *(LAS u32x2*)addr = v; }
DEVI void st32(u32 addr, u32 v) { *(LAS u32*)addr = v; }
DEVI u32x4 cat(u32x2 a, u32x2 b) { return u32x4{a.x, a.y, b.x, b.y}; }
DEVI float vfma(float a, float b, float c) { float d; asm("v_fma_f32 %0, %1, %2, %3" : "=v"(d) : "v"(a), "v"(b), "v"(c)); return d; }
DEVI float vmul(float a, float b) { float d; asm("v_mul_f32 %0, %1, %2" : "=v"(d) : "v"(a), "v"(b)); return d; }
DEVI float vadd(float a, float b) { float d; asm("v_add_f32 %0, %1, %2" : "=v"(d) : "v"(a), "v"(b)); return d; }
DEVI f32x4 vadd4(f32x4 a, f32x4 b) { return f32x4{vadd(a[0], b[0]), vadd(a[1], b[1]), vadd(a[2], b[2]), vadd(a[3], b[3])}; }
DEVI u32 opaque(u32 v) { asm volatile("" : "+v"(v)); return v; }
DEVI void dma16(const void* sbase, u32 voff, u32 ldsbase) {
  asm volatile("s_mov_b32 m0, %2\n\ts_nop 0\n\tglobal_load_lds_dwordx4 %0, %1" ::"v"(voff), "s"(sbase), "s"(ldsbase) : "memory");
}
template <int NL> DEVI void dma16n(const void* sbase, u32 voff, u32 ldsbase) {
  asm volatile("s_mov_b32 m0, %2\n\ts_mov_b32 exec_hi, %3\n\tglobal_load_lds_dwordx4 %0, %1\n\ts_mov_b32 exec_hi, -1" ::"v"(voff), "s"(sbase), "s"(ldsbase),
               "i"((1u << (NL - 32)) - 1u) : "memory");
}
template <int OFF> DEVI void gl128(u32x4& d, u32 voff, const void* sbase) { asm volatile("global_load_dwordx4 %0, %1, %2 offset:%3" : "=v"(d) : "v"(voff), "s"(sbase), "i"(OFF)); }
template <int OFF> DEVI void gl64(u32x2& d, u32 voff, const void* sbase) { asm volatile("global_load_dwordx2 %0, %1, %2 offset:%3" : "=v"(d) : "v"(voff), "s"(sbase), "i"(OFF)); }
DEVI void dma16c(const void* sbase, u32 voff, u32 ldsbase) {
  asm volatile("s_mov_b32 m0, %2\n\ts_nop 0\n\tglobal_load_lds_dwordx4 %0, %1 sc1" ::"v"(voff), "s"(sbase), "s"(ldsbase) : "memory");
}
template <int OFF> DEVI void gl128c(u32x4& d, u32 voff, const void* sbase) { asm volatile("global_load_dwordx4 %0, %1, %2 offset:%3 sc1" : "=v"(d) : "v"(voff), "s"(sbase), "i"(OFF)); }
template <int OFF> DEVI void gl32c(u32& d, u32 voff, const void* sbase) { asm volatile("global_load_dword %0, %1, %2 offset:%3 sc1" : "=v"(d) : "v"(voff), "s"(sbase), "i"(OFF)); }
DEVI void gst128(u32x4 v, u32 voff, void* sbase) { asm volatile("global_store_dwordx4 %0, %1, %2 sc1\n\ts_nop 1" ::"v"(voff), "v"(v), "s"(sbase) : "memory"); }
DEVI void gst64(u32x2 v, u32 voff, void* sbase) { asm volatile("global_store_dwordx2 %0, %1, %2 sc1" ::"v"(voff), "v"(v), "s"(sbase) : "memory"); }
DEVI void gst32(u32 v, u32 voff, void* sbase) { asm volatile("global_store_dword %0, %1, %2 sc1" ::"v"(voff), "v"(v), "s"(sbase) : "memory"); }
#define CODE_PF(from, to, ck, step, lds)                                                                                              \
  do {                                                                                                                                \
    u32 lo_, hi_, len_;                                                                                                               \
    asm volatile("s_getpc_b64 s[20:21]\n\ts_add_u32 %0, s20, " from "@rel32@lo+4\n\ts_addc_u32 %1, s21, " from "@rel32@hi+12\n\ts_mov_b32 %2, " to "-" from \
                 : "=s"(lo_), "=s"(hi_), "=s"(len_)::"s20", "s21", "scc");                                                            \
    const char* code_ = (const char*)(((unsigned long long)hi_ << 32) | lo_);                                                         \
    for (u32 o_ = (ck) * 1024; o_ < len_; o_ += (step) * 1024) { const u32 a_ = o_ + (threadIdx.x & 63) * 16; dma16(code_, a_ < len_ ? a_ : 0u, lds); } \
  } while (0)
template <int N> DEVI void wait_vm() { asm volatile("s_waitcnt vmcnt(%0)" ::"i"(N > 63 ? 63 : N) : "memory"); }
DEVI void wait_lgkm() { asm volatile("s_waitcnt lgkmcnt(0)" ::: "memory"); }
DEVI void bar() { asm volatile("s_barrier" ::: "memory"); }
DEVI u32 rfl(u32 v) { return __builtin_amdgcn_readfirstlane(v); }
#define SCHED() __builtin_amdgcn_sched_barrier(0)
#define STAMP(i)
#define STAMP_END()

constexpr float QSCALE = 0.23570226039551584f * 1.4426950408889634f;

#define POLL_SLEEP 8
#define POLL_FAST 4
#define SPDELAY 24
#define LDPOL "sc1"
#define FSPOL "sc1"
#define CVDELAY 0
#define PUBB 2
#define NMID 1
#define FLB 1
#define PUBD 0
#define PREPFIRST 2
#define OUTPOL "sc1"
#define BAL 0
#define CPRIO 2
#define WOGATE 3
#define YPOS 16
#define CMPRIO 2
#define POLLW 4
#define VDELAY 3

constexpr int L_SLOT = 32768, L_WOFF = 16384, L_STG = 94208, L_GW = 131072, L_PT = 133120, L_TILE = 17408;
constexpr int L_K = 0, L_V = 21760, L_C = 41536, L_R = L_PT + 1024, L_FL = 152320, L_DUMMY = L_FL + 256, L_Q = 153600, L_END = 163840;
constexpr int KC_STR = 4640, CS_U = 4 * KC_STR, CS_SZ = CS_U + 4096 + 128;
constexpr int L_W = L_C, L_X = L_W + 576 * 160, L_Y = L_X + 9216;
static_assert(L_Y + 9216 <= L_FL && L_C + 2 * CS_SZ <= L_STG && L_C + 4 * CS_SZ <= L_PT && L_PT + L_TILE <= L_FL, "lds map");
constexpr int W_PROG = L_FL, W_XCH = L_FL + 64, W_GO = L_FL + 128, W_TAG = L_FL + 144;

DEVI u32 lds_peek(u32 a) { return __hip_atomic_load((LAS u32*)a, __ATOMIC_RELAXED, __HIP_MEMORY_SCOPE_WORKGROUP); }
DEVI void prog_set(u32 a, u32 n) { asm volatile("" ::: "memory"); __hip_atomic_store((LAS u32*)a, n, __ATOMIC_RELAXED, __HIP_MEMORY_SCOPE_WORKGROUP); }
DEVI void prog_wait(u32 a, u32 n) { while (__builtin_amdgcn_ballot_w64(lds_peek(a) < n)) __builtin_amdgcn_s_sleep(1); asm volatile("" ::: "memory"); }
DEVI void publish(char* fb, u32 off, u32x2 tag, bool on) { wait_vm<0>(); if (on) asm volatile("global_store_dwordx2 %0, %1, %2 " FSPOL ::"v"(off), "v"(tag), "s"(fb) : "memory"); }
template <int SLP> DEVI void await(const char* fb, u32 off, u32x2 tag) {
  for (int spin = 0; spin < (1 << 20); spin++) {
    u32x2 v;
    asm volatile("global_load_dwordx2 %0, %1, %2 " LDPOL "\n\ts_waitcnt vmcnt(0)" : "=&v"(v) : "v"(off), "s"(fb) : "memory");
    if (!__builtin_amdgcn_ballot_w64(v.x != tag.x || v.y != tag.y)) break;
    __builtin_amdgcn_s_sleep(SLP);
  }
}

DEVI void prep_issue(const u16* Bk, const u16* Bv, int row0, int col, u32 tb, u32 pw, int lane) {
  const int r8 = lane >> 3, c8 = lane & 7;
  sfor<2>([&](auto I) {
    const int j = 2 * pw + I;
    int row = row0 + 8 * j + r8 - 3; row = row < 0 ? 0 : row;
    dma16(Bk + col, row * 1152 + 16 * (c8 ^ j), tb + 1024 * j);
    dma16(Bv + col, (row0 + 8 * j + r8) * 1152 + 16 * (c8 ^ j), tb + 9216 + 1024 * j);
  });
  dma16(Bk + col, (row0 + 61 + (r8 > 2 ? 2 : r8)) * 1152 + 16 * c8, tb + 8192);
}
template <typename F> DEVI void prep_compute(int row0, int col, u32 tb, u32 pw, int lane, u32x4 sh, char* ws, F&& mid) {
  const int c8 = lane & 7, cp = 8 * pw + (lane >> 3);
  const u32 o0 = tb + 1024 * c8 + 16 * ((cp >> 2) ^ c8) + 4 * (cp & 3), o1 = tb + 1024 * c8 + 16 * ((cp >> 2) ^ ((c8 + 1) & 7)) + 4 * (cp & 3);
  row0 += 8 * c8;
  u32 kk[11], vv[8];
#pragma unroll
  for (int i = 0; i < 11; i++) kk[i] = lds32((i < 8 ? o0 : o1) + 128 * i);
#pragma unroll
  for (int i = 0; i < 8; i++) vv[i] = lds32(o0 + 9216 + 128 * i);
  if (row0 == 0) { kk[0] = 0; kk[1] = 0; kk[2] = 0; }
  const u32 go = 2 * cp * 4096 + 2 * (2040 - row0);
  char* dst = ws + WS_UR + (size_t)col * 4096;
  float r[8];
  {
    const float a0 = lo2f(sh.x), a1 = hi2f(sh.x), a2 = lo2f(sh.y), a3 = hi2f(sh.y);
#pragma unroll
    for (int i = 0; i < 8; i++) r[i] = vmul(vfma(lo2f(kk[i + 3]), a0, vfma(lo2f(kk[i + 2]), a1, vfma(lo2f(kk[i + 1]), a2, vmul(lo2f(kk[i]), a3)))), lo2f(vv[i]));
    gst128(u32x4{pk2(r[7], r[6]), pk2(r[5], r[4]), pk2(r[3], r[2]), pk2(r[1], r[0])}, go, dst);
  }
  mid();
  {
    const float a0 = lo2f(sh.z), a1 = hi2f(sh.z), a2 = lo2f(sh.w), a3 = hi2f(sh.w);
#pragma unroll
    for (int i = 0; i < 8; i++) r[i] = vmul(vfma(hi2f(kk[i + 3]), a0, vfma(hi2f(kk[i + 2]), a1, vfma(hi2f(kk[i + 1]), a2, vmul(hi2f(kk[i]), a3)))), hi2f(vv[i]));
    gst128(u32x4{pk2(r[7], r[6]), pk2(r[5], r[4]), pk2(r[3], r[2]), pk2(r[1], r[0])}, go, dst + 4096);
  }
}

template <int NR> DEVI void conv_mfma(u32 cs, int rho0, int lane, f32x4 (&acc)[8]) {
  const int g = lane >> 4, l15 = lane & 15;
  u32 aa = cs + (l15 & 3) * KC_STR + 2 * (64 * (l15 >> 2) + 8 * g) + 64 * rho0;
  u32 ba = opaque(cs + CS_U + 2 * (1852 - 1792 - 4 * l15 + 8 * g) + 64 * rho0), bb = opaque(ba + 8);
  u32x4 A[8], G[8];
  sfor<5>([&](auto J) { constexpr int p = J < 4 ? J + 2 : 1; A[p] = lds128(aa + p * 512); G[p] = cat(lds64(ba + (7 - p) * 512), lds64(bb + (7 - p) * 512)); SCHED(); });
#pragma unroll 1
  for (int rho = 0; rho < NR; rho++) {
    constexpr int pa[36] = {2, 2, 3, 3, 2, 4, 3, 4, 2, 5, 1, 1, 2, 1, 3, 1, 4, 1, 5, 1, 6, 2, 0, 3, 0, 4, 0, 5, 0, 1, 0, 6, 0, 7, 0, 0};
    constexpr int pe[36] = {2, 3, 2, 3, 4, 2, 4, 3, 5, 2, 1, 2, 1, 3, 1, 4, 1, 5, 1, 6, 1, 0, 2, 0, 3, 0, 4, 0, 5, 0, 1, 0, 6, 0, 7, 0};
    constexpr int rdc[36] = {18, 19, 20, 21, 22, 23, 0, 1, 2, -1, -1, -1, -1, -1, -1, -1, -1, -1, -1, -1, -1, 6, 7, 8, 9, 10, 11, 12, 13, 14, 15, 16, 17, 3, 4, 5};
    sfor<36>([&](auto I) {
      constexpr int i = I, r = rdc[i], x = r / 3;
      if constexpr (i == 35) __builtin_amdgcn_s_setprio(CMPRIO);
      if constexpr (i == 9) { aa = opaque(aa + 64); ba = opaque(ba + 64); bb = opaque(bb + 64); }
      acc[pa[i] + pe[i]] = mfma16(A[pa[i]], G[pe[i]], acc[pa[i] + pe[i]]);
      if constexpr (r >= 0) {
        if constexpr (r % 3 == 0) A[x] = lds128(aa + x * 512);
        else if constexpr (r % 3 == 1) { const u32x2 t = lds64(ba + (7 - x) * 512); G[x].x = t.x; G[x].y = t.y; }
        else { const u32x2 t = lds64(bb + (7 - x) * 512); G[x].z = t.x; G[x].w = t.y; }
      }
      SCHED();
    });
  }
  SCHED();
}
DEVI void conv_out(f32x4 y, u32x2 u, float Dv, u32 off, char* ydst) {
  const float y0 = vfma(hi2f(u.y), Dv, y[0]), y1 = vfma(lo2f(u.y), Dv, y[1]), y2 = vfma(hi2f(u.x), Dv, y[2]), y3 = vfma(lo2f(u.x), Dv, y[3]);
  gst64(u32x2{pk2(y0, y1), pk2(y2, y3)}, off, ydst);
}

DEVI float xhalf_max(float m) { const u32 mu = f2u(m); auto r = __builtin_amdgcn_permlane32_swap(mu, mu, false, false); return fmaxf(u2f(r[0]), u2f(r[1])); }

extern "C" __global__ __launch_bounds__(768, 3) void gm_main(const u16* __restrict__ x, const u16* __restrict__ wqkv, char* __restrict__ ws,
                                                             const u16* __restrict__ Bk, const u16* __restrict__ Bv, const u16* __restrict__ shk,
                                                             const u16* __restrict__ ssmk, const u16* __restrict__ wo, const u16* __restrict__ ssmD,
                                                             const u16* __restrict__ gw, u16* __restrict__ out) {
  extern __shared__ __attribute__((aligned(16))) char smem[];
  const u32 sb = (u32)(size_t)(LAS char*)smem;
  const int b = blockIdx.x, xcd = b & 7, idx = b >> 3;
  const int lane = threadIdx.x & 63;
  const u32 wv = rfl(threadIdx.x >> 6);
  const bool spare = idx >= 28;
  char* const fb = ws + WS_F;
  const int qb = ((idx >> 2) + 1) & 7, h = 4 * xcd + (idx & 3);
  const int chb = wv >= 8 ? 64 * xcd + 2 * idx : 512 + 2 * h;
  u32x2 pre[2], fv; u32x4 cur[2];
  const u32 fo = F_U + ((chb >> 1) * 32 + 24 - 8 * (wv & 3) + (lane & 7)) * 8;
  STAMP(0);
  {
    const int hg = xcd & 1, ri = idx & 3, rb = 4 * (xcd >> 1) + ri, jt = idx >> 2;
    const int r8 = lane >> 3, c8 = lane & 7;
    const int rot = ((0x124924u >> (2 * (jt + ri))) & 3) * 3;
    const u32 xo = opaque((32 * wv + r8) * 1152 + 16 * (c8 ^ r8));
    asm volatile("" ::"s"(x), "s"(wqkv), "s"(Bk), "s"(Bv));
    if (wv < 4 && !spare) {
      const u32 lw = wv;
      const char* xb = (const char*)(x + (size_t)rb * 128 * 576);
      sfor<4>([&](auto I) { dma16(xb + rot * 128 + I * 9216, xo, sb + (4 * lw + I) * 1024); });
      u32 wof[2];
#pragma unroll
      for (int i = 0; i < 2; i++) {
        const int ch = (lane & 15) ^ (((lane >> 4) | (i << 2)) << 1), ln = 14 * hg + 2 * jt + (ch >> 3);
        wof[i] = ((lane >> 4) + 16 * lw) * 3456 + 128 * (ln > 26 ? 26 : ln) + 16 * (ch & 7);
      }
      const char* wb = (const char*)wqkv;
      auto issue = [&](int c) {
        const u32 sl = sb + (c & 3) * L_SLOT;
        c += rot; c = c >= 9 ? c - 9 : c;
        sfor<4>([&](auto I) { dma16(xb + c * 128 + I * 9216, xo, sl + (4 * lw + I) * 1024); });
        sfor<4>([&](auto I) { dma16(wb + (64 * c + 4 * I) * 3456, wof[I >> 1], sl + L_WOFF + (4 * lw + I) * 1024); });
      };
      sfor<4>([&](auto I) { dma16(wb + (64 * rot + 4 * I) * 3456, wof[I >> 1], sb + L_WOFF + (4 * lw + I) * 1024); });
      prep_issue(Bk, Bv, 64 * idx, 64 * xcd, sb + L_PT, lw, lane);
#pragma unroll 1
      for (int n = 1; n < 8; n++) { issue(n); wait_vm<8>(); bar(); }
      issue(8);
      wait_vm<8>(); bar();
      __builtin_amdgcn_s_sleep(2);
      {
        const char* wp = (const char*)wo + (idx == 27 ? 663552 - 24576 : idx * 23712) + lw * 6144;
        sfor<6>([&](auto I) { dma16(wp + I * 1024, lane * 16, sb + L_DUMMY); });
      }
      wait_vm<6>(); bar();
      bar();
    } else if (wv >= 8 || (spare && wv >= 4)) {
      const bool ex = wv < 8;
      const int row0 = ex ? 256 * xcd + 64 * ri : 64 * idx, col = ex ? 512 : 64 * xcd;
      const u32 tb = sb + (ex ? 0 : L_PT);
      u32x2 e;
      asm volatile("global_load_dwordx2 %0, %1, %2 sc1" : "=v"(e) : "v"(F_EP), "s"(fb));
      u32x4 sh;
      gl128<0>(sh, 8 * col + 16 * (8 * (wv & 3) + r8), shk);
      if (spare) prep_issue(Bk, Bv, row0, col, tb, wv & 3, lane);
      {
        const u32 o = (threadIdx.x & 255) * 16, op = o ? o - 8 : 0;
        sfor<2>([&](auto J) { const u16* kp = ssmk + (size_t)(chb + J) * 2048; gl128<0>(cur[J], o, kp); gl64<0>(pre[J], op, kp); });
      }
      {
        const int t3 = (4 * xcd + (idx & 3)) * 8 + (idx >> 2), k = wv & 3;
        const u32 mo = k == 0 ? F_1 + (h * 8 + qb) * 512 : k == 1 ? F_O + t3 * 512 : k == 2 ? F_Y + t3 * 640 : F_U + (chb >> 1) * 256;
        asm volatile("s_mov_b32 m0, %2\n\ts_nop 0\n\tglobal_load_lds_dword %0, %1 sc1" ::"v"(mo + (lane < (k == 3 ? 4 : k == 2 ? 10 : 8) ? lane : 0) * 64), "s"(fb), "s"(sb + L_DUMMY) : "memory");
      }
      if (wv == 8) st32(sb + L_FL + 4 * lane, 0u);
      if (spare) wait_vm<5>();
      bar();
      asm volatile("s_waitcnt vmcnt(5)" : "+v"(e), "+v"(sh)::"memory");
      e.x += 1u; e.y += e.x == 0u;
      if (wv == 8) st64(sb + W_TAG, e);
      prep_compute(row0, col, tb, wv & 3, lane, sh, ws, [&]() { if (!spare) { SCHED(); bar(); SCHED(); } });
      wait_lgkm();
      bar();
      if (!spare) sfor<PUBB>([&](auto) { bar(); });
      asm volatile("s_waitcnt vmcnt(0)" : "+v"(cur[0]), "+v"(cur[1]), "+v"(pre[0]), "+v"(pre[1])::"memory");
      __builtin_amdgcn_s_sleep(PUBD);
      publish(fb, F_U + (((col >> 1) + 8 * (wv & 3) + lane) * 32 + (row0 >> 6)) * 8, e, lane < 8);
      if (!spare) sfor<8 - NMID - PUBB - FLB>([&](auto) { bar(); });
      else if (wv >= 8) __builtin_amdgcn_s_sleep(SPDELAY);
      else __builtin_amdgcn_s_sleep(16);
      asm volatile("global_load_dwordx2 %0, %1, %2 sc1" : "=v"(fv) : "v"(fo), "s"(fb));
      if (!spare) sfor<FLB>([&](auto) { bar(); });
    } else if (spare) {
      CODE_PF("gm_main", "gm_end", 4 * ri + wv, 16, sb + L_DUMMY);
      bar(); bar();
    } else {
      const int cw = wv - 4, g = lane >> 4, l15 = lane & 15, wm = cw & 1, wn = cw >> 1;
      const u32 xra = sb + (64 * wm + l15) * 128 + 16 * (g ^ (l15 & 7));
      const u32 xb[2][2] = {{opaque(xra), opaque(xra + 2 * L_SLOT)}, {opaque(xra ^ 64), opaque((xra ^ 64) + 2 * L_SLOT)}};
      u32x4 wf[2][4], xf[2][4];
      f32x4 acc[4][4];
      const int ln = 14 * hg + 2 * jt + wn;
      u32 pof; bool pon;
      {
        const int r64 = 2 * rb + wm, seg = ln / 9, lc = ln - 9 * seg, hc = (64 * lc) / 18 + (lane >> 2), li = lane & 1, nx = (lane >> 1) & 1;
        pon = ln == 27 ? lane < 16 : (lane < 20 && hc < 32 && ((18 * hc + 17 * li) >> 6) == lc && (!nx || (seg > 0 && r64 < 28)));
        pof = opaque(ln == 27 ? F_Y + (((4 * (r64 >> 2) + 2 * (r64 & 1)) * 8 + lane) * 80 + 72 + ((r64 >> 1) & 1)) * 8 : F_1 + ((hc * 8 + (r64 >> 2) + nx) * 64 + seg * 16 + li * 8 + (r64 & 3) + (nx ? 0 : 4)) * 8);
      }
      __builtin_amdgcn_s_setprio(2);
      if (ln == 27) {
        dma16(gw, wm * 128 + lane * 16, sb + L_GW + wm * 128);
        wait_vm<0>();
#pragma unroll
        for (int i = 0; i < 4; i++) acc[i][0] = f32x4{0.f, 0.f, 0.f, 0.f};
#pragma unroll 1
        for (int c = 0; c < 9; c++) {
          bar();
          const int ca = c + rot >= 9 ? c + rot - 9 : c + rot;
#pragma unroll
          for (int ks = 0; ks < 2; ks++) {
            const u32x4 gv = lds128(sb + L_GW + ca * 128 + ks * 64 + 16 * g);
#pragma unroll
            for (int i = 0; i < 4; i++) acc[i][0] = mfma16(gv, lds128(xb[ks][0] + (c & 3) * L_SLOT + 2048 * i), acc[i][0]);
          }
          wait_lgkm();
        }
        bar();
        const float p = rbf(g == 0 ? acc[0][0][0] : g == 1 ? acc[1][0][0] : g == 2 ? acc[2][0][0] : acc[3][0][0]);
        gst32(f2u(rbf(1.f / (1.f + __expf(-p)))), lane * 4, ws + WS_G + (rb * 128 + 64 * wm) * 4);
      } else {
    u32 wb[4][2];
    {
      const int q = l15 >> 2, p = lane & 3, f = q | ((g & 1) << 2);
#pragma unroll
      for (int nt = 0; nt < 4; nt++) {
        const u32 a = sb + L_WOFF + (8 * g + q) * 256 + (((8 * wn + 2 * nt + (p >> 1)) ^ (f << 1)) << 4) + 8 * (p & 1);
        wb[nt][0] = opaque(a); wb[nt][1] = opaque(a + 2 * L_SLOT);
      }
    }
    auto rd = [&](auto KS, auto C) {
      constexpr int ks = KS, c = C, so = (c & 1) * L_SLOT, hi = (c >> 1) & 1;
      sfor<4>([&](auto I) {
        xf[ks][I] = lds128(xb[ks][hi] + so + 2048 * I);
        wf[ks][I] = cat(tr_read(wb[I][hi] + so + ks * 8192), tr_read(wb[I][hi] + so + ks * 8192 + 1024));
      });
    };
    auto mm = [&](auto KS, auto FIRST) {
      constexpr int ks = KS;
      sfor<16>([&](auto I) {
        constexpr int mo[16] = {0, 1, 0, 1, 2, 2, 0, 1, 2, 3, 3, 3, 0, 1, 2, 3}, no[16] = {0, 0, 1, 1, 0, 1, 2, 2, 2, 0, 1, 2, 3, 3, 3, 3};
        constexpr int mt = mo[I], nt = no[I];
        acc[mt][nt] = mfma16(xf[ks][mt], wf[ks][nt], FIRST ? f32x4{0.f, 0.f, 0.f, 0.f} : acc[mt][nt]);
      });
    };
    auto ilv = [&]() {
      sfor<12>([&](auto) { __builtin_amdgcn_sched_group_barrier(0x8, 1, 0); __builtin_amdgcn_sched_group_barrier(0x100, 1, 0); });
      __builtin_amdgcn_sched_group_barrier(0x8, 4, 0);
    };
    const float sc = ln < 9 ? QSCALE : 1.f;
    char* qo = ws + WS_QKV + (size_t)((2 * rb + wm) * 1728 + 64 * ln) * 128;
    SCHED();
    bar();
    STAMP(1);
    rd(IC(0), IC(0));
    sfor<9>([&](auto C) {
      constexpr int c = C;
      SCHED();
      rd(IC(1), C); mm(IC(0), IC(c == 0)); ilv();
      SCHED();
      if constexpr (c == 8) wait_lgkm();
      bar();
      SCHED();
      if constexpr (c < 8) { rd(IC(0), IC(c + 1)); mm(IC(1), IC(0)); ilv(); }
    });
    STAMP(2);
    const u32 stg = sb + L_STG + cw * 9216;
    const u32 sa = stg + l15 * 144 + 8 * g, ra = stg + r8 * 144 + 16 * c8, go = lane * 16;
    u32x4 t[4][2];
    sfor<6>([&](auto NT) {
      constexpr int nt = NT;
      if constexpr (nt < 4) sfor<4>([&](auto M) { acc[M][nt] = mfma16(xf[1][M], wf[1][nt], acc[M][nt]); });
      SCHED();
      if constexpr (nt > 0 && nt < 5) {
        sfor<4>([&](auto M) {
          const f32x4 v = acc[M][nt - 1] * sc;
          st64(sa + (nt - 1) * 2304 + 32 * M, u32x2{pk2(v[0], v[1]), pk2(v[2], v[3])});
        });
        t[nt - 1][0] = lds128(ra + (nt - 1) * 2304); t[nt - 1][1] = lds128(ra + (nt - 1) * 2304 + 1152);
      }
      if constexpr (nt > 1) { gst128(t[nt - 2][0], go, qo + (nt - 2) * 2048); gst128(t[nt - 2][1], go, qo + (nt - 2) * 2048 + 1024); }
      SCHED();
    });
      }
      STAMP(8);
      publish(fb, pof, lds64(sb + W_TAG), pon);
    }
  }
  const int rt = 4 * xcd + (idx & 3), ct = idx >> 2;
  auto wo_load = [&](int r0, int r1) {
    const int r6 = lane / 10, c10 = lane - 10 * r6;
    const u32 vo = (6 * (wv & 3) + r6) * 1152 + 16 * (c10 > 8 ? 8 : c10);
    const char* wb = (const char*)(wo + 72 * ct);
#pragma unroll 4
    for (int i = r0; i < r1; i++) dma16n<60>(wb + i * 24 * 1152, vo, sb + L_W + (4 * i + (wv & 3)) * 960);
  };
  STAMP(3);
  const u32x2 tag = lds64(sb + W_TAG);
  const int cw = wv & 3;
  u32 ypub = 0;
  if (wv >= (spare ? 4u : 8u)) {
    const int tid = threadIdx.x & 255;
    const u32 cb = sb + L_C + (wv >= 8 ? 0 : 2 * CS_SZ);
    __builtin_amdgcn_s_sleep(CVDELAY);
    asm volatile("s_waitcnt vmcnt(0)" : "+v"(fv)::"memory");
    if (__builtin_amdgcn_ballot_w64(fv.x != tag.x || fv.y != tag.y)) await<POLL_SLEEP>(fb, fo, tag);
    STAMP(2);
    sfor<2>([&](auto J) { dma16c(ws + WS_UR + (size_t)(chb + J) * 4096, tid * 16, cb + CS_U + cw * 1024 + J * CS_SZ); });
    const u32 dv = *(const __attribute__((address_space(4))) u32*)(ssmD + chb);
    {
      const u32 z0 = opaque(cw * KC_STR + 16 * lane), z1 = opaque(CS_U + 4096 + 16 * (lane & 7)), za = cb + (lane < 24 ? z0 : z1);
      st128(za, u32x4{0u, 0u, 0u, 0u}); st128(za + CS_SZ, u32x4{0u, 0u, 0u, 0u});
    }
    f32x4 acc[8];
#pragma unroll
    for (int c = 0; c < 8; c++) { acc[c] = f32x4{0.f, 0.f, 0.f, 0.f}; asm volatile("" : "+v"(acc[c])); }
#pragma unroll
    for (int j = 0; j < 2; j++) {
      const u32 cs = cb + j * CS_SZ + 384 + 16 * tid;
      const u32 w0 = tid ? pre[j].x : 0u, w1 = tid ? pre[j].y : 0u, w2 = cur[j].x, w3 = cur[j].y, w4 = cur[j].z, w5 = cur[j].w;
      const u32 a01 = __builtin_amdgcn_alignbyte(w1, w0, 2), a12 = __builtin_amdgcn_alignbyte(w2, w1, 2), a23 = __builtin_amdgcn_alignbyte(w3, w2, 2),
                a34 = __builtin_amdgcn_alignbyte(w4, w3, 2), a45 = __builtin_amdgcn_alignbyte(w5, w4, 2);
      st128(cs, u32x4{a01, a12, a23, a34});
      st128(cs + KC_STR, u32x4{w1, w2, w3, w4});
      st128(cs + 2 * KC_STR, u32x4{a12, a23, a34, a45});
      st128(cs + 3 * KC_STR, cur[j]);
    }
    STAMP(1);
    wait_vm<0>();
    prog_set(sb + W_XCH + 4 * wv, 1);
    const int tm = cw >> 1, pi = cw & 1;
    const u32 cs = cb + tm * CS_SZ;
    prog_wait(sb + W_XCH + 4 * (wv & 12) + 4 * (lane & 3), 1);
    STAMP(4);
    conv_mfma<4>(cs, 4 * pi, lane, acc);
    STAMP(5);
    __builtin_amdgcn_s_setprio(CPRIO);
    if (spare && wv >= 8) prog_wait(sb + W_XCH + 16 + 8 * tm + 4 * (lane & 1), 2);
    const u32 rA = (spare && wv >= 8 ? sb + L_C + (2 + tm) * CS_SZ : sb + L_R + tm * 8192) + lane * 16;
    if (pi) { sfor<4>([&](auto J) { st128(rA + 4096 + J * 1024, __builtin_bit_cast(u32x4, acc[J])); acc[J] = acc[4 + J]; }); }
    else sfor<4>([&](auto J) { st128(rA + J * 1024, __builtin_bit_cast(u32x4, acc[4 + J])); });
    u32x2 uu[4];
#pragma unroll
    for (int j = 0; j < 4; j++) uu[j] = lds64(cs + CS_U + 4088 - 8 * lane - 512 * (4 * pi + j));
    prog_set(sb + W_XCH + 4 * wv, 2);
    prog_wait(sb + W_XCH + 4 * (wv ^ 1), 2);
    f32x4 t[4];
#pragma unroll
    for (int j = 0; j < 4; j++) t[j] = __builtin_bit_cast(f32x4, lds128(rA + (pi ^ 1) * 4096 + j * 1024));
    wait_lgkm();
    prog_set(sb + W_XCH + 4 * wv, 3);
    const u32 yo = ((lane >> 3) & 3) * 73728 + (lane >> 5) * 64 + (lane & 7) * 8;
    char* yd = ws + WS_Y2 + (chb + tm) * 128 + pi * 1179648;
#pragma unroll
    for (int j = 0; j < 4; j++) conv_out(vadd4(acc[j], t[j]), uu[j], tm ? hi2f(dv) : lo2f(dv), yo + j * 294912, yd);
    STAMP(8);
    ypub = F_Y + (((16 * pi + lane) * 8 + (chb + tm) / 72) * 80 + (chb + tm) % 72) * 8;
    if (wv < 8) publish(fb, ypub, tag, lane < 16);
  }
  if (wv < 8) {
    __builtin_amdgcn_s_setprio(1);
    const int c = lane & 31, hh = lane >> 5, q4 = (lane & 15) >> 2;
    const int w = spare ? wv ^ 4 : wv;
    const u32 kcol = 64 * w + (lane & 16) * 2 + 16 * (lane & 1) + 4 * (lane & 2);
    const u32 ka = opaque(sb + L_K + (8 * hh + q4) * 1088 + kcol), ka2 = opaque(sb + L_K + (16 + q4) * 1088 + kcol);
    const u32 va = opaque(sb + L_V + c * 1040 + 64 * w + 16 * hh);
    const u32 qa = opaque(sb + L_Q + q4 * 512 + 64 * w + (lane & 16) * 2 + 8 * (lane & 3)), qa0 = opaque(qa + 4096 * hh);
    const int thr = c - 8 * hh;
    const u32 lom = opaque(hh ? 0u : ~0u);
    u32 msk[8], nmk[8];
    unsigned long long cnd[16];
#pragma unroll
    for (int e = 0; e < 16; e++) cnd[e] = __builtin_amdgcn_ballot_w64(16 * (e >> 3) + (e & 7) <= thr);
#pragma unroll
    for (int j = 0; j < 8; j++) { const int k0 = 16 * (j >> 2) + 2 * (j & 3); msk[j] = opaque(k0 + 1 <= thr ? 0xffffffffu : (k0 <= thr ? 0xffffu : 0u)); nmk[j] = opaque(~msk[j]); }
    f32x16 S0;
#pragma unroll
    for (int e = 0; e < 16; e++) S0[e] = u2f(opaque(spare ? 0xff800000u : 0u));
    const u32 oo = opaque((c >> 4) * 18432 + (c & 15) * 16 + hh * 256);
    const u32 fl = sb + W_PROG + 4 * (spare ? (lane & 3) : (lane & 7));
    if (wv < 3) st128(sb + (wv < 2 ? L_K + (18 + wv) * 1088 : L_V + 18 * 1040) + lane * 16, u32x4{0x3f803f80u, 0x3f803f80u, 0x3f803f80u, 0x3f803f80u});
    if (wv == (spare ? 0u : (u32)POLLW)) {
      await<POLL_FAST>(fb, F_1 + ((h * 8 + qb) * 64 + ((lane < 48 ? lane : lane - 16) | (lane < 16 || spare ? 4 : 0))) * 8, tag);
      prog_set(sb + W_GO, 1);
    } else prog_wait(sb + W_GO, 1);
    STAMP(4);
    __builtin_amdgcn_s_setprio(BAL ? 3 : 1);
    if (!spare || wv < 4) {
    const char* Ag = ws + WS_QKV + (size_t)(4 * qb * 1728 + 18 * h) * 128;
    const char* Kg = Ag + 576 * 128 - (qb ? 884736 : 0);
    const u32 lo = ((lane >> 3) & (qb ? 7 : 3)) * 221184 + (lane & 7) * 16, lq = ((lane >> 3) & 3) * 221184 + (lane >> 5) * 128 + (lane & 7) * 16;
#pragma unroll 1
    for (int s = wv; s <= w; s += 4) {
      dma16c(Ag + s * 256, lq, sb + L_Q + s * 1024);
      dma16c(Kg + s * 128, lo, sb + L_K + s * 1088); dma16c(Kg + s * 128 + 1024, lo, sb + L_K + (8 + s) * 1088);
      const char* eg = s < 4 ? Kg + (s < 2 ? 16 + s : 590 + s) * 128 : Ag + 2048;
      dma16c(eg, s < 4 ? lo : lq, sb + (s < 2 ? L_K + (16 + s) * 1088 : (s < 4 ? L_V + (14 + s) * 1040 : L_Q + 8192)));
    }
    __builtin_amdgcn_s_sleep(VDELAY);
    if (spare) { dma16c(Kg + (572 + w) * 128, lo, sb + L_V + (w - 4) * 1040); dma16c(Kg + (580 + w) * 128, lo, sb + L_V + (4 + w) * 1040); }
    dma16c(Kg + (576 + w) * 128, lo, sb + L_V + w * 1040); dma16c(Kg + (584 + w) * 128, lo, sb + L_V + (8 + w) * 1040);
    prog_set(sb + W_PROG + 4 * wv, 1);
    wait_vm<2>();
    prog_set(sb + W_PROG + 4 * wv, 2);
    }
    prog_wait(fl, 2);
    STAMP(5);
    struct KF { u32x4 a; u32x2 b; };
    struct VF { u32x4 a, b; };
    auto ldk = [&](int i) { KF k; k.a = cat(tr_read(ka + i * 64), tr_read(ka + 4352 + i * 64)); k.b = tr_read(ka2 + i * 64); return k; };
    auto ldv = [&](int i) { VF v; v.a = lds128(va + i * 64); v.b = lds128(va + i * 64 + 32); return v; };
    auto pk8 = [&](const f32x16& p, int o) { return u32x4{pk2(p[o], p[o + 1]), pk2(p[o + 2], p[o + 3]), pk2(p[o + 4], p[o + 5]), pk2(p[o + 6], p[o + 7])}; };
    const int ib = spare ? 8 - w : 1;
    const u32x4 q0 = cat(tr_read(qa0), tr_read(qa0 + 2048));
    const KF k8 = ldk(8);
    u32x2 q1 = tr_read(qa + 8192);
    const KF k0 = ldk(0);
    SCHED();
    q1.x &= lom; q1.y = 0u;
    auto qk = [&](const KF& k) { f32x16 z = {}; z = mfma32(k.a, q0, z); return mfma32k8(k.b, q1, z); };
    f32x16 Sm = {};
    Sm = mfma32(k8.a, q0, Sm); S0 = mfma32(k0.a, q0, S0);
    Sm = mfma32k8(k8.b, q1, Sm); S0 = mfma32k8(k0.b, q1, S0);
    KF kf[2]; VF vf[2], v8, v0;
    kf[1] = ldk(7); kf[0] = ldk(6);
    SCHED();
#pragma unroll
    for (int e = 0; e < 16; e++) Sm[e] = __builtin_amdgcn_inverse_ballot_w64(cnd[e]) ? Sm[e] : S0[e];
    float m = __builtin_fmaxf(__builtin_fmaxf(Sm[0], Sm[1]), Sm[2]), m2 = __builtin_fmaxf(__builtin_fmaxf(Sm[3], Sm[4]), Sm[5]);
#pragma unroll
    for (int e = 6; e < 14; e += 4) { m = fmaxf(fmaxf(m, Sm[e]), Sm[e + 1]); m2 = fmaxf(fmaxf(m2, Sm[e + 2]), Sm[e + 3]); }
    m = fmaxf(fmaxf(m, Sm[14]), Sm[15]);
    m = xhalf_max(fmaxf(m, m2));
    f32x16 O;
    float l;
#pragma unroll 1
    for (int pass = 0; pass < 2; pass++) {
      q1.y = pk2(-m, lo2f(pk2(m, m)) - m) & lom;
      f32x16 S[2];
      {
        f32x16 p = mfma32k8(kf[1].b, u32x2{0u, q1.y}, Sm);
        S[1] = qk(kf[1]);
        SCHED();
        wait_vm<0>();
        prog_set(sb + W_PROG + 4 * wv, 3);
#pragma unroll
        for (int e = 0; e < 8; e++) p[e] = __builtin_amdgcn_exp2f(p[e]);
        SCHED();
        const u32 vfl = lds_peek(fl);
        SCHED();
#pragma unroll
        for (int e = 8; e < 16; e++) p[e] = __builtin_amdgcn_exp2f(p[e]);
        SCHED();
        if (__builtin_amdgcn_ballot_w64(vfl < 3u)) prog_wait(fl, 3);
        asm volatile("" ::: "memory");
        v8 = ldv(8); v0 = ldv(0); vf[1] = ldv(7);
        SCHED();
        const u32x4 pa = pk8(p, 0), pb = pk8(p, 8);
        O = f32x16{};
        O = mfma32(v8.a, pa & u32x4{msk[0], msk[1], msk[2], msk[3]}, O); O = mfma32(v8.b, pb & u32x4{msk[4], msk[5], msk[6], msk[7]}, O);
        O = mfma32(v0.a, pa & u32x4{nmk[0], nmk[1], nmk[2], nmk[3]}, O); O = mfma32(v0.b, pb & u32x4{nmk[4], nmk[5], nmk[6], nmk[7]}, O);
      }
      auto tile = [&](auto self, auto I) -> void {
        constexpr int i = I;
        if (i < ib) return;
        SCHED();
        f32x16& Sc = S[i & 1];
        f32x16 z = {};
        if constexpr (i > 1) z = mfma32(kf[(i - 1) & 1].a, q0, z);
        if constexpr (i > 2) kf[i & 1] = ldk(i - 2);
        if constexpr (i > 1) vf[(i - 1) & 1] = ldv(i - 1);
        SCHED();
#pragma unroll
        for (int e = 0; e < 8; e++) Sc[e] = __builtin_amdgcn_exp2f(Sc[e]);
        SCHED();
        if constexpr (i > 1) S[(i - 1) & 1] = mfma32k8(kf[(i - 1) & 1].b, q1, z);
        const u32x4 pa = pk8(Sc, 0);
        SCHED();
#pragma unroll
        for (int e = 8; e < 12; e++) Sc[e] = __builtin_amdgcn_exp2f(Sc[e]);
        SCHED();
        O = mfma32(vf[i & 1].a, pa, O);
#pragma unroll
        for (int e = 12; e < 16; e++) Sc[e] = __builtin_amdgcn_exp2f(Sc[e]);
        const u32x4 pb = pk8(Sc, 8);
        SCHED();
        O = mfma32(vf[i & 1].b, pb, O);
        if constexpr (i > 1) self(self, IC(i - 1));
      };
      tile(tile, IC(7));
      SCHED();
      { const u32 lu = f2u(O[10]); auto r = __builtin_amdgcn_permlane32_swap(lu, lu, false, false); l = u2f(r[0]); }
      if (pass == 1 || __all(l > 8.67e-19f && l < 1.15e18f)) break;
      q1.y = 0u;
#pragma unroll 1
      for (int i = ib; i < 8; i++) {
        const f32x16 s = qk(ldk(i));
#pragma unroll
        for (int e = 0; e < 16; e += 2) m = fmaxf(fmaxf(m, s[e]), s[e + 1]);
      }
      m = xhalf_max(m);
      kf[1] = ldk(7); kf[0] = ldk(6);
    }
    STAMP(6);
    float inv = __builtin_amdgcn_rcpf(l);
    asm volatile("s_nop 1" : "+v"(inv));
    char* Og = ws + WS_O + (size_t)(16 * qb + 2 * w) * 18432;
    gst128(u32x4{pk2(vmul(O[0], inv), vmul(O[1], inv)), pk2(vmul(O[2], inv), vmul(O[3], inv)), pk2(vmul(O[4], inv), vmul(O[5], inv)), pk2(vmul(O[6], inv), vmul(O[7], inv))},
           oo, Og + (h >> 1) * 1024 + (h & 1) * 512);
    if (hh == 0) gst32(pk2(vmul(O[8], inv), vmul(O[9], inv)), (c >> 4) * 18432 + (c & 15) * 4, Og + 16384 + h * 64);
    publish(fb, F_O + (((4 * qb + (w & 3)) * 8 + lane) * 64 + (w >> 2) * 32 + h) * 8, tag, lane < 8);
    {
      __builtin_amdgcn_s_setprio(0);
      const int g = lane >> 4;
      prog_wait(sb + W_XCH + 32 + 4 * (lane & 3), 4);
      const u32 wa2 = sb + L_W + ((4 * g + (q4 >> 1)) * 18 + 16 + (q4 & 1)) * 160 + 8 * (lane & 3);
      u32x4 wx[2], wr[10];
      sfor<2>([&](auto J) { const int i = J ? 8 + (wv & 1) : wv; const u32 a = wa2 + (i / 5) * 46080 + (i % 5) * 32; wx[J] = cat(tr_read(a), tr_read(a + 5760)); });
      prog_set(sb + W_PROG + 4 * wv, 4);
      const u32 wa = sb + L_W + wv * 11520 + (18 * (g >> 1) + 4 * (g & 1) + q4) * 160 + 8 * (lane & 3), wd = sb + L_W + wv * 11520 + lane * 16;
      sfor<10>([&](auto I) { const u32 a = wa + (I / 5) * 5760 + (I % 5) * 32; wr[I] = cat(tr_read(a), tr_read(a + 1280)); });
      prog_wait(sb + W_PROG + 4 * (lane & 7), 4);
      sfor<2>([&](auto J) { const int i = J ? 8 + (wv & 1) : wv; st128(sb + L_Q + i * 1024 + lane * 16, wx[J]); });
      sfor<10>([&](auto I) { st128(wd + (I / 5) * 5760 + (I % 5) * 1024, wr[I]); });
      prog_set(sb + W_PROG + 4 * wv, 5);
    }
      if (b == 255 && wv == 4) {
#pragma unroll 1
        for (int spin = 0; spin < (1 << 18); spin++) {
          u32x4 v[9];
          sfor<9>([&](auto I) { gl128c<0>(v[I], (lane >> 4) * 2048 + (lane & 15) * 16, fb + F_U + I * 8192); });
          asm volatile("s_waitcnt vmcnt(0)" : "+v"(v[0]), "+v"(v[1]), "+v"(v[2]), "+v"(v[3]), "+v"(v[4]), "+v"(v[5]), "+v"(v[6]), "+v"(v[7]), "+v"(v[8])::"memory");
          u32 bad = 0;
#pragma unroll
          for (int i = 0; i < 9; i++) bad |= (v[i].x ^ tag.x) | (v[i].y ^ tag.y) | (v[i].z ^ tag.x) | (v[i].w ^ tag.y);
          if (!__builtin_amdgcn_ballot_w64(bad != 0)) break;
          __builtin_amdgcn_s_sleep(16);
        }
        if (lane == 0) gst64(tag, F_EP, fb);
      }
    STAMP_END();
    return;
  }
  {
    const int g = lane >> 4, l15 = lane & 15, q = l15 >> 2;
    const int r7 = lane / 9, c9 = lane - 9 * r7;
    const u32 xw = sb + L_X + cw * 2304;
    const int m16 = 16 * (rt >> 2) + 2 * (rt & 3) + 8 * (cw >> 1) + (cw & 1);
    if (__builtin_amdgcn_ballot_w64(lds_peek(sb + W_PROG + 4 * (spare ? (lane & 3) : (lane & 7))) < (u32)WOGATE)) { publish(fb, ypub, tag, lane < 16); ypub = 0; }
    prog_wait(sb + W_XCH + (spare ? 16 + 4 * (lane & 7) : 32 + 4 * (lane & 3)), 3);
    prog_wait(sb + W_PROG + 4 * (spare ? (lane & 3) : (lane & 7)), WOGATE);
    STAMP(9);
    {
      const u16* xp = x + (size_t)(16 * m16) * 576 + 72 * ct;
      dma16n<63>(xp, r7 * 1152 + 16 * c9, xw); dma16n<63>(xp + 7 * 576, r7 * 1152 + 16 * c9, xw + 1008); dma16n<63>(xp + 9 * 576, r7 * 1152 + 16 * c9, xw + 1296);
      wo_load(0, 4);
      wait_vm<7>();
      if (ypub && lane < 16) gst64(tag, ypub, fb);
      wo_load(4, YPOS);
    }
    const u32 yf = F_Y + ((rt * 8 + ct) * 80 + (lane < 18 ? 18 * cw + lane : 72 + (cw >> 1))) * 8;
    u32x2 yv;
    asm volatile("global_load_dwordx2 %0, %1, %2 sc1" : "=v"(yv) : "v"(yf), "s"(fb));
    wo_load(YPOS, 24);
    STAMP(10);
    asm volatile("s_waitcnt vmcnt(%1)" : "+v"(yv) : "i"(24 - YPOS) : "memory");
    if (__builtin_amdgcn_ballot_w64(yv.x != tag.x || yv.y != tag.y)) await<POLL_SLEEP>(fb, yf, tag);
    STAMP(11);
    {
      const char* yp = ws + WS_Y2 + (size_t)(rt * 576 + 72 * ct + 18 * cw) * 128;
      dma16c(yp, lane * 16, sb + L_Y + cw * 2304); dma16c(yp + 1024, lane * 16, sb + L_Y + cw * 2304 + 1024); dma16c(yp + 1280, lane * 16, sb + L_Y + cw * 2304 + 1280);
    }
    u32 gtu;
    gl32c<0>(gtu, (16 * m16 + l15) * 4, ws + WS_G);
    asm volatile("s_waitcnt vmcnt(0)" : "+v"(gtu)::"memory");
    prog_set(sb + W_XCH + 4 * wv, 4);
    prog_wait(sb + W_XCH + 32 + 4 * (lane & 3), 4);
    const float gt = u2f(gtu);
    f32x4 acc[5], tb[5];
    const u32 xa = xw + l15 * 144 + 8 * g;
    {
      const float g1 = rbf(1.f - gt);
      const u32 ya = sb + L_Y + (4 * g + q) * 128 + 32 * cw + 8 * (lane & 3);
      sfor<5>([&](auto NT) {
        const u32x2 y = tr_read(ya + NT * 2048), xv = lds64(xa + NT * 32);
        tb[NT] = f32x4{vmul(vmul(g1, lo2f(xv.x)), lo2f(y.x)), vmul(vmul(g1, hi2f(xv.x)), hi2f(y.x)), vmul(vmul(g1, lo2f(xv.y)), lo2f(y.y)), vmul(vmul(g1, hi2f(xv.y)), hi2f(y.y))};
      });
    }
    const u32 wl = opaque(sb + L_W + lane * 16), wh = opaque(wl + 46080), wq = opaque(sb + L_Q + lane * 16);
    u32x4 wf[2][5];
    auto rd = [&](auto KS) {
      sfor<5>([&](auto NT) {
        constexpr int ks = KS;
        if constexpr (ks < 16) wf[ks & 1][NT] = lds128((ks < 8 ? wl : wh) + (ks & 7) * 5760 + NT * 1024);
        else wf[ks & 1][NT] = lds128(wq + (5 * (ks - 16) + NT) * 1024);
      });
    };
    STAMP(6);
    u32x4 of[16]; u32 oe[8];
    const char* ob = ws + WS_O + (size_t)m16 * 18432;
    {
      const u32 oo = F_O + ((rt * 8 + ct) * 64 + (cw >> 1) * 32 + (lane & 31)) * 8;
      u32 miss = ~0u;
      sfor<4>([&](auto G) {
#pragma unroll 1
        for (int spin = 0; (miss & (0xffu << (8 * G))) && spin < (1 << 20); spin++) {
          u32x2 v;
          asm volatile("global_load_dwordx2 %0, %1, %2 sc1\n\ts_waitcnt vmcnt(0)" : "=&v"(v) : "v"(oo), "s"(fb) : "memory");
          miss = (u32)__builtin_amdgcn_ballot_w64(v.x != tag.x || v.y != tag.y);
          if (miss & (0xffu << (8 * G))) __builtin_amdgcn_s_sleep(POLL_FAST);
        }
        sfor<4>([&](auto F) { gl128c<F * 1024>(of[4 * G + F], lane * 16, ob + G * 4096); });
      });
    }
    STAMP(12);
    __builtin_amdgcn_s_setprio(3);
    sfor<8>([&](auto F) { gl32c<(F >> 2) * 1024 + (F & 3) * 64>(oe[F], 256 * g + 4 * l15, ob + 16384); });
    prog_wait(sb + W_PROG + 4 * (lane & 7), 5);
    rd(IC(0));
    sfor<16>([&](auto KS) {
      constexpr int ks = KS;
      SCHED();
      if constexpr (ks % 2 == 0) asm volatile("s_waitcnt vmcnt(%2)" : "+v"(of[ks]), "+v"(of[ks + 1]) : "i"(22 - ks) : "memory");
      rd(IC(ks + 1));
      sfor<5>([&](auto NT) { acc[NT] = mfma16(wf[ks & 1][NT], of[ks], ks == 0 ? f32x4{0.f, 0.f, 0.f, 0.f} : acc[NT]); });
      sfor<5>([&](auto) { __builtin_amdgcn_sched_group_barrier(0x8, 1, 0); __builtin_amdgcn_sched_group_barrier(0x100, 1, 0); });
    });
    SCHED();
    asm volatile("s_waitcnt vmcnt(0)" : "+v"(oe[0]), "+v"(oe[1]), "+v"(oe[2]), "+v"(oe[3]), "+v"(oe[4]), "+v"(oe[5]), "+v"(oe[6]), "+v"(oe[7])::"memory");
    rd(IC(17));
    sfor<5>([&](auto NT) { acc[NT] = mfma16(wf[0][NT], u32x4{oe[0], oe[1], oe[2], oe[3]}, acc[NT]); });
    sfor<5>([&](auto NT) { acc[NT] = mfma16(wf[1][NT], u32x4{oe[4], oe[5], oe[6], oe[7]}, acc[NT]); });
    SCHED();
    STAMP(13);
    sfor<5>([&](auto NT) {
      constexpr int nt = NT;
      if (nt < 4 || g < 2)
        st64(xa + nt * 32, u32x2{pk2(fmaf(gt, acc[nt][0], tb[nt][0]), fmaf(gt, acc[nt][1], tb[nt][1])), pk2(fmaf(gt, acc[nt][2], tb[nt][2]), fmaf(gt, acc[nt][3], tb[nt][3]))});
    });
    char* orow = (char*)(out + (size_t)(16 * m16) * 576 + 72 * ct);
    u32x4 t[3];
#pragma unroll
    for (int it = 0; it < 3; it++) t[it] = lds128(xw + it * 1008 + lane * 16);
#pragma unroll
    for (int it = 0; it < 3; it++)
      if (lane < (it < 2 ? 63 : 18)) asm volatile("global_store_dwordx4 %0, %1, %2 " OUTPOL "\n\ts_nop 1" ::"v"((7 * it + r7) * 1152 + 16 * c9), "v"(t[it]), "s"(orow) : "memory");
  }
  STAMP_END();
}
extern "C" __global__ void gm_end() {}

extern "C" void kernel_launch(void* const* d_in, const int* in_sizes, int n_in, void* d_out, int out_size, void* d_ws, size_t ws_size,
                              hipStream_t stream) {
  (void)hipFuncSetAttribute((const void*)gm_main, hipFuncAttributeMaxDynamicSharedMemorySize, L_END);
  gm_main<<<256, 768, L_END, stream>>>((const u16*)d_in[0], (const u16*)d_in[1], (char*)d_ws, (const u16*)d_in[4], (const u16*)d_in[5], (const u16*)d_in[6],
                                       (const u16*)d_in[7], (const u16*)d_in[2], (const u16*)d_in[8], (const u16*)d_in[9], (u16*)d_out);
}
```

```cpp
#include <hip/hip_runtime.h>
#include <hip/hip_bf16.h>
#include <utility>

#define STAMP_RD(v) asm volatile("s_memrealtime %0\n\ts_waitcnt lgkmcnt(0)" : "=s"(v)::"memory")
#define STAMP(i) do { if ((i) == 0) STAMP_RD(t0_); else if ((i) == 2) STAMP_RD(t1_); } while (0)
#define STAMP_END() do { unsigned long long te_; STAMP_RD(te_); if (2 == 15) t1_ = te_; if (blockIdx.x < 224 && (threadIdx.x >> 6) == 4) { const unsigned long long d_ = t1_ - t0_, e_ = t0_ + 3000ull + 10ull * (d_ > 5000ull ? 5000ull : d_); \
   while (__builtin_amdgcn_s_memrealtime() < e_) __builtin_amdgcn_s_sleep(2); } } while (0)

typedef unsigned short u16;
typedef unsigned int u32;
typedef u32 u32x2 __attribute__((ext_vector_type(2)));
typedef u32 u32x4 __attribute__((ext_vector_type(4)));
typedef short s16x4 __attribute__((ext_vector_type(4)));
typedef __bf16 bf16x8 __attribute__((ext_vector_type(8)));
typedef __bf16 bf16x2 __attribute__((ext_vector_type(2)));
typedef float f32x2 __attribute__((ext_vector_type(2)));
typedef float f32x4 __attribute__((ext_vector_type(4)));
typedef float f32x16 __attribute__((ext_vector_type(16)));
#define LAS __attribute__((address_space(3)))
#define GAS __attribute__((address_space(1)))
#define DEVI __device__ __forceinline__
#define IC(n) std::integral_constant<int, (n)>{}

constexpr size_t WS_QKV = 0;
constexpr size_t WS_O   = WS_QKV + 96ull * 2048 * 40;
constexpr size_t WS_UR  = WS_O + 32ull * 2048 * 40;
constexpr size_t WS_Y2  = WS_UR + 576ull * 4096;
constexpr size_t WS_G   = WS_Y2 + 576ull * 4096;
constexpr size_t WS_F   = WS_G + 8192;
constexpr u32 F_EP = 0, F_1 = 1024, F_U = F_1 + 256 * 512, F_O = F_U + 288 * 256, F_Y = F_O + 256 * 512;

template <typename F, int... I> DEVI void sfor_impl(F&& f, std::integer_sequence<int, I...>) { (f(std::integral_constant<int, I>{}), ...); }
template <int N, typename F> DEVI void sfor(F&& f) { sfor_impl(f, std::make_integer_sequence<int, N>{}); }

DEVI u32 pk2(float a, float b) { f32x2 v = {a, b}; return __builtin_bit_cast(u32, __builtin_convertvector(v, bf16x2)); }
DEVI u32 f2u(float a) { return __builtin_bit_cast(u32, a); }
DEVI float u2f(u32 a) { return __builtin_bit_cast(float, a); }
DEVI float lo2f(u32 w) { return __builtin_bit_cast(float, w << 16); }
DEVI float hi2f(u32 w) { return __builtin_bit_cast(float, w & 0xffff0000u); }
DEVI float rbf(float a) { return lo2f(pk2(a, a)); }
DEVI f32x4 mfma16(u32x4 a, u32x4 b, f32x4 c) { return __builtin_amdgcn_mfma_f32_16x16x32_bf16(__builtin_bit_cast(bf16x8, a), __builtin_bit_cast(bf16x8, b), c, 0, 0, 0); }
DEVI f32x16 mfma32(u32x4 a, u32x4 b, f32x16 c) { return __builtin_amdgcn_mfma_f32_32x32x16_bf16(__builtin_bit_cast(bf16x8, a), __builtin_bit_cast(bf16x8, b), c, 0, 0, 0); }
DEVI f32x16 mfma32k8(u32x2 a, u32x2 b, f32x16 c) { return __builtin_amdgcn_mfma_f32_32x32x8bf16_1k(__builtin_bit_cast(s16x4, a), __builtin_bit_cast(s16x4, b), c, 0, 0, 0); }
DEVI u32x2 tr_read(u32 addr) { return __builtin_bit_cast(u32x2, __builtin_amdgcn_ds_read_tr16_b64_v4i16((LAS s16x4*)addr)); }
DEVI u32x4 lds128(u32 addr) { return *(LAS u32x4*)addr; }
DEVI u32x2 lds64(u32 addr) { return *(LAS u32x2*)addr; }
DEVI u32 lds32(u32 addr) { return *(LAS u32*)addr; }
DEVI void st128(u32 addr, u32x4 v) { *(LAS u32x4*)addr = v; }
DEVI void st64(u32 addr, u32x2 v) { *(LAS u32x2*)addr = v; }
DEVI void st32(u32 addr, u32 v) { *(LAS u32*)addr = v; }
DEVI u32x4 cat(u32x2 a, u32x2 b) { return u32x4{a.x, a.y, b.x, b.y}; }
DEVI float vfma(float a, float b, float c) { float d; asm("v_fma_f32 %0, %1, %2, %3" : "=v"(d) : "v"(a), "v"(b), "v"(c)); return d; }
DEVI float vmul(float a, float b) { float d; asm("v_mul_f32 %0, %1, %2" : "=v"(d) : "v"(a), "v"(b)); return d; }
DEVI float vadd(float a, float b) { float d; asm("v_add_f32 %0, %1, %2" : "=v"(d) : "v"(a), "v"(b)); return d; }
DEVI f32x4 vadd4(f32x4 a, f32x4 b) { return f32x4{vadd(a[0], b[0]), vadd(a[1], b[1]), vadd(a[2], b[2]), vadd(a[3], b[3])}; }
DEVI u32 opaque(u32 v) { asm volatile("" : "+v"(v)); return v; }
DEVI void dma16(const void* sbase, u32 voff, u32 ldsbase) {
  asm volatile("s_mov_b32 m0, %2\n\ts_nop 0\n\tglobal_load_lds_dwordx4 %0, %1" ::"v"(voff), "s"(sbase), "s"(ldsbase) : "memory");
}
template <int NL> DEVI void dma16n(const void* sbase, u32 voff, u32 ldsbase) {
  asm volatile("s_mov_b32 m0, %2\n\ts_mov_b32 exec_hi, %3\n\tglobal_load_lds_dwordx4 %0, %1\n\ts_mov_b32 exec_hi, -1" ::"v"(voff), "s"(sbase), "s"(ldsbase),
               "i"((1u << (NL - 32)) - 1u) : "memory");
}
template <int OFF> DEVI void gl128(u32x4& d, u32 voff, const void* sbase) { asm volatile("global_load_dwordx4 %0, %1, %2 offset:%3" : "=v"(d) : "v"(voff), "s"(sbase), "i"(OFF)); }
template <int OFF> DEVI void gl64(u32x2& d, u32 voff, const void* sbase) { asm volatile("global_load_dwordx2 %0, %1, %2 offset:%3" : "=v"(d) : "v"(voff), "s"(sbase), "i"(OFF)); }
DEVI void dma16c(const void* sbase, u32 voff, u32 ldsbase) {
  asm volatile("s_mov_b32 m0, %2\n\ts_nop 0\n\tglobal_load_lds_dwordx4 %0, %1 sc1" ::"v"(voff), "s"(sbase), "s"(ldsbase) : "memory");
}
template <int OFF> DEVI void gl128c(u32x4& d, u32 voff, const void* sbase) { asm volatile("global_load_dwordx4 %0, %1, %2 offset:%3 sc1" : "=v"(d) : "v"(voff), "s"(sbase), "i"(OFF)); }
template <int OFF> DEVI void gl32c(u32& d, u32 voff, const void* sbase) { asm volatile("global_load_dword %0, %1, %2 offset:%3 sc1" : "=v"(d) : "v"(voff), "s"(sbase), "i"(OFF)); }
DEVI void gst128(u32x4 v, u32 voff, void* sbase) { asm volatile("global_store_dwordx4 %0, %1, %2 sc1\n\ts_nop 1" ::"v"(voff), "v"(v), "s"(sbase) : "memory"); }
DEVI void gst64(u32x2 v, u32 voff, void* sbase) { asm volatile("global_store_dwordx2 %0, %1, %2 sc1" ::"v"(voff), "v"(v), "s"(sbase) : "memory"); }
DEVI void gst32(u32 v, u32 voff, void* sbase) { asm volatile("global_store_dword %0, %1, %2 sc1" ::"v"(voff), "v"(v), "s"(sbase) : "memory"); }
#define CODE_PF(from, to, ck, step, lds)                                                                                              \
  do {                                                                                                                                \
    u32 lo_, hi_, len_;                                                                                                               \
    asm volatile("s_getpc_b64 s[20:21]\n\ts_add_u32 %0, s20, " from "@rel32@lo+4\n\ts_addc_u32 %1, s21, " from "@rel32@hi+12\n\ts_mov_b32 %2, " to "-" from \
                 : "=s"(lo_), "=s"(hi_), "=s"(len_)::"s20", "s21", "scc");                                                            \
    const char* code_ = (const char*)(((unsigned long long)hi_ << 32) | lo_);                                                         \
    for (u32 o_ = (ck) * 1024; o_ < len_; o_ += (step) * 1024) { const u32 a_ = o_ + (threadIdx.x & 63) * 16; dma16(code_, a_ < len_ ? a_ : 0u, lds); } \
  } while (0)
template <int N> DEVI void wait_vm() { asm volatile("s_waitcnt vmcnt(%0)" ::"i"(N > 63 ? 63 : N) : "memory"); }
DEVI void wait_lgkm() { asm volatile("s_waitcnt lgkmcnt(0)" ::: "memory"); }
DEVI void bar() { asm volatile("s_barrier" ::: "memory"); }
DEVI u32 rfl(u32 v) { return __builtin_amdgcn_readfirstlane(v); }
#define SCHED() __builtin_amdgcn_sched_barrier(0)

constexpr float QSCALE = 0.23570226039551584f * 1.4426950408889634f;

#define POLL_SLEEP 8
#define POLL_FAST 4
#define SPDELAY 24
#define LDPOL "sc1"
#define FSPOL "sc1"
#define CVDELAY 0
#define PUBB 2
#define NMID 1
#define FLB 1
#define PUBD 0
#define PREPFIRST 2
#define OUTPOL "sc1"
#define BAL 0
#define CPRIO 2
#define WOGATE 3
#define YPOS 16
#define CMPRIO 2
#define POLLW 4
#define VDELAY 3

constexpr int L_SLOT = 32768, L_WOFF = 16384, L_STG = 94208, L_GW = 131072, L_PT = 133120, L_TILE = 17408;
constexpr int L_K = 0, L_V = 21760, L_C = 41536, L_R = L_PT + 1024, L_FL = 152320, L_DUMMY = L_FL + 256, L_Q = 153600, L_END = 163840;
constexpr int KC_STR = 4640, CS_U = 4 * KC_STR, CS_SZ = CS_U + 4096 + 128;
constexpr int L_W = L_C, L_X = L_W + 576 * 160, L_Y = L_X + 9216;
static_assert(L_Y + 9216 <= L_FL && L_C + 2 * CS_SZ <= L_STG && L_C + 4 * CS_SZ <= L_PT && L_PT + L_TILE <= L_FL, "lds map");
constexpr int W_PROG = L_FL, W_XCH = L_FL + 64, W_GO = L_FL + 128, W_TAG = L_FL + 144;

DEVI u32 lds_peek(u32 a) { return __hip_atomic_load((LAS u32*)a, __ATOMIC_RELAXED, __HIP_MEMORY_SCOPE_WORKGROUP); }
DEVI void prog_set(u32 a, u32 n) { asm volatile("" ::: "memory"); __hip_atomic_store((LAS u32*)a, n, __ATOMIC_RELAXED, __HIP_MEMORY_SCOPE_WORKGROUP); }
DEVI void prog_wait(u32 a, u32 n) { while (__builtin_amdgcn_ballot_w64(lds_peek(a) < n)) __builtin_amdgcn_s_sleep(1); asm volatile("" ::: "memory"); }
DEVI void publish(char* fb, u32 off, u32x2 tag, bool on) { wait_vm<0>(); if (on) asm volatile("global_store_dwordx2 %0, %1, %2 " FSPOL ::"v"(off), "v"(tag), "s"(fb) : "memory"); }
template <int SLP> DEVI void await(const char* fb, u32 off, u32x2 tag) {
  for (int spin = 0; spin < (1 << 20); spin++) {
    u32x2 v;
    asm volatile("global_load_dwordx2 %0, %1, %2 " LDPOL "\n\ts_waitcnt vmcnt(0)" : "=&v"(v) : "v"(off), "s"(fb) : "memory");
    if (!__builtin_amdgcn_ballot_w64(v.x != tag.x || v.y != tag.y)) break;
    __builtin_amdgcn_s_sleep(SLP);
  }
}

DEVI void prep_issue(const u16* Bk, const u16* Bv, int row0, int col, u32 tb, u32 pw, int lane) {
  const int r8 = lane >> 3, c8 = lane & 7;
  sfor<2>([&](auto I) {
    const int j = 2 * pw + I;
    int row = row0 + 8 * j + r8 - 3; row = row < 0 ? 0 : row;
    dma16(Bk + col, row * 1152 + 16 * (c8 ^ j), tb + 1024 * j);
    dma16(Bv + col, (row0 + 8 * j + r8) * 1152 + 16 * (c8 ^ j), tb + 9216 + 1024 * j);
  });
  dma16(Bk + col, (row0 + 61 + (r8 > 2 ? 2 : r8)) * 1152 + 16 * c8, tb + 8192);
}
template <typename F> DEVI void prep_compute(int row0, int col, u32 tb, u32 pw, int lane, u32x4 sh, char* ws, F&& mid) {
  const int c8 = lane & 7, cp = 8 * pw + (lane >> 3);
  const u32 o0 = tb + 1024 * c8 + 16 * ((cp >> 2) ^ c8) + 4 * (cp & 3), o1 = tb + 1024 * c8 + 16 * ((cp >> 2) ^ ((c8 + 1) & 7)) + 4 * (cp & 3);
  row0 += 8 * c8;
  u32 kk[11], vv[8];
#pragma unroll
  for (int i = 0; i < 11; i++) kk[i] = lds32((i < 8 ? o0 : o1) + 128 * i);
#pragma unroll
  for (int i = 0; i < 8; i++) vv[i] = lds32(o0 + 9216 + 128 * i);
  if (row0 == 0) { kk[0] = 0; kk[1] = 0; kk[2] = 0; }
  const u32 go = 2 * cp * 4096 + 2 * (2040 - row0);
  char* dst = ws + WS_UR + (size_t)col * 4096;
  float r[8];
  {
    const float a0 = lo2f(sh.x), a1 = hi2f(sh.x), a2 = lo2f(sh.y), a3 = hi2f(sh.y);
#pragma unroll
    for (int i = 0; i < 8; i++) r[i] = vmul(vfma(lo2f(kk[i + 3]), a0, vfma(lo2f(kk[i + 2]), a1, vfma(lo2f(kk[i + 1]), a2, vmul(lo2f(kk[i]), a3)))), lo2f(vv[i]));
    gst128(u32x4{pk2(r[7], r[6]), pk2(r[5], r[4]), pk2(r[3], r[2]), pk2(r[1], r[0])}, go, dst);
  }
  mid();
  {
    const float a0 = lo2f(sh.z), a1 = hi2f(sh.z), a2 = lo2f(sh.w), a3 = hi2f(sh.w);
#pragma unroll
    for (int i = 0; i < 8; i++) r[i] = vmul(vfma(hi2f(kk[i + 3]), a0, vfma(hi2f(kk[i + 2]), a1, vfma(hi2f(kk[i + 1]), a2, vmul(hi2f(kk[i]), a3)))), hi2f(vv[i]));
    gst128(u32x4{pk2(r[7], r[6]), pk2(r[5], r[4]), pk2(r[3], r[2]), pk2(r[1], r[0])}, go, dst + 4096);
  }
}

template <int NR> DEVI void conv_mfma(u32 cs, int rho0, int lane, f32x4 (&acc)[8]) {
  const int g = lane >> 4, l15 = lane & 15;
  u32 aa = cs + (l15 & 3) * KC_STR + 2 * (64 * (l15 >> 2) + 8 * g) + 64 * rho0;
  u32 ba = opaque(cs + CS_U + 2 * (1852 - 1792 - 4 * l15 + 8 * g) + 64 * rho0), bb = opaque(ba + 8);
  u32x4 A[8], G[8];
  sfor<5>([&](auto J) { constexpr int p = J < 4 ? J + 2 : 1; A[p] = lds128(aa + p * 512); G[p] = cat(lds64(ba + (7 - p) * 512), lds64(bb + (7 - p) * 512)); SCHED(); });
#pragma unroll 1
  for (int rho = 0; rho < NR; rho++) {
    constexpr int pa[36] = {2, 2, 3, 3, 2, 4, 3, 4, 2, 5, 1, 1, 2, 1, 3, 1, 4, 1, 5, 1, 6, 2, 0, 3, 0, 4, 0, 5, 0, 1, 0, 6, 0, 7, 0, 0};
    constexpr int pe[36] = {2, 3, 2, 3, 4, 2, 4, 3, 5, 2, 1, 2, 1, 3, 1, 4, 1, 5, 1, 6, 1, 0, 2, 0, 3, 0, 4, 0, 5, 0, 1, 0, 6, 0, 7, 0};
    constexpr int rdc[36] = {18, 19, 20, 21, 22, 23, 0, 1, 2, -1, -1, -1, -1, -1, -1, -1, -1, -1, -1, -1, -1, 6, 7, 8, 9, 10, 11, 12, 13, 14, 15, 16, 17, 3, 4, 5};
    sfor<36>([&](auto I) {
      constexpr int i = I, r = rdc[i], x = r / 3;
      if constexpr (i == 35) __builtin_amdgcn_s_setprio(CMPRIO);
      if constexpr (i == 9) { aa = opaque(aa + 64); ba = opaque(ba + 64); bb = opaque(bb + 64); }
      acc[pa[i] + pe[i]] = mfma16(A[pa[i]], G[pe[i]], acc[pa[i] + pe[i]]);
      if constexpr (r >= 0) {
        if constexpr (r % 3 == 0) A[x] = lds128(aa + x * 512);
        else if constexpr (r % 3 == 1) { const u32x2 t = lds64(ba + (7 - x) * 512); G[x].x = t.x; G[x].y = t.y; }
        else { const u32x2 t = lds64(bb + (7 - x) * 512); G[x].z = t.x; G[x].w = t.y; }
      }
      SCHED();
    });
  }
  SCHED();
}
DEVI void conv_out(f32x4 y, u32x2 u, float Dv, u32 off, char* ydst) {
  const float y0 = vfma(hi2f(u.y), Dv, y[0]), y1 = vfma(lo2f(u.y), Dv, y[1]), y2 = vfma(hi2f(u.x), Dv, y[2]), y3 = vfma(lo2f(u.x), Dv, y[3]);
  gst64(u32x2{pk2(y0, y1), pk2(y2, y3)}, off, ydst);
}

DEVI float xhalf_max(float m) { const u32 mu = f2u(m); auto r = __builtin_amdgcn_permlane32_swap(mu, mu, false, false); return fmaxf(u2f(r[0]), u2f(r[1])); }

extern "C" __global__ __launch_bounds__(768, 3) void gm_main(const u16* __restrict__ x, const u16* __restrict__ wqkv, char* __restrict__ ws,
                                                             const u16* __restrict__ Bk, const u16* __restrict__ Bv, const u16* __restrict__ shk,
                                                             const u16* __restrict__ ssmk, const u16* __restrict__ wo, const u16* __restrict__ ssmD,
                                                             const u16* __restrict__ gw, u16* __restrict__ out) {
  extern __shared__ __attribute__((aligned(16))) char smem[];
  const u32 sb = (u32)(size_t)(LAS char*)smem;
  const int b = blockIdx.x, xcd = b & 7, idx = b >> 3;
  const int lane = threadIdx.x & 63;
  const u32 wv = rfl(threadIdx.x >> 6);
  const bool spare = idx >= 28;
  char* const fb = ws + WS_F;
  const int qb = ((idx >> 2) + 1) & 7, h = 4 * xcd + (idx & 3);
  const int chb = wv >= 8 ? 64 * xcd + 2 * idx : 512 + 2 * h;
  u32x2 pre[2], fv; u32x4 cur[2];
  const u32 fo = F_U + ((chb >> 1) * 32 + 24 - 8 * (wv & 3) + (lane & 7)) * 8;
  unsigned long long t0_ = 0, t1_ = 0;
  STAMP(0);
  {
    const int hg = xcd & 1, ri = idx & 3, rb = 4 * (xcd >> 1) + ri, jt = idx >> 2;
    const int r8 = lane >> 3, c8 = lane & 7;
    const int rot = ((0x124924u >> (2 * (jt + ri))) & 3) * 3;
    const u32 xo = opaque((32 * wv + r8) * 1152 + 16 * (c8 ^ r8));
    asm volatile("" ::"s"(x), "s"(wqkv), "s"(Bk), "s"(Bv));
    if (wv < 4 && !spare) {
      const u32 lw = wv;
      const char* xb = (const char*)(x + (size_t)rb * 128 * 576);
      sfor<4>([&](auto I) { dma16(xb + rot * 128 + I * 9216, xo, sb + (4 * lw + I) * 1024); });
      u32 wof[2];
#pragma unroll
      for (int i = 0; i < 2; i++) {
        const int ch = (lane & 15) ^ (((lane >> 4) | (i << 2)) << 1), ln = 14 * hg + 2 * jt + (ch >> 3);
        wof[i] = ((lane >> 4) + 16 * lw) * 3456 + 128 * (ln > 26 ? 26 : ln) + 16 * (ch & 7);
      }
      const char* wb = (const char*)wqkv;
      auto issue = [&](int c) {
        const u32 sl = sb + (c & 3) * L_SLOT;
        c += rot; c = c >= 9 ? c - 9 : c;
        sfor<4>([&](auto I) { dma16(xb + c * 128 + I * 9216, xo, sl + (4 * lw + I) * 1024); });
        sfor<4>([&](auto I) { dma16(wb + (64 * c + 4 * I) * 3456, wof[I >> 1], sl + L_WOFF + (4 * lw + I) * 1024); });
      };
      sfor<4>([&](auto I) { dma16(wb + (64 * rot + 4 * I) * 3456, wof[I >> 1], sb + L_WOFF + (4 * lw + I) * 1024); });
      prep_issue(Bk, Bv, 64 * idx, 64 * xcd, sb + L_PT, lw, lane);
#pragma unroll 1
      for (int n = 1; n < 8; n++) { issue(n); wait_vm<8>(); bar(); }
      issue(8);
      wait_vm<8>(); bar();
      __builtin_amdgcn_s_sleep(2);
      {
        const char* wp = (const char*)wo + (idx == 27 ? 663552 - 24576 : idx * 23712) + lw * 6144;
        sfor<6>([&](auto I) { dma16(wp + I * 1024, lane * 16, sb + L_DUMMY); });
      }
      wait_vm<6>(); bar();
      bar();
    } else if (wv >= 8 || (spare && wv >= 4)) {
      const bool ex = wv < 8;
      const int row0 = ex ? 256 * xcd + 64 * ri : 64 * idx, col = ex ? 512 : 64 * xcd;
      const u32 tb = sb + (ex ? 0 : L_PT);
      u32x2 e;
      asm volatile("global_load_dwordx2 %0, %1, %2 sc1" : "=v"(e) : "v"(F_EP), "s"(fb));
      u32x4 sh;
      gl128<0>(sh, 8 * col + 16 * (8 * (wv & 3) + r8), shk);
      if (spare) prep_issue(Bk, Bv, row0, col, tb, wv & 3, lane);
      {
        const u32 o = (threadIdx.x & 255) * 16, op = o ? o - 8 : 0;
        sfor<2>([&](auto J) { const u16* kp = ssmk + (size_t)(chb + J) * 2048; gl128<0>(cur[J], o, kp); gl64<0>(pre[J], op, kp); });
      }
      {
        const int t3 = (4 * xcd + (idx & 3)) * 8 + (idx >> 2), k = wv & 3;
        const u32 mo = k == 0 ? F_1 + (h * 8 + qb) * 512 : k == 1 ? F_O + t3 * 512 : k == 2 ? F_Y + t3 * 640 : F_U + (chb >> 1) * 256;
        asm volatile("s_mov_b32 m0, %2\n\ts_nop 0\n\tglobal_load_lds_dword %0, %1 sc1" ::"v"(mo + (lane < (k == 3 ? 4 : k == 2 ? 10 : 8) ? lane : 0) * 64), "s"(fb), "s"(sb + L_DUMMY) : "memory");
      }
      if (wv == 8) st32(sb + L_FL + 4 * lane, 0u);
      if (spare) wait_vm<5>();
      bar();
      asm volatile("s_waitcnt vmcnt(5)" : "+v"(e), "+v"(sh)::"memory");
      e.x += 1u; e.y += e.x == 0u;
      if (wv == 8) st64(sb + W_TAG, e);
      prep_compute(row0, col, tb, wv & 3, lane, sh, ws, [&]() { if (!spare) { SCHED(); bar(); SCHED(); } });
      wait_lgkm();
      bar();
      if (!spare) sfor<PUBB>([&](auto) { bar(); });
      asm volatile("s_waitcnt vmcnt(0)" : "+v"(cur[0]), "+v"(cur[1]), "+v"(pre[0]), "+v"(pre[1])::"memory");
      __builtin_amdgcn_s_sleep(PUBD);
      publish(fb, F_U + (((col >> 1) + 8 * (wv & 3) + lane) * 32 + (row0 >> 6)) * 8, e, lane < 8);
      if (!spare) sfor<8 - NMID - PUBB - FLB>([&](auto) { bar(); });
      else if (wv >= 8) __builtin_amdgcn_s_sleep(SPDELAY);
      else __builtin_amdgcn_s_sleep(16);
      asm volatile("global_load_dwordx2 %0, %1, %2 sc1" : "=v"(fv) : "v"(fo), "s"(fb));
      if (!spare) sfor<FLB>([&](auto) { bar(); });
    } else if (spare) {
      CODE_PF("gm_main", "gm_end", 4 * ri + wv, 16, sb + L_DUMMY);
      bar(); bar();
    } else {
      const int cw = wv - 4, g = lane >> 4, l15 = lane & 15, wm = cw & 1, wn = cw >> 1;
      const u32 xra = sb + (64 * wm + l15) * 128 + 16 * (g ^ (l15 & 7));
      const u32 xb[2][2] = {{opaque(xra), opaque(xra + 2 * L_SLOT)}, {opaque(xra ^ 64), opaque((xra ^ 64) + 2 * L_SLOT)}};
      u32x4 wf[2][4], xf[2][4];
      f32x4 acc[4][4];
      const int ln = 14 * hg + 2 * jt + wn;
      u32 pof; bool pon;
      {
        const int r64 = 2 * rb + wm, seg = ln / 9, lc = ln - 9 * seg, hc = (64 * lc) / 18 + (lane >> 2), li = lane & 1, nx = (lane >> 1) & 1;
        pon = ln == 27 ? lane < 16 : (lane < 20 && hc < 32 && ((18 * hc + 17 * li) >> 6) == lc && (!nx || (seg > 0 && r64 < 28)));
        pof = opaque(ln == 27 ? F_Y + (((4 * (r64 >> 2) + 2 * (r64 & 1)) * 8 + lane) * 80 + 72 + ((r64 >> 1) & 1)) * 8 : F_1 + ((hc * 8 + (r64 >> 2) + nx) * 64 + seg * 16 + li * 8 + (r64 & 3) + (nx ? 0 : 4)) * 8);
      }
      __builtin_amdgcn_s_setprio(2);
      if (ln == 27) {
        dma16(gw, wm * 128 + lane * 16, sb + L_GW + wm * 128);
        wait_vm<0>();
#pragma unroll
        for (int i = 0; i < 4; i++) acc[i][0] = f32x4{0.f, 0.f, 0.f, 0.f};
#pragma unroll 1
        for (int c = 0; c < 9; c++) {
          bar();
          const int ca = c + rot >= 9 ? c + rot - 9 : c + rot;
#pragma unroll
          for (int ks = 0; ks < 2; ks++) {
            const u32x4 gv = lds128(sb + L_GW + ca * 128 + ks * 64 + 16 * g);
#pragma unroll
            for (int i = 0; i < 4; i++) acc[i][0] = mfma16(gv, lds128(xb[ks][0] + (c & 3) * L_SLOT + 2048 * i), acc[i][0]);
          }
          wait_lgkm();
        }
        bar();
        const float p = rbf(g == 0 ? acc[0][0][0] : g == 1 ? acc[1][0][0] : g == 2 ? acc[2][0][0] : acc[3][0][0]);
        gst32(f2u(rbf(1.f / (1.f + __expf(-p)))), lane * 4, ws + WS_G + (rb * 128 + 64 * wm) * 4);
      } else {
    u32 wb[4][2];
    {
      const int q = l15 >> 2, p = lane & 3, f = q | ((g & 1) << 2);
#pragma unroll
      for (int nt = 0; nt < 4; nt++) {
        const u32 a = sb + L_WOFF + (8 * g + q) * 256 + (((8 * wn + 2 * nt + (p >> 1)) ^ (f << 1)) << 4) + 8 * (p & 1);
        wb[nt][0] = opaque(a); wb[nt][1] = opaque(a + 2 * L_SLOT);
      }
    }
    auto rd = [&](auto KS, auto C) {
      constexpr int ks = KS, c = C, so = (c & 1) * L_SLOT, hi = (c >> 1) & 1;
      sfor<4>([&](auto I) {
        xf[ks][I] = lds128(xb[ks][hi] + so + 2048 * I);
        wf[ks][I] = cat(tr_read(wb[I][hi] + so + ks * 8192), tr_read(wb[I][hi] + so + ks * 8192 + 1024));
      });
    };
    auto mm = [&](auto KS, auto FIRST) {
      constexpr int ks = KS;
      sfor<16>([&](auto I) {
        constexpr int mo[16] = {0, 1, 0, 1, 2, 2, 0, 1, 2, 3, 3, 3, 0, 1, 2, 3}, no[16] = {0, 0, 1, 1, 0, 1, 2, 2, 2, 0, 1, 2, 3, 3, 3, 3};
        constexpr int mt = mo[I], nt = no[I];
        acc[mt][nt] = mfma16(xf[ks][mt], wf[ks][nt], FIRST ? f32x4{0.f, 0.f, 0.f, 0.f} : acc[mt][nt]);
      });
    };
    auto ilv = [&]() {
      sfor<12>([&](auto) { __builtin_amdgcn_sched_group_barrier(0x8, 1, 0); __builtin_amdgcn_sched_group_barrier(0x100, 1, 0); });
      __builtin_amdgcn_sched_group_barrier(0x8, 4, 0);
    };
    const float sc = ln < 9 ? QSCALE : 1.f;
    char* qo = ws + WS_QKV + (size_t)((2 * rb + wm) * 1728 + 64 * ln) * 128;
    SCHED();
    bar();
    STAMP(1);
    rd(IC(0), IC(0));
    sfor<9>([&](auto C) {
      constexpr int c = C;
      SCHED();
      rd(IC(1), C); mm(IC(0), IC(c == 0)); ilv();
      SCHED();
      if constexpr (c == 8) wait_lgkm();
      bar();
      SCHED();
      if constexpr (c < 8) { rd(IC(0), IC(c + 1)); mm(IC(1), IC(0)); ilv(); }
    });
    STAMP(2);
    const u32 stg = sb + L_STG + cw * 9216;
    const u32 sa = stg + l15 * 144 + 8 * g, ra = stg + r8 * 144 + 16 * c8, go = lane * 16;
    u32x4 t[4][2];
    sfor<6>([&](auto NT) {
      constexpr int nt = NT;
      if constexpr (nt < 4) sfor<4>([&](auto M) { acc[M][nt] = mfma16(xf[1][M], wf[1][nt], acc[M][nt]); });
      SCHED();
      if constexpr (nt > 0 && nt < 5) {
        sfor<4>([&](auto M) {
          const f32x4 v = acc[M][nt - 1] * sc;
          st64(sa + (nt - 1) * 2304 + 32 * M, u32x2{pk2(v[0], v[1]), pk2(v[2], v[3])});
        });
        t[nt - 1][0] = lds128(ra + (nt - 1) * 2304); t[nt - 1][1] = lds128(ra + (nt - 1) * 2304 + 1152);
      }
      if constexpr (nt > 1) { gst128(t[nt - 2][0], go, qo + (nt - 2) * 2048); gst128(t[nt - 2][1], go, qo + (nt - 2) * 2048 + 1024); }
      SCHED();
    });
      }
      STAMP(8);
      publish(fb, pof, lds64(sb + W_TAG), pon);
    }
  }
  const int rt = 4 * xcd + (idx & 3), ct = idx >> 2;
  auto wo_load = [&](int r0, int r1) {
    const int r6 = lane / 10, c10 = lane - 10 * r6;
    const u32 vo = (6 * (wv & 3) + r6) * 1152 + 16 * (c10 > 8 ? 8 : c10);
    const char* wb = (const char*)(wo + 72 * ct);
#pragma unroll 4
    for (int i = r0; i < r1; i++) dma16n<60>(wb + i * 24 * 1152, vo, sb + L_W + (4 * i + (wv & 3)) * 960);
  };
  STAMP(3);
  const u32x2 tag = lds64(sb + W_TAG);
  const int cw = wv & 3;
  u32 ypub = 0;
  if (wv >= (spare ? 4u : 8u)) {
    const int tid = threadIdx.x & 255;
    const u32 cb = sb + L_C + (wv >= 8 ? 0 : 2 * CS_SZ);
    __builtin_amdgcn_s_sleep(CVDELAY);
    asm volatile("s_waitcnt vmcnt(0)" : "+v"(fv)::"memory");
    if (__builtin_amdgcn_ballot_w64(fv.x != tag.x || fv.y != tag.y)) await<POLL_SLEEP>(fb, fo, tag);
    STAMP(2);
    sfor<2>([&](auto J) { dma16c(ws + WS_UR + (size_t)(chb + J) * 4096, tid * 16, cb + CS_U + cw * 1024 + J * CS_SZ); });
    const u32 dv = *(const __attribute__((address_space(4))) u32*)(ssmD + chb);
    {
      const u32 z0 = opaque(cw * KC_STR + 16 * lane), z1 = opaque(CS_U + 4096 + 16 * (lane & 7)), za = cb + (lane < 24 ? z0 : z1);
      st128(za, u32x4{0u, 0u, 0u, 0u}); st128(za + CS_SZ, u32x4{0u, 0u, 0u, 0u});
    }
    f32x4 acc[8];
#pragma unroll
    for (int c = 0; c < 8; c++) { acc[c] = f32x4{0.f, 0.f, 0.f, 0.f}; asm volatile("" : "+v"(acc[c])); }
#pragma unroll
    for (int j = 0; j < 2; j++) {
      const u32 cs = cb + j * CS_SZ + 384 + 16 * tid;
      const u32 w0 = tid ? pre[j].x : 0u, w1 = tid ? pre[j].y : 0u, w2 = cur[j].x, w3 = cur[j].y, w4 = cur[j].z, w5 = cur[j].w;
      const u32 a01 = __builtin_amdgcn_alignbyte(w1, w0, 2), a12 = __builtin_amdgcn_alignbyte(w2, w1, 2), a23 = __builtin_amdgcn_alignbyte(w3, w2, 2),
                a34 = __builtin_amdgcn_alignbyte(w4, w3, 2), a45 = __builtin_amdgcn_alignbyte(w5, w4, 2);
      st128(cs, u32x4{a01, a12, a23, a34});
      st128(cs + KC_STR, u32x4{w1, w2, w3, w4});
      st128(cs + 2 * KC_STR, u32x4{a12, a23, a34, a45});
      st128(cs + 3 * KC_STR, cur[j]);
    }
    STAMP(1);
    wait_vm<0>();
    prog_set(sb + W_XCH + 4 * wv, 1);
    const int tm = cw >> 1, pi = cw & 1;
    const u32 cs = cb + tm * CS_SZ;
    prog_wait(sb + W_XCH + 4 * (wv & 12) + 4 * (lane & 3), 1);
    STAMP(4);
    conv_mfma<4>(cs, 4 * pi, lane, acc);
    STAMP(5);
    __builtin_amdgcn_s_setprio(CPRIO);
    if (spare && wv >= 8) prog_wait(sb + W_XCH + 16 + 8 * tm + 4 * (lane & 1), 2);
    const u32 rA = (spare && wv >= 8 ? sb + L_C + (2 + tm) * CS_SZ : sb + L_R + tm * 8192) + lane * 16;
    if (pi) { sfor<4>([&](auto J) { st128(rA + 4096 + J * 1024, __builtin_bit_cast(u32x4, acc[J])); acc[J] = acc[4 + J]; }); }
    else sfor<4>([&](auto J) { st128(rA + J * 1024, __builtin_bit_cast(u32x4, acc[4 + J])); });
    u32x2 uu[4];
#pragma unroll
    for (int j = 0; j < 4; j++) uu[j] = lds64(cs + CS_U + 4088 - 8 * lane - 512 * (4 * pi + j));
    prog_set(sb + W_XCH + 4 * wv, 2);
    prog_wait(sb + W_XCH + 4 * (wv ^ 1), 2);
    f32x4 t[4];
#pragma unroll
    for (int j = 0; j < 4; j++) t[j] = __builtin_bit_cast(f32x4, lds128(rA + (pi ^ 1) * 4096 + j * 1024));
    wait_lgkm();
    prog_set(sb + W_XCH + 4 * wv, 3);
    const u32 yo = ((lane >> 3) & 3) * 73728 + (lane >> 5) * 64 + (lane & 7) * 8;
    char* yd = ws + WS_Y2 + (chb + tm) * 128 + pi * 1179648;
#pragma unroll
    for (int j = 0; j < 4; j++) conv_out(vadd4(acc[j], t[j]), uu[j], tm ? hi2f(dv) : lo2f(dv), yo + j * 294912, yd);
    STAMP(8);
    ypub = F_Y + (((16 * pi + lane) * 8 + (chb + tm) / 72) * 80 + (chb + tm) % 72) * 8;
    if (wv < 8) publish(fb, ypub, tag, lane < 16);
  }
  if (wv < 8) {
    __builtin_amdgcn_s_setprio(1);
    const int c = lane & 31, hh = lane >> 5, q4 = (lane & 15) >> 2;
    const int w = spare ? wv ^ 4 : wv;
    const u32 kcol = 64 * w + (lane & 16) * 2 + 16 * (lane & 1) + 4 * (lane & 2);
    const u32 ka = opaque(sb + L_K + (8 * hh + q4) * 1088 + kcol), ka2 = opaque(sb + L_K + (16 + q4) * 1088 + kcol);
    const u32 va = opaque(sb + L_V + c * 1040 + 64 * w + 16 * hh);
    const u32 qa = opaque(sb + L_Q + q4 * 512 + 64 * w + (lane & 16) * 2 + 8 * (lane & 3)), qa0 = opaque(qa + 4096 * hh);
    const int thr = c - 8 * hh;
    const u32 lom = opaque(hh ? 0u : ~0u);
    u32 msk[8], nmk[8];
    unsigned long long cnd[16];
#pragma unroll
    for (int e = 0; e < 16; e++) cnd[e] = __builtin_amdgcn_ballot_w64(16 * (e >> 3) + (e & 7) <= thr);
#pragma unroll
    for (int j = 0; j < 8; j++) { const int k0 = 16 * (j >> 2) + 2 * (j & 3); msk[j] = opaque(k0 + 1 <= thr ? 0xffffffffu : (k0 <= thr ? 0xffffu : 0u)); nmk[j] = opaque(~msk[j]); }
    f32x16 S0;
#pragma unroll
    for (int e = 0; e < 16; e++) S0[e] = u2f(opaque(spare ? 0xff800000u : 0u));
    const u32 oo = opaque((c >> 4) * 18432 + (c & 15) * 16 + hh * 256);
    const u32 fl = sb + W_PROG + 4 * (spare ? (lane & 3) : (lane & 7));
    if (wv < 3) st128(sb + (wv < 2 ? L_K + (18 + wv) * 1088 : L_V + 18 * 1040) + lane * 16, u32x4{0x3f803f80u, 0x3f803f80u, 0x3f803f80u, 0x3f803f80u});
    if (wv == (spare ? 0u : (u32)POLLW)) {
      await<POLL_FAST>(fb, F_1 + ((h * 8 + qb) * 64 + ((lane < 48 ? lane : lane - 16) | (lane < 16 || spare ? 4 : 0))) * 8, tag);
      prog_set(sb + W_GO, 1);
    } else prog_wait(sb + W_GO, 1);
    STAMP(4);
    __builtin_amdgcn_s_setprio(BAL ? 3 : 1);
    if (!spare || wv < 4) {
    const char* Ag = ws + WS_QKV + (size_t)(4 * qb * 1728 + 18 * h) * 128;
    const char* Kg = Ag + 576 * 128 - (qb ? 884736 : 0);
    const u32 lo = ((lane >> 3) & (qb ? 7 : 3)) * 221184 + (lane & 7) * 16, lq = ((lane >> 3) & 3) * 221184 + (lane >> 5) * 128 + (lane & 7) * 16;
#pragma unroll 1
    for (int s = wv; s <= w; s += 4) {
      dma16c(Ag + s * 256, lq, sb + L_Q + s * 1024);
      dma16c(Kg + s * 128, lo, sb + L_K + s * 1088); dma16c(Kg + s * 128 + 1024, lo, sb + L_K + (8 + s) * 1088);
      const char* eg = s < 4 ? Kg + (s < 2 ? 16 + s : 590 + s) * 128 : Ag + 2048;
      dma16c(eg, s < 4 ? lo : lq, sb + (s < 2 ? L_K + (16 + s) * 1088 : (s < 4 ? L_V + (14 + s) * 1040 : L_Q + 8192)));
    }
    __builtin_amdgcn_s_sleep(VDELAY);
    if (spare) { dma16c(Kg + (572 + w) * 128, lo, sb + L_V + (w - 4) * 1040); dma16c(Kg + (580 + w) * 128, lo, sb + L_V + (4 + w) * 1040); }
    dma16c(Kg + (576 + w) * 128, lo, sb + L_V + w * 1040); dma16c(Kg + (584 + w) * 128, lo, sb + L_V + (8 + w) * 1040);
    prog_set(sb + W_PROG + 4 * wv, 1);
    wait_vm<2>();
    prog_set(sb + W_PROG + 4 * wv, 2);
    }
    prog_wait(fl, 2);
    STAMP(5);
    struct KF { u32x4 a; u32x2 b; };
    struct VF { u32x4 a, b; };
    auto ldk = [&](int i) { KF k; k.a = cat(tr_read(ka + i * 64), tr_read(ka + 4352 + i * 64)); k.b = tr_read(ka2 + i * 64); return k; };
    auto ldv = [&](int i) { VF v; v.a = lds128(va + i * 64); v.b = lds128(va + i * 64 + 32); return v; };
    auto pk8 = [&](const f32x16& p, int o) { return u32x4{pk2(p[o], p[o + 1]), pk2(p[o + 2], p[o + 3]), pk2(p[o + 4], p[o + 5]), pk2(p[o + 6], p[o + 7])}; };
    const int ib = spare ? 8 - w : 1;
    const u32x4 q0 = cat(tr_read(qa0), tr_read(qa0 + 2048));
    const KF k8 = ldk(8);
    u32x2 q1 = tr_read(qa + 8192);
    const KF k0 = ldk(0);
    SCHED();
    q1.x &= lom; q1.y = 0u;
    auto qk = [&](const KF& k) { f32x16 z = {}; z = mfma32(k.a, q0, z); return mfma32k8(k.b, q1, z); };
    f32x16 Sm = {};
    Sm = mfma32(k8.a, q0, Sm); S0 = mfma32(k0.a, q0, S0);
    Sm = mfma32k8(k8.b, q1, Sm); S0 = mfma32k8(k0.b, q1, S0);
    KF kf[2]; VF vf[2], v8, v0;
    kf[1] = ldk(7); kf[0] = ldk(6);
    SCHED();
#pragma unroll
    for (int e = 0; e < 16; e++) Sm[e] = __builtin_amdgcn_inverse_ballot_w64(cnd[e]) ? Sm[e] : S0[e];
    float m = __builtin_fmaxf(__builtin_fmaxf(Sm[0], Sm[1]), Sm[2]), m2 = __builtin_fmaxf(__builtin_fmaxf(Sm[3], Sm[4]), Sm[5]);
#pragma unroll
    for (int e = 6; e < 14; e += 4) { m = fmaxf(fmaxf(m, Sm[e]), Sm[e + 1]); m2 = fmaxf(fmaxf(m2, Sm[e + 2]), Sm[e + 3]); }
    m = fmaxf(fmaxf(m, Sm[14]), Sm[15]);
    m = xhalf_max(fmaxf(m, m2));
    f32x16 O;
    float l;
#pragma unroll 1
    for (int pass = 0; pass < 2; pass++) {
      q1.y = pk2(-m, lo2f(pk2(m, m)) - m) & lom;
      f32x16 S[2];
      {
        f32x16 p = mfma32k8(kf[1].b, u32x2{0u, q1.y}, Sm);
        S[1] = qk(kf[1]);
        SCHED();
        wait_vm<0>();
        prog_set(sb + W_PROG + 4 * wv, 3);
#pragma unroll
        for (int e = 0; e < 8; e++) p[e] = __builtin_amdgcn_exp2f(p[e]);
        SCHED();
        const u32 vfl = lds_peek(fl);
        SCHED();
#pragma unroll
        for (int e = 8; e < 16; e++) p[e] = __builtin_amdgcn_exp2f(p[e]);
        SCHED();
        if (__builtin_amdgcn_ballot_w64(vfl < 3u)) prog_wait(fl, 3);
        asm volatile("" ::: "memory");
        v8 = ldv(8); v0 = ldv(0); vf[1] = ldv(7);
        SCHED();
        const u32x4 pa = pk8(p, 0), pb = pk8(p, 8);
        O = f32x16{};
        O = mfma32(v8.a, pa & u32x4{msk[0], msk[1], msk[2], msk[3]}, O); O = mfma32(v8.b, pb & u32x4{msk[4], msk[5], msk[6], msk[7]}, O);
        O = mfma32(v0.a, pa & u32x4{nmk[0], nmk[1], nmk[2], nmk[3]}, O); O = mfma32(v0.b, pb & u32x4{nmk[4], nmk[5], nmk[6], nmk[7]}, O);
      }
      auto tile = [&](auto self, auto I) -> void {
        constexpr int i = I;
        if (i < ib) return;
        SCHED();
        f32x16& Sc = S[i & 1];
        f32x16 z = {};
        if constexpr (i > 1) z = mfma32(kf[(i - 1) & 1].a, q0, z);
        if constexpr (i > 2) kf[i & 1] = ldk(i - 2);
        if constexpr (i > 1) vf[(i - 1) & 1] = ldv(i - 1);
        SCHED();
#pragma unroll
        for (int e = 0; e < 8; e++) Sc[e] = __builtin_amdgcn_exp2f(Sc[e]);
        SCHED();
        if constexpr (i > 1) S[(i - 1) & 1] = mfma32k8(kf[(i - 1) & 1].b, q1, z);
        const u32x4 pa = pk8(Sc, 0);
        SCHED();
#pragma unroll
        for (int e = 8; e < 12; e++) Sc[e] = __builtin_amdgcn_exp2f(Sc[e]);
        SCHED();
        O = mfma32(vf[i & 1].a, pa, O);
#pragma unroll
        for (int e = 12; e < 16; e++) Sc[e] = __builtin_amdgcn_exp2f(Sc[e]);
        const u32x4 pb = pk8(Sc, 8);
        SCHED();
        O = mfma32(vf[i & 1].b, pb, O);
        if constexpr (i > 1) self(self, IC(i - 1));
      };
      tile(tile, IC(7));
      SCHED();
      { const u32 lu = f2u(O[10]); auto r = __builtin_amdgcn_permlane32_swap(lu, lu, false, false); l = u2f(r[0]); }
      if (pass == 1 || __all(l > 8.67e-19f && l < 1.15e18f)) break;
      q1.y = 0u;
#pragma unroll 1
      for (int i = ib; i < 8; i++) {
        const f32x16 s = qk(ldk(i));
#pragma unroll
        for (int e = 0; e < 16; e += 2) m = fmaxf(fmaxf(m, s[e]), s[e + 1]);
      }
      m = xhalf_max(m);
      kf[1] = ldk(7); kf[0] = ldk(6);
    }
    STAMP(6);
    float inv = __builtin_amdgcn_rcpf(l);
    asm volatile("s_nop 1" : "+v"(inv));
    char* Og = ws + WS_O + (size_t)(16 * qb + 2 * w) * 18432;
    gst128(u32x4{pk2(vmul(O[0], inv), vmul(O[1], inv)), pk2(vmul(O[2], inv), vmul(O[3], inv)), pk2(vmul(O[4], inv), vmul(O[5], inv)), pk2(vmul(O[6], inv), vmul(O[7], inv))},
           oo, Og + (h >> 1) * 1024 + (h & 1) * 512);
    if (hh == 0) gst32(pk2(vmul(O[8], inv), vmul(O[9], inv)), (c >> 4) * 18432 + (c & 15) * 4, Og + 16384 + h * 64);
    publish(fb, F_O + (((4 * qb + (w & 3)) * 8 + lane) * 64 + (w >> 2) * 32 + h) * 8, tag, lane < 8);
    {
      __builtin_amdgcn_s_setprio(0);
      const int g = lane >> 4;
      prog_wait(sb + W_XCH + 32 + 4 * (lane & 3), 4);
      const u32 wa2 = sb + L_W + ((4 * g + (q4 >> 1)) * 18 + 16 + (q4 & 1)) * 160 + 8 * (lane & 3);
      u32x4 wx[2], wr[10];
      sfor<2>([&](auto J) { const int i = J ? 8 + (wv & 1) : wv; const u32 a = wa2 + (i / 5) * 46080 + (i % 5) * 32; wx[J] = cat(tr_read(a), tr_read(a + 5760)); });
      prog_set(sb + W_PROG + 4 * wv, 4);
      const u32 wa = sb + L_W + wv * 11520 + (18 * (g >> 1) + 4 * (g & 1) + q4) * 160 + 8 * (lane & 3), wd = sb + L_W + wv * 11520 + lane * 16;
      sfor<10>([&](auto I) { const u32 a = wa + (I / 5) * 5760 + (I % 5) * 32; wr[I] = cat(tr_read(a), tr_read(a + 1280)); });
      prog_wait(sb + W_PROG + 4 * (lane & 7), 4);
      sfor<2>([&](auto J) { const int i = J ? 8 + (wv & 1) : wv; st128(sb + L_Q + i * 1024 + lane * 16, wx[J]); });
      sfor<10>([&](auto I) { st128(wd + (I / 5) * 5760 + (I % 5) * 1024, wr[I]); });
      prog_set(sb + W_PROG + 4 * wv, 5);
    }
      if (b == 255 && wv == 4) {
#pragma unroll 1
        for (int spin = 0; spin < (1 << 18); spin++) {
          u32x4 v[9];
          sfor<9>([&](auto I) { gl128c<0>(v[I], (lane >> 4) * 2048 + (lane & 15) * 16, fb + F_U + I * 8192); });
          asm volatile("s_waitcnt vmcnt(0)" : "+v"(v[0]), "+v"(v[1]), "+v"(v[2]), "+v"(v[3]), "+v"(v[4]), "+v"(v[5]), "+v"(v[6]), "+v"(v[7]), "+v"(v[8])::"memory");
          u32 bad = 0;
#pragma unroll
          for (int i = 0; i < 9; i++) bad |= (v[i].x ^ tag.x) | (v[i].y ^ tag.y) | (v[i].z ^ tag.x) | (v[i].w ^ tag.y);
          if (!__builtin_amdgcn_ballot_w64(bad != 0)) break;
          __builtin_amdgcn_s_sleep(16);
        }
        if (lane == 0) gst64(tag, F_EP, fb);
      }
    STAMP_END();
    return;
  }
  {
    const int g = lane >> 4, l15 = lane & 15, q = l15 >> 2;
    const int r7 = lane / 9, c9 = lane - 9 * r7;
    const u32 xw = sb + L_X + cw * 2304;
    const int m16 = 16 * (rt >> 2) + 2 * (rt & 3) + 8 * (cw >> 1) + (cw & 1);
    if (__builtin_amdgcn_ballot_w64(lds_peek(sb + W_PROG + 4 * (spare ? (lane & 3) : (lane & 7))) < (u32)WOGATE)) { publish(fb, ypub, tag, lane < 16); ypub = 0; }
    prog_wait(sb + W_XCH + (spare ? 16 + 4 * (lane & 7) : 32 + 4 * (lane & 3)), 3);
    prog_wait(sb + W_PROG + 4 * (spare ? (lane & 3) : (lane & 7)), WOGATE);
    STAMP(9);
    {
      const u16* xp = x + (size_t)(16 * m16) * 576 + 72 * ct;
      dma16n<63>(xp, r7 * 1152 + 16 * c9, xw); dma16n<63>(xp + 7 * 576, r7 * 1152 + 16 * c9, xw + 1008); dma16n<63>(xp + 9 * 576, r7 * 1152 + 16 * c9, xw + 1296);
      wo_load(0, 4);
      wait_vm<7>();
      if (ypub && lane < 16) gst64(tag, ypub, fb);
      wo_load(4, YPOS);
    }
    const u32 yf = F_Y + ((rt * 8 + ct) * 80 + (lane < 18 ? 18 * cw + lane : 72 + (cw >> 1))) * 8;
    u32x2 yv;
    asm volatile("global_load_dwordx2 %0, %1, %2 sc1" : "=v"(yv) : "v"(yf), "s"(fb));
    wo_load(YPOS, 24);
    STAMP(10);
    asm volatile("s_waitcnt vmcnt(%1)" : "+v"(yv) : "i"(24 - YPOS) : "memory");
    if (__builtin_amdgcn_ballot_w64(yv.x != tag.x || yv.y != tag.y)) await<POLL_SLEEP>(fb, yf, tag);
    STAMP(11);
    {
      const char* yp = ws + WS_Y2 + (size_t)(rt * 576 + 72 * ct + 18 * cw) * 128;
      dma16c(yp, lane * 16, sb + L_Y + cw * 2304); dma16c(yp + 1024, lane * 16, sb + L_Y + cw * 2304 + 1024); dma16c(yp + 1280, lane * 16, sb + L_Y + cw * 2304 + 1280);
    }
    u32 gtu;
    gl32c<0>(gtu, (16 * m16 + l15) * 4, ws + WS_G);
    asm volatile("s_waitcnt vmcnt(0)" : "+v"(gtu)::"memory");
    prog_set(sb + W_XCH + 4 * wv, 4);
    prog_wait(sb + W_XCH + 32 + 4 * (lane & 3), 4);
    const float gt = u2f(gtu);
    f32x4 acc[5], tb[5];
    const u32 xa = xw + l15 * 144 + 8 * g;
    {
      const float g1 = rbf(1.f - gt);
      const u32 ya = sb + L_Y + (4 * g + q) * 128 + 32 * cw + 8 * (lane & 3);
      sfor<5>([&](auto NT) {
        const u32x2 y = tr_read(ya + NT * 2048), xv = lds64(xa + NT * 32);
        tb[NT] = f32x4{vmul(vmul(g1, lo2f(xv.x)), lo2f(y.x)), vmul(vmul(g1, hi2f(xv.x)), hi2f(y.x)), vmul(vmul(g1, lo2f(xv.y)), lo2f(y.y)), vmul(vmul(g1, hi2f(xv.y)), hi2f(y.y))};
      });
    }
    const u32 wl = opaque(sb + L_W + lane * 16), wh = opaque(wl + 46080), wq = opaque(sb + L_Q + lane * 16);
    u32x4 wf[2][5];
    auto rd = [&](auto KS) {
      sfor<5>([&](auto NT) {
        constexpr int ks = KS;
        if constexpr (ks < 16) wf[ks & 1][NT] = lds128((ks < 8 ? wl : wh) + (ks & 7) * 5760 + NT * 1024);
        else wf[ks & 1][NT] = lds128(wq + (5 * (ks - 16) + NT) * 1024);
      });
    };
    STAMP(6);
    u32x4 of[16]; u32 oe[8];
    const char* ob = ws + WS_O + (size_t)m16 * 18432;
    {
      const u32 oo = F_O + ((rt * 8 + ct) * 64 + (cw >> 1) * 32 + (lane & 31)) * 8;
      u32 miss = ~0u;
      sfor<4>([&](auto G) {
#pragma unroll 1
        for (int spin = 0; (miss & (0xffu << (8 * G))) && spin < (1 << 20); spin++) {
          u32x2 v;
          asm volatile("global_load_dwordx2 %0, %1, %2 sc1\n\ts_waitcnt vmcnt(0)" : "=&v"(v) : "v"(oo), "s"(fb) : "memory");
          miss = (u32)__builtin_amdgcn_ballot_w64(v.x != tag.x || v.y != tag.y);
          if (miss & (0xffu << (8 * G))) __builtin_amdgcn_s_sleep(POLL_FAST);
        }
        sfor<4>([&](auto F) { gl128c<F * 1024>(of[4 * G + F], lane * 16, ob + G * 4096); });
      });
    }
    STAMP(12);
    __builtin_amdgcn_s_setprio(3);
    sfor<8>([&](auto F) { gl32c<(F >> 2) * 1024 + (F & 3) * 64>(oe[F], 256 * g + 4 * l15, ob + 16384); });
    prog_wait(sb + W_PROG + 4 * (lane & 7), 5);
    rd(IC(0));
    sfor<16>([&](auto KS) {
      constexpr int ks = KS;
      SCHED();
      if constexpr (ks % 2 == 0) asm volatile("s_waitcnt vmcnt(%2)" : "+v"(of[ks]), "+v"(of[ks + 1]) : "i"(22 - ks) : "memory");
      rd(IC(ks + 1));
      sfor<5>([&](auto NT) { acc[NT] = mfma16(wf[ks & 1][NT], of[ks], ks == 0 ? f32x4{0.f, 0.f, 0.f, 0.f} : acc[NT]); });
      sfor<5>([&](auto) { __builtin_amdgcn_sched_group_barrier(0x8, 1, 0); __builtin_amdgcn_sched_group_barrier(0x100, 1, 0); });
    });
    SCHED();
    asm volatile("s_waitcnt vmcnt(0)" : "+v"(oe[0]), "+v"(oe[1]), "+v"(oe[2]), "+v"(oe[3]), "+v"(oe[4]), "+v"(oe[5]), "+v"(oe[6]), "+v"(oe[7])::"memory");
    rd(IC(17));
    sfor<5>([&](auto NT) { acc[NT] = mfma16(wf[0][NT], u32x4{oe[0], oe[1], oe[2], oe[3]}, acc[NT]); });
    sfor<5>([&](auto NT) { acc[NT] = mfma16(wf[1][NT], u32x4{oe[4], oe[5], oe[6], oe[7]}, acc[NT]); });
    SCHED();
    STAMP(13);
    sfor<5>([&](auto NT) {
      constexpr int nt = NT;
      if (nt < 4 || g < 2)
        st64(xa + nt * 32, u32x2{pk2(fmaf(gt, acc[nt][0], tb[nt][0]), fmaf(gt, acc[nt][1], tb[nt][1])), pk2(fmaf(gt, acc[nt][2], tb[nt][2]), fmaf(gt, acc[nt][3], tb[nt][3]))});
    });
    char* orow = (char*)(out + (size_t)(16 * m16) * 576 + 72 * ct);
    u32x4 t[3];
#pragma unroll
    for (int it = 0; it < 3; it++) t[it] = lds128(xw + it * 1008 + lane * 16);
#pragma unroll
    for (int it = 0; it < 3; it++)
      if (lane < (it < 2 ? 63 : 18)) asm volatile("global_store_dwordx4 %0, %1, %2 " OUTPOL "\n\ts_nop 1" ::"v"((7 * it + r7) * 1152 + 16 * c9), "v"(t[it]), "s"(orow) : "memory");
  }
  STAMP_END();
}
extern "C" __global__ void gm_end() {}

extern "C" void kernel_launch(void* const* d_in, const int* in_sizes, int n_in, void* d_out, int out_size, void* d_ws, size_t ws_size,
                              hipStream_t stream) {
  (void)hipFuncSetAttribute((const void*)gm_main, hipFuncAttributeMaxDynamicSharedMemorySize, L_END);
  gm_main<<<256, 768, L_END, stream>>>((const u16*)d_in[0], (const u16*)d_in[1], (char*)d_ws, (const u16*)d_in[4], (const u16*)d_in[5], (const u16*)d_in[6],
                                       (const u16*)d_in[7], (const u16*)d_in[2], (const u16*)d_in[8], (const u16*)d_in[9], (u16*)d_out);
}
```

```cpp
#include <hip/hip_runtime.h>
#include <hip/hip_bf16.h>
#include <utility>

typedef unsigned short u16;
typedef unsigned int u32;
typedef u32 u32x2 __attribute__((ext_vector_type(2)));
typedef u32 u32x4 __attribute__((ext_vector_type(4)));
typedef short s16x4 __attribute__((ext_vector_type(4)));
typedef __bf16 bf16x8 __attribute__((ext_vector_type(8)));
typedef __bf16 bf16x2 __attribute__((ext_vector_type(2)));
typedef float f32x2 __attribute__((ext_vector_type(2)));
typedef float f32x4 __attribute__((ext_vector_type(4)));
typedef float f32x16 __attribute__((ext_vector_type(16)));
#define LAS __attribute__((address_space(3)))
#define GAS __attribute__((address_space(1)))
#define DEVI __device__ __forceinline__
#define IC(n) std::integral_constant<int, (n)>{}

constexpr size_t WS_QKV = 0;
constexpr size_t WS_O   = WS_QKV + 96ull * 2048 * 40;
constexpr size_t WS_UR  = WS_O + 32ull * 2048 * 40;
constexpr size_t WS_Y2  = WS_UR + 576ull * 4096;
constexpr size_t WS_G   = WS_Y2 + 576ull * 4096;
constexpr size_t WS_F   = WS_G + 8192;
constexpr u32 F_EP = 0, F_1 = 1024, F_U = F_1 + 256 * 512, F_O = F_U + 288 * 256, F_Y = F_O + 256 * 512;

template <typename F, int... I> DEVI void sfor_impl(F&& f, std::integer_sequence<int, I...>) { (f(std::integral_constant<int, I>{}), ...); }
template <int N, typename F> DEVI void sfor(F&& f) { sfor_impl(f, std::make_integer_sequence<int, N>{}); }

DEVI u32 pk2(float a, float b) { f32x2 v = {a, b}; return __builtin_bit_cast(u32, __builtin_convertvector(v, bf16x2)); }
DEVI u32 f2u(float a) { return __builtin_bit_cast(u32, a); }
DEVI float u2f(u32 a) { return __builtin_bit_cast(float, a); }
DEVI float lo2f(u32 w) { return __builtin_bit_cast(float, w << 16); }
DEVI float hi2f(u32 w) { return __builtin_bit_cast(float, w & 0xffff0000u); }
DEVI float rbf(float a) { return lo2f(pk2(a, a)); }
DEVI f32x4 mfma16(u32x4 a, u32x4 b, f32x4 c) { return __builtin_amdgcn_mfma_f32_16x16x32_bf16(__builtin_bit_cast(bf16x8, a), __builtin_bit_cast(bf16x8, b), c, 0, 0, 0); }
DEVI f32x16 mfma32(u32x4 a, u32x4 b, f32x16 c) { return __builtin_amdgcn_mfma_f32_32x32x16_bf16(__builtin_bit_cast(bf16x8, a), __builtin_bit_cast(bf16x8, b), c, 0, 0, 0); }
DEVI f32x16 mfma32k8(u32x2 a, u32x2 b, f32x16 c) { return __builtin_amdgcn_mfma_f32_32x32x8bf16_1k(__builtin_bit_cast(s16x4, a), __builtin_bit_cast(s16x4, b), c, 0, 0, 0); }
DEVI u32x2 tr_read(u32 addr) { return __builtin_bit_cast(u32x2, __builtin_amdgcn_ds_read_tr16_b64_v4i16((LAS s16x4*)addr)); }
DEVI u32x4 lds128(u32 addr) { return *(LAS u32x4*)addr; }
DEVI u32x2 lds64(u32 addr) { return *(LAS u32x2*)addr; }
DEVI u32 lds32(u32 addr) { return *(LAS u32*)addr; }
DEVI void st128(u32 addr, u32x4 v) { *(LAS u32x4*)addr = v; }
DEVI void st64(u32 addr, u32x2 v) { *(LAS u32x2*)addr = v; }
DEVI void st32(u32 addr, u32 v) { *(LAS u32*)addr = v; }
DEVI u32x4 cat(u32x2 a, u32x2 b) { return u32x4{a.x, a.y, b.x, b.y}; }
DEVI float vfma(float a, float b, float c) { float d; asm("v_fma_f32 %0, %1, %2, %3" : "=v"(d) : "v"(a), "v"(b), "v"(c)); return d; }
DEVI float vmul(float a, float b) { float d; asm("v_mul_f32 %0, %1, %2" : "=v"(d) : "v"(a), "v"(b)); return d; }
DEVI float vadd(float a, float b) { float d; asm("v_add_f32 %0, %1, %2" : "=v"(d) : "v"(a), "v"(b)); return d; }
DEVI f32x4 vadd4(f32x4 a, f32x4 b) { return f32x4{vadd(a[0], b[0]), vadd(a[1], b[1]), vadd(a[2], b[2]), vadd(a[3], b[3])}; }
DEVI u32 opaque(u32 v) { asm volatile("" : "+v"(v)); return v; }
DEVI void dma16(const void* sbase, u32 voff, u32 ldsbase) {
  asm volatile("s_mov_b32 m0, %2\n\ts_nop 0\n\tglobal_load_lds_dwordx4 %0, %1" ::"v"(voff), "s"(sbase), "s"(ldsbase) : "memory");
}
template <int NL> DEVI void dma16n(const void* sbase, u32 voff, u32 ldsbase) {
  asm volatile("s_mov_b32 m0, %2\n\ts_mov_b32 exec_hi, %3\n\tglobal_load_lds_dwordx4 %0, %1\n\ts_mov_b32 exec_hi, -1" ::"v"(voff), "s"(sbase), "s"(ldsbase),
               "i"((1u << (NL - 32)) - 1u) : "memory");
}
template <int OFF> DEVI void gl128(u32x4& d, u32 voff, const void* sbase) { asm volatile("global_load_dwordx4 %0, %1, %2 offset:%3" : "=v"(d) : "v"(voff), "s"(sbase), "i"(OFF)); }
template <int OFF> DEVI void gl64(u32x2& d, u32 voff, const void* sbase) { asm volatile("global_load_dwordx2 %0, %1, %2 offset:%3" : "=v"(d) : "v"(voff), "s"(sbase), "i"(OFF)); }
DEVI void dma16c(const void* sbase, u32 voff, u32 ldsbase) {
  asm volatile("s_mov_b32 m0, %2\n\ts_nop 0\n\tglobal_load_lds_dwordx4 %0, %1 sc1" ::"v"(voff), "s"(sbase), "s"(ldsbase) : "memory");
}
template <int OFF> DEVI void gl128c(u32x4& d, u32 voff, const void* sbase) { asm volatile("global_load_dwordx4 %0, %1, %2 offset:%3 sc1" : "=v"(d) : "v"(voff), "s"(sbase), "i"(OFF)); }
template <int OFF> DEVI void gl32c(u32& d, u32 voff, const void* sbase) { asm volatile("global_load_dword %0, %1, %2 offset:%3 sc1" : "=v"(d) : "v"(voff), "s"(sbase), "i"(OFF)); }
DEVI void gst128(u32x4 v, u32 voff, void* sbase) { asm volatile("global_store_dwordx4 %0, %1, %2 sc1\n\ts_nop 1" ::"v"(voff), "v"(v), "s"(sbase) : "memory"); }
DEVI void gst64(u32x2 v, u32 voff, void* sbase) { asm volatile("global_store_dwordx2 %0, %1, %2 sc1" ::"v"(voff), "v"(v), "s"(sbase) : "memory"); }
DEVI void gst32(u32 v, u32 voff, void* sbase) { asm volatile("global_store_dword %0, %1, %2 sc1" ::"v"(voff), "v"(v), "s"(sbase) : "memory"); }
#define CODE_PF(from, to, ck, step, lds)                                                                                              \
  do {                                                                                                                                \
    u32 lo_, hi_, len_;                                                                                                               \
    asm volatile("s_getpc_b64 s[20:21]\n\ts_add_u32 %0, s20, " from "@rel32@lo+4\n\ts_addc_u32 %1, s21, " from "@rel32@hi+12\n\ts_mov_b32 %2, " to "-" from \
                 : "=s"(lo_), "=s"(hi_), "=s"(len_)::"s20", "s21", "scc");                                                            \
    const char* code_ = (const char*)(((unsigned long long)hi_ << 32) | lo_);                                                         \
    for (u32 o_ = (ck) * 1024; o_ < len_; o_ += (step) * 1024) { const u32 a_ = o_ + (threadIdx.x & 63) * 16; dma16(code_, a_ < len_ ? a_ : 0u, lds); } \
  } while (0)
template <int N> DEVI void wait_vm() { asm volatile("s_waitcnt vmcnt(%0)" ::"i"(N > 63 ? 63 : N) : "memory"); }
DEVI void wait_lgkm() { asm volatile("s_waitcnt lgkmcnt(0)" ::: "memory"); }
DEVI void bar() { asm volatile("s_barrier" ::: "memory"); }
DEVI u32 rfl(u32 v) { return __builtin_amdgcn_readfirstlane(v); }
#define SCHED() __builtin_amdgcn_sched_barrier(0)
#define STAMP(i)
#define STAMP_END()

constexpr float QSCALE = 0.23570226039551584f * 1.4426950408889634f;

#define POLL_SLEEP 8
#define POLL_FAST 2
#define SPDELAY 24
#define LDPOL "sc1"
#define FSPOL "sc1"
#define CVDELAY 0
#define PUBB 2
#define NMID 1
#define FLB 1
#define PUBD 0
#define PREPFIRST 2
#define OUTPOL "sc1"
#define BAL 0
#define CPRIO 2
#define WOGATE 3
#define YPOS 16
#define CMPRIO 2
#define QDELAY 8
#define OGATE 1
#define ODELAY 6
#define POLLW 4
#define VDELAY 3

constexpr int L_SLOT = 32768, L_WOFF = 16384, L_STG = 94208, L_GW = 131072, L_PT = 133120, L_TILE = 17408;
constexpr int L_K = 0, L_V = 21760, L_C = 41536, L_R = L_PT + 1024, L_FL = 152320, L_DUMMY = L_FL + 256, L_Q = 153600, L_END = 163840;
constexpr int KC_STR = 4640, CS_U = 4 * KC_STR, CS_SZ = CS_U + 4096 + 128;
constexpr int L_W = L_C, L_X = L_W + 576 * 160, L_Y = L_X + 9216;
static_assert(L_Y + 9216 <= L_FL && L_C + 2 * CS_SZ <= L_STG && L_C + 4 * CS_SZ <= L_PT && L_PT + L_TILE <= L_FL, "lds map");
constexpr int W_PROG = L_FL, W_XCH = L_FL + 64, W_GO = L_FL + 128, W_TAG = L_FL + 144;

DEVI u32 lds_peek(u32 a) { return __hip_atomic_load((LAS u32*)a, __ATOMIC_RELAXED, __HIP_MEMORY_SCOPE_WORKGROUP); }
DEVI void prog_set(u32 a, u32 n) { asm volatile("" ::: "memory"); __hip_atomic_store((LAS u32*)a, n, __ATOMIC_RELAXED, __HIP_MEMORY_SCOPE_WORKGROUP); }
DEVI void prog_wait(u32 a, u32 n) { while (__builtin_amdgcn_ballot_w64(lds_peek(a) < n)) __builtin_amdgcn_s_sleep(1); asm volatile("" ::: "memory"); }
DEVI void publish(char* fb, u32 off, u32x2 tag, bool on) { wait_vm<0>(); if (on) asm volatile("global_store_dwordx2 %0, %1, %2 " FSPOL ::"v"(off), "v"(tag), "s"(fb) : "memory"); }
template <int SLP> DEVI void await(const char* fb, u32 off, u32x2 tag) {
  for (int spin = 0; spin < (1 << 20); spin++) {
    u32x2 v;
    asm volatile("global_load_dwordx2 %0, %1, %2 " LDPOL "\n\ts_waitcnt vmcnt(0)" : "=&v"(v) : "v"(off), "s"(fb) : "memory");
    if (!__builtin_amdgcn_ballot_w64(v.x != tag.x || v.y != tag.y)) break;
    __builtin_amdgcn_s_sleep(SLP);
  }
}

DEVI void prep_issue(const u16* Bk, const u16* Bv, int row0, int col, u32 tb, u32 pw, int lane) {
  const int r8 = lane >> 3, c8 = lane & 7;
  sfor<2>([&](auto I) {
    const int j = 2 * pw + I;
    int row = row0 + 8 * j + r8 - 3; row = row < 0 ? 0 : row;
    dma16(Bk + col, row * 1152 + 16 * (c8 ^ j), tb + 1024 * j);
    dma16(Bv + col, (row0 + 8 * j + r8) * 1152 + 16 * (c8 ^ j), tb + 9216 + 1024 * j);
  });
  dma16(Bk + col, (row0 + 61 + (r8 > 2 ? 2 : r8)) * 1152 + 16 * c8, tb + 8192);
}
template <typename F> DEVI void prep_compute(int row0, int col, u32 tb, u32 pw, int lane, u32x4 sh, char* ws, F&& mid) {
  const int c8 = lane & 7, cp = 8 * pw + (lane >> 3);
  const u32 o0 = tb + 1024 * c8 + 16 * ((cp >> 2) ^ c8) + 4 * (cp & 3), o1 = tb + 1024 * c8 + 16 * ((cp >> 2) ^ ((c8 + 1) & 7)) + 4 * (cp & 3);
  row0 += 8 * c8;
  u32 kk[11], vv[8];
#pragma unroll
  for (int i = 0; i < 11; i++) kk[i] = lds32((i < 8 ? o0 : o1) + 128 * i);
#pragma unroll
  for (int i = 0; i < 8; i++) vv[i] = lds32(o0 + 9216 + 128 * i);
  if (row0 == 0) { kk[0] = 0; kk[1] = 0; kk[2] = 0; }
  const u32 go = 2 * cp * 4096 + 2 * (2040 - row0);
  char* dst = ws + WS_UR + (size_t)col * 4096;
  float r[8];
  {
    const float a0 = lo2f(sh.x), a1 = hi2f(sh.x), a2 = lo2f(sh.y), a3 = hi2f(sh.y);
#pragma unroll
    for (int i = 0; i < 8; i++) r[i] = vmul(vfma(lo2f(kk[i + 3]), a0, vfma(lo2f(kk[i + 2]), a1, vfma(lo2f(kk[i + 1]), a2, vmul(lo2f(kk[i]), a3)))), lo2f(vv[i]));
    gst128(u32x4{pk2(r[7], r[6]), pk2(r[5], r[4]), pk2(r[3], r[2]), pk2(r[1], r[0])}, go, dst);
  }
  mid();
  {
    const float a0 = lo2f(sh.z), a1 = hi2f(sh.z), a2 = lo2f(sh.w), a3 = hi2f(sh.w);
#pragma unroll
    for (int i = 0; i < 8; i++) r[i] = vmul(vfma(hi2f(kk[i + 3]), a0, vfma(hi2f(kk[i + 2]), a1, vfma(hi2f(kk[i + 1]), a2, vmul(hi2f(kk[i]), a3)))), hi2f(vv[i]));
    gst128(u32x4{pk2(r[7], r[6]), pk2(r[5], r[4]), pk2(r[3], r[2]), pk2(r[1], r[0])}, go, dst + 4096);
  }
}

template <int NR> DEVI void conv_mfma(u32 cs, int rho0, int lane, f32x4 (&acc)[8]) {
  const int g = lane >> 4, l15 = lane & 15;
  u32 aa = cs + (l15 & 3) * KC_STR + 2 * (64 * (l15 >> 2) + 8 * g) + 64 * rho0;
  u32 ba = opaque(cs + CS_U + 2 * (1852 - 1792 - 4 * l15 + 8 * g) + 64 * rho0), bb = opaque(ba + 8);
  u32x4 A[8], G[8];
  sfor<5>([&](auto J) { constexpr int p = J < 4 ? J + 2 : 1; A[p] = lds128(aa + p * 512); G[p] = cat(lds64(ba + (7 - p) * 512), lds64(bb + (7 - p) * 512)); SCHED(); });
#pragma unroll 1
  for (int rho = 0; rho < NR; rho++) {
    constexpr int pa[36] = {2, 2, 3, 3, 2, 4, 3, 4, 2, 5, 1, 1, 2, 1, 3, 1, 4, 1, 5, 1, 6, 2, 0, 3, 0, 4, 0, 5, 0, 1, 0, 6, 0, 7, 0, 0};
    constexpr int pe[36] = {2, 3, 2, 3, 4, 2, 4, 3, 5, 2, 1, 2, 1, 3, 1, 4, 1, 5, 1, 6, 1, 0, 2, 0, 3, 0, 4, 0, 5, 0, 1, 0, 6, 0, 7, 0};
    constexpr int rdc[36] = {18, 19, 20, 21, 22, 23, 0, 1, 2, -1, -1, -1, -1, -1, -1, -1, -1, -1, -1, -1, -1, 6, 7, 8, 9, 10, 11, 12, 13, 14, 15, 16, 17, 3, 4, 5};
    sfor<36>([&](auto I) {
      constexpr int i = I, r = rdc[i], x = r / 3;
      if constexpr (i == 35) __builtin_amdgcn_s_setprio(CMPRIO);
      if constexpr (i == 9) { aa = opaque(aa + 64); ba = opaque(ba + 64); bb = opaque(bb + 64); }
      acc[pa[i] + pe[i]] = mfma16(A[pa[i]], G[pe[i]], acc[pa[i] + pe[i]]);
      if constexpr (r >= 0) {
        if constexpr (r % 3 == 0) A[x] = lds128(aa + x * 512);
        else if constexpr (r % 3 == 1) { const u32x2 t = lds64(ba + (7 - x) * 512); G[x].x = t.x; G[x].y = t.y; }
        else { const u32x2 t = lds64(bb + (7 - x) * 512); G[x].z = t.x; G[x].w = t.y; }
      }
      SCHED();
    });
  }
  SCHED();
}
DEVI void conv_out(f32x4 y, u32x2 u, float Dv, u32 off, char* ydst) {
  const float y0 = vfma(hi2f(u.y), Dv, y[0]), y1 = vfma(lo2f(u.y), Dv, y[1]), y2 = vfma(hi2f(u.x), Dv, y[2]), y3 = vfma(lo2f(u.x), Dv, y[3]);
  gst64(u32x2{pk2(y0, y1), pk2(y2, y3)}, off, ydst);
}

DEVI float xhalf_max(float m) { const u32 mu = f2u(m); auto r = __builtin_amdgcn_permlane32_swap(mu, mu, false, false); return fmaxf(u2f(r[0]), u2f(r[1])); }

extern "C" __global__ __launch_bounds__(768, 3) void gm_main(const u16* __restrict__ x, const u16* __restrict__ wqkv, char* __restrict__ ws,
                                                             const u16* __restrict__ Bk, const u16* __restrict__ Bv, const u16* __restrict__ shk,
                                                             const u16* __restrict__ ssmk, const u16* __restrict__ wo, const u16* __restrict__ ssmD,
                                                             const u16* __restrict__ gw, u16* __restrict__ out) {
  extern __shared__ __attribute__((aligned(16))) char smem[];
  const u32 sb = (u32)(size_t)(LAS char*)smem;
  const int b = blockIdx.x, xcd = b & 7, idx = b >> 3;
  const int lane = threadIdx.x & 63;
  const u32 wv = rfl(threadIdx.x >> 6);
  const bool spare = idx >= 28;
  char* const fb = ws + WS_F;
  const int qb = ((idx >> 2) + 1) & 7, h = 4 * xcd + (idx & 3);
  const int chb = wv >= 8 ? 64 * xcd + 2 * idx : 512 + 2 * h;
  u32x2 pre[2], fv; u32x4 cur[2];
  const u32 fo = F_U + ((chb >> 1) * 32 + 24 - 8 * (wv & 3) + (lane & 7)) * 8;
  u32 pof = 0; bool pon = false;
  STAMP(0);
  {
    const int hg = xcd & 1, ri = idx & 3, rb = 4 * (xcd >> 1) + ri, jt = idx >> 2;
    const int r8 = lane >> 3, c8 = lane & 7;
    const int rot = ((0x124924u >> (2 * (jt + ri))) & 3) * 3;
    const u32 xo = opaque((32 * wv + r8) * 1152 + 16 * (c8 ^ r8));
    asm volatile("" ::"s"(x), "s"(wqkv), "s"(Bk), "s"(Bv));
    if (wv < 4 && !spare) {
      const u32 lw = wv;
      const char* xb = (const char*)(x + (size_t)rb * 128 * 576);
      sfor<4>([&](auto I) { dma16(xb + rot * 128 + I * 9216, xo, sb + (4 * lw + I) * 1024); });
      u32 wof[2];
#pragma unroll
      for (int i = 0; i < 2; i++) {
        const int ch = (lane & 15) ^ (((lane >> 4) | (i << 2)) << 1), ln = 14 * hg + 2 * jt + (ch >> 3);
        wof[i] = ((lane >> 4) + 16 * lw) * 3456 + 128 * (ln > 26 ? 26 : ln) + 16 * (ch & 7);
      }
      const char* wb = (const char*)wqkv;
      auto issue = [&](int c) {
        const u32 sl = sb + (c & 3) * L_SLOT;
        c += rot; c = c >= 9 ? c - 9 : c;
        sfor<4>([&](auto I) { dma16(xb + c * 128 + I * 9216, xo, sl + (4 * lw + I) * 1024); });
        sfor<4>([&](auto I) { dma16(wb + (64 * c + 4 * I) * 3456, wof[I >> 1], sl + L_WOFF + (4 * lw + I) * 1024); });
      };
      sfor<4>([&](auto I) { dma16(wb + (64 * rot + 4 * I) * 3456, wof[I >> 1], sb + L_WOFF + (4 * lw + I) * 1024); });
      prep_issue(Bk, Bv, 64 * idx, 64 * xcd, sb + L_PT, lw, lane);
#pragma unroll 1
      for (int n = 1; n < 8; n++) { issue(n); wait_vm<8>(); bar(); }
      issue(8);
      wait_vm<8>(); bar();
      __builtin_amdgcn_s_sleep(2);
      {
        const char* wp = (const char*)wo + (idx == 27 ? 663552 - 24576 : idx * 23712) + lw * 6144;
        sfor<6>([&](auto I) { dma16(wp + I * 1024, lane * 16, sb + L_DUMMY); });
      }
      wait_vm<6>(); bar();
      bar();
    } else if (wv >= 8 || (spare && wv >= 4)) {
      const bool ex = wv < 8;
      const int row0 = ex ? 256 * xcd + 64 * ri : 64 * idx, col = ex ? 512 : 64 * xcd;
      const u32 tb = sb + (ex ? 0 : L_PT);
      u32x2 e;
      asm volatile("global_load_dwordx2 %0, %1, %2 sc1" : "=v"(e) : "v"(F_EP), "s"(fb));
      u32x4 sh;
      gl128<0>(sh, 8 * col + 16 * (8 * (wv & 3) + r8), shk);
      if (spare) prep_issue(Bk, Bv, row0, col, tb, wv & 3, lane);
      {
        const u32 o = (threadIdx.x & 255) * 16, op = o ? o - 8 : 0;
        sfor<2>([&](auto J) { const u16* kp = ssmk + (size_t)(chb + J) * 2048; gl128<0>(cur[J], o, kp); gl64<0>(pre[J], op, kp); });
      }
      {
        const int t3 = (4 * xcd + (idx & 3)) * 8 + (idx >> 2), k = wv & 3;
        const u32 mo = k == 0 ? F_1 + (h * 8 + qb) * 512 : k == 1 ? F_O + t3 * 512 : k == 2 ? F_Y + t3 * 640 : F_U + (chb >> 1) * 256;
        asm volatile("s_mov_b32 m0, %2\n\ts_nop 0\n\tglobal_load_lds_dword %0, %1 sc1" ::"v"(mo + (lane < (k == 3 ? 4 : k == 2 ? 10 : 8) ? lane : 0) * 64), "s"(fb), "s"(sb + L_DUMMY) : "memory");
      }
      if (wv == 8) st32(sb + L_FL + 4 * lane, 0u);
      if (spare) wait_vm<5>();
      bar();
      asm volatile("s_waitcnt vmcnt(5)" : "+v"(e), "+v"(sh)::"memory");
      e.x += 1u; e.y += e.x == 0u;
      if (wv == 8) st64(sb + W_TAG, e);
      prep_compute(row0, col, tb, wv & 3, lane, sh, ws, [&]() { if (!spare) { SCHED(); bar(); SCHED(); } });
      wait_lgkm();
      bar();
      if (!spare) sfor<PUBB>([&](auto) { bar(); });
      asm volatile("s_waitcnt vmcnt(0)" : "+v"(cur[0]), "+v"(cur[1]), "+v"(pre[0]), "+v"(pre[1])::"memory");
      __builtin_amdgcn_s_sleep(PUBD);
      publish(fb, F_U + (((col >> 1) + 8 * (wv & 3) + lane) * 32 + (row0 >> 6)) * 8, e, lane < 8);
      if (!spare) sfor<8 - NMID - PUBB - FLB>([&](auto) { bar(); });
      else if (wv >= 8) __builtin_amdgcn_s_sleep(SPDELAY);
      else __builtin_amdgcn_s_sleep(16);
      asm volatile("global_load_dwordx2 %0, %1, %2 sc1" : "=v"(fv) : "v"(fo), "s"(fb));
      if (!spare) sfor<FLB>([&](auto) { bar(); });
    } else if (spare) {
      CODE_PF("gm_main", "gm_end", 4 * ri + wv, 16, sb + L_DUMMY);
      bar(); bar();
    } else {
      const int cw = wv - 4, g = lane >> 4, l15 = lane & 15, wm = cw & 1, wn = cw >> 1;
      const u32 xra = sb + (64 * wm + l15) * 128 + 16 * (g ^ (l15 & 7));
      const u32 xb[2][2] = {{opaque(xra), opaque(xra + 2 * L_SLOT)}, {opaque(xra ^ 64), opaque((xra ^ 64) + 2 * L_SLOT)}};
      u32x4 wf[2][4], xf[2][4];
      f32x4 acc[4][4];
      const int ln = 14 * hg + 2 * jt + wn;
      {
        const int r64 = 2 * rb + wm, seg = ln / 9, lc = ln - 9 * seg, hc = (64 * lc) / 18 + (lane >> 2), li = lane & 1, nx = (lane >> 1) & 1;
        pon = ln == 27 ? lane < 16 : (lane < 20 && hc < 32 && ((18 * hc + 17 * li) >> 6) == lc && (!nx || (seg > 0 && r64 < 28)));
        pof = opaque(ln == 27 ? F_Y + (((4 * (r64 >> 2) + 2 * (r64 & 1)) * 8 + lane) * 80 + 72 + ((r64 >> 1) & 1)) * 8 : F_1 + ((hc * 8 + (r64 >> 2) + nx) * 64 + seg * 16 + li * 8 + (r64 & 3) + (nx ? 0 : 4)) * 8);
      }
      __builtin_amdgcn_s_setprio(2);
      if (ln == 27) {
        dma16(gw, wm * 128 + lane * 16, sb + L_GW + wm * 128);
        wait_vm<0>();
#pragma unroll
        for (int i = 0; i < 4; i++) acc[i][0] = f32x4{0.f, 0.f, 0.f, 0.f};
#pragma unroll 1
        for (int c = 0; c < 9; c++) {
          bar();
          const int ca = c + rot >= 9 ? c + rot - 9 : c + rot;
#pragma unroll
          for (int ks = 0; ks < 2; ks++) {
            const u32x4 gv = lds128(sb + L_GW + ca * 128 + ks * 64 + 16 * g);
#pragma unroll
            for (int i = 0; i < 4; i++) acc[i][0] = mfma16(gv, lds128(xb[ks][0] + (c & 3) * L_SLOT + 2048 * i), acc[i][0]);
          }
          wait_lgkm();
        }
        bar();
        const float p = rbf(g == 0 ? acc[0][0][0] : g == 1 ? acc[1][0][0] : g == 2 ? acc[2][0][0] : acc[3][0][0]);
        gst32(f2u(rbf(1.f / (1.f + __expf(-p)))), lane * 4, ws + WS_G + (rb * 128 + 64 * wm) * 4);
      } else {
    u32 wb[4][2];
    {
      const int q = l15 >> 2, p = lane & 3, f = q | ((g & 1) << 2);
#pragma unroll
      for (int nt = 0; nt < 4; nt++) {
        const u32 a = sb + L_WOFF + (8 * g + q) * 256 + (((8 * wn + 2 * nt + (p >> 1)) ^ (f << 1)) << 4) + 8 * (p & 1);
        wb[nt][0] = opaque(a); wb[nt][1] = opaque(a + 2 * L_SLOT);
      }
    }
    auto rd = [&](auto KS, auto C) {
      constexpr int ks = KS, c = C, so = (c & 1) * L_SLOT, hi = (c >> 1) & 1;
      sfor<4>([&](auto I) {
        xf[ks][I] = lds128(xb[ks][hi] + so + 2048 * I);
        wf[ks][I] = cat(tr_read(wb[I][hi] + so + ks * 8192), tr_read(wb[I][hi] + so + ks * 8192 + 1024));
      });
    };
    auto mm = [&](auto KS, auto FIRST) {
      constexpr int ks = KS;
      sfor<16>([&](auto I) {
        constexpr int mo[16] = {0, 1, 0, 1, 2, 2, 0, 1, 2, 3, 3, 3, 0, 1, 2, 3}, no[16] = {0, 0, 1, 1, 0, 1, 2, 2, 2, 0, 1, 2, 3, 3, 3, 3};
        constexpr int mt = mo[I], nt = no[I];
        acc[mt][nt] = mfma16(xf[ks][mt], wf[ks][nt], FIRST ? f32x4{0.f, 0.f, 0.f, 0.f} : acc[mt][nt]);
      });
    };
    auto ilv = [&]() {
      sfor<12>([&](auto) { __builtin_amdgcn_sched_group_barrier(0x8, 1, 0); __builtin_amdgcn_sched_group_barrier(0x100, 1, 0); });
      __builtin_amdgcn_sched_group_barrier(0x8, 4, 0);
    };
    const float sc = ln < 9 ? QSCALE : 1.f;
    char* qo = ws + WS_QKV + (size_t)((2 * rb + wm) * 1728 + 64 * ln) * 128;
    SCHED();
    bar();
    STAMP(1);
    rd(IC(0), IC(0));
    sfor<9>([&](auto C) {
      constexpr int c = C;
      SCHED();
      rd(IC(1), C); mm(IC(0), IC(c == 0)); ilv();
      SCHED();
      if constexpr (c == 8) wait_lgkm();
      bar();
      SCHED();
      if constexpr (c < 8) { rd(IC(0), IC(c + 1)); mm(IC(1), IC(0)); ilv(); }
    });
    STAMP(2);
    const u32 stg = sb + L_STG + cw * 9216;
    const u32 sa = stg + l15 * 144 + 8 * g, ra = stg + r8 * 144 + 16 * c8, go = lane * 16;
    u32x4 t[4][2];
    sfor<6>([&](auto NT) {
      constexpr int nt = NT;
      if constexpr (nt < 4) sfor<4>([&](auto M) { acc[M][nt] = mfma16(xf[1][M], wf[1][nt], acc[M][nt]); });
      SCHED();
      if constexpr (nt > 0 && nt < 5) {
        sfor<4>([&](auto M) {
          const f32x4 v = acc[M][nt - 1] * sc;
          st64(sa + (nt - 1) * 2304 + 32 * M, u32x2{pk2(v[0], v[1]), pk2(v[2], v[3])});
        });
        t[nt - 1][0] = lds128(ra + (nt - 1) * 2304); t[nt - 1][1] = lds128(ra + (nt - 1) * 2304 + 1152);
      }
      if constexpr (nt > 1) { gst128(t[nt - 2][0], go, qo + (nt - 2) * 2048); gst128(t[nt - 2][1], go, qo + (nt - 2) * 2048 + 1024); }
      SCHED();
    });
      }
      STAMP(8);
    }
  }
  const int rt = 4 * xcd + (idx & 3), ct = idx >> 2;
  auto wo_load = [&](int r0, int r1) {
    const int r6 = lane / 10, c10 = lane - 10 * r6;
    const u32 vo = (6 * (wv & 3) + r6) * 1152 + 16 * (c10 > 8 ? 8 : c10);
    const char* wb = (const char*)(wo + 72 * ct);
#pragma unroll 4
    for (int i = r0; i < r1; i++) dma16n<60>(wb + i * 24 * 1152, vo, sb + L_W + (4 * i + (wv & 3)) * 960);
  };
  const u32x2 tag = lds64(sb + W_TAG);
  const int cw = wv & 3;
  u32 ypub = 0;
  if (wv >= (spare ? 4u : 8u)) {
    const int tid = threadIdx.x & 255;
    const u32 cb = sb + L_C + (wv >= 8 ? 0 : 2 * CS_SZ);
    __builtin_amdgcn_s_sleep(CVDELAY);
    asm volatile("s_waitcnt vmcnt(0)" : "+v"(fv)::"memory");
    if (__builtin_amdgcn_ballot_w64(fv.x != tag.x || fv.y != tag.y)) await<POLL_SLEEP>(fb, fo, tag);
    STAMP(2);
    sfor<2>([&](auto J) { dma16c(ws + WS_UR + (size_t)(chb + J) * 4096, tid * 16, cb + CS_U + cw * 1024 + J * CS_SZ); });
    const u32 dv = *(const __attribute__((address_space(4))) u32*)(ssmD + chb);
    {
      const u32 z0 = opaque(cw * KC_STR + 16 * lane), z1 = opaque(CS_U + 4096 + 16 * (lane & 7)), za = cb + (lane < 24 ? z0 : z1);
      st128(za, u32x4{0u, 0u, 0u, 0u}); st128(za + CS_SZ, u32x4{0u, 0u, 0u, 0u});
    }
    f32x4 acc[8];
#pragma unroll
    for (int c = 0; c < 8; c++) { acc[c] = f32x4{0.f, 0.f, 0.f, 0.f}; asm volatile("" : "+v"(acc[c])); }
#pragma unroll
    for (int j = 0; j < 2; j++) {
      const u32 cs = cb + j * CS_SZ + 384 + 16 * tid;
      const u32 w0 = tid ? pre[j].x : 0u, w1 = tid ? pre[j].y : 0u, w2 = cur[j].x, w3 = cur[j].y, w4 = cur[j].z, w5 = cur[j].w;
      const u32 a01 = __builtin_amdgcn_alignbyte(w1, w0, 2), a12 = __builtin_amdgcn_alignbyte(w2, w1, 2), a23 = __builtin_amdgcn_alignbyte(w3, w2, 2),
                a34 = __builtin_amdgcn_alignbyte(w4, w3, 2), a45 = __builtin_amdgcn_alignbyte(w5, w4, 2);
      st128(cs, u32x4{a01, a12, a23, a34});
      st128(cs + KC_STR, u32x4{w1, w2, w3, w4});
      st128(cs + 2 * KC_STR, u32x4{a12, a23, a34, a45});
      st128(cs + 3 * KC_STR, cur[j]);
    }
    STAMP(1);
    wait_vm<0>();
    prog_set(sb + W_XCH + 4 * wv, 1);
    const int tm = cw >> 1, pi = cw & 1;
    const u32 cs = cb + tm * CS_SZ;
    prog_wait(sb + W_XCH + 4 * (wv & 12) + 4 * (lane & 3), 1);
    STAMP(4);
    conv_mfma<4>(cs, 4 * pi, lane, acc);
    STAMP(5);
    __builtin_amdgcn_s_setprio(CPRIO);
    if (spare && wv >= 8) prog_wait(sb + W_XCH + 16 + 8 * tm + 4 * (lane & 1), 2);
    const u32 rA = (spare && wv >= 8 ? sb + L_C + (2 + tm) * CS_SZ : sb + L_R + tm * 8192) + lane * 16;
    if (pi) { sfor<4>([&](auto J) { st128(rA + 4096 + J * 1024, __builtin_bit_cast(u32x4, acc[J])); acc[J] = acc[4 + J]; }); }
    else sfor<4>([&](auto J) { st128(rA + J * 1024, __builtin_bit_cast(u32x4, acc[4 + J])); });
    u32x2 uu[4];
#pragma unroll
    for (int j = 0; j < 4; j++) uu[j] = lds64(cs + CS_U + 4088 - 8 * lane - 512 * (4 * pi + j));
    prog_set(sb + W_XCH + 4 * wv, 2);
    prog_wait(sb + W_XCH + 4 * (wv ^ 1), 2);
    f32x4 t[4];
#pragma unroll
    for (int j = 0; j < 4; j++) t[j] = __builtin_bit_cast(f32x4, lds128(rA + (pi ^ 1) * 4096 + j * 1024));
    wait_lgkm();
    prog_set(sb + W_XCH + 4 * wv, 3);
    const u32 yo = ((lane >> 3) & 3) * 73728 + (lane >> 5) * 64 + (lane & 7) * 8;
    char* yd = ws + WS_Y2 + (chb + tm) * 128 + pi * 1179648;
#pragma unroll
    for (int j = 0; j < 4; j++) conv_out(vadd4(acc[j], t[j]), uu[j], tm ? hi2f(dv) : lo2f(dv), yo + j * 294912, yd);
    STAMP(8);
    ypub = F_Y + (((16 * pi + lane) * 8 + (chb + tm) / 72) * 80 + (chb + tm) % 72) * 8;
    if (wv < 8) publish(fb, ypub, tag, lane < 16);
  }
  if (wv < 8) {
    __builtin_amdgcn_s_setprio(1);
    const int c = lane & 31, hh = lane >> 5, q4 = (lane & 15) >> 2;
    const int w = spare ? wv ^ 4 : wv;
    const u32 kcol = 64 * w + (lane & 16) * 2 + 16 * (lane & 1) + 4 * (lane & 2);
    const u32 ka = opaque(sb + L_K + (8 * hh + q4) * 1088 + kcol), ka2 = opaque(sb + L_K + (16 + q4) * 1088 + kcol);
    const u32 va = opaque(sb + L_V + c * 1040 + 64 * w + 16 * hh);
    const u32 qa = opaque(sb + L_Q + q4 * 512 + 64 * w + (lane & 16) * 2 + 8 * (lane & 3)), qa0 = opaque(qa + 4096 * hh);
    const int thr = c - 8 * hh;
    const u32 lom = opaque(hh ? 0u : ~0u);
    u32 msk[8], nmk[8];
    unsigned long long cnd[16];
#pragma unroll
    for (int e = 0; e < 16; e++) cnd[e] = __builtin_amdgcn_ballot_w64(16 * (e >> 3) + (e & 7) <= thr);
#pragma unroll
    for (int j = 0; j < 8; j++) { const int k0 = 16 * (j >> 2) + 2 * (j & 3); msk[j] = opaque(k0 + 1 <= thr ? 0xffffffffu : (k0 <= thr ? 0xffffu : 0u)); nmk[j] = opaque(~msk[j]); }
    f32x16 S0;
#pragma unroll
    for (int e = 0; e < 16; e++) S0[e] = u2f(opaque(spare ? 0xff800000u : 0u));
    const u32 oo = opaque((c >> 4) * 18432 + (c & 15) * 16 + hh * 256);
    const u32 fl = sb + W_PROG + 4 * (spare ? (lane & 3) : (lane & 7));
    if (wv < 3) st128(sb + (wv < 2 ? L_K + (18 + wv) * 1088 : L_V + 18 * 1040) + lane * 16, u32x4{0x3f803f80u, 0x3f803f80u, 0x3f803f80u, 0x3f803f80u});
    SCHED();
    if (wv >= 4 && !spare) publish(fb, pof, tag, pon);
    STAMP(3);
    if (wv == (spare ? 0u : (u32)POLLW)) {
      __builtin_amdgcn_s_sleep(QDELAY);
      await<POLL_FAST>(fb, F_1 + ((h * 8 + qb) * 64 + ((lane < 48 ? lane : lane - 16) | (lane < 16 || spare ? 4 : 0))) * 8, tag);
      prog_set(sb + W_GO, 1);
    } else prog_wait(sb + W_GO, 1);
    STAMP(4);
    __builtin_amdgcn_s_setprio(BAL ? 3 : 1);
    if (!spare || wv < 4) {
    const char* Ag = ws + WS_QKV + (size_t)(4 * qb * 1728 + 18 * h) * 128;
    const char* Kg = Ag + 576 * 128 - (qb ? 884736 : 0);
    const u32 lo = ((lane >> 3) & (qb ? 7 : 3)) * 221184 + (lane & 7) * 16, lq = ((lane >> 3) & 3) * 221184 + (lane >> 5) * 128 + (lane & 7) * 16;
#pragma unroll 1
    for (int s = wv; s <= w; s += 4) {
      dma16c(Ag + s * 256, lq, sb + L_Q + s * 1024);
      dma16c(Kg + s * 128, lo, sb + L_K + s * 1088); dma16c(Kg + s * 128 + 1024, lo, sb + L_K + (8 + s) * 1088);
      const char* eg = s < 4 ? Kg + (s < 2 ? 16 + s : 590 + s) * 128 : Ag + 2048;
      dma16c(eg, s < 4 ? lo : lq, sb + (s < 2 ? L_K + (16 + s) * 1088 : (s < 4 ? L_V + (14 + s) * 1040 : L_Q + 8192)));
    }
    __builtin_amdgcn_s_sleep(VDELAY);
    if (spare) { dma16c(Kg + (572 + w) * 128, lo, sb + L_V + (w - 4) * 1040); dma16c(Kg + (580 + w) * 128, lo, sb + L_V + (4 + w) * 1040); }
    dma16c(Kg + (576 + w) * 128, lo, sb + L_V + w * 1040); dma16c(Kg + (584 + w) * 128, lo, sb + L_V + (8 + w) * 1040);
    prog_set(sb + W_PROG + 4 * wv, 1);
    wait_vm<2>();
    prog_set(sb + W_PROG + 4 * wv, 2);
    }
    prog_wait(fl, 2);
    STAMP(5);
    struct KF { u32x4 a; u32x2 b; };
    struct VF { u32x4 a, b; };
    auto ldk = [&](int i) { KF k; k.a = cat(tr_read(ka + i * 64), tr_read(ka + 4352 + i * 64)); k.b = tr_read(ka2 + i * 64); return k; };
    auto ldv = [&](int i) { VF v; v.a = lds128(va + i * 64); v.b = lds128(va + i * 64 + 32); return v; };
    auto pk8 = [&](const f32x16& p, int o) { return u32x4{pk2(p[o], p[o + 1]), pk2(p[o + 2], p[o + 3]), pk2(p[o + 4], p[o + 5]), pk2(p[o + 6], p[o + 7])}; };
    const int ib = spare ? 8 - w : 1;
    const u32x4 q0 = cat(tr_read(qa0), tr_read(qa0 + 2048));
    const KF k8 = ldk(8);
    u32x2 q1 = tr_read(qa + 8192);
    const KF k0 = ldk(0);
    SCHED();
    q1.x &= lom; q1.y = 0u;
    auto qk = [&](const KF& k) { f32x16 z = {}; z = mfma32(k.a, q0, z); return mfma32k8(k.b, q1, z); };
    f32x16 Sm = {};
    Sm = mfma32(k8.a, q0, Sm); S0 = mfma32(k0.a, q0, S0);
    Sm = mfma32k8(k8.b, q1, Sm); S0 = mfma32k8(k0.b, q1, S0);
    KF kf[2]; VF vf[2], v8, v0;
    kf[1] = ldk(7); kf[0] = ldk(6);
    SCHED();
#pragma unroll
    for (int e = 0; e < 16; e++) Sm[e] = __builtin_amdgcn_inverse_ballot_w64(cnd[e]) ? Sm[e] : S0[e];
    float m = __builtin_fmaxf(__builtin_fmaxf(Sm[0], Sm[1]), Sm[2]), m2 = __builtin_fmaxf(__builtin_fmaxf(Sm[3], Sm[4]), Sm[5]);
#pragma unroll
    for (int e = 6; e < 14; e += 4) { m = fmaxf(fmaxf(m, Sm[e]), Sm[e + 1]); m2 = fmaxf(fmaxf(m2, Sm[e + 2]), Sm[e + 3]); }
    m = fmaxf(fmaxf(m, Sm[14]), Sm[15]);
    m = xhalf_max(fmaxf(m, m2));
    f32x16 O;
    float l;
#pragma unroll 1
    for (int pass = 0; pass < 2; pass++) {
      { const float r1 = lo2f(pk2(m, m)) - m; q1.y = hh ? pk2(r1 - lo2f(pk2(r1, r1)), 0.f) : pk2(-m, r1); }
      f32x16 S[2];
      {
        f32x16 p = mfma32k8(kf[1].b, u32x2{0u, q1.y}, Sm);
        S[1] = qk(kf[1]);
        SCHED();
        wait_vm<0>();
        prog_set(sb + W_PROG + 4 * wv, 3);
#pragma unroll
        for (int e = 0; e < 8; e++) p[e] = __builtin_amdgcn_exp2f(p[e]);
        SCHED();
        const u32 vfl = lds_peek(fl);
        SCHED();
#pragma unroll
        for (int e = 8; e < 16; e++) p[e] = __builtin_amdgcn_exp2f(p[e]);
        SCHED();
        if (__builtin_amdgcn_ballot_w64(vfl < 3u)) prog_wait(fl, 3);
        asm volatile("" ::: "memory");
        v8 = ldv(8); v0 = ldv(0); vf[1] = ldv(7);
        SCHED();
        const u32x4 pa = pk8(p, 0), pb = pk8(p, 8);
        O = f32x16{};
        O = mfma32(v8.a, pa & u32x4{msk[0], msk[1], msk[2], msk[3]}, O); O = mfma32(v8.b, pb & u32x4{msk[4], msk[5], msk[6], msk[7]}, O);
        O = mfma32(v0.a, pa & u32x4{nmk[0], nmk[1], nmk[2], nmk[3]}, O); O = mfma32(v0.b, pb & u32x4{nmk[4], nmk[5], nmk[6], nmk[7]}, O);
      }
      auto tile = [&](auto self, auto I) -> void {
        constexpr int i = I;
        if (i < ib) return;
        SCHED();
        f32x16& Sc = S[i & 1];
        f32x16 z = {};
        if constexpr (i > 1) z = mfma32(kf[(i - 1) & 1].a, q0, z);
        if constexpr (i > 2) kf[i & 1] = ldk(i - 2);
        if constexpr (i > 1) vf[(i - 1) & 1] = ldv(i - 1);
        SCHED();
#pragma unroll
        for (int e = 0; e < 8; e++) Sc[e] = __builtin_amdgcn_exp2f(Sc[e]);
        SCHED();
        if constexpr (i > 1) S[(i - 1) & 1] = mfma32k8(kf[(i - 1) & 1].b, q1, z);
        const u32x4 pa = pk8(Sc, 0);
        SCHED();
#pragma unroll
        for (int e = 8; e < 12; e++) Sc[e] = __builtin_amdgcn_exp2f(Sc[e]);
        SCHED();
        O = mfma32(vf[i & 1].a, pa, O);
#pragma unroll
        for (int e = 12; e < 16; e++) Sc[e] = __builtin_amdgcn_exp2f(Sc[e]);
        const u32x4 pb = pk8(Sc, 8);
        SCHED();
        O = mfma32(vf[i & 1].b, pb, O);
        if constexpr (i > 1) self(self, IC(i - 1));
      };
      tile(tile, IC(7));
      SCHED();
      { const u32 lu = f2u(O[10]); auto r = __builtin_amdgcn_permlane32_swap(lu, lu, false, false); l = u2f(r[0]); }
      if (pass == 1 || __all(l > 8.67e-19f && l < 1.15e18f)) break;
      q1.y = 0u;
#pragma unroll 1
      for (int i = ib; i < 8; i++) {
        const f32x16 s = qk(ldk(i));
#pragma unroll
        for (int e = 0; e < 16; e += 2) m = fmaxf(fmaxf(m, s[e]), s[e + 1]);
      }
      m = xhalf_max(m);
      kf[1] = ldk(7); kf[0] = ldk(6);
    }
    STAMP(6);
    float inv = __builtin_amdgcn_rcpf(l);
    asm volatile("s_nop 1" : "+v"(inv));
    char* Og = ws + WS_O + (size_t)(16 * qb + 2 * w) * 18432;
    gst128(u32x4{pk2(vmul(O[0], inv), vmul(O[1], inv)), pk2(vmul(O[2], inv), vmul(O[3], inv)), pk2(vmul(O[4], inv), vmul(O[5], inv)), pk2(vmul(O[6], inv), vmul(O[7], inv))},
           oo, Og + (h >> 1) * 1024 + (h & 1) * 512);
    if (hh == 0) gst32(pk2(vmul(O[8], inv), vmul(O[9], inv)), (c >> 4) * 18432 + (c & 15) * 4, Og + 16384 + h * 64);
    publish(fb, F_O + (((4 * qb + (w & 3)) * 8 + lane) * 64 + (w >> 2) * 32 + h) * 8, tag, lane < 8);
    {
      __builtin_amdgcn_s_setprio(0);
      const int g = lane >> 4;
      prog_wait(sb + W_XCH + 32 + 4 * (lane & 3), 4);
      const u32 wa2 = sb + L_W + ((4 * g + (q4 >> 1)) * 18 + 16 + (q4 & 1)) * 160 + 8 * (lane & 3);
      u32x4 wx[2], wr[10];
      sfor<2>([&](auto J) { const int i = J ? 8 + (wv & 1) : wv; const u32 a = wa2 + (i / 5) * 46080 + (i % 5) * 32; wx[J] = cat(tr_read(a), tr_read(a + 5760)); });
      prog_set(sb + W_PROG + 4 * wv, 4);
      const u32 wa = sb + L_W + wv * 11520 + (18 * (g >> 1) + 4 * (g & 1) + q4) * 160 + 8 * (lane & 3), wd = sb + L_W + wv * 11520 + lane * 16;
      sfor<10>([&](auto I) { const u32 a = wa + (I / 5) * 5760 + (I % 5) * 32; wr[I] = cat(tr_read(a), tr_read(a + 1280)); });
      prog_wait(sb + W_PROG + 4 * (lane & 7), 4);
      sfor<2>([&](auto J) { const int i = J ? 8 + (wv & 1) : wv; st128(sb + L_Q + i * 1024 + lane * 16, wx[J]); });
      sfor<10>([&](auto I) { st128(wd + (I / 5) * 5760 + (I % 5) * 1024, wr[I]); });
      prog_set(sb + W_PROG + 4 * wv, 5);
    }
      if (b == 255 && wv == 4) {
#pragma unroll 1
        for (int spin = 0; spin < (1 << 18); spin++) {
          u32x4 v[9];
          sfor<9>([&](auto I) { gl128c<0>(v[I], (lane >> 4) * 2048 + (lane & 15) * 16, fb + F_U + I * 8192); });
          asm volatile("s_waitcnt vmcnt(0)" : "+v"(v[0]), "+v"(v[1]), "+v"(v[2]), "+v"(v[3]), "+v"(v[4]), "+v"(v[5]), "+v"(v[6]), "+v"(v[7]), "+v"(v[8])::"memory");
          u32 bad = 0;
#pragma unroll
          for (int i = 0; i < 9; i++) bad |= (v[i].x ^ tag.x) | (v[i].y ^ tag.y) | (v[i].z ^ tag.x) | (v[i].w ^ tag.y);
          if (!__builtin_amdgcn_ballot_w64(bad != 0)) break;
          __builtin_amdgcn_s_sleep(16);
        }
        if (lane == 0) gst64(tag, F_EP, fb);
      }
    STAMP_END();
    return;
  }
  {
    const int g = lane >> 4, l15 = lane & 15, q = l15 >> 2;
    const int r7 = lane / 9, c9 = lane - 9 * r7;
    const u32 xw = sb + L_X + cw * 2304;
    const int m16 = 16 * (rt >> 2) + 2 * (rt & 3) + 8 * (cw >> 1) + (cw & 1);
    if (__builtin_amdgcn_ballot_w64(lds_peek(sb + W_PROG + 4 * (spare ? (lane & 3) : (lane & 7))) < (u32)WOGATE)) { publish(fb, ypub, tag, lane < 16); ypub = 0; }
    prog_wait(sb + W_XCH + (spare ? 16 + 4 * (lane & 7) : 32 + 4 * (lane & 3)), 3);
    prog_wait(sb + W_PROG + 4 * (spare ? (lane & 3) : (lane & 7)), WOGATE);
    STAMP(9);
    {
      const u16* xp = x + (size_t)(16 * m16) * 576 + 72 * ct;
      dma16n<63>(xp, r7 * 1152 + 16 * c9, xw); dma16n<63>(xp + 7 * 576, r7 * 1152 + 16 * c9, xw + 1008); dma16n<63>(xp + 9 * 576, r7 * 1152 + 16 * c9, xw + 1296);
      wo_load(0, 4);
      wait_vm<7>();
      if (ypub && lane < 16) gst64(tag, ypub, fb);
      wo_load(4, YPOS);
    }
    const u32 yf = F_Y + ((rt * 8 + ct) * 80 + (lane < 18 ? 18 * cw + lane : 72 + (cw >> 1))) * 8;
    u32x2 yv;
    asm volatile("global_load_dwordx2 %0, %1, %2 sc1" : "=v"(yv) : "v"(yf), "s"(fb));
    wo_load(YPOS, 24);
    STAMP(10);
    asm volatile("s_waitcnt vmcnt(%1)" : "+v"(yv) : "i"(24 - YPOS) : "memory");
    if (__builtin_amdgcn_ballot_w64(yv.x != tag.x || yv.y != tag.y)) await<POLL_SLEEP>(fb, yf, tag);
    STAMP(11);
    {
      const char* yp = ws + WS_Y2 + (size_t)(rt * 576 + 72 * ct + 18 * cw) * 128;
      dma16c(yp, lane * 16, sb + L_Y + cw * 2304); dma16c(yp + 1024, lane * 16, sb + L_Y + cw * 2304 + 1024); dma16c(yp + 1280, lane * 16, sb + L_Y + cw * 2304 + 1280);
    }
    u32 gtu;
    gl32c<0>(gtu, (16 * m16 + l15) * 4, ws + WS_G);
    asm volatile("s_waitcnt vmcnt(0)" : "+v"(gtu)::"memory");
    prog_set(sb + W_XCH + 4 * wv, 4);
    prog_wait(sb + W_XCH + 32 + 4 * (lane & 3), 4);
    const float gt = u2f(gtu);
    f32x4 acc[5], tb[5];
    const u32 xa = xw + l15 * 144 + 8 * g;
    {
      const float g1 = rbf(1.f - gt);
      const u32 ya = sb + L_Y + (4 * g + q) * 128 + 32 * cw + 8 * (lane & 3);
      sfor<5>([&](auto NT) {
        const u32x2 y = tr_read(ya + NT * 2048), xv = lds64(xa + NT * 32);
        tb[NT] = f32x4{vmul(vmul(g1, lo2f(xv.x)), lo2f(y.x)), vmul(vmul(g1, hi2f(xv.x)), hi2f(y.x)), vmul(vmul(g1, lo2f(xv.y)), lo2f(y.y)), vmul(vmul(g1, hi2f(xv.y)), hi2f(y.y))};
      });
    }
    const u32 wl = opaque(sb + L_W + lane * 16), wh = opaque(wl + 46080), wq = opaque(sb + L_Q + lane * 16);
    u32x4 wf[2][5];
    auto rd = [&](auto KS) {
      sfor<5>([&](auto NT) {
        constexpr int ks = KS;
        if constexpr (ks < 16) wf[ks & 1][NT] = lds128((ks < 8 ? wl : wh) + (ks & 7) * 5760 + NT * 1024);
        else wf[ks & 1][NT] = lds128(wq + (5 * (ks - 16) + NT) * 1024);
      });
    };
    STAMP(6);
    u32x4 of[16]; u32 oe[8];
    const char* ob = ws + WS_O + (size_t)m16 * 18432;
    {
      const u32 oo = F_O + ((rt * 8 + ct) * 64 + (cw >> 1) * 32 + (lane & 31)) * 8;
      prog_wait(sb + W_PROG + (spare || OGATE == 2 ? 4 * (lane & 7) : 16 * (cw >> 1) + 4 * (lane & 3)), 4);
      __builtin_amdgcn_s_sleep(ODELAY);
      u32 miss = ~0u;
      sfor<4>([&](auto G) {
#pragma unroll 1
        for (int spin = 0; (miss & (0xffu << (8 * G))) && spin < (1 << 20); spin++) {
          u32x2 v;
          asm volatile("global_load_dwordx2 %0, %1, %2 sc1\n\ts_waitcnt vmcnt(0)" : "=&v"(v) : "v"(oo), "s"(fb) : "memory");
          miss = (u32)__builtin_amdgcn_ballot_w64(v.x != tag.x || v.y != tag.y);
          if (miss & (0xffu << (8 * G))) __builtin_amdgcn_s_sleep(POLL_FAST);
        }
        sfor<4>([&](auto F) { gl128c<F * 1024>(of[4 * G + F], lane * 16, ob + G * 4096); });
      });
    }
    STAMP(12);
    __builtin_amdgcn_s_setprio(3);
    sfor<8>([&](auto F) { gl32c<(F >> 2) * 1024 + (F & 3) * 64>(oe[F], 256 * g + 4 * l15, ob + 16384); });
    prog_wait(sb + W_PROG + 4 * (lane & 7), 5);
    rd(IC(0));
    sfor<16>([&](auto KS) {
      constexpr int ks = KS;
      SCHED();
      if constexpr (ks % 2 == 0) asm volatile("s_waitcnt vmcnt(%2)" : "+v"(of[ks]), "+v"(of[ks + 1]) : "i"(22 - ks) : "memory");
      rd(IC(ks + 1));
      sfor<5>([&](auto NT) { acc[NT] = mfma16(wf[ks & 1][NT], of[ks], ks == 0 ? f32x4{0.f, 0.f, 0.f, 0.f} : acc[NT]); });
      sfor<5>([&](auto) { __builtin_amdgcn_sched_group_barrier(0x8, 1, 0); __builtin_amdgcn_sched_group_barrier(0x100, 1, 0); });
    });
    SCHED();
    asm volatile("s_waitcnt vmcnt(0)" : "+v"(oe[0]), "+v"(oe[1]), "+v"(oe[2]), "+v"(oe[3]), "+v"(oe[4]), "+v"(oe[5]), "+v"(oe[6]), "+v"(oe[7])::"memory");
    rd(IC(17));
    sfor<5>([&](auto NT) { acc[NT] = mfma16(wf[0][NT], u32x4{oe[0], oe[1], oe[2], oe[3]}, acc[NT]); });
    sfor<5>([&](auto NT) { acc[NT] = mfma16(wf[1][NT], u32x4{oe[4], oe[5], oe[6], oe[7]}, acc[NT]); });
    SCHED();
    STAMP(13);
    sfor<5>([&](auto NT) {
      constexpr int nt = NT;
      if (nt < 4 || g < 2)
        st64(xa + nt * 32, u32x2{pk2(fmaf(gt, acc[nt][0], tb[nt][0]), fmaf(gt, acc[nt][1], tb[nt][1])), pk2(fmaf(gt, acc[nt][2], tb[nt][2]), fmaf(gt, acc[nt][3], tb[nt][3]))});
    });
    char* orow = (char*)(out + (size_t)(16 * m16) * 576 + 72 * ct);
    u32x4 t[3];
#pragma unroll
    for (int it = 0; it < 3; it++) t[it] = lds128(xw + it * 1008 + lane * 16);
#pragma unroll
    for (int it = 0; it < 3; it++)
      if (lane < (it < 2 ? 63 : 18)) asm volatile("global_store_dwordx4 %0, %1, %2 " OUTPOL "\n\ts_nop 1" ::"v"((7 * it + r7) * 1152 + 16 * c9), "v"(t[it]), "s"(orow) : "memory");
  }
  STAMP_END();
}
extern "C" __global__ void gm_end() {}

extern "C" void kernel_launch(void* const* d_in, const int* in_sizes, int n_in, void* d_out, int out_size, void* d_ws, size_t ws_size,
                              hipStream_t stream) {
  (void)hipFuncSetAttribute((const void*)gm_main, hipFuncAttributeMaxDynamicSharedMemorySize, L_END);
  gm_main<<<256, 768, L_END, stream>>>((const u16*)d_in[0], (const u16*)d_in[1], (char*)d_ws, (const u16*)d_in[4], (const u16*)d_in[5], (const u16*)d_in[6],
                                       (const u16*)d_in[7], (const u16*)d_in[2], (const u16*)d_in[8], (const u16*)d_in[9], (u16*)d_out);
}
```

```cpp
#include <hip/hip_runtime.h>
#include <hip/hip_bf16.h>
#include <utility>

typedef unsigned short u16;
typedef unsigned int u32;
typedef u32 u32x2 __attribute__((ext_vector_type(2)));
typedef u32 u32x4 __attribute__((ext_vector_type(4)));
typedef short s16x4 __attribute__((ext_vector_type(4)));
typedef __bf16 bf16x8 __attribute__((ext_vector_type(8)));
typedef __bf16 bf16x2 __attribute__((ext_vector_type(2)));
typedef float f32x2 __attribute__((ext_vector_type(2)));
typedef float f32x4 __attribute__((ext_vector_type(4)));
typedef float f32x16 __attribute__((ext_vector_type(16)));
#define LAS __attribute__((address_space(3)))
#define GAS __attribute__((address_space(1)))
#define DEVI __device__ __forceinline__
#define IC(n) std::integral_constant<int, (n)>{}

constexpr size_t WS_QKV = 0;
constexpr size_t WS_O   = WS_QKV + 96ull * 2048 * 40;
constexpr size_t WS_UR  = WS_O + 32ull * 2048 * 40;
constexpr size_t WS_Y2  = WS_UR + 576ull * 4096;
constexpr size_t WS_G   = WS_Y2 + 576ull * 4096;
constexpr size_t WS_F   = WS_G + 8192;
constexpr u32 F_EP = 0, F_1 = 1024, F_U = F_1 + 256 * 512, F_O = F_U + 288 * 256, F_Y = F_O + 256 * 512;

template <typename F, int... I> DEVI void sfor_impl(F&& f, std::integer_sequence<int, I...>) { (f(std::integral_constant<int, I>{}), ...); }
template <int N, typename F> DEVI void sfor(F&& f) { sfor_impl(f, std::make_integer_sequence<int, N>{}); }

DEVI u32 pk2(float a, float b) { f32x2 v = {a, b}; return __builtin_bit_cast(u32, __builtin_convertvector(v, bf16x2)); }
DEVI u32 f2u(float a) { return __builtin_bit_cast(u32, a); }
DEVI float u2f(u32 a) { return __builtin_bit_cast(float, a); }
DEVI float lo2f(u32 w) { return __builtin_bit_cast(float, w << 16); }
DEVI float hi2f(u32 w) { return __builtin_bit_cast(float, w & 0xffff0000u); }
DEVI float rbf(float a) { return lo2f(pk2(a, a)); }
DEVI f32x4 mfma16(u32x4 a, u32x4 b, f32x4 c) { return __builtin_amdgcn_mfma_f32_16x16x32_bf16(__builtin_bit_cast(bf16x8, a), __builtin_bit_cast(bf16x8, b), c, 0, 0, 0); }
DEVI f32x16 mfma32(u32x4 a, u32x4 b, f32x16 c) { return __builtin_amdgcn_mfma_f32_32x32x16_bf16(__builtin_bit_cast(bf16x8, a), __builtin_bit_cast(bf16x8, b), c, 0, 0, 0); }
DEVI f32x16 mfma32k8(u32x2 a, u32x2 b, f32x16 c) { return __builtin_amdgcn_mfma_f32_32x32x8bf16_1k(__builtin_bit_cast(s16x4, a), __builtin_bit_cast(s16x4, b), c, 0, 0, 0); }
DEVI u32x2 tr_read(u32 addr) { return __builtin_bit_cast(u32x2, __builtin_amdgcn_ds_read_tr16_b64_v4i16((LAS s16x4*)addr)); }
DEVI u32x4 lds128(u32 addr) { return *(LAS u32x4*)addr; }
DEVI u32x2 lds64(u32 addr) { return *(LAS u32x2*)addr; }
DEVI u32 lds32(u32 addr) { return *(LAS u32*)addr; }
DEVI void st128(u32 addr, u32x4 v) { *(LAS u32x4*)addr = v; }
DEVI void st64(u32 addr, u32x2 v) { *(LAS u32x2*)addr = v; }
DEVI void st32(u32 addr, u32 v) { *(LAS u32*)addr = v; }
DEVI u32x4 cat(u32x2 a, u32x2 b) { return u32x4{a.x, a.y, b.x, b.y}; }
DEVI float vfma(float a, float b, float c) { float d; asm("v_fma_f32 %0, %1, %2, %3" : "=v"(d) : "v"(a), "v"(b), "v"(c)); return d; }
DEVI float vmul(float a, float b) { float d; asm("v_mul_f32 %0, %1, %2" : "=v"(d) : "v"(a), "v"(b)); return d; }
DEVI float vadd(float a, float b) { float d; asm("v_add_f32 %0, %1, %2" : "=v"(d) : "v"(a), "v"(b)); return d; }
DEVI f32x4 vadd4(f32x4 a, f32x4 b) { return f32x4{vadd(a[0], b[0]), vadd(a[1], b[1]), vadd(a[2], b[2]), vadd(a[3], b[3])}; }
DEVI u32 opaque(u32 v) { asm volatile("" : "+v"(v)); return v; }
DEVI void dma16(const void* sbase, u32 voff, u32 ldsbase) {
  asm volatile("s_mov_b32 m0, %2\n\ts_nop 0\n\tglobal_load_lds_dwordx4 %0, %1" ::"v"(voff), "s"(sbase), "s"(ldsbase) : "memory");
}
template <int NL> DEVI void dma16n(const void* sbase, u32 voff, u32 ldsbase) {
  asm volatile("s_mov_b32 m0, %2\n\ts_mov_b32 exec_hi, %3\n\tglobal_load_lds_dwordx4 %0, %1\n\ts_mov_b32 exec_hi, -1" ::"v"(voff), "s"(sbase), "s"(ldsbase),
               "i"((1u << (NL - 32)) - 1u) : "memory");
}
template <int OFF> DEVI void gl128(u32x4& d, u32 voff, const void* sbase) { asm volatile("global_load_dwordx4 %0, %1, %2 offset:%3" : "=v"(d) : "v"(voff), "s"(sbase), "i"(OFF)); }
template <int OFF> DEVI void gl64(u32x2& d, u32 voff, const void* sbase) { asm volatile("global_load_dwordx2 %0, %1, %2 offset:%3" : "=v"(d) : "v"(voff), "s"(sbase), "i"(OFF)); }
DEVI void dma16c(const void* sbase, u32 voff, u32 ldsbase) {
  asm volatile("s_mov_b32 m0, %2\n\ts_nop 0\n\tglobal_load_lds_dwordx4 %0, %1 sc1" ::"v"(voff), "s"(sbase), "s"(ldsbase) : "memory");
}
template <int OFF> DEVI void gl128c(u32x4& d, u32 voff, const void* sbase) { asm volatile("global_load_dwordx4 %0, %1, %2 offset:%3 sc1" : "=v"(d) : "v"(voff), "s"(sbase), "i"(OFF)); }
template <int OFF> DEVI void gl32c(u32& d, u32 voff, const void* sbase) { asm volatile("global_load_dword %0, %1, %2 offset:%3 sc1" : "=v"(d) : "v"(voff), "s"(sbase), "i"(OFF)); }
DEVI void gst128(u32x4 v, u32 voff, void* sbase) { asm volatile("global_store_dwordx4 %0, %1, %2 sc1\n\ts_nop 1" ::"v"(voff), "v"(v), "s"(sbase) : "memory"); }
DEVI void gst64(u32x2 v, u32 voff, void* sbase) { asm volatile("global_store_dwordx2 %0, %1, %2 sc1" ::"v"(voff), "v"(v), "s"(sbase) : "memory"); }
DEVI void gst32(u32 v, u32 voff, void* sbase) { asm volatile("global_store_dword %0, %1, %2 sc1" ::"v"(voff), "v"(v), "s"(sbase) : "memory"); }
#define CODE_PF(from, to, ck, step, lds)                                                                                              \
  do {                                                                                                                                \
    u32 lo_, hi_, len_;                                                                                                               \
    asm volatile("s_getpc_b64 s[20:21]\n\ts_add_u32 %0, s20, " from "@rel32@lo+4\n\ts_addc_u32 %1, s21, " from "@rel32@hi+12\n\ts_mov_b32 %2, " to "-" from \
                 : "=s"(lo_), "=s"(hi_), "=s"(len_)::"s20", "s21", "scc");                                                            \
    const char* code_ = (const char*)(((unsigned long long)hi_ << 32) | lo_);                                                         \
    for (u32 o_ = (ck) * 1024; o_ < len_; o_ += (step) * 1024) { const u32 a_ = o_ + (threadIdx.x & 63) * 16; dma16(code_, a_ < len_ ? a_ : 0u, lds); } \
  } while (0)
template <int N> DEVI void wait_vm() { asm volatile("s_waitcnt vmcnt(%0)" ::"i"(N > 63 ? 63 : N) : "memory"); }
DEVI void wait_lgkm() { asm volatile("s_waitcnt lgkmcnt(0)" ::: "memory"); }
DEVI void bar() { asm volatile("s_barrier" ::: "memory"); }
DEVI u32 rfl(u32 v) { return __builtin_amdgcn_readfirstlane(v); }
#define SCHED() __builtin_amdgcn_sched_barrier(0)
#define STAMP(i)
#define STAMP_END()

constexpr float QSCALE = 0.23570226039551584f * 1.4426950408889634f;

#define POLL_SLEEP 8
#define POLL_FAST 2
#define SPDELAY 14
#define LDPOL "sc1"
#define FSPOL "sc1"
#define CVDELAY 0
#define PUBB 2
#define NMID 1
#define FLB 1
#define PUBD 0
#define PREPFIRST 2
#define OUTPOL "sc1"
#define BAL 1
#define CPRIO 3
#define WOGATE 3
#define YPOS 16
#define CMPRIO 3
#define QDELAY 8
#define OGATE 1
#define ODELAY 6
#define BAL1 4
#define BAL0 1
#define SQD 16
#define POLLW 4
#define VDELAY 3

constexpr int L_SLOT = 32768, L_WOFF = 16384, L_STG = 94208, L_GW = 131072, L_PT = 133120, L_TILE = 17408;
constexpr int L_K = 0, L_V = 21760, L_C = 41536, L_R = L_PT + 1024, L_FL = 152320, L_DUMMY = L_FL + 256, L_Q = 153600, L_END = 163840;
constexpr int KC_STR = 4640, CS_U = 4 * KC_STR, CS_SZ = CS_U + 4096 + 128;
constexpr int L_W = L_C, L_X = L_W + 576 * 160, L_Y = L_X + 9216;
static_assert(L_Y + 9216 <= L_FL && L_C + 2 * CS_SZ <= L_STG && L_C + 4 * CS_SZ <= L_PT && L_PT + L_TILE <= L_FL, "lds map");
constexpr int W_PROG = L_FL, W_XCH = L_FL + 64, W_GO = L_FL + 128, W_TAG = L_FL + 144;

DEVI u32 lds_peek(u32 a) { return __hip_atomic_load((LAS u32*)a, __ATOMIC_RELAXED, __HIP_MEMORY_SCOPE_WORKGROUP); }
DEVI void prog_set(u32 a, u32 n) { asm volatile("" ::: "memory"); __hip_atomic_store((LAS u32*)a, n, __ATOMIC_RELAXED, __HIP_MEMORY_SCOPE_WORKGROUP); }
DEVI void prog_wait(u32 a, u32 n) { while (__builtin_amdgcn_ballot_w64(lds_peek(a) < n)) __builtin_amdgcn_s_sleep(1); asm volatile("" ::: "memory"); }
DEVI void publish(char* fb, u32 off, u32x2 tag, bool on) { wait_vm<0>(); if (on) asm volatile("global_store_dwordx2 %0, %1, %2 " FSPOL ::"v"(off), "v"(tag), "s"(fb) : "memory"); }
template <int SLP> DEVI void await(const char* fb, u32 off, u32x2 tag) {
  for (int spin = 0; spin < (1 << 20); spin++) {
    u32x2 v;
    asm volatile("global_load_dwordx2 %0, %1, %2 " LDPOL "\n\ts_waitcnt vmcnt(0)" : "=&v"(v) : "v"(off), "s"(fb) : "memory");
    if (!__builtin_amdgcn_ballot_w64(v.x != tag.x || v.y != tag.y)) break;
    __builtin_amdgcn_s_sleep(SLP);
  }
}

DEVI void prep_issue(const u16* Bk, const u16* Bv, int row0, int col, u32 tb, u32 pw, int lane) {
  const int r8 = lane >> 3, c8 = lane & 7;
  sfor<2>([&](auto I) {
    const int j = 2 * pw + I;
    int row = row0 + 8 * j + r8 - 3; row = row < 0 ? 0 : row;
    dma16(Bk + col, row * 1152 + 16 * (c8 ^ j), tb + 1024 * j);
    dma16(Bv + col, (row0 + 8 * j + r8) * 1152 + 16 * (c8 ^ j), tb + 9216 + 1024 * j);
  });
  dma16(Bk + col, (row0 + 61 + (r8 > 2 ? 2 : r8)) * 1152 + 16 * c8, tb + 8192);
}
template <typename F> DEVI void prep_compute(int row0, int col, u32 tb, u32 pw, int lane, u32x4 sh, char* ws, F&& mid) {
  const int c8 = lane & 7, cp = 8 * pw + (lane >> 3);
  const u32 o0 = tb + 1024 * c8 + 16 * ((cp >> 2) ^ c8) + 4 * (cp & 3), o1 = tb + 1024 * c8 + 16 * ((cp >> 2) ^ ((c8 + 1) & 7)) + 4 * (cp & 3);
  row0 += 8 * c8;
  u32 kk[11], vv[8];
#pragma unroll
  for (int i = 0; i < 11; i++) kk[i] = lds32((i < 8 ? o0 : o1) + 128 * i);
#pragma unroll
  for (int i = 0; i < 8; i++) vv[i] = lds32(o0 + 9216 + 128 * i);
  if (row0 == 0) { kk[0] = 0; kk[1] = 0; kk[2] = 0; }
  const u32 go = 2 * cp * 4096 + 2 * (2040 - row0);
  char* dst = ws + WS_UR + (size_t)col * 4096;
  float r[8];
  {
    const float a0 = lo2f(sh.x), a1 = hi2f(sh.x), a2 = lo2f(sh.y), a3 = hi2f(sh.y);
#pragma unroll
    for (int i = 0; i < 8; i++) r[i] = vmul(vfma(lo2f(kk[i + 3]), a0, vfma(lo2f(kk[i + 2]), a1, vfma(lo2f(kk[i + 1]), a2, vmul(lo2f(kk[i]), a3)))), lo2f(vv[i]));
    gst128(u32x4{pk2(r[7], r[6]), pk2(r[5], r[4]), pk2(r[3], r[2]), pk2(r[1], r[0])}, go, dst);
  }
  mid();
  {
    const float a0 = lo2f(sh.z), a1 = hi2f(sh.z), a2 = lo2f(sh.w), a3 = hi2f(sh.w);
#pragma unroll
    for (int i = 0; i < 8; i++) r[i] = vmul(vfma(hi2f(kk[i + 3]), a0, vfma(hi2f(kk[i + 2]), a1, vfma(hi2f(kk[i + 1]), a2, vmul(hi2f(kk[i]), a3)))), hi2f(vv[i]));
    gst128(u32x4{pk2(r[7], r[6]), pk2(r[5], r[4]), pk2(r[3], r[2]), pk2(r[1], r[0])}, go, dst + 4096);
  }
}

template <int NR> DEVI void conv_mfma(u32 cs, int rho0, int lane, f32x4 (&acc)[8]) {
  const int g = lane >> 4, l15 = lane & 15;
  u32 aa = cs + (l15 & 3) * KC_STR + 2 * (64 * (l15 >> 2) + 8 * g) + 64 * rho0;
  u32 ba = opaque(cs + CS_U + 2 * (1852 - 1792 - 4 * l15 + 8 * g) + 64 * rho0), bb = opaque(ba + 8);
  u32x4 A[8], G[8];
  sfor<5>([&](auto J) { constexpr int p = J < 4 ? J + 2 : 1; A[p] = lds128(aa + p * 512); G[p] = cat(lds64(ba + (7 - p) * 512), lds64(bb + (7 - p) * 512)); SCHED(); });
#pragma unroll 1
  for (int rho = 0; rho < NR; rho++) {
    constexpr int pa[36] = {2, 2, 3, 3, 2, 4, 3, 4, 2, 5, 1, 1, 2, 1, 3, 1, 4, 1, 5, 1, 6, 2, 0, 3, 0, 4, 0, 5, 0, 1, 0, 6, 0, 7, 0, 0};
    constexpr int pe[36] = {2, 3, 2, 3, 4, 2, 4, 3, 5, 2, 1, 2, 1, 3, 1, 4, 1, 5, 1, 6, 1, 0, 2, 0, 3, 0, 4, 0, 5, 0, 1, 0, 6, 0, 7, 0};
    constexpr int rdc[36] = {18, 19, 20, 21, 22, 23, 0, 1, 2, -1, -1, -1, -1, -1, -1, -1, -1, -1, -1, -1, -1, 6, 7, 8, 9, 10, 11, 12, 13, 14, 15, 16, 17, 3, 4, 5};
    sfor<36>([&](auto I) {
      constexpr int i = I, r = rdc[i], x = r / 3;
      if constexpr (i == 35) __builtin_amdgcn_s_setprio(CMPRIO);
      if constexpr (i == 9) { aa = opaque(aa + 64); ba = opaque(ba + 64); bb = opaque(bb + 64); }
      acc[pa[i] + pe[i]] = mfma16(A[pa[i]], G[pe[i]], acc[pa[i] + pe[i]]);
      if constexpr (r >= 0) {
        if constexpr (r % 3 == 0) A[x] = lds128(aa + x * 512);
        else if constexpr (r % 3 == 1) { const u32x2 t = lds64(ba + (7 - x) * 512); G[x].x = t.x; G[x].y = t.y; }
        else { const u32x2 t = lds64(bb + (7 - x) * 512); G[x].z = t.x; G[x].w = t.y; }
      }
      SCHED();
    });
  }
  SCHED();
}
DEVI void conv_out(f32x4 y, u32x2 u, float Dv, u32 off, char* ydst) {
  const float y0 = vfma(hi2f(u.y), Dv, y[0]), y1 = vfma(lo2f(u.y), Dv, y[1]), y2 = vfma(hi2f(u.x), Dv, y[2]), y3 = vfma(lo2f(u.x), Dv, y[3]);
  gst64(u32x2{pk2(y0, y1), pk2(y2, y3)}, off, ydst);
}

DEVI float xhalf_max(float m) { const u32 mu = f2u(m); auto r = __builtin_amdgcn_permlane32_swap(mu, mu, false, false); return fmaxf(u2f(r[0]), u2f(r[1])); }

extern "C" __global__ __launch_bounds__(768, 3) void gm_main(const u16* __restrict__ x, const u16* __restrict__ wqkv, char* __restrict__ ws,
                                                             const u16* __restrict__ Bk, const u16* __restrict__ Bv, const u16* __restrict__ shk,
                                                             const u16* __restrict__ ssmk, const u16* __restrict__ wo, const u16* __restrict__ ssmD,
                                                             const u16* __restrict__ gw, u16* __restrict__ out) {
  extern __shared__ __attribute__((aligned(16))) char smem[];
  const u32 sb = (u32)(size_t)(LAS char*)smem;
  const int b = blockIdx.x, xcd = b & 7, idx = b >> 3;
  const int lane = threadIdx.x & 63;
  const u32 wv = rfl(threadIdx.x >> 6);
  const bool spare = idx >= 28;
  char* const fb = ws + WS_F;
  const int qb = ((idx >> 2) + 1) & 7, h = 4 * xcd + (idx & 3);
  const int chb = wv >= 8 ? 64 * xcd + 2 * idx : 512 + 2 * h;
  u32x2 pre[2], fv; u32x4 cur[2];
  const u32 fo = F_U + ((chb >> 1) * 32 + 24 - 8 * (wv & 3) + (lane & 7)) * 8;
  auto build = [&]() {
    const int tid = threadIdx.x & 255, cw = wv & 3;
    const u32 cb = sb + L_C + (wv >= 8 ? 0 : 2 * CS_SZ);
    {
      const u32 z0 = opaque(cw * KC_STR + 16 * lane), z1 = opaque(CS_U + 4096 + 16 * (lane & 7)), za = cb + (lane < 24 ? z0 : z1);
      st128(za, u32x4{0u, 0u, 0u, 0u}); st128(za + CS_SZ, u32x4{0u, 0u, 0u, 0u});
    }
#pragma unroll
    for (int j = 0; j < 2; j++) {
      const u32 cs = cb + j * CS_SZ + 384 + 16 * tid;
      const u32 w0 = tid ? pre[j].x : 0u, w1 = tid ? pre[j].y : 0u, w2 = cur[j].x, w3 = cur[j].y, w4 = cur[j].z, w5 = cur[j].w;
      const u32 a01 = __builtin_amdgcn_alignbyte(w1, w0, 2), a12 = __builtin_amdgcn_alignbyte(w2, w1, 2), a23 = __builtin_amdgcn_alignbyte(w3, w2, 2),
                a34 = __builtin_amdgcn_alignbyte(w4, w3, 2), a45 = __builtin_amdgcn_alignbyte(w5, w4, 2);
      st128(cs, u32x4{a01, a12, a23, a34});
      st128(cs + KC_STR, u32x4{w1, w2, w3, w4});
      st128(cs + 2 * KC_STR, u32x4{a12, a23, a34, a45});
      st128(cs + 3 * KC_STR, cur[j]);
    }
  };
  u32 pof = 0; bool pon = false;
  STAMP(0);
  {
    const int hg = xcd & 1, ri = idx & 3, rb = 4 * (xcd >> 1) + ri, jt = idx >> 2;
    const int r8 = lane >> 3, c8 = lane & 7;
    const int rot = ((0x124924u >> (2 * (jt + ri))) & 3) * 3;
    const u32 xo = opaque((32 * wv + r8) * 1152 + 16 * (c8 ^ r8));
    asm volatile("" ::"s"(x), "s"(wqkv), "s"(Bk), "s"(Bv));
    if (wv < 4 && !spare) {
      const u32 lw = wv;
      const char* xb = (const char*)(x + (size_t)rb * 128 * 576);
      sfor<4>([&](auto I) { dma16(xb + rot * 128 + I * 9216, xo, sb + (4 * lw + I) * 1024); });
      u32 wof[2];
#pragma unroll
      for (int i = 0; i < 2; i++) {
        const int ch = (lane & 15) ^ (((lane >> 4) | (i << 2)) << 1), ln = 14 * hg + 2 * jt + (ch >> 3);
        wof[i] = ((lane >> 4) + 16 * lw) * 3456 + 128 * (ln > 26 ? 26 : ln) + 16 * (ch & 7);
      }
      const char* wb = (const char*)wqkv;
      auto issue = [&](int c) {
        const u32 sl = sb + (c & 3) * L_SLOT;
        c += rot; c = c >= 9 ? c - 9 : c;
        sfor<4>([&](auto I) { dma16(xb + c * 128 + I * 9216, xo, sl + (4 * lw + I) * 1024); });
        sfor<4>([&](auto I) { dma16(wb + (64 * c + 4 * I) * 3456, wof[I >> 1], sl + L_WOFF + (4 * lw + I) * 1024); });
      };
      sfor<4>([&](auto I) { dma16(wb + (64 * rot + 4 * I) * 3456, wof[I >> 1], sb + L_WOFF + (4 * lw + I) * 1024); });
      prep_issue(Bk, Bv, 64 * idx, 64 * xcd, sb + L_PT, lw, lane);
#pragma unroll 1
      for (int n = 1; n < 8; n++) { issue(n); wait_vm<8>(); bar(); }
      issue(8);
      wait_vm<8>(); bar();
      __builtin_amdgcn_s_sleep(2);
      {
        const char* wp = (const char*)wo + (idx == 27 ? 663552 - 24576 : idx * 23712) + lw * 6144;
        sfor<6>([&](auto I) { dma16(wp + I * 1024, lane * 16, sb + L_DUMMY); });
      }
      wait_vm<6>(); bar();
      bar();
    } else if (wv >= 8 || (spare && wv >= 4)) {
      const bool ex = wv < 8;
      const int row0 = ex ? 256 * xcd + 64 * ri : 64 * idx, col = ex ? 512 : 64 * xcd;
      const u32 tb = sb + (ex ? 0 : L_PT);
      u32x2 e;
      asm volatile("global_load_dwordx2 %0, %1, %2 sc1" : "=v"(e) : "v"(F_EP), "s"(fb));
      u32x4 sh;
      gl128<0>(sh, 8 * col + 16 * (8 * (wv & 3) + r8), shk);
      if (spare) prep_issue(Bk, Bv, row0, col, tb, wv & 3, lane);
      {
        const u32 o = (threadIdx.x & 255) * 16, op = o ? o - 8 : 0;
        sfor<2>([&](auto J) { const u16* kp = ssmk + (size_t)(chb + J) * 2048; gl128<0>(cur[J], o, kp); gl64<0>(pre[J], op, kp); });
      }
      {
        const int t3 = (4 * xcd + (idx & 3)) * 8 + (idx >> 2), k = wv & 3;
        const u32 mo = k == 0 ? F_1 + (h * 8 + qb) * 512 : k == 1 ? F_O + t3 * 512 : k == 2 ? F_Y + t3 * 640 : F_U + (chb >> 1) * 256;
        asm volatile("s_mov_b32 m0, %2\n\ts_nop 0\n\tglobal_load_lds_dword %0, %1 sc1" ::"v"(mo + (lane < (k == 3 ? 4 : k == 2 ? 10 : 8) ? lane : 0) * 64), "s"(fb), "s"(sb + L_DUMMY) : "memory");
      }
      if (wv == 8) st32(sb + L_FL + 4 * lane, 0u);
      if (spare) wait_vm<5>();
      bar();
      asm volatile("s_waitcnt vmcnt(5)" : "+v"(e), "+v"(sh)::"memory");
      e.x += 1u; e.y += e.x == 0u;
      if (wv == 8) st64(sb + W_TAG, e);
      prep_compute(row0, col, tb, wv & 3, lane, sh, ws, [&]() { if (!spare) { SCHED(); bar(); SCHED(); } });
      wait_lgkm();
      bar();
      if (!spare) sfor<PUBB>([&](auto) { bar(); });
      asm volatile("s_waitcnt vmcnt(0)" : "+v"(cur[0]), "+v"(cur[1]), "+v"(pre[0]), "+v"(pre[1])::"memory");
      publish(fb, F_U + (((col >> 1) + 8 * (wv & 3) + lane) * 32 + (row0 >> 6)) * 8, e, lane < 8);
      if (!spare) sfor<8 - NMID - PUBB - FLB>([&](auto) { bar(); });
      else { build(); if (wv >= 8) __builtin_amdgcn_s_sleep(SPDELAY); }
      asm volatile("global_load_dwordx2 %0, %1, %2 sc1" : "=v"(fv) : "v"(fo), "s"(fb));
      if (!spare) sfor<FLB>([&](auto) { bar(); });
    } else if (spare) {
      CODE_PF("gm_main", "gm_end", 4 * ri + wv, 16, sb + L_DUMMY);
      bar(); bar();
    } else {
      const int cw = wv - 4, g = lane >> 4, l15 = lane & 15, wm = cw & 1, wn = cw >> 1;
      const u32 xra = sb + (64 * wm + l15) * 128 + 16 * (g ^ (l15 & 7));
      const u32 xb[2][2] = {{opaque(xra), opaque(xra + 2 * L_SLOT)}, {opaque(xra ^ 64), opaque((xra ^ 64) + 2 * L_SLOT)}};
      u32x4 wf[2][4], xf[2][4];
      f32x4 acc[4][4];
      const int ln = 14 * hg + 2 * jt + wn;
      {
        const int r64 = 2 * rb + wm, seg = ln / 9, lc = ln - 9 * seg, hc = (64 * lc) / 18 + (lane >> 2), li = lane & 1, nx = (lane >> 1) & 1;
        pon = ln == 27 ? lane < 16 : (lane < 20 && hc < 32 && ((18 * hc + 17 * li) >> 6) == lc && (!nx || (seg > 0 && r64 < 28)));
        pof = opaque(ln == 27 ? F_Y + (((4 * (r64 >> 2) + 2 * (r64 & 1)) * 8 + lane) * 80 + 72 + ((r64 >> 1) & 1)) * 8 : F_1 + ((hc * 8 + (r64 >> 2) + nx) * 64 + seg * 16 + li * 8 + (r64 & 3) + (nx ? 0 : 4)) * 8);
      }
      __builtin_amdgcn_s_setprio(2);
      if (ln == 27) {
        dma16(gw, wm * 128 + lane * 16, sb + L_GW + wm * 128);
        wait_vm<0>();
#pragma unroll
        for (int i = 0; i < 4; i++) acc[i][0] = f32x4{0.f, 0.f, 0.f, 0.f};
#pragma unroll 1
        for (int c = 0; c < 9; c++) {
          bar();
          const int ca = c + rot >= 9 ? c + rot - 9 : c + rot;
#pragma unroll
          for (int ks = 0; ks < 2; ks++) {
            const u32x4 gv = lds128(sb + L_GW + ca * 128 + ks * 64 + 16 * g);
#pragma unroll
            for (int i = 0; i < 4; i++) acc[i][0] = mfma16(gv, lds128(xb[ks][0] + (c & 3) * L_SLOT + 2048 * i), acc[i][0]);
          }
          wait_lgkm();
        }
        bar();
        const float p = rbf(g == 0 ? acc[0][0][0] : g == 1 ? acc[1][0][0] : g == 2 ? acc[2][0][0] : acc[3][0][0]);
        gst32(f2u(rbf(1.f / (1.f + __expf(-p)))), lane * 4, ws + WS_G + (rb * 128 + 64 * wm) * 4);
      } else {
    u32 wb[4][2];
    {
      const int q = l15 >> 2, p = lane & 3, f = q | ((g & 1) << 2);
#pragma unroll
      for (int nt = 0; nt < 4; nt++) {
        const u32 a = sb + L_WOFF + (8 * g + q) * 256 + (((8 * wn + 2 * nt + (p >> 1)) ^ (f << 1)) << 4) + 8 * (p & 1);
        wb[nt][0] = opaque(a); wb[nt][1] = opaque(a + 2 * L_SLOT);
      }
    }
    auto rd = [&](auto KS, auto C) {
      constexpr int ks = KS, c = C, so = (c & 1) * L_SLOT, hi = (c >> 1) & 1;
      sfor<4>([&](auto I) {
        xf[ks][I] = lds128(xb[ks][hi] + so + 2048 * I);
        wf[ks][I] = cat(tr_read(wb[I][hi] + so + ks * 8192), tr_read(wb[I][hi] + so + ks * 8192 + 1024));
      });
    };
    auto mm = [&](auto KS, auto FIRST) {
      constexpr int ks = KS;
      sfor<16>([&](auto I) {
        constexpr int mo[16] = {0, 1, 0, 1, 2, 2, 0, 1, 2, 3, 3, 3, 0, 1, 2, 3}, no[16] = {0, 0, 1, 1, 0, 1, 2, 2, 2, 0, 1, 2, 3, 3, 3, 3};
        constexpr int mt = mo[I], nt = no[I];
        acc[mt][nt] = mfma16(xf[ks][mt], wf[ks][nt], FIRST ? f32x4{0.f, 0.f, 0.f, 0.f} : acc[mt][nt]);
      });
    };
    auto ilv = [&]() {
      sfor<12>([&](auto) { __builtin_amdgcn_sched_group_barrier(0x8, 1, 0); __builtin_amdgcn_sched_group_barrier(0x100, 1, 0); });
      __builtin_amdgcn_sched_group_barrier(0x8, 4, 0);
    };
    const float sc = ln < 9 ? QSCALE : 1.f;
    char* qo = ws + WS_QKV + (size_t)((2 * rb + wm) * 1728 + 64 * ln) * 128;
    SCHED();
    bar();
    STAMP(1);
    rd(IC(0), IC(0));
    sfor<9>([&](auto C) {
      constexpr int c = C;
      SCHED();
      rd(IC(1), C); mm(IC(0), IC(c == 0)); ilv();
      SCHED();
      if constexpr (c == 8) wait_lgkm();
      bar();
      SCHED();
      if constexpr (c < 8) { rd(IC(0), IC(c + 1)); mm(IC(1), IC(0)); ilv(); }
    });
    STAMP(2);
    const u32 stg = sb + L_STG + cw * 9216;
    const u32 sa = stg + l15 * 144 + 8 * g, ra = stg + r8 * 144 + 16 * c8, go = lane * 16;
    u32x4 t[4][2];
    sfor<6>([&](auto NT) {
      constexpr int nt = NT;
      if constexpr (nt < 4) sfor<4>([&](auto M) { acc[M][nt] = mfma16(xf[1][M], wf[1][nt], acc[M][nt]); });
      SCHED();
      if constexpr (nt > 0 && nt < 5) {
        sfor<4>([&](auto M) {
          const f32x4 v = acc[M][nt - 1] * sc;
          st64(sa + (nt - 1) * 2304 + 32 * M, u32x2{pk2(v[0], v[1]), pk2(v[2], v[3])});
        });
        t[nt - 1][0] = lds128(ra + (nt - 1) * 2304); t[nt - 1][1] = lds128(ra + (nt - 1) * 2304 + 1152);
      }
      if constexpr (nt > 1) { gst128(t[nt - 2][0], go, qo + (nt - 2) * 2048); gst128(t[nt - 2][1], go, qo + (nt - 2) * 2048 + 1024); }
      SCHED();
    });
      }
      STAMP(8);
    }
  }
  const int rt = 4 * xcd + (idx & 3), ct = idx >> 2;
  auto wo_load = [&](int r0, int r1) {
    const int r6 = lane / 10, c10 = lane - 10 * r6;
    const u32 vo = (6 * (wv & 3) + r6) * 1152 + 16 * (c10 > 8 ? 8 : c10);
    const char* wb = (const char*)(wo + 72 * ct);
#pragma unroll 4
    for (int i = r0; i < r1; i++) dma16n<60>(wb + i * 24 * 1152, vo, sb + L_W + (4 * i + (wv & 3)) * 960);
  };
  const u32x2 tag = lds64(sb + W_TAG);
  const int cw = wv & 3;
  u32 ypub = 0;
  if (wv >= (spare ? 4u : 8u)) {
    const int tid = threadIdx.x & 255;
    const u32 cb = sb + L_C + (wv >= 8 ? 0 : 2 * CS_SZ);
    __builtin_amdgcn_s_sleep(CVDELAY);
    asm volatile("s_waitcnt vmcnt(0)" : "+v"(fv)::"memory");
    if (__builtin_amdgcn_ballot_w64(fv.x != tag.x || fv.y != tag.y)) await<POLL_SLEEP>(fb, fo, tag);
    STAMP(2);
    sfor<2>([&](auto J) { dma16c(ws + WS_UR + (size_t)(chb + J) * 4096, tid * 16, cb + CS_U + cw * 1024 + J * CS_SZ); });
    const u32 dv = *(const __attribute__((address_space(4))) u32*)(ssmD + chb);
    f32x4 acc[8];
#pragma unroll
    for (int c = 0; c < 8; c++) { acc[c] = f32x4{0.f, 0.f, 0.f, 0.f}; asm volatile("" : "+v"(acc[c])); }
    if (!spare) build();
    STAMP(1);
    wait_vm<0>();
    prog_set(sb + W_XCH + 4 * wv, 1);
    const int tm = cw >> 1, pi = cw & 1;
    const u32 cs = cb + tm * CS_SZ;
    prog_wait(sb + W_XCH + 4 * (wv & 12) + 4 * (lane & 3), 1);
    STAMP(4);
    conv_mfma<4>(cs, 4 * pi, lane, acc);
    STAMP(5);
    __builtin_amdgcn_s_setprio(CPRIO);
    if (spare && wv >= 8) prog_wait(sb + W_XCH + 16 + 8 * tm + 4 * (lane & 1), 2);
    const u32 rA = (spare && wv >= 8 ? sb + L_C + (2 + tm) * CS_SZ : sb + L_R + tm * 8192) + lane * 16;
    if (pi) { sfor<4>([&](auto J) { st128(rA + 4096 + J * 1024, __builtin_bit_cast(u32x4, acc[J])); acc[J] = acc[4 + J]; }); }
    else sfor<4>([&](auto J) { st128(rA + J * 1024, __builtin_bit_cast(u32x4, acc[4 + J])); });
    u32x2 uu[4];
#pragma unroll
    for (int j = 0; j < 4; j++) uu[j] = lds64(cs + CS_U + 4088 - 8 * lane - 512 * (4 * pi + j));
    prog_set(sb + W_XCH + 4 * wv, 2);
    prog_wait(sb + W_XCH + 4 * (wv ^ 1), 2);
    f32x4 t[4];
#pragma unroll
    for (int j = 0; j < 4; j++) t[j] = __builtin_bit_cast(f32x4, lds128(rA + (pi ^ 1) * 4096 + j * 1024));
    wait_lgkm();
    prog_set(sb + W_XCH + 4 * wv, 3);
    const u32 yo = ((lane >> 3) & 3) * 73728 + (lane >> 5) * 64 + (lane & 7) * 8;
    char* yd = ws + WS_Y2 + (chb + tm) * 128 + pi * 1179648;
#pragma unroll
    for (int j = 0; j < 4; j++) conv_out(vadd4(acc[j], t[j]), uu[j], tm ? hi2f(dv) : lo2f(dv), yo + j * 294912, yd);
    STAMP(8);
    ypub = F_Y + (((16 * pi + lane) * 8 + (chb + tm) / 72) * 80 + (chb + tm) % 72) * 8;
    if (wv < 8) publish(fb, ypub, tag, lane < 16);
  }
  if (wv < 8) {
    __builtin_amdgcn_s_setprio(1);
    const int c = lane & 31, hh = lane >> 5, q4 = (lane & 15) >> 2;
    const int w = spare ? wv ^ 4 : wv;
    const u32 kcol = 64 * w + (lane & 16) * 2 + 16 * (lane & 1) + 4 * (lane & 2);
    const u32 ka = opaque(sb + L_K + (8 * hh + q4) * 1088 + kcol), ka2 = opaque(sb + L_K + (16 + q4) * 1088 + kcol);
    const u32 va = opaque(sb + L_V + c * 1040 + 64 * w + 16 * hh);
    const u32 qa = opaque(sb + L_Q + q4 * 512 + 64 * w + (lane & 16) * 2 + 8 * (lane & 3)), qa0 = opaque(qa + 4096 * hh);
    const int thr = c - 8 * hh;
    const u32 lom = opaque(hh ? 0u : ~0u);
    u32 msk[8], nmk[8];
    unsigned long long cnd[16];
#pragma unroll
    for (int e = 0; e < 16; e++) cnd[e] = __builtin_amdgcn_ballot_w64(16 * (e >> 3) + (e & 7) <= thr);
#pragma unroll
    for (int j = 0; j < 8; j++) { const int k0 = 16 * (j >> 2) + 2 * (j & 3); msk[j] = opaque(k0 + 1 <= thr ? 0xffffffffu : (k0 <= thr ? 0xffffu : 0u)); nmk[j] = opaque(~msk[j]); }
    f32x16 S0;
#pragma unroll
    for (int e = 0; e < 16; e++) S0[e] = u2f(opaque(spare ? 0xff800000u : 0u));
    const u32 oo = opaque((c >> 4) * 18432 + (c & 15) * 16 + hh * 256);
    const u32 fl = sb + W_PROG + 4 * (spare ? (lane & 3) : (lane & 7));
    if (wv < 3) st128(sb + (wv < 2 ? L_K + (18 + wv) * 1088 : L_V + 18 * 1040) + lane * 16, u32x4{0x3f803f80u, 0x3f803f80u, 0x3f803f80u, 0x3f803f80u});
    const char* Ag = ws + WS_QKV + (size_t)(4 * qb * 1728 + 18 * h) * 128;
    const char* Kg = Ag + 576 * 128 - (qb ? 884736 : 0);
    const u32 lo = opaque(((lane >> 3) & (qb ? 7 : 3)) * 221184 + (lane & 7) * 16), lq = opaque(((lane >> 3) & 3) * 221184 + (lane >> 5) * 128 + (lane & 7) * 16);
    const char *pq = Ag + wv * 256, *pk = Kg + wv * 128, *pe = wv < 4 ? Kg + (wv < 2 ? 16 + wv : 590 + wv) * 128 : Ag + 2048;
    u32 le = sb + (wv < 2 ? L_K + (16 + wv) * 1088 : (wv < 4 ? L_V + (14 + wv) * 1040 : L_Q + 8192)), lk = sb + L_K + wv * 1088, lv = sb + L_V + wv * 1040;
    const u32 lx = opaque(wv < 4 ? lo : lq);
    asm volatile("" : "+s"(pq), "+s"(pk), "+s"(pe), "+s"(le), "+s"(lk), "+s"(lv));
    SCHED();
    if (wv >= 4 && !spare) publish(fb, pof, tag, pon);
    STAMP(3);
    if (wv == (spare ? 0u : (u32)POLLW)) {
      if (spare) { prog_wait(sb + W_XCH + 32, 1); __builtin_amdgcn_s_sleep(SQD); }
      __builtin_amdgcn_s_sleep(QDELAY);
      await<POLL_FAST>(fb, F_1 + ((h * 8 + qb) * 64 + ((lane < 48 ? lane : lane - 16) | (lane < 16 || spare ? 4 : 0))) * 8, tag);
      prog_set(sb + W_GO, 1);
    } else prog_wait(sb + W_GO, 1);
    STAMP(4);
    __builtin_amdgcn_s_setprio(BAL ? 2 : 1);
    if (!spare || wv < 4) {
    dma16c(pk, lo, lk); dma16c(pk + 1024, lo, lk + 8704); dma16c(pq, lq, sb + L_Q + wv * 1024); dma16c(pe, lx, le);
    if (spare) {
      dma16c(pk + 512, lo, lk + 4352); dma16c(pk + 1536, lo, lk + 13056); dma16c(pq + 1024, lq, sb + L_Q + 4096 + wv * 1024); dma16c(Ag + 2048, lq, sb + L_Q + 8192);
      dma16c(pk + 580 * 128, lo, lv + 4160); dma16c(pk + 588 * 128, lo, lv + 12480);
    }
    __builtin_amdgcn_s_sleep(VDELAY);
    dma16c(pk + 576 * 128, lo, lv); dma16c(pk + 584 * 128, lo, lv + 8320);
    STAMP(7);
    wait_vm<2>();
    prog_set(sb + W_PROG + 4 * wv, 2);
    }
    prog_wait(fl, 2);
    STAMP(5);
    struct KF { u32x4 a; u32x2 b; };
    struct VF { u32x4 a, b; };
    auto ldk = [&](int i) { KF k; k.a = cat(tr_read(ka + i * 64), tr_read(ka + 4352 + i * 64)); k.b = tr_read(ka2 + i * 64); return k; };
    auto ldv = [&](int i) { VF v; v.a = lds128(va + i * 64); v.b = lds128(va + i * 64 + 32); return v; };
    auto pk8 = [&](const f32x16& p, int o) { return u32x4{pk2(p[o], p[o + 1]), pk2(p[o + 2], p[o + 3]), pk2(p[o + 4], p[o + 5]), pk2(p[o + 6], p[o + 7])}; };
    const int ib = spare ? 8 - w : 1;
    const u32x4 q0 = cat(tr_read(qa0), tr_read(qa0 + 2048));
    const KF k8 = ldk(8);
    u32x2 q1 = tr_read(qa + 8192);
    const KF k0 = ldk(0);
    SCHED();
    q1.x &= lom; q1.y = 0u;
    auto qk = [&](const KF& k) { f32x16 z = {}; z = mfma32(k.a, q0, z); return mfma32k8(k.b, q1, z); };
    f32x16 Sm = {};
    Sm = mfma32(k8.a, q0, Sm); S0 = mfma32(k0.a, q0, S0);
    Sm = mfma32k8(k8.b, q1, Sm); S0 = mfma32k8(k0.b, q1, S0);
    KF kf[2]; VF vf[2], v8, v0;
    kf[1] = ldk(7); kf[0] = ldk(6);
    SCHED();
#pragma unroll
    for (int e = 0; e < 16; e++) Sm[e] = __builtin_amdgcn_inverse_ballot_w64(cnd[e]) ? Sm[e] : S0[e];
    float m = __builtin_fmaxf(__builtin_fmaxf(Sm[0], Sm[1]), Sm[2]), m2 = __builtin_fmaxf(__builtin_fmaxf(Sm[3], Sm[4]), Sm[5]);
#pragma unroll
    for (int e = 6; e < 14; e += 4) { m = fmaxf(fmaxf(m, Sm[e]), Sm[e + 1]); m2 = fmaxf(fmaxf(m2, Sm[e + 2]), Sm[e + 3]); }
    m = fmaxf(fmaxf(m, Sm[14]), Sm[15]);
    m = xhalf_max(fmaxf(m, m2));
    f32x16 O;
    float l;
#pragma unroll 1
    for (int pass = 0; pass < 2; pass++) {
      { const float r1 = lo2f(pk2(m, m)) - m; q1.y = hh ? pk2(r1 - lo2f(pk2(r1, r1)), 0.f) : pk2(-m, r1); }
      f32x16 S[2];
      {
        f32x16 p = mfma32k8(kf[1].b, u32x2{0u, q1.y}, Sm);
        S[1] = qk(kf[1]);
        SCHED();
        wait_vm<0>();
        prog_set(sb + W_PROG + 4 * wv, 3);
#pragma unroll
        for (int e = 0; e < 8; e++) p[e] = __builtin_amdgcn_exp2f(p[e]);
        SCHED();
        const u32 vfl = lds_peek(fl);
        SCHED();
#pragma unroll
        for (int e = 8; e < 16; e++) p[e] = __builtin_amdgcn_exp2f(p[e]);
        SCHED();
        if (__builtin_amdgcn_ballot_w64(vfl < 3u)) prog_wait(fl, 3);
        asm volatile("" ::: "memory");
        v8 = ldv(8); v0 = ldv(0); vf[1] = ldv(7);
        SCHED();
        const u32x4 pa = pk8(p, 0), pb = pk8(p, 8);
        O = f32x16{};
        O = mfma32(v8.a, pa & u32x4{msk[0], msk[1], msk[2], msk[3]}, O); O = mfma32(v8.b, pb & u32x4{msk[4], msk[5], msk[6], msk[7]}, O);
        O = mfma32(v0.a, pa & u32x4{nmk[0], nmk[1], nmk[2], nmk[3]}, O); O = mfma32(v0.b, pb & u32x4{nmk[4], nmk[5], nmk[6], nmk[7]}, O);
      }
      auto tile = [&](auto self, auto I) -> void {
        constexpr int i = I;
        if (i < ib) return;
        if constexpr (i == BAL1) __builtin_amdgcn_s_setprio(1);
        if constexpr (i == BAL0) __builtin_amdgcn_s_setprio(0);
        SCHED();
        f32x16& Sc = S[i & 1];
        f32x16 z = {};
        if constexpr (i > 1) z = mfma32(kf[(i - 1) & 1].a, q0, z);
        if constexpr (i > 2) kf[i & 1] = ldk(i - 2);
        if constexpr (i > 1) vf[(i - 1) & 1] = ldv(i - 1);
        SCHED();
#pragma unroll
        for (int e = 0; e < 8; e++) Sc[e] = __builtin_amdgcn_exp2f(Sc[e]);
        SCHED();
        if constexpr (i > 1) S[(i - 1) & 1] = mfma32k8(kf[(i - 1) & 1].b, q1, z);
        const u32x4 pa = pk8(Sc, 0);
        SCHED();
#pragma unroll
        for (int e = 8; e < 12; e++) Sc[e] = __builtin_amdgcn_exp2f(Sc[e]);
        SCHED();
        O = mfma32(vf[i & 1].a, pa, O);
#pragma unroll
        for (int e = 12; e < 16; e++) Sc[e] = __builtin_amdgcn_exp2f(Sc[e]);
        const u32x4 pb = pk8(Sc, 8);
        SCHED();
        O = mfma32(vf[i & 1].b, pb, O);
        if constexpr (i > 1) self(self, IC(i - 1));
      };
      tile(tile, IC(7));
      SCHED();
      { const u32 lu = f2u(O[10]); auto r = __builtin_amdgcn_permlane32_swap(lu, lu, false, false); l = u2f(r[0]); }
      if (pass == 1 || __all(l > 8.67e-19f && l < 1.15e18f)) break;
      q1.y = 0u;
#pragma unroll 1
      for (int i = ib; i < 8; i++) {
        const f32x16 s = qk(ldk(i));
#pragma unroll
        for (int e = 0; e < 16; e += 2) m = fmaxf(fmaxf(m, s[e]), s[e + 1]);
      }
      m = xhalf_max(m);
      kf[1] = ldk(7); kf[0] = ldk(6);
    }
    STAMP(6);
    float inv = __builtin_amdgcn_rcpf(l);
    asm volatile("s_nop 1" : "+v"(inv));
    char* Og = ws + WS_O + (size_t)(16 * qb + 2 * w) * 18432;
    gst128(u32x4{pk2(vmul(O[0], inv), vmul(O[1], inv)), pk2(vmul(O[2], inv), vmul(O[3], inv)), pk2(vmul(O[4], inv), vmul(O[5], inv)), pk2(vmul(O[6], inv), vmul(O[7], inv))},
           oo, Og + (h >> 1) * 1024 + (h & 1) * 512);
    if (hh == 0) gst32(pk2(vmul(O[8], inv), vmul(O[9], inv)), (c >> 4) * 18432 + (c & 15) * 4, Og + 16384 + h * 64);
    publish(fb, F_O + (((4 * qb + (w & 3)) * 8 + lane) * 64 + (w >> 2) * 32 + h) * 8, tag, lane < 8);
    {
      __builtin_amdgcn_s_setprio(0);
      const int g = lane >> 4;
      prog_wait(sb + W_XCH + 32 + 4 * (lane & 3), 4);
      const u32 wa2 = sb + L_W + ((4 * g + (q4 >> 1)) * 18 + 16 + (q4 & 1)) * 160 + 8 * (lane & 3);
      u32x4 wx[2], wr[10];
      sfor<2>([&](auto J) { const int i = J ? 8 + (wv & 1) : wv; const u32 a = wa2 + (i / 5) * 46080 + (i % 5) * 32; wx[J] = cat(tr_read(a), tr_read(a + 5760)); });
      prog_set(sb + W_PROG + 4 * wv, 4);
      const u32 wa = sb + L_W + wv * 11520 + (18 * (g >> 1) + 4 * (g & 1) + q4) * 160 + 8 * (lane & 3), wd = sb + L_W + wv * 11520 + lane * 16;
      sfor<10>([&](auto I) { const u32 a = wa + (I / 5) * 5760 + (I % 5) * 32; wr[I] = cat(tr_read(a), tr_read(a + 1280)); });
      prog_wait(sb + W_PROG + 4 * (lane & 7), 4);
      sfor<2>([&](auto J) { const int i = J ? 8 + (wv & 1) : wv; st128(sb + L_Q + i * 1024 + lane * 16, wx[J]); });
      sfor<10>([&](auto I) { st128(wd + (I / 5) * 5760 + (I % 5) * 1024, wr[I]); });
      prog_set(sb + W_PROG + 4 * wv, 5);
    }
      if (b == 255 && wv == 4) {
#pragma unroll 1
        for (int spin = 0; spin < (1 << 18); spin++) {
          u32x4 v[9];
          sfor<9>([&](auto I) { gl128c<0>(v[I], (lane >> 4) * 2048 + (lane & 15) * 16, fb + F_U + I * 8192); });
          asm volatile("s_waitcnt vmcnt(0)" : "+v"(v[0]), "+v"(v[1]), "+v"(v[2]), "+v"(v[3]), "+v"(v[4]), "+v"(v[5]), "+v"(v[6]), "+v"(v[7]), "+v"(v[8])::"memory");
          u32 bad = 0;
#pragma unroll
          for (int i = 0; i < 9; i++) bad |= (v[i].x ^ tag.x) | (v[i].y ^ tag.y) | (v[i].z ^ tag.x) | (v[i].w ^ tag.y);
          if (!__builtin_amdgcn_ballot_w64(bad != 0)) break;
          __builtin_amdgcn_s_sleep(16);
        }
        if (lane == 0) gst64(tag, F_EP, fb);
      }
    STAMP_END();
    return;
  }
  {
    const int g = lane >> 4, l15 = lane & 15, q = l15 >> 2;
    const int r7 = lane / 9, c9 = lane - 9 * r7;
    const u32 xw = sb + L_X + cw * 2304;
    const int m16 = 16 * (rt >> 2) + 2 * (rt & 3) + 8 * (cw >> 1) + (cw & 1);
    if (__builtin_amdgcn_ballot_w64(lds_peek(sb + W_PROG + 4 * (spare ? (lane & 3) : (lane & 7))) < (u32)WOGATE)) { publish(fb, ypub, tag, lane < 16); ypub = 0; }
    prog_wait(sb + W_XCH + (spare ? 16 + 4 * (lane & 7) : 32 + 4 * (lane & 3)), 3);
    prog_wait(sb + W_PROG + 4 * (spare ? (lane & 3) : (lane & 7)), WOGATE);
    STAMP(9);
    {
      const u16* xp = x + (size_t)(16 * m16) * 576 + 72 * ct;
      dma16n<63>(xp, r7 * 1152 + 16 * c9, xw); dma16n<63>(xp + 7 * 576, r7 * 1152 + 16 * c9, xw + 1008); dma16n<63>(xp + 9 * 576, r7 * 1152 + 16 * c9, xw + 1296);
      wo_load(0, 4);
      wait_vm<7>();
      if (ypub && lane < 16) gst64(tag, ypub, fb);
      wo_load(4, YPOS);
    }
    const u32 yf = F_Y + ((rt * 8 + ct) * 80 + (lane < 18 ? 18 * cw + lane : 72 + (cw >> 1))) * 8;
    u32x2 yv;
    asm volatile("global_load_dwordx2 %0, %1, %2 sc1" : "=v"(yv) : "v"(yf), "s"(fb));
    wo_load(YPOS, 24);
    STAMP(10);
    asm volatile("s_waitcnt vmcnt(%1)" : "+v"(yv) : "i"(24 - YPOS) : "memory");
    if (__builtin_amdgcn_ballot_w64(yv.x != tag.x || yv.y != tag.y)) await<POLL_SLEEP>(fb, yf, tag);
    STAMP(11);
    {
      const char* yp = ws + WS_Y2 + (size_t)(rt * 576 + 72 * ct + 18 * cw) * 128;
      dma16c(yp, lane * 16, sb + L_Y + cw * 2304); dma16c(yp + 1024, lane * 16, sb + L_Y + cw * 2304 + 1024); dma16c(yp + 1280, lane * 16, sb + L_Y + cw * 2304 + 1280);
    }
    u32 gtu;
    gl32c<0>(gtu, (16 * m16 + l15) * 4, ws + WS_G);
    wait_vm<4>();
    prog_set(sb + W_XCH + 4 * wv, 4);
    asm volatile("s_waitcnt vmcnt(0)" : "+v"(gtu)::"memory");
    prog_set(sb + W_XCH + 4 * wv, 5);
    prog_wait(sb + W_XCH + 32 + 4 * (lane & 3), 5);
    const float gt = u2f(gtu);
    f32x4 acc[5]; u32x2 tb[5];
    const u32 xa = xw + l15 * 144 + 8 * g;
    {
      const float g1 = rbf(1.f - gt);
      const u32 ya = sb + L_Y + (4 * g + q) * 128 + 32 * cw + 8 * (lane & 3);
      sfor<5>([&](auto NT) {
        const u32x2 y = tr_read(ya + NT * 2048), xv = lds64(xa + NT * 32);
        tb[NT] = u32x2{opaque(pk2(vmul(vmul(g1, lo2f(xv.x)), lo2f(y.x)), vmul(vmul(g1, hi2f(xv.x)), hi2f(y.x)))), opaque(pk2(vmul(vmul(g1, lo2f(xv.y)), lo2f(y.y)), vmul(vmul(g1, hi2f(xv.y)), hi2f(y.y))))};
      });
    }
    const u32 wl = opaque(sb + L_W + lane * 16), wh = opaque(wl + 46080), wq = opaque(sb + L_Q + lane * 16);
    u32x4 wf[3][5];
    auto rd = [&](auto KS) {
      sfor<5>([&](auto NT) {
        constexpr int ks = KS;
        if constexpr (ks < 16) wf[ks % 3][NT] = lds128((ks < 8 ? wl : wh) + (ks & 7) * 5760 + NT * 1024);
        else wf[ks % 3][NT] = lds128(wq + (5 * (ks - 16) + NT) * 1024);
      });
    };
    STAMP(6);
    u32x4 of[16]; u32 oe[8];
    const char* ob = ws + WS_O + (size_t)m16 * 18432;
    {
      const u32 oo = F_O + ((rt * 8 + ct) * 64 + (cw >> 1) * 32 + (lane & 31)) * 8;
      prog_wait(sb + W_PROG + (spare || OGATE == 2 ? 4 * (lane & 7) : 16 * (cw >> 1) + 4 * (lane & 3)), 4);
      __builtin_amdgcn_s_sleep(ODELAY);
      u32 miss = ~0u;
      sfor<4>([&](auto G) {
#pragma unroll 1
        for (int spin = 0; (miss & (0xffu << (8 * G))) && spin < (1 << 20); spin++) {
          u32x2 v;
          asm volatile("global_load_dwordx2 %0, %1, %2 sc1\n\ts_waitcnt vmcnt(0)" : "=&v"(v) : "v"(oo), "s"(fb) : "memory");
          miss = (u32)__builtin_amdgcn_ballot_w64(v.x != tag.x || v.y != tag.y);
          if (miss & (0xffu << (8 * G))) __builtin_amdgcn_s_sleep(POLL_FAST);
        }
        sfor<4>([&](auto F) { gl128c<F * 1024>(of[4 * G + F], lane * 16, ob + G * 4096); });
      });
    }
    STAMP(12);
    __builtin_amdgcn_s_setprio(3);
    sfor<8>([&](auto F) { gl32c<(F >> 2) * 1024 + (F & 3) * 64>(oe[F], 256 * g + 4 * l15, ob + 16384); });
    prog_wait(sb + W_PROG + 4 * (lane & 7), 5);
    rd(IC(0)); rd(IC(1));
    sfor<16>([&](auto KS) {
      constexpr int ks = KS;
      SCHED();
      if constexpr (ks % 2 == 0) asm volatile("s_waitcnt vmcnt(%2)" : "+v"(of[ks]), "+v"(of[ks + 1]) : "i"(22 - ks) : "memory");
      rd(IC(ks + 2));
      sfor<5>([&](auto NT) { acc[NT] = mfma16(wf[ks % 3][NT], of[ks], ks == 0 ? f32x4{0.f, 0.f, 0.f, 0.f} : acc[NT]); });
      sfor<5>([&](auto) { __builtin_amdgcn_sched_group_barrier(0x8, 1, 0); __builtin_amdgcn_sched_group_barrier(0x100, 1, 0); });
    });
    SCHED();
    asm volatile("s_waitcnt vmcnt(0)" : "+v"(oe[0]), "+v"(oe[1]), "+v"(oe[2]), "+v"(oe[3]), "+v"(oe[4]), "+v"(oe[5]), "+v"(oe[6]), "+v"(oe[7])::"memory");
    sfor<5>([&](auto NT) { acc[NT] = mfma16(wf[1][NT], u32x4{oe[0], oe[1], oe[2], oe[3]}, acc[NT]); });
    sfor<5>([&](auto NT) { acc[NT] = mfma16(wf[2][NT], u32x4{oe[4], oe[5], oe[6], oe[7]}, acc[NT]); });
    SCHED();
    STAMP(13);
    sfor<5>([&](auto NT) {
      constexpr int nt = NT;
      if (nt < 4 || g < 2)
        st64(xa + nt * 32, u32x2{pk2(fmaf(gt, acc[nt][0], lo2f(tb[nt].x)), fmaf(gt, acc[nt][1], hi2f(tb[nt].x))), pk2(fmaf(gt, acc[nt][2], lo2f(tb[nt].y)), fmaf(gt, acc[nt][3], hi2f(tb[nt].y)))});
    });
    char* orow = (char*)(out + (size_t)(16 * m16) * 576 + 72 * ct);
    u32x4 t[3];
#pragma unroll
    for (int it = 0; it < 3; it++) t[it] = lds128(xw + it * 1008 + lane * 16);
#pragma unroll
    for (int it = 0; it < 3; it++)
      if (lane < (it < 2 ? 63 : 18)) asm volatile("global_store_dwordx4 %0, %1, %2 " OUTPOL "\n\ts_nop 1" ::"v"((7 * it + r7) * 1152 + 16 * c9), "v"(t[it]), "s"(orow) : "memory");
  }
  STAMP_END();
}
extern "C" __global__ void gm_end() {}

extern "C" void kernel_launch(void* const* d_in, const int* in_sizes, int n_in, void* d_out, int out_size, void* d_ws, size_t ws_size,
                              hipStream_t stream) {
  (void)hipFuncSetAttribute((const void*)gm_main, hipFuncAttributeMaxDynamicSharedMemorySize, L_END);
  gm_main<<<256, 768, L_END, stream>>>((const u16*)d_in[0], (const u16*)d_in[1], (char*)d_ws, (const u16*)d_in[4], (const u16*)d_in[5], (const u16*)d_in[6],
                                       (const u16*)d_in[7], (const u16*)d_in[2], (const u16*)d_in[8], (const u16*)d_in[9], (u16*)d_out);
}
```

```cpp
#include <hip/hip_runtime.h>
#include <hip/hip_bf16.h>
#include <utility>

typedef unsigned short u16;
typedef unsigned int u32;
typedef u32 u32x2 __attribute__((ext_vector_type(2)));
typedef u32 u32x4 __attribute__((ext_vector_type(4)));
typedef short s16x4 __attribute__((ext_vector_type(4)));
typedef __bf16 bf16x8 __attribute__((ext_vector_type(8)));
typedef __bf16 bf16x2 __attribute__((ext_vector_type(2)));
typedef float f32x2 __attribute__((ext_vector_type(2)));
typedef float f32x4 __attribute__((ext_vector_type(4)));
typedef float f32x16 __attribute__((ext_vector_type(16)));
#define LAS __attribute__((address_space(3)))
#define GAS __attribute__((address_space(1)))
#define DEVI __device__ __forceinline__
#define IC(n) std::integral_constant<int, (n)>{}

constexpr size_t WS_QKV = 0;
constexpr size_t WS_O   = WS_QKV + 96ull * 2048 * 40;
constexpr size_t WS_UR  = WS_O + 32ull * 2048 * 40;
constexpr size_t WS_Y2  = WS_UR + 576ull * 4096;
constexpr size_t WS_G   = WS_Y2 + 576ull * 4096;
constexpr size_t WS_F   = WS_G + 8192;
constexpr u32 F_EP = 0, F_1 = 1024, F_U = F_1 + 256 * 512, F_O = F_U + 288 * 256, F_Y = F_O + 256 * 512;

template <typename F, int... I> DEVI void sfor_impl(F&& f, std::integer_sequence<int, I...>) { (f(std::integral_constant<int, I>{}), ...); }
template <int N, typename F> DEVI void sfor(F&& f) { sfor_impl(f, std::make_integer_sequence<int, N>{}); }

DEVI u32 pk2(float a, float b) { f32x2 v = {a, b}; return __builtin_bit_cast(u32, __builtin_convertvector(v, bf16x2)); }
DEVI u32 f2u(float a) { return __builtin_bit_cast(u32, a); }
DEVI float u2f(u32 a) { return __builtin_bit_cast(float, a); }
DEVI float lo2f(u32 w) { return __builtin_bit_cast(float, w << 16); }
DEVI float hi2f(u32 w) { return __builtin_bit_cast(float, w & 0xffff0000u); }
DEVI float rbf(float a) { return lo2f(pk2(a, a)); }
DEVI f32x4 mfma16(u32x4 a, u32x4 b, f32x4 c) { return __builtin_amdgcn_mfma_f32_16x16x32_bf16(__builtin_bit_cast(bf16x8, a), __builtin_bit_cast(bf16x8, b), c, 0, 0, 0); }
DEVI f32x16 mfma32(u32x4 a, u32x4 b, f32x16 c) { return __builtin_amdgcn_mfma_f32_32x32x16_bf16(__builtin_bit_cast(bf16x8, a), __builtin_bit_cast(bf16x8, b), c, 0, 0, 0); }
DEVI f32x16 mfma32k8(u32x2 a, u32x2 b, f32x16 c) { return __builtin_amdgcn_mfma_f32_32x32x8bf16_1k(__builtin_bit_cast(s16x4, a), __builtin_bit_cast(s16x4, b), c, 0, 0, 0); }
DEVI u32x2 tr_read(u32 addr) { return __builtin_bit_cast(u32x2, __builtin_amdgcn_ds_read_tr16_b64_v4i16((LAS s16x4*)addr)); }
DEVI u32x4 lds128(u32 addr) { return *(LAS u32x4*)addr; }
DEVI u32x2 lds64(u32 addr) { return *(LAS u32x2*)addr; }
DEVI u32 lds32(u32 addr) { return *(LAS u32*)addr; }
DEVI void st128(u32 addr, u32x4 v) { *(LAS u32x4*)addr = v; }
DEVI void st64(u32 addr, u32x2 v) { *(LAS u32x2*)addr = v; }
DEVI void st32(u32 addr, u32 v) { *(LAS u32*)addr = v; }
DEVI u32x4 cat(u32x2 a, u32x2 b) { return u32x4{a.x, a.y, b.x, b.y}; }
DEVI float vfma(float a, float b, float c) { float d; asm("v_fma_f32 %0, %1, %2, %3" : "=v"(d) : "v"(a), "v"(b), "v"(c)); return d; }
DEVI float vmul(float a, float b) { float d; asm("v_mul_f32 %0, %1, %2" : "=v"(d) : "v"(a), "v"(b)); return d; }
DEVI float vadd(float a, float b) { float d; asm("v_add_f32 %0, %1, %2" : "=v"(d) : "v"(a), "v"(b)); return d; }
DEVI f32x4 vadd4(f32x4 a, f32x4 b) { return f32x4{vadd(a[0], b[0]), vadd(a[1], b[1]), vadd(a[2], b[2]), vadd(a[3], b[3])}; }
DEVI u32 opaque(u32 v) { asm volatile("" : "+v"(v)); return v; }
DEVI void dma16(const void* sbase, u32 voff, u32 ldsbase) {
  asm volatile("s_mov_b32 m0, %2\n\ts_nop 0\n\tglobal_load_lds_dwordx4 %0, %1" ::"v"(voff), "s"(sbase), "s"(ldsbase) : "memory");
}
template <int NL> DEVI void dma16n(const void* sbase, u32 voff, u32 ldsbase) {
  asm volatile("s_mov_b32 m0, %2\n\ts_mov_b32 exec_hi, %3\n\tglobal_load_lds_dwordx4 %0, %1\n\ts_mov_b32 exec_hi, -1" ::"v"(voff), "s"(sbase), "s"(ldsbase),
               "i"((1u << (NL - 32)) - 1u) : "memory");
}
template <int OFF> DEVI void gl128(u32x4& d, u32 voff, const void* sbase) { asm volatile("global_load_dwordx4 %0, %1, %2 offset:%3" : "=v"(d) : "v"(voff), "s"(sbase), "i"(OFF)); }
template <int OFF> DEVI void gl64(u32x2& d, u32 voff, const void* sbase) { asm volatile("global_load_dwordx2 %0, %1, %2 offset:%3" : "=v"(d) : "v"(voff), "s"(sbase), "i"(OFF)); }
DEVI void dma16c(const void* sbase, u32 voff, u32 ldsbase) {
  asm volatile("s_mov_b32 m0, %2\n\ts_nop 0\n\tglobal_load_lds_dwordx4 %0, %1 sc1" ::"v"(voff), "s"(sbase), "s"(ldsbase) : "memory");
}
template <int OFF> DEVI void gl128c(u32x4& d, u32 voff, const void* sbase) { asm volatile("global_load_dwordx4 %0, %1, %2 offset:%3 sc1" : "=v"(d) : "v"(voff), "s"(sbase), "i"(OFF)); }
template <int OFF> DEVI void gl32c(u32& d, u32 voff, const void* sbase) { asm volatile("global_load_dword %0, %1, %2 offset:%3 sc1" : "=v"(d) : "v"(voff), "s"(sbase), "i"(OFF)); }
DEVI void gst128(u32x4 v, u32 voff, void* sbase) { asm volatile("global_store_dwordx4 %0, %1, %2 sc1\n\ts_nop 1" ::"v"(voff), "v"(v), "s"(sbase) : "memory"); }
DEVI void gst64(u32x2 v, u32 voff, void* sbase) { asm volatile("global_store_dwordx2 %0, %1, %2 sc1" ::"v"(voff), "v"(v), "s"(sbase) : "memory"); }
DEVI void gst32(u32 v, u32 voff, void* sbase) { asm volatile("global_store_dword %0, %1, %2 sc1" ::"v"(voff), "v"(v), "s"(sbase) : "memory"); }
#define CODE_PF(from, to, ck, step, lds)                                                                                              \
  do {                                                                                                                                \
    u32 lo_, hi_, len_;                                                                                                               \
    asm volatile("s_getpc_b64 s[20:21]\n\ts_add_u32 %0, s20, " from "@rel32@lo+4\n\ts_addc_u32 %1, s21, " from "@rel32@hi+12\n\ts_mov_b32 %2, " to "-" from \
                 : "=s"(lo_), "=s"(hi_), "=s"(len_)::"s20", "s21", "scc");                                                            \
    const char* code_ = (const char*)(((unsigned long long)hi_ << 32) | lo_);                                                         \
    for (u32 o_ = (ck) * 1024; o_ < len_; o_ += (step) * 1024) { const u32 a_ = o_ + (threadIdx.x & 63) * 16; dma16(code_, a_ < len_ ? a_ : 0u, lds); } \
  } while (0)
template <int N> DEVI void wait_vm() { asm volatile("s_waitcnt vmcnt(%0)" ::"i"(N > 63 ? 63 : N) : "memory"); }
DEVI void wait_lgkm() { asm volatile("s_waitcnt lgkmcnt(0)" ::: "memory"); }
DEVI void bar() { asm volatile("s_barrier" ::: "memory"); }
DEVI u32 rfl(u32 v) { return __builtin_amdgcn_readfirstlane(v); }
#define SCHED() __builtin_amdgcn_sched_barrier(0)
#define STAMP(i)
#define STAMP_END()

constexpr float QSCALE = 0.23570226039551584f * 1.4426950408889634f;

#define POLL_SLEEP 8
#define POLL_FAST 2
#define SPDELAY 24
#define LDPOL "sc1"
#define FSPOL "sc1"
#define CVDELAY 0
#define PUBB 2
#define NMID 1
#define FLB 1
#define PUBD 0
#define PREPFIRST 2
#define OUTPOL "sc1"
#define BAL 1
#define CPRIO 3
#define WOGATE 3
#define YPOS 16
#define CMPRIO 3
#define QDELAY 8
#define OGATE 1
#define ODELAY 6
#define BAL1 4
#define BAL0 1
#define SQD 16
#define UBACK 20
#define POLLW 4
#define VDELAY 3

constexpr int L_SLOT = 32768, L_WOFF = 16384, L_STG = 94208, L_GW = 131072, L_PT = 133120, L_TILE = 17408;
constexpr int L_K = 0, L_V = 21760, L_C = 41536, L_R = L_PT + 1024, L_FL = 152320, L_DUMMY = L_FL + 256, L_Q = 153600, L_END = 163840;
constexpr int KC_STR = 4640, CS_U = 4 * KC_STR, CS_SZ = CS_U + 4096 + 128;
constexpr int L_W = L_C, L_X = L_W + 576 * 160, L_Y = L_X + 9216;
static_assert(L_Y + 9216 <= L_FL && L_C + 2 * CS_SZ <= L_STG && L_C + 4 * CS_SZ <= L_PT && L_PT + L_TILE <= L_FL, "lds map");
constexpr int W_PROG = L_FL, W_XCH = L_FL + 64, W_GO = L_FL + 128, W_TAG = L_FL + 144;

DEVI u32 lds_peek(u32 a) { return __hip_atomic_load((LAS u32*)a, __ATOMIC_RELAXED, __HIP_MEMORY_SCOPE_WORKGROUP); }
DEVI void prog_set(u32 a, u32 n) { asm volatile("" ::: "memory"); __hip_atomic_store((LAS u32*)a, n, __ATOMIC_RELAXED, __HIP_MEMORY_SCOPE_WORKGROUP); }
DEVI void prog_wait(u32 a, u32 n) { while (__builtin_amdgcn_ballot_w64(lds_peek(a) < n)) __builtin_amdgcn_s_sleep(1); asm volatile("" ::: "memory"); }
DEVI void publish(char* fb, u32 off, u32x2 tag, bool on) { wait_vm<0>(); if (on) asm volatile("global_store_dwordx2 %0, %1, %2 " FSPOL ::"v"(off), "v"(tag), "s"(fb) : "memory"); }
template <int SLP> DEVI void await(const char* fb, u32 off, u32x2 tag) {
  for (int spin = 0; spin < (1 << 20); spin++) {
    u32x2 v;
    asm volatile("global_load_dwordx2 %0, %1, %2 " LDPOL "\n\ts_waitcnt vmcnt(0)" : "=&v"(v) : "v"(off), "s"(fb) : "memory");
    if (!__builtin_amdgcn_ballot_w64(v.x != tag.x || v.y != tag.y)) break;
    __builtin_amdgcn_s_sleep(SLP);
  }
}

DEVI void prep_issue(const u16* Bk, const u16* Bv, int row0, int col, u32 tb, u32 pw, int lane) {
  const int r8 = lane >> 3, c8 = lane & 7;
  sfor<2>([&](auto I) {
    const int j = 2 * pw + I;
    int row = row0 + 8 * j + r8 - 3; row = row < 0 ? 0 : row;
    dma16(Bk + col, row * 1152 + 16 * (c8 ^ j), tb + 1024 * j);
    dma16(Bv + col, (row0 + 8 * j + r8) * 1152 + 16 * (c8 ^ j), tb + 9216 + 1024 * j);
  });
  dma16(Bk + col, (row0 + 61 + (r8 > 2 ? 2 : r8)) * 1152 + 16 * c8, tb + 8192);
}
template <typename F> DEVI void prep_compute(int row0, int col, u32 tb, u32 pw, int lane, u32x4 sh, char* ws, F&& mid) {
  const int c8 = lane & 7, cp = 8 * pw + (lane >> 3);
  const u32 o0 = tb + 1024 * c8 + 16 * ((cp >> 2) ^ c8) + 4 * (cp & 3), o1 = tb + 1024 * c8 + 16 * ((cp >> 2) ^ ((c8 + 1) & 7)) + 4 * (cp & 3);
  row0 += 8 * c8;
  u32 kk[11], vv[8];
#pragma unroll
  for (int i = 0; i < 11; i++) kk[i] = lds32((i < 8 ? o0 : o1) + 128 * i);
#pragma unroll
  for (int i = 0; i < 8; i++) vv[i] = lds32(o0 + 9216 + 128 * i);
  if (row0 == 0) { kk[0] = 0; kk[1] = 0; kk[2] = 0; }
  const u32 go = 2 * cp * 4096 + 2 * (2040 - row0);
  char* dst = ws + WS_UR + (size_t)col * 4096;
  float r[8];
  {
    const float a0 = lo2f(sh.x), a1 = hi2f(sh.x), a2 = lo2f(sh.y), a3 = hi2f(sh.y);
#pragma unroll
    for (int i = 0; i < 8; i++) r[i] = vmul(vfma(lo2f(kk[i + 3]), a0, vfma(lo2f(kk[i + 2]), a1, vfma(lo2f(kk[i + 1]), a2, vmul(lo2f(kk[i]), a3)))), lo2f(vv[i]));
    gst128(u32x4{pk2(r[7], r[6]), pk2(r[5], r[4]), pk2(r[3], r[2]), pk2(r[1], r[0])}, go, dst);
  }
  mid();
  {
    const float a0 = lo2f(sh.z), a1 = hi2f(sh.z), a2 = lo2f(sh.w), a3 = hi2f(sh.w);
#pragma unroll
    for (int i = 0; i < 8; i++) r[i] = vmul(vfma(hi2f(kk[i + 3]), a0, vfma(hi2f(kk[i + 2]), a1, vfma(hi2f(kk[i + 1]), a2, vmul(hi2f(kk[i]), a3)))), hi2f(vv[i]));
    gst128(u32x4{pk2(r[7], r[6]), pk2(r[5], r[4]), pk2(r[3], r[2]), pk2(r[1], r[0])}, go, dst + 4096);
  }
}

template <int NR> DEVI void conv_mfma(u32 cs, int rho0, int lane, f32x4 (&acc)[8]) {
  const int g = lane >> 4, l15 = lane & 15;
  u32 aa = cs + (l15 & 3) * KC_STR + 2 * (64 * (l15 >> 2) + 8 * g) + 64 * rho0;
  u32 ba = opaque(cs + CS_U + 2 * (1852 - 1792 - 4 * l15 + 8 * g) + 64 * rho0), bb = opaque(ba + 8);
  u32x4 A[8], G[8];
  sfor<5>([&](auto J) { constexpr int p = J < 4 ? J + 2 : 1; A[p] = lds128(aa + p * 512); G[p] = cat(lds64(ba + (7 - p) * 512), lds64(bb + (7 - p) * 512)); SCHED(); });
#pragma unroll 1
  for (int rho = 0; rho < NR; rho++) {
    constexpr int pa[36] = {2, 2, 3, 3, 2, 4, 3, 4, 2, 5, 1, 1, 2, 1, 3, 1, 4, 1, 5, 1, 6, 2, 0, 3, 0, 4, 0, 5, 0, 1, 0, 6, 0, 7, 0, 0};
    constexpr int pe[36] = {2, 3, 2, 3, 4, 2, 4, 3, 5, 2, 1, 2, 1, 3, 1, 4, 1, 5, 1, 6, 1, 0, 2, 0, 3, 0, 4, 0, 5, 0, 1, 0, 6, 0, 7, 0};
    constexpr int rdc[36] = {18, 19, 20, 21, 22, 23, 0, 1, 2, -1, -1, -1, -1, -1, -1, -1, -1, -1, -1, -1, -1, 6, 7, 8, 9, 10, 11, 12, 13, 14, 15, 16, 17, 3, 4, 5};
    sfor<36>([&](auto I) {
      constexpr int i = I, r = rdc[i], x = r / 3;
      if constexpr (i == 35) __builtin_amdgcn_s_setprio(CMPRIO);
      if constexpr (i == 9) { aa = opaque(aa + 64); ba = opaque(ba + 64); bb = opaque(bb + 64); }
      acc[pa[i] + pe[i]] = mfma16(A[pa[i]], G[pe[i]], acc[pa[i] + pe[i]]);
      if constexpr (r >= 0) {
        if constexpr (r % 3 == 0) A[x] = lds128(aa + x * 512);
        else if constexpr (r % 3 == 1) { const u32x2 t = lds64(ba + (7 - x) * 512); G[x].x = t.x; G[x].y = t.y; }
        else { const u32x2 t = lds64(bb + (7 - x) * 512); G[x].z = t.x; G[x].w = t.y; }
      }
      SCHED();
    });
  }
  SCHED();
}
DEVI void conv_out(f32x4 y, u32x2 u, float Dv, u32 off, char* ydst) {
  const float y0 = vfma(hi2f(u.y), Dv, y[0]), y1 = vfma(lo2f(u.y), Dv, y[1]), y2 = vfma(hi2f(u.x), Dv, y[2]), y3 = vfma(lo2f(u.x), Dv, y[3]);
  gst64(u32x2{pk2(y0, y1), pk2(y2, y3)}, off, ydst);
}

DEVI float xhalf_max(float m) { const u32 mu = f2u(m); auto r = __builtin_amdgcn_permlane32_swap(mu, mu, false, false); return fmaxf(u2f(r[0]), u2f(r[1])); }

extern "C" __global__ __launch_bounds__(768, 3) void gm_main(const u16* __restrict__ x, const u16* __restrict__ wqkv, char* __restrict__ ws,
                                                             const u16* __restrict__ Bk, const u16* __restrict__ Bv, const u16* __restrict__ shk,
                                                             const u16* __restrict__ ssmk, const u16* __restrict__ wo, const u16* __restrict__ ssmD,
                                                             const u16* __restrict__ gw, u16* __restrict__ out) {
  extern __shared__ __attribute__((aligned(16))) char smem[];
  const u32 sb = (u32)(size_t)(LAS char*)smem;
  const int b = blockIdx.x, xcd = b & 7, idx = b >> 3;
  const int lane = threadIdx.x & 63;
  const u32 wv = rfl(threadIdx.x >> 6);
  const bool spare = idx >= 28;
  char* const fb = ws + WS_F;
  const int qb = ((idx >> 2) + 1) & 7, h = 4 * xcd + (idx & 3);
  const int chb = wv >= 8 ? 64 * xcd + 2 * idx : 512 + 2 * h;
  u32x2 pre[2], fv; u32x4 cur[2];
  const u32 fo = F_U + ((chb >> 1) * 32 + 24 - 8 * (wv & 3) + (lane & 7)) * 8;
  u32 pof = 0; bool pon = false;
  STAMP(0);
  {
    const int hg = xcd & 1, ri = idx & 3, rb = 4 * (xcd >> 1) + ri, jt = idx >> 2;
    const int r8 = lane >> 3, c8 = lane & 7;
    const int rot = ((0x124924u >> (2 * (jt + ri))) & 3) * 3;
    const u32 xo = opaque((32 * wv + r8) * 1152 + 16 * (c8 ^ r8));
    asm volatile("" ::"s"(x), "s"(wqkv), "s"(Bk), "s"(Bv));
    if (wv < 4 && !spare) {
      const u32 lw = wv;
      const char* xb = (const char*)(x + (size_t)rb * 128 * 576);
      sfor<4>([&](auto I) { dma16(xb + rot * 128 + I * 9216, xo, sb + (4 * lw + I) * 1024); });
      u32 wof[2];
#pragma unroll
      for (int i = 0; i < 2; i++) {
        const int ch = (lane & 15) ^ (((lane >> 4) | (i << 2)) << 1), ln = 14 * hg + 2 * jt + (ch >> 3);
        wof[i] = ((lane >> 4) + 16 * lw) * 3456 + 128 * (ln > 26 ? 26 : ln) + 16 * (ch & 7);
      }
      const char* wb = (const char*)wqkv;
      auto issue = [&](int c) {
        const u32 sl = sb + (c & 3) * L_SLOT;
        c += rot; c = c >= 9 ? c - 9 : c;
        sfor<4>([&](auto I) { dma16(xb + c * 128 + I * 9216, xo, sl + (4 * lw + I) * 1024); });
        sfor<4>([&](auto I) { dma16(wb + (64 * c + 4 * I) * 3456, wof[I >> 1], sl + L_WOFF + (4 * lw + I) * 1024); });
      };
      sfor<4>([&](auto I) { dma16(wb + (64 * rot + 4 * I) * 3456, wof[I >> 1], sb + L_WOFF + (4 * lw + I) * 1024); });
      prep_issue(Bk, Bv, 64 * idx, 64 * xcd, sb + L_PT, lw, lane);
#pragma unroll 1
      for (int n = 1; n < 8; n++) { issue(n); wait_vm<8>(); bar(); }
      issue(8);
      wait_vm<8>(); bar();
      __builtin_amdgcn_s_sleep(2);
      {
        const char* wp = (const char*)wo + (idx == 27 ? 663552 - 24576 : idx * 23712) + lw * 6144;
        sfor<6>([&](auto I) { dma16(wp + I * 1024, lane * 16, sb + L_DUMMY); });
      }
      wait_vm<6>(); bar();
      bar();
    } else if (wv >= 8 || (spare && wv >= 4)) {
      const bool ex = wv < 8;
      const int row0 = ex ? 256 * xcd + 64 * ri : 64 * idx, col = ex ? 512 : 64 * xcd;
      const u32 tb = sb + (ex ? 0 : L_PT);
      u32x2 e;
      asm volatile("global_load_dwordx2 %0, %1, %2 sc1" : "=v"(e) : "v"(F_EP), "s"(fb));
      u32x4 sh;
      gl128<0>(sh, 8 * col + 16 * (8 * (wv & 3) + r8), shk);
      if (spare) prep_issue(Bk, Bv, row0, col, tb, wv & 3, lane);
      {
        const u32 o = (threadIdx.x & 255) * 16, op = o ? o - 8 : 0;
        sfor<2>([&](auto J) { const u16* kp = ssmk + (size_t)(chb + J) * 2048; gl128<0>(cur[J], o, kp); gl64<0>(pre[J], op, kp); });
      }
      {
        const int t3 = (4 * xcd + (idx & 3)) * 8 + (idx >> 2), k = wv & 3;
        const u32 mo = k == 0 ? F_1 + (h * 8 + qb) * 512 : k == 1 ? F_O + t3 * 512 : k == 2 ? F_Y + t3 * 640 : F_U + (chb >> 1) * 256;
        asm volatile("s_mov_b32 m0, %2\n\ts_nop 0\n\tglobal_load_lds_dword %0, %1 sc1" ::"v"(mo + (lane < (k == 3 ? 4 : k == 2 ? 10 : 8) ? lane : 0) * 64), "s"(fb), "s"(sb + L_DUMMY) : "memory");
      }
      if (wv == 8) st32(sb + L_FL + 4 * lane, 0u);
      if (spare) wait_vm<5>();
      bar();
      asm volatile("s_waitcnt vmcnt(5)" : "+v"(e), "+v"(sh)::"memory");
      e.x += 1u; e.y += e.x == 0u;
      if (wv == 8) st64(sb + W_TAG, e);
      prep_compute(row0, col, tb, wv & 3, lane, sh, ws, [&]() { if (!spare) { SCHED(); bar(); SCHED(); } });
      wait_lgkm();
      bar();
      if (!spare) sfor<PUBB>([&](auto) { bar(); });
      asm volatile("s_waitcnt vmcnt(0)" : "+v"(cur[0]), "+v"(cur[1]), "+v"(pre[0]), "+v"(pre[1])::"memory");
      __builtin_amdgcn_s_sleep(PUBD);
      publish(fb, F_U + (((col >> 1) + 8 * (wv & 3) + lane) * 32 + (row0 >> 6)) * 8, e, lane < 8);
      if (!spare) sfor<8 - NMID - PUBB - FLB>([&](auto) { bar(); });
      else if (wv >= 8) __builtin_amdgcn_s_sleep(SPDELAY);
      else __builtin_amdgcn_s_sleep(16);
      asm volatile("global_load_dwordx2 %0, %1, %2 sc1" : "=v"(fv) : "v"(fo), "s"(fb));
      if (!spare) sfor<FLB>([&](auto) { bar(); });
    } else if (spare) {
      CODE_PF("gm_main", "gm_end", 4 * ri + wv, 16, sb + L_DUMMY);
      bar(); bar();
    } else {
      const int cw = wv - 4, g = lane >> 4, l15 = lane & 15, wm = cw & 1, wn = cw >> 1;
      const u32 xra = sb + (64 * wm + l15) * 128 + 16 * (g ^ (l15 & 7));
      const u32 xb[2][2] = {{opaque(xra), opaque(xra + 2 * L_SLOT)}, {opaque(xra ^ 64), opaque((xra ^ 64) + 2 * L_SLOT)}};
      u32x4 wf[2][4], xf[2][4];
      f32x4 acc[4][4];
      const int ln = 14 * hg + 2 * jt + wn;
      {
        const int r64 = 2 * rb + wm, seg = ln / 9, lc = ln - 9 * seg, hc = (64 * lc) / 18 + (lane >> 2), li = lane & 1, nx = (lane >> 1) & 1;
        pon = ln == 27 ? lane < 16 : (lane < 20 && hc < 32 && ((18 * hc + 17 * li) >> 6) == lc && (!nx || (seg > 0 && r64 < 28)));
        pof = opaque(ln == 27 ? F_Y + (((4 * (r64 >> 2) + 2 * (r64 & 1)) * 8 + lane) * 80 + 72 + ((r64 >> 1) & 1)) * 8 : F_1 + ((hc * 8 + (r64 >> 2) + nx) * 64 + seg * 16 + li * 8 + (r64 & 3) + (nx ? 0 : 4)) * 8);
      }
      __builtin_amdgcn_s_setprio(2);
      if (ln == 27) {
        dma16(gw, wm * 128 + lane * 16, sb + L_GW + wm * 128);
        wait_vm<0>();
#pragma unroll
        for (int i = 0; i < 4; i++) acc[i][0] = f32x4{0.f, 0.f, 0.f, 0.f};
#pragma unroll 1
        for (int c = 0; c < 9; c++) {
          bar();
          const int ca = c + rot >= 9 ? c + rot - 9 : c + rot;
#pragma unroll
          for (int ks = 0; ks < 2; ks++) {
            const u32x4 gv = lds128(sb + L_GW + ca * 128 + ks * 64 + 16 * g);
#pragma unroll
            for (int i = 0; i < 4; i++) acc[i][0] = mfma16(gv, lds128(xb[ks][0] + (c & 3) * L_SLOT + 2048 * i), acc[i][0]);
          }
          wait_lgkm();
        }
        bar();
        const float p = rbf(g == 0 ? acc[0][0][0] : g == 1 ? acc[1][0][0] : g == 2 ? acc[2][0][0] : acc[3][0][0]);
        gst32(f2u(rbf(1.f / (1.f + __expf(-p)))), lane * 4, ws + WS_G + (rb * 128 + 64 * wm) * 4);
      } else {
    u32 wb[4][2];
    {
      const int q = l15 >> 2, p = lane & 3, f = q | ((g & 1) << 2);
#pragma unroll
      for (int nt = 0; nt < 4; nt++) {
        const u32 a = sb + L_WOFF + (8 * g + q) * 256 + (((8 * wn + 2 * nt + (p >> 1)) ^ (f << 1)) << 4) + 8 * (p & 1);
        wb[nt][0] = opaque(a); wb[nt][1] = opaque(a + 2 * L_SLOT);
      }
    }
    auto rd = [&](auto KS, auto C) {
      constexpr int ks = KS, c = C, so = (c & 1) * L_SLOT, hi = (c >> 1) & 1;
      sfor<4>([&](auto I) {
        xf[ks][I] = lds128(xb[ks][hi] + so + 2048 * I);
        wf[ks][I] = cat(tr_read(wb[I][hi] + so + ks * 8192), tr_read(wb[I][hi] + so + ks * 8192 + 1024));
      });
    };
    auto mm = [&](auto KS, auto FIRST) {
      constexpr int ks = KS;
      sfor<16>([&](auto I) {
        constexpr int mo[16] = {0, 1, 0, 1, 2, 2, 0, 1, 2, 3, 3, 3, 0, 1, 2, 3}, no[16] = {0, 0, 1, 1, 0, 1, 2, 2, 2, 0, 1, 2, 3, 3, 3, 3};
        constexpr int mt = mo[I], nt = no[I];
        acc[mt][nt] = mfma16(xf[ks][mt], wf[ks][nt], FIRST ? f32x4{0.f, 0.f, 0.f, 0.f} : acc[mt][nt]);
      });
    };
    auto ilv = [&]() {
      sfor<12>([&](auto) { __builtin_amdgcn_sched_group_barrier(0x8, 1, 0); __builtin_amdgcn_sched_group_barrier(0x100, 1, 0); });
      __builtin_amdgcn_sched_group_barrier(0x8, 4, 0);
    };
    char* qo = ws + WS_QKV + (size_t)((2 * rb + wm) * 1728 + 64 * ln) * 128;
    SCHED();
    bar();
    STAMP(1);
    rd(IC(0), IC(0));
    sfor<9>([&](auto C) {
      constexpr int c = C;
      SCHED();
      rd(IC(1), C); mm(IC(0), IC(c == 0)); ilv();
      SCHED();
      if constexpr (c == 8) wait_lgkm();
      bar();
      SCHED();
      if constexpr (c < 8) { rd(IC(0), IC(c + 1)); mm(IC(1), IC(0)); ilv(); }
    });
    STAMP(2);
    const u32 stg = sb + L_STG + cw * 9216;
    const u32 sa = stg + l15 * 144 + 8 * g, ra = stg + r8 * 144 + 16 * c8, go = lane * 16;
    u32x4 t[4][2];
    sfor<6>([&](auto NT) {
      constexpr int nt = NT;
      if constexpr (nt < 4) sfor<4>([&](auto M) { acc[M][nt] = mfma16(xf[1][M], wf[1][nt], acc[M][nt]); });
      SCHED();
      if constexpr (nt > 0 && nt < 5) {
        sfor<4>([&](auto M) {
          const f32x4 v = acc[M][nt - 1];
          st64(sa + (nt - 1) * 2304 + 32 * M, u32x2{pk2(v[0], v[1]), pk2(v[2], v[3])});
        });
        t[nt - 1][0] = lds128(ra + (nt - 1) * 2304); t[nt - 1][1] = lds128(ra + (nt - 1) * 2304 + 1152);
      }
      if constexpr (nt > 1) { gst128(t[nt - 2][0], go, qo + (nt - 2) * 2048); gst128(t[nt - 2][1], go, qo + (nt - 2) * 2048 + 1024); }
      SCHED();
    });
      }
      STAMP(8);
    }
  }
  const int rt = 4 * xcd + (idx & 3), ct = idx >> 2;
  auto wo_load = [&](int r0, int r1) {
    const int r6 = lane / 10, c10 = lane - 10 * r6;
    const u32 vo = (6 * (wv & 3) + r6) * 1152 + 16 * (c10 > 8 ? 8 : c10);
    const char* wb = (const char*)(wo + 72 * ct);
#pragma unroll 4
    for (int i = r0; i < r1; i++) dma16n<60>(wb + i * 24 * 1152, vo, sb + L_W + (4 * i + (wv & 3)) * 960);
  };
  const u32x2 tag = lds64(sb + W_TAG);
  const int cw = wv & 3;
  u32 ypub = 0;
  if (wv >= (spare ? 4u : 8u)) {
    const int tid = threadIdx.x & 255;
    const u32 cb = sb + L_C + (wv >= 8 ? 0 : 2 * CS_SZ);
    __builtin_amdgcn_s_sleep(CVDELAY);
    asm volatile("s_waitcnt vmcnt(0)" : "+v"(fv)::"memory");
    if (__builtin_amdgcn_ballot_w64(fv.x != tag.x || fv.y != tag.y)) { __builtin_amdgcn_s_sleep(UBACK); await<UBACK>(fb, fo, tag); }
    STAMP(2);
    sfor<2>([&](auto J) { dma16c(ws + WS_UR + (size_t)(chb + J) * 4096, tid * 16, cb + CS_U + cw * 1024 + J * CS_SZ); });
    const u32 dv = *(const __attribute__((address_space(4))) u32*)(ssmD + chb);
    {
      const u32 z0 = opaque(cw * KC_STR + 16 * lane), z1 = opaque(CS_U + 4096 + 16 * (lane & 7)), za = cb + (lane < 24 ? z0 : z1);
      st128(za, u32x4{0u, 0u, 0u, 0u}); st128(za + CS_SZ, u32x4{0u, 0u, 0u, 0u});
    }
    f32x4 acc[8];
#pragma unroll
    for (int c = 0; c < 8; c++) { acc[c] = f32x4{0.f, 0.f, 0.f, 0.f}; asm volatile("" : "+v"(acc[c])); }
#pragma unroll
    for (int j = 0; j < 2; j++) {
      const u32 cs = cb + j * CS_SZ + 384 + 16 * tid;
      const u32 w0 = tid ? pre[j].x : 0u, w1 = tid ? pre[j].y : 0u, w2 = cur[j].x, w3 = cur[j].y, w4 = cur[j].z, w5 = cur[j].w;
      const u32 a01 = __builtin_amdgcn_alignbyte(w1, w0, 2), a12 = __builtin_amdgcn_alignbyte(w2, w1, 2), a23 = __builtin_amdgcn_alignbyte(w3, w2, 2),
                a34 = __builtin_amdgcn_alignbyte(w4, w3, 2), a45 = __builtin_amdgcn_alignbyte(w5, w4, 2);
      st128(cs, u32x4{a01, a12, a23, a34});
      st128(cs + KC_STR, u32x4{w1, w2, w3, w4});
      st128(cs + 2 * KC_STR, u32x4{a12, a23, a34, a45});
      st128(cs + 3 * KC_STR, cur[j]);
    }
    STAMP(1);
    wait_vm<0>();
    prog_set(sb + W_XCH + 4 * wv, 1);
    const int tm = cw >> 1, pi = cw & 1;
    const u32 cs = cb + tm * CS_SZ;
    prog_wait(sb + W_XCH + 4 * (wv & 12) + 4 * (lane & 3), 1);
    STAMP(4);
    conv_mfma<4>(cs, 4 * pi, lane, acc);
    STAMP(5);
    __builtin_amdgcn_s_setprio(CPRIO);
    if (spare && wv >= 8) prog_wait(sb + W_XCH + 16 + 8 * tm + 4 * (lane & 1), 2);
    const u32 rA = (spare && wv >= 8 ? sb + L_C + (2 + tm) * CS_SZ : sb + L_R + tm * 8192) + lane * 16;
    if (pi) { sfor<4>([&](auto J) { st128(rA + 4096 + J * 1024, __builtin_bit_cast(u32x4, acc[J])); acc[J] = acc[4 + J]; }); }
    else sfor<4>([&](auto J) { st128(rA + J * 1024, __builtin_bit_cast(u32x4, acc[4 + J])); });
    u32x2 uu[4];
#pragma unroll
    for (int j = 0; j < 4; j++) uu[j] = lds64(cs + CS_U + 4088 - 8 * lane - 512 * (4 * pi + j));
    prog_set(sb + W_XCH + 4 * wv, 2);
    prog_wait(sb + W_XCH + 4 * (wv ^ 1), 2);
    f32x4 t[4];
#pragma unroll
    for (int j = 0; j < 4; j++) t[j] = __builtin_bit_cast(f32x4, lds128(rA + (pi ^ 1) * 4096 + j * 1024));
    wait_lgkm();
    prog_set(sb + W_XCH + 4 * wv, 3);
    const u32 yo = ((lane >> 3) & 3) * 73728 + (lane >> 5) * 64 + (lane & 7) * 8;
    char* yd = ws + WS_Y2 + (chb + tm) * 128 + pi * 1179648;
#pragma unroll
    for (int j = 0; j < 4; j++) conv_out(vadd4(acc[j], t[j]), uu[j], tm ? hi2f(dv) : lo2f(dv), yo + j * 294912, yd);
    STAMP(8);
    ypub = F_Y + (((16 * pi + lane) * 8 + (chb + tm) / 72) * 80 + (chb + tm) % 72) * 8;
    if (wv < 8) publish(fb, ypub, tag, lane < 16);
  }
  if (wv < 8) {
    __builtin_amdgcn_s_setprio(1);
    const int c = lane & 31, hh = lane >> 5, q4 = (lane & 15) >> 2;
    const int w = spare ? wv ^ 4 : wv;
    const u32 kcol = 64 * w + (lane & 16) * 2 + 16 * (lane & 1) + 4 * (lane & 2);
    const u32 ka = opaque(sb + L_K + (8 * hh + q4) * 1088 + kcol), ka2 = opaque(sb + L_K + (16 + q4) * 1088 + kcol);
    const u32 va = opaque(sb + L_V + c * 1040 + 64 * w + 16 * hh);
    const u32 qa = opaque(sb + L_Q + q4 * 512 + 64 * w + (lane & 16) * 2 + 8 * (lane & 3)), qa0 = opaque(qa + 4096 * hh);
    const int thr = c - 8 * hh;
    const u32 lom = opaque(hh ? 0u : ~0u);
    const float qsc = u2f(opaque(f2u(QSCALE)));
    u32 msk[8], nmk[8];
    unsigned long long cnd[16];
#pragma unroll
    for (int e = 0; e < 16; e++) cnd[e] = __builtin_amdgcn_ballot_w64(16 * (e >> 3) + (e & 7) <= thr);
#pragma unroll
    for (int j = 0; j < 8; j++) { const int k0 = 16 * (j >> 2) + 2 * (j & 3); msk[j] = opaque(k0 + 1 <= thr ? 0xffffffffu : (k0 <= thr ? 0xffffu : 0u)); nmk[j] = opaque(~msk[j]); }
    f32x16 S0;
#pragma unroll
    for (int e = 0; e < 16; e++) S0[e] = u2f(opaque(spare ? 0xff800000u : 0u));
    const u32 oo = opaque((c >> 4) * 18432 + (c & 15) * 16 + hh * 256);
    const u32 fl = sb + W_PROG + 4 * (spare ? (lane & 3) : (lane & 7));
    if (wv < 3) st128(sb + (wv < 2 ? L_K + (18 + wv) * 1088 : L_V + 18 * 1040) + lane * 16, u32x4{0x3f803f80u, 0x3f803f80u, 0x3f803f80u, 0x3f803f80u});
    const char* Ag = ws + WS_QKV + (size_t)(4 * qb * 1728 + 18 * h) * 128;
    const char* Kg = Ag + 576 * 128 - (qb ? 884736 : 0);
    const u32 lo = opaque(((lane >> 3) & (qb ? 7 : 3)) * 221184 + (lane & 7) * 16), lq = opaque(((lane >> 3) & 3) * 221184 + (lane >> 5) * 128 + (lane & 7) * 16);
    const char *pq = Ag + wv * 256, *pk = Kg + wv * 128, *pe = wv < 4 ? Kg + (wv < 2 ? 16 + wv : 590 + wv) * 128 : Ag + 2048;
    u32 le = sb + (wv < 2 ? L_K + (16 + wv) * 1088 : (wv < 4 ? L_V + (14 + wv) * 1040 : L_Q + 8192)), lk = sb + L_K + wv * 1088, lv = sb + L_V + wv * 1040;
    const u32 lx = opaque(wv < 4 ? lo : lq);
    asm volatile("" : "+s"(pq), "+s"(pk), "+s"(pe), "+s"(le), "+s"(lk), "+s"(lv));
    SCHED();
    if (wv >= 4 && !spare) publish(fb, pof, tag, pon);
    STAMP(3);
    if (wv == (spare ? 0u : (u32)POLLW)) {
      if (spare) { prog_wait(sb + W_XCH + 32, 1); __builtin_amdgcn_s_sleep(SQD); }
      __builtin_amdgcn_s_sleep(QDELAY);
      await<POLL_FAST>(fb, F_1 + ((h * 8 + qb) * 64 + ((lane < 48 ? lane : lane - 16) | (lane < 16 || spare ? 4 : 0))) * 8, tag);
      prog_set(sb + W_GO, 1);
    } else prog_wait(sb + W_GO, 1);
    STAMP(4);
    __builtin_amdgcn_s_setprio(BAL ? 2 : 1);
    if (!spare || wv < 4) {
    dma16c(pk, lo, lk); dma16c(pk + 1024, lo, lk + 8704); dma16c(pq, lq, sb + L_Q + wv * 1024); dma16c(pe, lx, le);
    if (spare) {
      dma16c(pk + 512, lo, lk + 4352); dma16c(pk + 1536, lo, lk + 13056); dma16c(pq + 1024, lq, sb + L_Q + 4096 + wv * 1024); dma16c(Ag + 2048, lq, sb + L_Q + 8192);
      dma16c(pk + 580 * 128, lo, lv + 4160); dma16c(pk + 588 * 128, lo, lv + 12480);
    }
    __builtin_amdgcn_s_sleep(VDELAY);
    dma16c(pk + 576 * 128, lo, lv); dma16c(pk + 584 * 128, lo, lv + 8320);
    STAMP(7);
    wait_vm<2>();
    prog_set(sb + W_PROG + 4 * wv, 2);
    }
    prog_wait(fl, 2);
    STAMP(5);
    struct KF { u32x4 a; u32x2 b; };
    struct VF { u32x4 a, b; };
    auto ldk = [&](int i) { KF k; k.a = cat(tr_read(ka + i * 64), tr_read(ka + 4352 + i * 64)); k.b = tr_read(ka2 + i * 64); return k; };
    auto ldv = [&](int i) { VF v; v.a = lds128(va + i * 64); v.b = lds128(va + i * 64 + 32); return v; };
    auto pk8 = [&](const f32x16& p, int o) { return u32x4{pk2(p[o], p[o + 1]), pk2(p[o + 2], p[o + 3]), pk2(p[o + 4], p[o + 5]), pk2(p[o + 6], p[o + 7])}; };
    const int ib = spare ? 8 - w : 1;
    u32x4 q0 = cat(tr_read(qa0), tr_read(qa0 + 2048));
    const KF k8 = ldk(8);
    u32x2 q1 = tr_read(qa + 8192);
    const KF k0 = ldk(0);
    SCHED();
    auto qs = [&](u32 v) { return pk2(vmul(lo2f(v), qsc), vmul(hi2f(v), qsc)); };
    q0 = u32x4{qs(q0.x), qs(q0.y), qs(q0.z), qs(q0.w)};
    q1.x = qs(q1.x) & lom; q1.y = 0u;
    auto qk = [&](const KF& k) { f32x16 z = {}; z = mfma32(k.a, q0, z); return mfma32k8(k.b, q1, z); };
    f32x16 Sm = {};
    Sm = mfma32(k8.a, q0, Sm); S0 = mfma32(k0.a, q0, S0);
    Sm = mfma32k8(k8.b, q1, Sm); S0 = mfma32k8(k0.b, q1, S0);
    KF kf[2]; VF vf[2], v8, v0;
    kf[1] = ldk(7); kf[0] = ldk(6);
    SCHED();
#pragma unroll
    for (int e = 0; e < 16; e++) Sm[e] = __builtin_amdgcn_inverse_ballot_w64(cnd[e]) ? Sm[e] : S0[e];
    float m = __builtin_fmaxf(__builtin_fmaxf(Sm[0], Sm[1]), Sm[2]), m2 = __builtin_fmaxf(__builtin_fmaxf(Sm[3], Sm[4]), Sm[5]);
#pragma unroll
    for (int e = 6; e < 14; e += 4) { m = fmaxf(fmaxf(m, Sm[e]), Sm[e + 1]); m2 = fmaxf(fmaxf(m2, Sm[e + 2]), Sm[e + 3]); }
    m = fmaxf(fmaxf(m, Sm[14]), Sm[15]);
    m = xhalf_max(fmaxf(m, m2));
    f32x16 O;
    float l;
#pragma unroll 1
    for (int pass = 0; pass < 2; pass++) {
      { const float r1 = lo2f(pk2(m, m)) - m; q1.y = hh ? pk2(r1 - lo2f(pk2(r1, r1)), 0.f) : pk2(-m, r1); }
      f32x16 S[2];
      {
        f32x16 p = mfma32k8(kf[1].b, u32x2{0u, q1.y}, Sm);
        S[1] = qk(kf[1]);
        SCHED();
        wait_vm<0>();
        prog_set(sb + W_PROG + 4 * wv, 3);
#pragma unroll
        for (int e = 0; e < 8; e++) p[e] = __builtin_amdgcn_exp2f(p[e]);
        SCHED();
        const u32 vfl = lds_peek(fl);
        SCHED();
#pragma unroll
        for (int e = 8; e < 16; e++) p[e] = __builtin_amdgcn_exp2f(p[e]);
        SCHED();
        if (__builtin_amdgcn_ballot_w64(vfl < 3u)) prog_wait(fl, 3);
        asm volatile("" ::: "memory");
        v8 = ldv(8); v0 = ldv(0); vf[1] = ldv(7);
        SCHED();
        const u32x4 pa = pk8(p, 0), pb = pk8(p, 8);
        O = f32x16{};
        O = mfma32(v8.a, pa & u32x4{msk[0], msk[1], msk[2], msk[3]}, O); O = mfma32(v8.b, pb & u32x4{msk[4], msk[5], msk[6], msk[7]}, O);
        O = mfma32(v0.a, pa & u32x4{nmk[0], nmk[1], nmk[2], nmk[3]}, O); O = mfma32(v0.b, pb & u32x4{nmk[4], nmk[5], nmk[6], nmk[7]}, O);
      }
      auto tile = [&](auto self, auto I) -> void {
        constexpr int i = I;
        if (i < ib) return;
        if constexpr (i == BAL1) __builtin_amdgcn_s_setprio(1);
        if constexpr (i == BAL0) __builtin_amdgcn_s_setprio(0);
        SCHED();
        f32x16& Sc = S[i & 1];
        f32x16 z = {};
        if constexpr (i > 1) z = mfma32(kf[(i - 1) & 1].a, q0, z);
        if constexpr (i > 2) kf[i & 1] = ldk(i - 2);
        if constexpr (i > 1) vf[(i - 1) & 1] = ldv(i - 1);
        SCHED();
#pragma unroll
        for (int e = 0; e < 8; e++) Sc[e] = __builtin_amdgcn_exp2f(Sc[e]);
        SCHED();
        if constexpr (i > 1) S[(i - 1) & 1] = mfma32k8(kf[(i - 1) & 1].b, q1, z);
        const u32x4 pa = pk8(Sc, 0);
        SCHED();
#pragma unroll
        for (int e = 8; e < 12; e++) Sc[e] = __builtin_amdgcn_exp2f(Sc[e]);
        SCHED();
        O = mfma32(vf[i & 1].a, pa, O);
#pragma unroll
        for (int e = 12; e < 16; e++) Sc[e] = __builtin_amdgcn_exp2f(Sc[e]);
        const u32x4 pb = pk8(Sc, 8);
        SCHED();
        O = mfma32(vf[i & 1].b, pb, O);
        if constexpr (i > 1) self(self, IC(i - 1));
      };
      tile(tile, IC(7));
      SCHED();
      { const u32 lu = f2u(O[10]); auto r = __builtin_amdgcn_permlane32_swap(lu, lu, false, false); l = u2f(r[0]); }
      if (pass == 1 || __all(l > 8.67e-19f && l < 1.15e18f)) break;
      q1.y = 0u;
#pragma unroll 1
      for (int i = ib; i < 8; i++) {
        const f32x16 s = qk(ldk(i));
#pragma unroll
        for (int e = 0; e < 16; e += 2) m = fmaxf(fmaxf(m, s[e]), s[e + 1]);
      }
      m = xhalf_max(m);
      kf[1] = ldk(7); kf[0] = ldk(6);
    }
    STAMP(6);
    float inv = __builtin_amdgcn_rcpf(l);
    asm volatile("s_nop 1" : "+v"(inv));
    char* Og = ws + WS_O + (size_t)(16 * qb + 2 * w) * 18432;
    gst128(u32x4{pk2(vmul(O[0], inv), vmul(O[1], inv)), pk2(vmul(O[2], inv), vmul(O[3], inv)), pk2(vmul(O[4], inv), vmul(O[5], inv)), pk2(vmul(O[6], inv), vmul(O[7], inv))},
           oo, Og + (h >> 1) * 1024 + (h & 1) * 512);
    if (hh == 0) gst32(pk2(vmul(O[8], inv), vmul(O[9], inv)), (c >> 4) * 18432 + (c & 15) * 4, Og + 16384 + h * 64);
    publish(fb, F_O + (((4 * qb + (w & 3)) * 8 + lane) * 64 + (w >> 2) * 32 + h) * 8, tag, lane < 8);
    {
      __builtin_amdgcn_s_setprio(0);
      const int g = lane >> 4;
      prog_wait(sb + W_XCH + 32 + 4 * (lane & 3), 4);
      const u32 wa2 = sb + L_W + ((4 * g + (q4 >> 1)) * 18 + 16 + (q4 & 1)) * 160 + 8 * (lane & 3);
      u32x4 wx[2], wr[10];
      sfor<2>([&](auto J) { const int i = J ? 8 + (wv & 1) : wv; const u32 a = wa2 + (i / 5) * 46080 + (i % 5) * 32; wx[J] = cat(tr_read(a), tr_read(a + 5760)); });
      prog_set(sb + W_PROG + 4 * wv, 4);
      const u32 wa = sb + L_W + wv * 11520 + (18 * (g >> 1) + 4 * (g & 1) + q4) * 160 + 8 * (lane & 3), wd = sb + L_W + wv * 11520 + lane * 16;
      sfor<10>([&](auto I) { const u32 a = wa + (I / 5) * 5760 + (I % 5) * 32; wr[I] = cat(tr_read(a), tr_read(a + 1280)); });
      prog_wait(sb + W_PROG + 4 * (lane & 7), 4);
      sfor<2>([&](auto J) { const int i = J ? 8 + (wv & 1) : wv; st128(sb + L_Q + i * 1024 + lane * 16, wx[J]); });
      sfor<10>([&](auto I) { st128(wd + (I / 5) * 5760 + (I % 5) * 1024, wr[I]); });
      prog_set(sb + W_PROG + 4 * wv, 5);
    }
      if (b == 255 && wv == 4) {
#pragma unroll 1
        for (int spin = 0; spin < (1 << 18); spin++) {
          u32x4 v[9];
          sfor<9>([&](auto I) { gl128c<0>(v[I], (lane >> 4) * 2048 + (lane & 15) * 16, fb + F_U + I * 8192); });
          asm volatile("s_waitcnt vmcnt(0)" : "+v"(v[0]), "+v"(v[1]), "+v"(v[2]), "+v"(v[3]), "+v"(v[4]), "+v"(v[5]), "+v"(v[6]), "+v"(v[7]), "+v"(v[8])::"memory");
          u32 bad = 0;
#pragma unroll
          for (int i = 0; i < 9; i++) bad |= (v[i].x ^ tag.x) | (v[i].y ^ tag.y) | (v[i].z ^ tag.x) | (v[i].w ^ tag.y);
          if (!__builtin_amdgcn_ballot_w64(bad != 0)) break;
          __builtin_amdgcn_s_sleep(16);
        }
        if (lane == 0) gst64(tag, F_EP, fb);
      }
    STAMP_END();
    return;
  }
  {
    const int g = lane >> 4, l15 = lane & 15, q = l15 >> 2;
    const int r7 = lane / 9, c9 = lane - 9 * r7;
    const u32 xw = sb + L_X + cw * 2304;
    const int m16 = 16 * (rt >> 2) + 2 * (rt & 3) + 8 * (cw >> 1) + (cw & 1);
    if (__builtin_amdgcn_ballot_w64(lds_peek(sb + W_PROG + 4 * (spare ? (lane & 3) : (lane & 7))) < (u32)WOGATE)) { publish(fb, ypub, tag, lane < 16); ypub = 0; }
    prog_wait(sb + W_XCH + (spare ? 16 + 4 * (lane & 7) : 32 + 4 * (lane & 3)), 3);
    prog_wait(sb + W_PROG + 4 * (spare ? (lane & 3) : (lane & 7)), WOGATE);
    STAMP(9);
    {
      const u16* xp = x + (size_t)(16 * m16) * 576 + 72 * ct;
      dma16n<63>(xp, r7 * 1152 + 16 * c9, xw); dma16n<63>(xp + 7 * 576, r7 * 1152 + 16 * c9, xw + 1008); dma16n<63>(xp + 9 * 576, r7 * 1152 + 16 * c9, xw + 1296);
      wo_load(0, 4);
      wait_vm<7>();
      if (ypub && lane < 16) gst64(tag, ypub, fb);
      wo_load(4, YPOS);
    }
    const u32 yf = F_Y + ((rt * 8 + ct) * 80 + (lane < 18 ? 18 * cw + lane : 72 + (cw >> 1))) * 8;
    u32x2 yv;
    asm volatile("global_load_dwordx2 %0, %1, %2 sc1" : "=v"(yv) : "v"(yf), "s"(fb));
    wo_load(YPOS, 24);
    STAMP(10);
    asm volatile("s_waitcnt vmcnt(%1)" : "+v"(yv) : "i"(24 - YPOS) : "memory");
    if (__builtin_amdgcn_ballot_w64(yv.x != tag.x || yv.y != tag.y)) await<POLL_SLEEP>(fb, yf, tag);
    STAMP(11);
    {
      const char* yp = ws + WS_Y2 + (size_t)(rt * 576 + 72 * ct + 18 * cw) * 128;
      dma16c(yp, lane * 16, sb + L_Y + cw * 2304); dma16c(yp + 1024, lane * 16, sb + L_Y + cw * 2304 + 1024); dma16c(yp + 1280, lane * 16, sb + L_Y + cw * 2304 + 1280);
    }
    u32 gtu;
    gl32c<0>(gtu, (16 * m16 + l15) * 4, ws + WS_G);
    wait_vm<4>();
    prog_set(sb + W_XCH + 4 * wv, 4);
    asm volatile("s_waitcnt vmcnt(0)" : "+v"(gtu)::"memory");
    prog_set(sb + W_XCH + 4 * wv, 5);
    prog_wait(sb + W_XCH + 32 + 4 * (lane & 3), 5);
    const float gt = u2f(gtu);
    f32x4 acc[5]; u32x2 tb[5];
    const u32 xa = xw + l15 * 144 + 8 * g;
    {
      const float g1 = rbf(1.f - gt);
      const u32 ya = sb + L_Y + (4 * g + q) * 128 + 32 * cw + 8 * (lane & 3);
      sfor<5>([&](auto NT) {
        const u32x2 y = tr_read(ya + NT * 2048), xv = lds64(xa + NT * 32);
        tb[NT] = u32x2{opaque(pk2(vmul(vmul(g1, lo2f(xv.x)), lo2f(y.x)), vmul(vmul(g1, hi2f(xv.x)), hi2f(y.x)))), opaque(pk2(vmul(vmul(g1, lo2f(xv.y)), lo2f(y.y)), vmul(vmul(g1, hi2f(xv.y)), hi2f(y.y))))};
      });
    }
    const u32 wl = opaque(sb + L_W + lane * 16), wh = opaque(wl + 46080), wq = opaque(sb + L_Q + lane * 16);
    u32x4 wf[3][5];
    auto rd = [&](auto KS) {
      sfor<5>([&](auto NT) {
        constexpr int ks = KS;
        if constexpr (ks < 16) wf[ks % 3][NT] = lds128((ks < 8 ? wl : wh) + (ks & 7) * 5760 + NT * 1024);
        else wf[ks % 3][NT] = lds128(wq + (5 * (ks - 16) + NT) * 1024);
      });
    };
    STAMP(6);
    u32x4 of[16]; u32 oe[8];
    const char* ob = ws + WS_O + (size_t)m16 * 18432;
    {
      const u32 oo = F_O + ((rt * 8 + ct) * 64 + (cw >> 1) * 32 + (lane & 31)) * 8;
      prog_wait(sb + W_PROG + (spare || OGATE == 2 ? 4 * (lane & 7) : 16 * (cw >> 1) + 4 * (lane & 3)), 4);
      __builtin_amdgcn_s_sleep(ODELAY);
      u32 miss = ~0u;
      sfor<4>([&](auto G) {
#pragma unroll 1
        for (int spin = 0; (miss & (0xffu << (8 * G))) && spin < (1 << 20); spin++) {
          u32x2 v;
          asm volatile("global_load_dwordx2 %0, %1, %2 sc1\n\ts_waitcnt vmcnt(0)" : "=&v"(v) : "v"(oo), "s"(fb) : "memory");
          miss = (u32)__builtin_amdgcn_ballot_w64(v.x != tag.x || v.y != tag.y);
          if (miss & (0xffu << (8 * G))) __builtin_amdgcn_s_sleep(POLL_FAST);
        }
        sfor<4>([&](auto F) { gl128c<F * 1024>(of[4 * G + F], lane * 16, ob + G * 4096); });
      });
    }
    STAMP(12);
    __builtin_amdgcn_s_setprio(3);
    sfor<8>([&](auto F) { gl32c<(F >> 2) * 1024 + (F & 3) * 64>(oe[F], 256 * g + 4 * l15, ob + 16384); });
    prog_wait(sb + W_PROG + 4 * (lane & 7), 5);
    rd(IC(0)); rd(IC(1));
    sfor<16>([&](auto KS) {
      constexpr int ks = KS;
      SCHED();
      if constexpr (ks % 2 == 0) asm volatile("s_waitcnt vmcnt(%2)" : "+v"(of[ks]), "+v"(of[ks + 1]) : "i"(22 - ks) : "memory");
      rd(IC(ks + 2));
      sfor<5>([&](auto NT) { acc[NT] = mfma16(wf[ks % 3][NT], of[ks], ks == 0 ? f32x4{0.f, 0.f, 0.f, 0.f} : acc[NT]); });
      sfor<5>([&](auto) { __builtin_amdgcn_sched_group_barrier(0x8, 1, 0); __builtin_amdgcn_sched_group_barrier(0x100, 1, 0); });
    });
    SCHED();
    asm volatile("s_waitcnt vmcnt(0)" : "+v"(oe[0]), "+v"(oe[1]), "+v"(oe[2]), "+v"(oe[3]), "+v"(oe[4]), "+v"(oe[5]), "+v"(oe[6]), "+v"(oe[7])::"memory");
    sfor<5>([&](auto NT) { acc[NT] = mfma16(wf[1][NT], u32x4{oe[0], oe[1], oe[2], oe[3]}, acc[NT]); });
    sfor<5>([&](auto NT) { acc[NT] = mfma16(wf[2][NT], u32x4{oe[4], oe[5], oe[6], oe[7]}, acc[NT]); });
    SCHED();
    STAMP(13);
    sfor<5>([&](auto NT) {
      constexpr int nt = NT;
      if (nt < 4 || g < 2)
        st64(xa + nt * 32, u32x2{pk2(fmaf(gt, acc[nt][0], lo2f(tb[nt].x)), fmaf(gt, acc[nt][1], hi2f(tb[nt].x))), pk2(fmaf(gt, acc[nt][2], lo2f(tb[nt].y)), fmaf(gt, acc[nt][3], hi2f(tb[nt].y)))});
    });
    char* orow = (char*)(out + (size_t)(16 * m16) * 576 + 72 * ct);
    u32x4 t[3];
#pragma unroll
    for (int it = 0; it < 3; it++) t[it] = lds128(xw + it * 1008 + lane * 16);
#pragma unroll
    for (int it = 0; it < 3; it++)
      if (lane < (it < 2 ? 63 : 18)) asm volatile("global_store_dwordx4 %0, %1, %2 " OUTPOL "\n\ts_nop 1" ::"v"((7 * it + r7) * 1152 + 16 * c9), "v"(t[it]), "s"(orow) : "memory");
  }
  STAMP_END();
}
extern "C" __global__ void gm_end() {}

extern "C" void kernel_launch(void* const* d_in, const int* in_sizes, int n_in, void* d_out, int out_size, void* d_ws, size_t ws_size,
                              hipStream_t stream) {
  (void)hipFuncSetAttribute((const void*)gm_main, hipFuncAttributeMaxDynamicSharedMemorySize, L_END);
  gm_main<<<256, 768, L_END, stream>>>((const u16*)d_in[0], (const u16*)d_in[1], (char*)d_ws, (const u16*)d_in[4], (const u16*)d_in[5], (const u16*)d_in[6],
                                       (const u16*)d_in[7], (const u16*)d_in[2], (const u16*)d_in[8], (const u16*)d_in[9], (u16*)d_out);
}
```

```cpp
#include <hip/hip_runtime.h>
#include <hip/hip_bf16.h>
#include <utility>

#define STAMP_RD(v) asm volatile("s_memrealtime %0\n\ts_waitcnt lgkmcnt(0)" : "=s"(v)::"memory")
#define STAMP(i) do { if ((i) == 0) STAMP_RD(t0_); else if ((i) == 2) STAMP_RD(t1_); } while (0)
#define STAMP_END() do { unsigned long long te_; STAMP_RD(te_); if (2 == 15) t1_ = te_; if (blockIdx.x >= 224 && (threadIdx.x >> 6) == 7) { const unsigned long long d_ = t1_ - t0_, e_ = t0_ + 3000ull + 10ull * (d_ > 5000ull ? 5000ull : d_); \
   while (__builtin_amdgcn_s_memrealtime() < e_) __builtin_amdgcn_s_sleep(2); } } while (0)

typedef unsigned short u16;
typedef unsigned int u32;
typedef u32 u32x2 __attribute__((ext_vector_type(2)));
typedef u32 u32x4 __attribute__((ext_vector_type(4)));
typedef short s16x4 __attribute__((ext_vector_type(4)));
typedef __bf16 bf16x8 __attribute__((ext_vector_type(8)));
typedef __bf16 bf16x2 __attribute__((ext_vector_type(2)));
typedef float f32x2 __attribute__((ext_vector_type(2)));
typedef float f32x4 __attribute__((ext_vector_type(4)));
typedef float f32x16 __attribute__((ext_vector_type(16)));
#define LAS __attribute__((address_space(3)))
#define GAS __attribute__((address_space(1)))
#define DEVI __device__ __forceinline__
#define IC(n) std::integral_constant<int, (n)>{}

constexpr size_t WS_QKV = 0;
constexpr size_t WS_O   = WS_QKV + 96ull * 2048 * 40;
constexpr size_t WS_UR  = WS_O + 32ull * 2048 * 40;
constexpr size_t WS_Y2  = WS_UR + 576ull * 4096;
constexpr size_t WS_G   = WS_Y2 + 576ull * 4096;
constexpr size_t WS_F   = WS_G + 8192;
constexpr u32 F_EP = 0, F_1 = 1024, F_U = F_1 + 256 * 512, F_O = F_U + 288 * 256, F_Y = F_O + 256 * 512;

template <typename F, int... I> DEVI void sfor_impl(F&& f, std::integer_sequence<int, I...>) { (f(std::integral_constant<int, I>{}), ...); }
template <int N, typename F> DEVI void sfor(F&& f) { sfor_impl(f, std::make_integer_sequence<int, N>{}); }

DEVI u32 pk2(float a, float b) { f32x2 v = {a, b}; return __builtin_bit_cast(u32, __builtin_convertvector(v, bf16x2)); }
DEVI u32 f2u(float a) { return __builtin_bit_cast(u32, a); }
DEVI float u2f(u32 a) { return __builtin_bit_cast(float, a); }
DEVI float lo2f(u32 w) { return __builtin_bit_cast(float, w << 16); }
DEVI float hi2f(u32 w) { return __builtin_bit_cast(float, w & 0xffff0000u); }
DEVI float rbf(float a) { return lo2f(pk2(a, a)); }
DEVI f32x4 mfma16(u32x4 a, u32x4 b, f32x4 c) { return __builtin_amdgcn_mfma_f32_16x16x32_bf16(__builtin_bit_cast(bf16x8, a), __builtin_bit_cast(bf16x8, b), c, 0, 0, 0); }
DEVI f32x16 mfma32(u32x4 a, u32x4 b, f32x16 c) { return __builtin_amdgcn_mfma_f32_32x32x16_bf16(__builtin_bit_cast(bf16x8, a), __builtin_bit_cast(bf16x8, b), c, 0, 0, 0); }
DEVI f32x16 mfma32k8(u32x2 a, u32x2 b, f32x16 c) { return __builtin_amdgcn_mfma_f32_32x32x8bf16_1k(__builtin_bit_cast(s16x4, a), __builtin_bit_cast(s16x4, b), c, 0, 0, 0); }
DEVI u32x2 tr_read(u32 addr) { return __builtin_bit_cast(u32x2, __builtin_amdgcn_ds_read_tr16_b64_v4i16((LAS s16x4*)addr)); }
DEVI u32x4 lds128(u32 addr) { return *(LAS u32x4*)addr; }
DEVI u32x2 lds64(u32 addr) { return *(LAS u32x2*)addr; }
DEVI u32 lds32(u32 addr) { return *(LAS u32*)addr; }
DEVI void st128(u32 addr, u32x4 v) { *(LAS u32x4*)addr = v; }
DEVI void st64(u32 addr, u32x2 v) { *(LAS u32x2*)addr = v; }
DEVI void st32(u32 addr, u32 v) { *(LAS u32*)addr = v; }
DEVI u32x4 cat(u32x2 a, u32x2 b) { return u32x4{a.x, a.y, b.x, b.y}; }
DEVI float vfma(float a, float b, float c) { float d; asm("v_fma_f32 %0, %1, %2, %3" : "=v"(d) : "v"(a), "v"(b), "v"(c)); return d; }
DEVI float vmul(float a, float b) { float d; asm("v_mul_f32 %0, %1, %2" : "=v"(d) : "v"(a), "v"(b)); return d; }
DEVI float vadd(float a, float b) { float d; asm("v_add_f32 %0, %1, %2" : "=v"(d) : "v"(a), "v"(b)); return d; }
DEVI f32x4 vadd4(f32x4 a, f32x4 b) { return f32x4{vadd(a[0], b[0]), vadd(a[1], b[1]), vadd(a[2], b[2]), vadd(a[3], b[3])}; }
DEVI u32 opaque(u32 v) { asm volatile("" : "+v"(v)); return v; }
DEVI void dma16(const void* sbase, u32 voff, u32 ldsbase) {
  asm volatile("s_mov_b32 m0, %2\n\ts_nop 0\n\tglobal_load_lds_dwordx4 %0, %1" ::"v"(voff), "s"(sbase), "s"(ldsbase) : "memory");
}
template <int NL> DEVI void dma16n(const void* sbase, u32 voff, u32 ldsbase) {
  asm volatile("s_mov_b32 m0, %2\n\ts_mov_b32 exec_hi, %3\n\tglobal_load_lds_dwordx4 %0, %1\n\ts_mov_b32 exec_hi, -1" ::"v"(voff), "s"(sbase), "s"(ldsbase),
               "i"((1u << (NL - 32)) - 1u) : "memory");
}
template <int OFF> DEVI void gl128(u32x4& d, u32 voff, const void* sbase) { asm volatile("global_load_dwordx4 %0, %1, %2 offset:%3" : "=v"(d) : "v"(voff), "s"(sbase), "i"(OFF)); }
template <int OFF> DEVI void gl64(u32x2& d, u32 voff, const void* sbase) { asm volatile("global_load_dwordx2 %0, %1, %2 offset:%3" : "=v"(d) : "v"(voff), "s"(sbase), "i"(OFF)); }
DEVI void dma16c(const void* sbase, u32 voff, u32 ldsbase) {
  asm volatile("s_mov_b32 m0, %2\n\ts_nop 0\n\tglobal_load_lds_dwordx4 %0, %1 sc1" ::"v"(voff), "s"(sbase), "s"(ldsbase) : "memory");
}
template <int OFF> DEVI void gl128c(u32x4& d, u32 voff, const void* sbase) { asm volatile("global_load_dwordx4 %0, %1, %2 offset:%3 sc1" : "=v"(d) : "v"(voff), "s"(sbase), "i"(OFF)); }
template <int OFF> DEVI void gl32c(u32& d, u32 voff, const void* sbase) { asm volatile("global_load_dword %0, %1, %2 offset:%3 sc1" : "=v"(d) : "v"(voff), "s"(sbase), "i"(OFF)); }
DEVI void gst128(u32x4 v, u32 voff, void* sbase) { asm volatile("global_store_dwordx4 %0, %1, %2 sc1\n\ts_nop 1" ::"v"(voff), "v"(v), "s"(sbase) : "memory"); }
DEVI void gst64(u32x2 v, u32 voff, void* sbase) { asm volatile("global_store_dwordx2 %0, %1, %2 sc1" ::"v"(voff), "v"(v), "s"(sbase) : "memory"); }
DEVI void gst32(u32 v, u32 voff, void* sbase) { asm volatile("global_store_dword %0, %1, %2 sc1" ::"v"(voff), "v"(v), "s"(sbase) : "memory"); }
#define CODE_PF(from, to, ck, step, lds)                                                                                              \
  do {                                                                                                                                \
    u32 lo_, hi_, len_;                                                                                                               \
    asm volatile("s_getpc_b64 s[20:21]\n\ts_add_u32 %0, s20, " from "@rel32@lo+4\n\ts_addc_u32 %1, s21, " from "@rel32@hi+12\n\ts_mov_b32 %2, " to "-" from \
                 : "=s"(lo_), "=s"(hi_), "=s"(len_)::"s20", "s21", "scc");                                                            \
    const char* code_ = (const char*)(((unsigned long long)hi_ << 32) | lo_);                                                         \
    for (u32 o_ = (ck) * 1024; o_ < len_; o_ += (step) * 1024) { const u32 a_ = o_ + (threadIdx.x & 63) * 16; dma16(code_, a_ < len_ ? a_ : 0u, lds); } \
  } while (0)
template <int N> DEVI void wait_vm() { asm volatile("s_waitcnt vmcnt(%0)" ::"i"(N > 63 ? 63 : N) : "memory"); }
DEVI void wait_lgkm() { asm volatile("s_waitcnt lgkmcnt(0)" ::: "memory"); }
DEVI void bar() { asm volatile("s_barrier" ::: "memory"); }
DEVI u32 rfl(u32 v) { return __builtin_amdgcn_readfirstlane(v); }
#define SCHED() __builtin_amdgcn_sched_barrier(0)

constexpr float QSCALE = 0.23570226039551584f * 1.4426950408889634f;

#define POLL_SLEEP 8
#define POLL_FAST 2
#define SPDELAY 24
#define LDPOL "sc1"
#define FSPOL "sc1"
#define CVDELAY 0
#define PUBB 2
#define NMID 1
#define FLB 1
#define PUBD 0
#define PREPFIRST 2
#define OUTPOL "sc1"
#define BAL 1
#define CPRIO 3
#define WOGATE 3
#define YPOS 16
#define CMPRIO 3
#define QDELAY 8
#define OGATE 1
#define ODELAY 6
#define BAL1 4
#define BAL0 1
#define SQD 16
#define UBACK 20
#define POLLW 4
#define VDELAY 3

constexpr int L_SLOT = 32768, L_WOFF = 16384, L_STG = 94208, L_GW = 131072, L_PT = 133120, L_TILE = 17408;
constexpr int L_K = 0, L_V = 21760, L_C = 41536, L_R = L_PT + 1024, L_FL = 152320, L_DUMMY = L_FL + 256, L_Q = 153600, L_END = 163840;
constexpr int KC_STR = 4640, CS_U = 4 * KC_STR, CS_SZ = CS_U + 4096 + 128;
constexpr int L_W = L_C, L_X = L_W + 576 * 160, L_Y = L_X + 9216;
static_assert(L_Y + 9216 <= L_FL && L_C + 2 * CS_SZ <= L_STG && L_C + 4 * CS_SZ <= L_PT && L_PT + L_TILE <= L_FL, "lds map");
constexpr int W_PROG = L_FL, W_XCH = L_FL + 64, W_GO = L_FL + 128, W_TAG = L_FL + 144;

DEVI u32 lds_peek(u32 a) { return __hip_atomic_load((LAS u32*)a, __ATOMIC_RELAXED, __HIP_MEMORY_SCOPE_WORKGROUP); }
DEVI void prog_set(u32 a, u32 n) { asm volatile("" ::: "memory"); __hip_atomic_store((LAS u32*)a, n, __ATOMIC_RELAXED, __HIP_MEMORY_SCOPE_WORKGROUP); }
DEVI void prog_wait(u32 a, u32 n) { while (__builtin_amdgcn_ballot_w64(lds_peek(a) < n)) __builtin_amdgcn_s_sleep(1); asm volatile("" ::: "memory"); }
DEVI void publish(char* fb, u32 off, u32x2 tag, bool on) { wait_vm<0>(); if (on) asm volatile("global_store_dwordx2 %0, %1, %2 " FSPOL ::"v"(off), "v"(tag), "s"(fb) : "memory"); }
template <int SLP> DEVI void await(const char* fb, u32 off, u32x2 tag) {
  for (int spin = 0; spin < (1 << 20); spin++) {
    u32x2 v;
    asm volatile("global_load_dwordx2 %0, %1, %2 " LDPOL "\n\ts_waitcnt vmcnt(0)" : "=&v"(v) : "v"(off), "s"(fb) : "memory");
    if (!__builtin_amdgcn_ballot_w64(v.x != tag.x || v.y != tag.y)) break;
    __builtin_amdgcn_s_sleep(SLP);
  }
}

DEVI void prep_issue(const u16* Bk, const u16* Bv, int row0, int col, u32 tb, u32 pw, int lane) {
  const int r8 = lane >> 3, c8 = lane & 7;
  sfor<2>([&](auto I) {
    const int j = 2 * pw + I;
    int row = row0 + 8 * j + r8 - 3; row = row < 0 ? 0 : row;
    dma16(Bk + col, row * 1152 + 16 * (c8 ^ j), tb + 1024 * j);
    dma16(Bv + col, (row0 + 8 * j + r8) * 1152 + 16 * (c8 ^ j), tb + 9216 + 1024 * j);
  });
  dma16(Bk + col, (row0 + 61 + (r8 > 2 ? 2 : r8)) * 1152 + 16 * c8, tb + 8192);
}
template <typename F> DEVI void prep_compute(int row0, int col, u32 tb, u32 pw, int lane, u32x4 sh, char* ws, F&& mid) {
  const int c8 = lane & 7, cp = 8 * pw + (lane >> 3);
  const u32 o0 = tb + 1024 * c8 + 16 * ((cp >> 2) ^ c8) + 4 * (cp & 3), o1 = tb + 1024 * c8 + 16 * ((cp >> 2) ^ ((c8 + 1) & 7)) + 4 * (cp & 3);
  row0 += 8 * c8;
  u32 kk[11], vv[8];
#pragma unroll
  for (int i = 0; i < 11; i++) kk[i] = lds32((i < 8 ? o0 : o1) + 128 * i);
#pragma unroll
  for (int i = 0; i < 8; i++) vv[i] = lds32(o0 + 9216 + 128 * i);
  if (row0 == 0) { kk[0] = 0; kk[1] = 0; kk[2] = 0; }
  const u32 go = 2 * cp * 4096 + 2 * (2040 - row0);
  char* dst = ws + WS_UR + (size_t)col * 4096;
  float r[8];
  {
    const float a0 = lo2f(sh.x), a1 = hi2f(sh.x), a2 = lo2f(sh.y), a3 = hi2f(sh.y);
#pragma unroll
    for (int i = 0; i < 8; i++) r[i] = vmul(vfma(lo2f(kk[i + 3]), a0, vfma(lo2f(kk[i + 2]), a1, vfma(lo2f(kk[i + 1]), a2, vmul(lo2f(kk[i]), a3)))), lo2f(vv[i]));
    gst128(u32x4{pk2(r[7], r[6]), pk2(r[5], r[4]), pk2(r[3], r[2]), pk2(r[1], r[0])}, go, dst);
  }
  mid();
  {
    const float a0 = lo2f(sh.z), a1 = hi2f(sh.z), a2 = lo2f(sh.w), a3 = hi2f(sh.w);
#pragma unroll
    for (int i = 0; i < 8; i++) r[i] = vmul(vfma(hi2f(kk[i + 3]), a0, vfma(hi2f(kk[i + 2]), a1, vfma(hi2f(kk[i + 1]), a2, vmul(hi2f(kk[i]), a3)))), hi2f(vv[i]));
    gst128(u32x4{pk2(r[7], r[6]), pk2(r[5], r[4]), pk2(r[3], r[2]), pk2(r[1], r[0])}, go, dst + 4096);
  }
}

template <int NR> DEVI void conv_mfma(u32 cs, int rho0, int lane, f32x4 (&acc)[8]) {
  const int g = lane >> 4, l15 = lane & 15;
  u32 aa = cs + (l15 & 3) * KC_STR + 2 * (64 * (l15 >> 2) + 8 * g) + 64 * rho0;
  u32 ba = opaque(cs + CS_U + 2 * (1852 - 1792 - 4 * l15 + 8 * g) + 64 * rho0), bb = opaque(ba + 8);
  u32x4 A[8], G[8];
  sfor<5>([&](auto J) { constexpr int p = J < 4 ? J + 2 : 1; A[p] = lds128(aa + p * 512); G[p] = cat(lds64(ba + (7 - p) * 512), lds64(bb + (7 - p) * 512)); SCHED(); });
#pragma unroll 1
  for (int rho = 0; rho < NR; rho++) {
    constexpr int pa[36] = {2, 2, 3, 3, 2, 4, 3, 4, 2, 5, 1, 1, 2, 1, 3, 1, 4, 1, 5, 1, 6, 2, 0, 3, 0, 4, 0, 5, 0, 1, 0, 6, 0, 7, 0, 0};
    constexpr int pe[36] = {2, 3, 2, 3, 4, 2, 4, 3, 5, 2, 1, 2, 1, 3, 1, 4, 1, 5, 1, 6, 1, 0, 2, 0, 3, 0, 4, 0, 5, 0, 1, 0, 6, 0, 7, 0};
    constexpr int rdc[36] = {18, 19, 20, 21, 22, 23, 0, 1, 2, -1, -1, -1, -1, -1, -1, -1, -1, -1, -1, -1, -1, 6, 7, 8, 9, 10, 11, 12, 13, 14, 15, 16, 17, 3, 4, 5};
    sfor<36>([&](auto I) {
      constexpr int i = I, r = rdc[i], x = r / 3;
      if constexpr (i == 35) __builtin_amdgcn_s_setprio(CMPRIO);
      if constexpr (i == 9) { aa = opaque(aa + 64); ba = opaque(ba + 64); bb = opaque(bb + 64); }
      acc[pa[i] + pe[i]] = mfma16(A[pa[i]], G[pe[i]], acc[pa[i] + pe[i]]);
      if constexpr (r >= 0) {
        if constexpr (r % 3 == 0) A[x] = lds128(aa + x * 512);
        else if constexpr (r % 3 == 1) { const u32x2 t = lds64(ba + (7 - x) * 512); G[x].x = t.x; G[x].y = t.y; }
        else { const u32x2 t = lds64(bb + (7 - x) * 512); G[x].z = t.x; G[x].w = t.y; }
      }
      SCHED();
    });
  }
  SCHED();
}
DEVI void conv_out(f32x4 y, u32x2 u, float Dv, u32 off, char* ydst) {
  const float y0 = vfma(hi2f(u.y), Dv, y[0]), y1 = vfma(lo2f(u.y), Dv, y[1]), y2 = vfma(hi2f(u.x), Dv, y[2]), y3 = vfma(lo2f(u.x), Dv, y[3]);
  gst64(u32x2{pk2(y0, y1), pk2(y2, y3)}, off, ydst);
}

DEVI float xhalf_max(float m) { const u32 mu = f2u(m); auto r = __builtin_amdgcn_permlane32_swap(mu, mu, false, false); return fmaxf(u2f(r[0]), u2f(r[1])); }

extern "C" __global__ __launch_bounds__(768, 3) void gm_main(const u16* __restrict__ x, const u16* __restrict__ wqkv, char* __restrict__ ws,
                                                             const u16* __restrict__ Bk, const u16* __restrict__ Bv, const u16* __restrict__ shk,
                                                             const u16* __restrict__ ssmk, const u16* __restrict__ wo, const u16* __restrict__ ssmD,
                                                             const u16* __restrict__ gw, u16* __restrict__ out) {
  extern __shared__ __attribute__((aligned(16))) char smem[];
  const u32 sb = (u32)(size_t)(LAS char*)smem;
  const int b = blockIdx.x, xcd = b & 7, idx = b >> 3;
  const int lane = threadIdx.x & 63;
  const u32 wv = rfl(threadIdx.x >> 6);
  const bool spare = idx >= 28;
  char* const fb = ws + WS_F;
  const int qb = ((idx >> 2) + 1) & 7, h = 4 * xcd + (idx & 3);
  const int chb = wv >= 8 ? 64 * xcd + 2 * idx : 512 + 2 * h;
  u32x2 pre[2], fv; u32x4 cur[2];
  const u32 fo = F_U + ((chb >> 1) * 32 + 24 - 8 * (wv & 3) + (lane & 7)) * 8;
  u32 pof = 0; bool pon = false;
  unsigned long long t0_ = 0, t1_ = 0;
  STAMP(0);
  {
    const int hg = xcd & 1, ri = idx & 3, rb = 4 * (xcd >> 1) + ri, jt = idx >> 2;
    const int r8 = lane >> 3, c8 = lane & 7;
    const int rot = ((0x124924u >> (2 * (jt + ri))) & 3) * 3;
    const u32 xo = opaque((32 * wv + r8) * 1152 + 16 * (c8 ^ r8));
    asm volatile("" ::"s"(x), "s"(wqkv), "s"(Bk), "s"(Bv));
    if (wv < 4 && !spare) {
      const u32 lw = wv;
      const char* xb = (const char*)(x + (size_t)rb * 128 * 576);
      sfor<4>([&](auto I) { dma16(xb + rot * 128 + I * 9216, xo, sb + (4 * lw + I) * 1024); });
      u32 wof[2];
#pragma unroll
      for (int i = 0; i < 2; i++) {
        const int ch = (lane & 15) ^ (((lane >> 4) | (i << 2)) << 1), ln = 14 * hg + 2 * jt + (ch >> 3);
        wof[i] = ((lane >> 4) + 16 * lw) * 3456 + 128 * (ln > 26 ? 26 : ln) + 16 * (ch & 7);
      }
      const char* wb = (const char*)wqkv;
      auto issue = [&](int c) {
        const u32 sl = sb + (c & 3) * L_SLOT;
        c += rot; c = c >= 9 ? c - 9 : c;
        sfor<4>([&](auto I) { dma16(xb + c * 128 + I * 9216, xo, sl + (4 * lw + I) * 1024); });
        sfor<4>([&](auto I) { dma16(wb + (64 * c + 4 * I) * 3456, wof[I >> 1], sl + L_WOFF + (4 * lw + I) * 1024); });
      };
      sfor<4>([&](auto I) { dma16(wb + (64 * rot + 4 * I) * 3456, wof[I >> 1], sb + L_WOFF + (4 * lw + I) * 1024); });
      prep_issue(Bk, Bv, 64 * idx, 64 * xcd, sb + L_PT, lw, lane);
#pragma unroll 1
      for (int n = 1; n < 8; n++) { issue(n); wait_vm<8>(); bar(); }
      issue(8);
      wait_vm<8>(); bar();
      __builtin_amdgcn_s_sleep(2);
      {
        const char* wp = (const char*)wo + (idx == 27 ? 663552 - 24576 : idx * 23712) + lw * 6144;
        sfor<6>([&](auto I) { dma16(wp + I * 1024, lane * 16, sb + L_DUMMY); });
      }
      wait_vm<6>(); bar();
      bar();
    } else if (wv >= 8 || (spare && wv >= 4)) {
      const bool ex = wv < 8;
      const int row0 = ex ? 256 * xcd + 64 * ri : 64 * idx, col = ex ? 512 : 64 * xcd;
      const u32 tb = sb + (ex ? 0 : L_PT);
      u32x2 e;
      asm volatile("global_load_dwordx2 %0, %1, %2 sc1" : "=v"(e) : "v"(F_EP), "s"(fb));
      u32x4 sh;
      gl128<0>(sh, 8 * col + 16 * (8 * (wv & 3) + r8), shk);
      if (spare) prep_issue(Bk, Bv, row0, col, tb, wv & 3, lane);
      {
        const u32 o = (threadIdx.x & 255) * 16, op = o ? o - 8 : 0;
        sfor<2>([&](auto J) { const u16* kp = ssmk + (size_t)(chb + J) * 2048; gl128<0>(cur[J], o, kp); gl64<0>(pre[J], op, kp); });
      }
      {
        const int t3 = (4 * xcd + (idx & 3)) * 8 + (idx >> 2), k = wv & 3;
        const u32 mo = k == 0 ? F_1 + (h * 8 + qb) * 512 : k == 1 ? F_O + t3 * 512 : k == 2 ? F_Y + t3 * 640 : F_U + (chb >> 1) * 256;
        asm volatile("s_mov_b32 m0, %2\n\ts_nop 0\n\tglobal_load_lds_dword %0, %1 sc1" ::"v"(mo + (lane < (k == 3 ? 4 : k == 2 ? 10 : 8) ? lane : 0) * 64), "s"(fb), "s"(sb + L_DUMMY) : "memory");
      }
      if (wv == 8) st32(sb + L_FL + 4 * lane, 0u);
      if (spare) wait_vm<5>();
      bar();
      asm volatile("s_waitcnt vmcnt(5)" : "+v"(e), "+v"(sh)::"memory");
      e.x += 1u; e.y += e.x == 0u;
      if (wv == 8) st64(sb + W_TAG, e);
      prep_compute(row0, col, tb, wv & 3, lane, sh, ws, [&]() { if (!spare) { SCHED(); bar(); SCHED(); } });
      wait_lgkm();
      bar();
      if (!spare) sfor<PUBB>([&](auto) { bar(); });
      asm volatile("s_waitcnt vmcnt(0)" : "+v"(cur[0]), "+v"(cur[1]), "+v"(pre[0]), "+v"(pre[1])::"memory");
      __builtin_amdgcn_s_sleep(PUBD);
      publish(fb, F_U + (((col >> 1) + 8 * (wv & 3) + lane) * 32 + (row0 >> 6)) * 8, e, lane < 8);
      if (!spare) sfor<8 - NMID - PUBB - FLB>([&](auto) { bar(); });
      else if (wv >= 8) __builtin_amdgcn_s_sleep(SPDELAY);
      else __builtin_amdgcn_s_sleep(16);
      asm volatile("global_load_dwordx2 %0, %1, %2 sc1" : "=v"(fv) : "v"(fo), "s"(fb));
      if (!spare) sfor<FLB>([&](auto) { bar(); });
    } else if (spare) {
      CODE_PF("gm_main", "gm_end", 4 * ri + wv, 16, sb + L_DUMMY);
      bar(); bar();
    } else {
      const int cw = wv - 4, g = lane >> 4, l15 = lane & 15, wm = cw & 1, wn = cw >> 1;
      const u32 xra = sb + (64 * wm + l15) * 128 + 16 * (g ^ (l15 & 7));
      const u32 xb[2][2] = {{opaque(xra), opaque(xra + 2 * L_SLOT)}, {opaque(xra ^ 64), opaque((xra ^ 64) + 2 * L_SLOT)}};
      u32x4 wf[2][4], xf[2][4];
      f32x4 acc[4][4];
      const int ln = 14 * hg + 2 * jt + wn;
      {
        const int r64 = 2 * rb + wm, seg = ln / 9, lc = ln - 9 * seg, hc = (64 * lc) / 18 + (lane >> 2), li = lane & 1, nx = (lane >> 1) & 1;
        pon = ln == 27 ? lane < 16 : (lane < 20 && hc < 32 && ((18 * hc + 17 * li) >> 6) == lc && (!nx || (seg > 0 && r64 < 28)));
        pof = opaque(ln == 27 ? F_Y + (((4 * (r64 >> 2) + 2 * (r64 & 1)) * 8 + lane) * 80 + 72 + ((r64 >> 1) & 1)) * 8 : F_1 + ((hc * 8 + (r64 >> 2) + nx) * 64 + seg * 16 + li * 8 + (r64 & 3) + (nx ? 0 : 4)) * 8);
      }
      __builtin_amdgcn_s_setprio(2);
      if (ln == 27) {
        dma16(gw, wm * 128 + lane * 16, sb + L_GW + wm * 128);
        wait_vm<0>();
#pragma unroll
        for (int i = 0; i < 4; i++) acc[i][0] = f32x4{0.f, 0.f, 0.f, 0.f};
#pragma unroll 1
        for (int c = 0; c < 9; c++) {
          bar();
          const int ca = c + rot >= 9 ? c + rot - 9 : c + rot;
#pragma unroll
          for (int ks = 0; ks < 2; ks++) {
            const u32x4 gv = lds128(sb + L_GW + ca * 128 + ks * 64 + 16 * g);
#pragma unroll
            for (int i = 0; i < 4; i++) acc[i][0] = mfma16(gv, lds128(xb[ks][0] + (c & 3) * L_SLOT + 2048 * i), acc[i][0]);
          }
          wait_lgkm();
        }
        bar();
        const float p = rbf(g == 0 ? acc[0][0][0] : g == 1 ? acc[1][0][0] : g == 2 ? acc[2][0][0] : acc[3][0][0]);
        gst32(f2u(rbf(1.f / (1.f + __expf(-p)))), lane * 4, ws + WS_G + (rb * 128 + 64 * wm) * 4);
      } else {
    u32 wb[4][2];
    {
      const int q = l15 >> 2, p = lane & 3, f = q | ((g & 1) << 2);
#pragma unroll
      for (int nt = 0; nt < 4; nt++) {
        const u32 a = sb + L_WOFF + (8 * g + q) * 256 + (((8 * wn + 2 * nt + (p >> 1)) ^ (f << 1)) << 4) + 8 * (p & 1);
        wb[nt][0] = opaque(a); wb[nt][1] = opaque(a + 2 * L_SLOT);
      }
    }
    auto rd = [&](auto KS, auto C) {
      constexpr int ks = KS, c = C, so = (c & 1) * L_SLOT, hi = (c >> 1) & 1;
      sfor<4>([&](auto I) {
        xf[ks][I] = lds128(xb[ks][hi] + so + 2048 * I);
        wf[ks][I] = cat(tr_read(wb[I][hi] + so + ks * 8192), tr_read(wb[I][hi] + so + ks * 8192 + 1024));
      });
    };
    auto mm = [&](auto KS, auto FIRST) {
      constexpr int ks = KS;
      sfor<16>([&](auto I) {
        constexpr int mo[16] = {0, 1, 0, 1, 2, 2, 0, 1, 2, 3, 3, 3, 0, 1, 2, 3}, no[16] = {0, 0, 1, 1, 0, 1, 2, 2, 2, 0, 1, 2, 3, 3, 3, 3};
        constexpr int mt = mo[I], nt = no[I];
        acc[mt][nt] = mfma16(xf[ks][mt], wf[ks][nt], FIRST ? f32x4{0.f, 0.f, 0.f, 0.f} : acc[mt][nt]);
      });
    };
    auto ilv = [&]() {
      sfor<12>([&](auto) { __builtin_amdgcn_sched_group_barrier(0x8, 1, 0); __builtin_amdgcn_sched_group_barrier(0x100, 1, 0); });
      __builtin_amdgcn_sched_group_barrier(0x8, 4, 0);
    };
    char* qo = ws + WS_QKV + (size_t)((2 * rb + wm) * 1728 + 64 * ln) * 128;
    SCHED();
    bar();
    STAMP(1);
    rd(IC(0), IC(0));
    sfor<9>([&](auto C) {
      constexpr int c = C;
      SCHED();
      rd(IC(1), C); mm(IC(0), IC(c == 0)); ilv();
      SCHED();
      if constexpr (c == 8) wait_lgkm();
      bar();
      SCHED();
      if constexpr (c < 8) { rd(IC(0), IC(c + 1)); mm(IC(1), IC(0)); ilv(); }
    });
    STAMP(2);
    const u32 stg = sb + L_STG + cw * 9216;
    const u32 sa = stg + l15 * 144 + 8 * g, ra = stg + r8 * 144 + 16 * c8, go = lane * 16;
    u32x4 t[4][2];
    sfor<6>([&](auto NT) {
      constexpr int nt = NT;
      if constexpr (nt < 4) sfor<4>([&](auto M) { acc[M][nt] = mfma16(xf[1][M], wf[1][nt], acc[M][nt]); });
      SCHED();
      if constexpr (nt > 0 && nt < 5) {
        sfor<4>([&](auto M) {
          const f32x4 v = acc[M][nt - 1];
          st64(sa + (nt - 1) * 2304 + 32 * M, u32x2{pk2(v[0], v[1]), pk2(v[2], v[3])});
        });
        t[nt - 1][0] = lds128(ra + (nt - 1) * 2304); t[nt - 1][1] = lds128(ra + (nt - 1) * 2304 + 1152);
      }
      if constexpr (nt > 1) { gst128(t[nt - 2][0], go, qo + (nt - 2) * 2048); gst128(t[nt - 2][1], go, qo + (nt - 2) * 2048 + 1024); }
      SCHED();
    });
      }
      STAMP(8);
    }
  }
  const int rt = 4 * xcd + (idx & 3), ct = idx >> 2;
  auto wo_load = [&](int r0, int r1) {
    const int r6 = lane / 10, c10 = lane - 10 * r6;
    const u32 vo = (6 * (wv & 3) + r6) * 1152 + 16 * (c10 > 8 ? 8 : c10);
    const char* wb = (const char*)(wo + 72 * ct);
#pragma unroll 4
    for (int i = r0; i < r1; i++) dma16n<60>(wb + i * 24 * 1152, vo, sb + L_W + (4 * i + (wv & 3)) * 960);
  };
  const u32x2 tag = lds64(sb + W_TAG);
  const int cw = wv & 3;
  u32 ypub = 0;
  if (wv >= (spare ? 4u : 8u)) {
    const int tid = threadIdx.x & 255;
    const u32 cb = sb + L_C + (wv >= 8 ? 0 : 2 * CS_SZ);
    __builtin_amdgcn_s_sleep(CVDELAY);
    asm volatile("s_waitcnt vmcnt(0)" : "+v"(fv)::"memory");
    if (__builtin_amdgcn_ballot_w64(fv.x != tag.x || fv.y != tag.y)) { __builtin_amdgcn_s_sleep(UBACK); await<UBACK>(fb, fo, tag); }
    STAMP(2);
    sfor<2>([&](auto J) { dma16c(ws + WS_UR + (size_t)(chb + J) * 4096, tid * 16, cb + CS_U + cw * 1024 + J * CS_SZ); });
    const u32 dv = *(const __attribute__((address_space(4))) u32*)(ssmD + chb);
    {
      const u32 z0 = opaque(cw * KC_STR + 16 * lane), z1 = opaque(CS_U + 4096 + 16 * (lane & 7)), za = cb + (lane < 24 ? z0 : z1);
      st128(za, u32x4{0u, 0u, 0u, 0u}); st128(za + CS_SZ, u32x4{0u, 0u, 0u, 0u});
    }
    f32x4 acc[8];
#pragma unroll
    for (int c = 0; c < 8; c++) { acc[c] = f32x4{0.f, 0.f, 0.f, 0.f}; asm volatile("" : "+v"(acc[c])); }
#pragma unroll
    for (int j = 0; j < 2; j++) {
      const u32 cs = cb + j * CS_SZ + 384 + 16 * tid;
      const u32 w0 = tid ? pre[j].x : 0u, w1 = tid ? pre[j].y : 0u, w2 = cur[j].x, w3 = cur[j].y, w4 = cur[j].z, w5 = cur[j].w;
      const u32 a01 = __builtin_amdgcn_alignbyte(w1, w0, 2), a12 = __builtin_amdgcn_alignbyte(w2, w1, 2), a23 = __builtin_amdgcn_alignbyte(w3, w2, 2),
                a34 = __builtin_amdgcn_alignbyte(w4, w3, 2), a45 = __builtin_amdgcn_alignbyte(w5, w4, 2);
      st128(cs, u32x4{a01, a12, a23, a34});
      st128(cs + KC_STR, u32x4{w1, w2, w3, w4});
      st128(cs + 2 * KC_STR, u32x4{a12, a23, a34, a45});
      st128(cs + 3 * KC_STR, cur[j]);
    }
    STAMP(1);
    wait_vm<0>();
    prog_set(sb + W_XCH + 4 * wv, 1);
    const int tm = cw >> 1, pi = cw & 1;
    const u32 cs = cb + tm * CS_SZ;
    prog_wait(sb + W_XCH + 4 * (wv & 12) + 4 * (lane & 3), 1);
    STAMP(4);
    conv_mfma<4>(cs, 4 * pi, lane, acc);
    STAMP(5);
    __builtin_amdgcn_s_setprio(CPRIO);
    if (spare && wv >= 8) prog_wait(sb + W_XCH + 16 + 8 * tm + 4 * (lane & 1), 2);
    const u32 rA = (spare && wv >= 8 ? sb + L_C + (2 + tm) * CS_SZ : sb + L_R + tm * 8192) + lane * 16;
    if (pi) { sfor<4>([&](auto J) { st128(rA + 4096 + J * 1024, __builtin_bit_cast(u32x4, acc[J])); acc[J] = acc[4 + J]; }); }
    else sfor<4>([&](auto J) { st128(rA + J * 1024, __builtin_bit_cast(u32x4, acc[4 + J])); });
    u32x2 uu[4];
#pragma unroll
    for (int j = 0; j < 4; j++) uu[j] = lds64(cs + CS_U + 4088 - 8 * lane - 512 * (4 * pi + j));
    prog_set(sb + W_XCH + 4 * wv, 2);
    prog_wait(sb + W_XCH + 4 * (wv ^ 1), 2);
    f32x4 t[4];
#pragma unroll
    for (int j = 0; j < 4; j++) t[j] = __builtin_bit_cast(f32x4, lds128(rA + (pi ^ 1) * 4096 + j * 1024));
    wait_lgkm();
    prog_set(sb + W_XCH + 4 * wv, 3);
    const u32 yo = ((lane >> 3) & 3) * 73728 + (lane >> 5) * 64 + (lane & 7) * 8;
    char* yd = ws + WS_Y2 + (chb + tm) * 128 + pi * 1179648;
#pragma unroll
    for (int j = 0; j < 4; j++) conv_out(vadd4(acc[j], t[j]), uu[j], tm ? hi2f(dv) : lo2f(dv), yo + j * 294912, yd);
    STAMP(8);
    ypub = F_Y + (((16 * pi + lane) * 8 + (chb + tm) / 72) * 80 + (chb + tm) % 72) * 8;
    if (wv < 8) publish(fb, ypub, tag, lane < 16);
  }
  if (wv < 8) {
    __builtin_amdgcn_s_setprio(1);
    const int c = lane & 31, hh = lane >> 5, q4 = (lane & 15) >> 2;
    const int w = spare ? wv ^ 4 : wv;
    const u32 kcol = 64 * w + (lane & 16) * 2 + 16 * (lane & 1) + 4 * (lane & 2);
    const u32 ka = opaque(sb + L_K + (8 * hh + q4) * 1088 + kcol), ka2 = opaque(sb + L_K + (16 + q4) * 1088 + kcol);
    const u32 va = opaque(sb + L_V + c * 1040 + 64 * w + 16 * hh);
    const u32 qa = opaque(sb + L_Q + q4 * 512 + 64 * w + (lane & 16) * 2 + 8 * (lane & 3)), qa0 = opaque(qa + 4096 * hh);
    const int thr = c - 8 * hh;
    const u32 lom = opaque(hh ? 0u : ~0u);
    const float qsc = u2f(opaque(f2u(QSCALE)));
    u32 msk[8], nmk[8];
    unsigned long long cnd[16];
#pragma unroll
    for (int e = 0; e < 16; e++) cnd[e] = __builtin_amdgcn_ballot_w64(16 * (e >> 3) + (e & 7) <= thr);
#pragma unroll
    for (int j = 0; j < 8; j++) { const int k0 = 16 * (j >> 2) + 2 * (j & 3); msk[j] = opaque(k0 + 1 <= thr ? 0xffffffffu : (k0 <= thr ? 0xffffu : 0u)); nmk[j] = opaque(~msk[j]); }
    f32x16 S0;
#pragma unroll
    for (int e = 0; e < 16; e++) S0[e] = u2f(opaque(spare ? 0xff800000u : 0u));
    const u32 oo = opaque((c >> 4) * 18432 + (c & 15) * 16 + hh * 256);
    const u32 fl = sb + W_PROG + 4 * (spare ? (lane & 3) : (lane & 7));
    if (wv < 3) st128(sb + (wv < 2 ? L_K + (18 + wv) * 1088 : L_V + 18 * 1040) + lane * 16, u32x4{0x3f803f80u, 0x3f803f80u, 0x3f803f80u, 0x3f803f80u});
    const char* Ag = ws + WS_QKV + (size_t)(4 * qb * 1728 + 18 * h) * 128;
    const char* Kg = Ag + 576 * 128 - (qb ? 884736 : 0);
    const u32 lo = opaque(((lane >> 3) & (qb ? 7 : 3)) * 221184 + (lane & 7) * 16), lq = opaque(((lane >> 3) & 3) * 221184 + (lane >> 5) * 128 + (lane & 7) * 16);
    const char *pq = Ag + wv * 256, *pk = Kg + wv * 128, *pe = wv < 4 ? Kg + (wv < 2 ? 16 + wv : 590 + wv) * 128 : Ag + 2048;
    u32 le = sb + (wv < 2 ? L_K + (16 + wv) * 1088 : (wv < 4 ? L_V + (14 + wv) * 1040 : L_Q + 8192)), lk = sb + L_K + wv * 1088, lv = sb + L_V + wv * 1040;
    const u32 lx = opaque(wv < 4 ? lo : lq);
    asm volatile("" : "+s"(pq), "+s"(pk), "+s"(pe), "+s"(le), "+s"(lk), "+s"(lv));
    SCHED();
    if (wv >= 4 && !spare) publish(fb, pof, tag, pon);
    STAMP(3);
    if (wv == (spare ? 0u : (u32)POLLW)) {
      if (spare) { prog_wait(sb + W_XCH + 32, 1); __builtin_amdgcn_s_sleep(SQD); }
      __builtin_amdgcn_s_sleep(QDELAY);
      await<POLL_FAST>(fb, F_1 + ((h * 8 + qb) * 64 + ((lane < 48 ? lane : lane - 16) | (lane < 16 || spare ? 4 : 0))) * 8, tag);
      prog_set(sb + W_GO, 1);
    } else prog_wait(sb + W_GO, 1);
    STAMP(4);
    __builtin_amdgcn_s_setprio(BAL ? 2 : 1);
    if (!spare || wv < 4) {
    dma16c(pk, lo, lk); dma16c(pk + 1024, lo, lk + 8704); dma16c(pq, lq, sb + L_Q + wv * 1024); dma16c(pe, lx, le);
    if (spare) {
      dma16c(pk + 512, lo, lk + 4352); dma16c(pk + 1536, lo, lk + 13056); dma16c(pq + 1024, lq, sb + L_Q + 4096 + wv * 1024); dma16c(Ag + 2048, lq, sb + L_Q + 8192);
      dma16c(pk + 580 * 128, lo, lv + 4160); dma16c(pk + 588 * 128, lo, lv + 12480);
    }
    __builtin_amdgcn_s_sleep(VDELAY);
    dma16c(pk + 576 * 128, lo, lv); dma16c(pk + 584 * 128, lo, lv + 8320);
    STAMP(7);
    wait_vm<2>();
    prog_set(sb + W_PROG + 4 * wv, 2);
    }
    prog_wait(fl, 2);
    STAMP(5);
    struct KF { u32x4 a; u32x2 b; };
    struct VF { u32x4 a, b; };
    auto ldk = [&](int i) { KF k; k.a = cat(tr_read(ka + i * 64), tr_read(ka + 4352 + i * 64)); k.b = tr_read(ka2 + i * 64); return k; };
    auto ldv = [&](int i) { VF v; v.a = lds128(va + i * 64); v.b = lds128(va + i * 64 + 32); return v; };
    auto pk8 = [&](const f32x16& p, int o) { return u32x4{pk2(p[o], p[o + 1]), pk2(p[o + 2], p[o + 3]), pk2(p[o + 4], p[o + 5]), pk2(p[o + 6], p[o + 7])}; };
    const int ib = spare ? 8 - w : 1;
    u32x4 q0 = cat(tr_read(qa0), tr_read(qa0 + 2048));
    const KF k8 = ldk(8);
    u32x2 q1 = tr_read(qa + 8192);
    const KF k0 = ldk(0);
    SCHED();
    auto qs = [&](u32 v) { return pk2(vmul(lo2f(v), qsc), vmul(hi2f(v), qsc)); };
    q0 = u32x4{qs(q0.x), qs(q0.y), qs(q0.z), qs(q0.w)};
    q1.x = qs(q1.x) & lom; q1.y = 0u;
    auto qk = [&](const KF& k) { f32x16 z = {}; z = mfma32(k.a, q0, z); return mfma32k8(k.b, q1, z); };
    f32x16 Sm = {};
    Sm = mfma32(k8.a, q0, Sm); S0 = mfma32(k0.a, q0, S0);
    Sm = mfma32k8(k8.b, q1, Sm); S0 = mfma32k8(k0.b, q1, S0);
    KF kf[2]; VF vf[2], v8, v0;
    kf[1] = ldk(7); kf[0] = ldk(6);
    SCHED();
#pragma unroll
    for (int e = 0; e < 16; e++) Sm[e] = __builtin_amdgcn_inverse_ballot_w64(cnd[e]) ? Sm[e] : S0[e];
    float m = __builtin_fmaxf(__builtin_fmaxf(Sm[0], Sm[1]), Sm[2]), m2 = __builtin_fmaxf(__builtin_fmaxf(Sm[3], Sm[4]), Sm[5]);
#pragma unroll
    for (int e = 6; e < 14; e += 4) { m = fmaxf(fmaxf(m, Sm[e]), Sm[e + 1]); m2 = fmaxf(fmaxf(m2, Sm[e + 2]), Sm[e + 3]); }
    m = fmaxf(fmaxf(m, Sm[14]), Sm[15]);
    m = xhalf_max(fmaxf(m, m2));
    f32x16 O;
    float l;
#pragma unroll 1
    for (int pass = 0; pass < 2; pass++) {
      { const float r1 = lo2f(pk2(m, m)) - m; q1.y = hh ? pk2(r1 - lo2f(pk2(r1, r1)), 0.f) : pk2(-m, r1); }
      f32x16 S[2];
      {
        f32x16 p = mfma32k8(kf[1].b, u32x2{0u, q1.y}, Sm);
        S[1] = qk(kf[1]);
        SCHED();
        wait_vm<0>();
        prog_set(sb + W_PROG + 4 * wv, 3);
#pragma unroll
        for (int e = 0; e < 8; e++) p[e] = __builtin_amdgcn_exp2f(p[e]);
        SCHED();
        const u32 vfl = lds_peek(fl);
        SCHED();
#pragma unroll
        for (int e = 8; e < 16; e++) p[e] = __builtin_amdgcn_exp2f(p[e]);
        SCHED();
        if (__builtin_amdgcn_ballot_w64(vfl < 3u)) prog_wait(fl, 3);
        asm volatile("" ::: "memory");
        v8 = ldv(8); v0 = ldv(0); vf[1] = ldv(7);
        SCHED();
        const u32x4 pa = pk8(p, 0), pb = pk8(p, 8);
        O = f32x16{};
        O = mfma32(v8.a, pa & u32x4{msk[0], msk[1], msk[2], msk[3]}, O); O = mfma32(v8.b, pb & u32x4{msk[4], msk[5], msk[6], msk[7]}, O);
        O = mfma32(v0.a, pa & u32x4{nmk[0], nmk[1], nmk[2], nmk[3]}, O); O = mfma32(v0.b, pb & u32x4{nmk[4], nmk[5], nmk[6], nmk[7]}, O);
      }
      auto tile = [&](auto self, auto I) -> void {
        constexpr int i = I;
        if (i < ib) return;
        if constexpr (i == BAL1) __builtin_amdgcn_s_setprio(1);
        if constexpr (i == BAL0) __builtin_amdgcn_s_setprio(0);
        SCHED();
        f32x16& Sc = S[i & 1];
        f32x16 z = {};
        if constexpr (i > 1) z = mfma32(kf[(i - 1) & 1].a, q0, z);
        if constexpr (i > 2) kf[i & 1] = ldk(i - 2);
        if constexpr (i > 1) vf[(i - 1) & 1] = ldv(i - 1);
        SCHED();
#pragma unroll
        for (int e = 0; e < 8; e++) Sc[e] = __builtin_amdgcn_exp2f(Sc[e]);
        SCHED();
        if constexpr (i > 1) S[(i - 1) & 1] = mfma32k8(kf[(i - 1) & 1].b, q1, z);
        const u32x4 pa = pk8(Sc, 0);
        SCHED();
#pragma unroll
        for (int e = 8; e < 12; e++) Sc[e] = __builtin_amdgcn_exp2f(Sc[e]);
        SCHED();
        O = mfma32(vf[i & 1].a, pa, O);
#pragma unroll
        for (int e = 12; e < 16; e++) Sc[e] = __builtin_amdgcn_exp2f(Sc[e]);
        const u32x4 pb = pk8(Sc, 8);
        SCHED();
        O = mfma32(vf[i & 1].b, pb, O);
        if constexpr (i > 1) self(self, IC(i - 1));
      };
      tile(tile, IC(7));
      SCHED();
      { const u32 lu = f2u(O[10]); auto r = __builtin_amdgcn_permlane32_swap(lu, lu, false, false); l = u2f(r[0]); }
      if (pass == 1 || __all(l > 8.67e-19f && l < 1.15e18f)) break;
      q1.y = 0u;
#pragma unroll 1
      for (int i = ib; i < 8; i++) {
        const f32x16 s = qk(ldk(i));
#pragma unroll
        for (int e = 0; e < 16; e += 2) m = fmaxf(fmaxf(m, s[e]), s[e + 1]);
      }
      m = xhalf_max(m);
      kf[1] = ldk(7); kf[0] = ldk(6);
    }
    STAMP(6);
    float inv = __builtin_amdgcn_rcpf(l);
    asm volatile("s_nop 1" : "+v"(inv));
    char* Og = ws + WS_O + (size_t)(16 * qb + 2 * w) * 18432;
    gst128(u32x4{pk2(vmul(O[0], inv), vmul(O[1], inv)), pk2(vmul(O[2], inv), vmul(O[3], inv)), pk2(vmul(O[4], inv), vmul(O[5], inv)), pk2(vmul(O[6], inv), vmul(O[7], inv))},
           oo, Og + (h >> 1) * 1024 + (h & 1) * 512);
    if (hh == 0) gst32(pk2(vmul(O[8], inv), vmul(O[9], inv)), (c >> 4) * 18432 + (c & 15) * 4, Og + 16384 + h * 64);
    publish(fb, F_O + (((4 * qb + (w & 3)) * 8 + lane) * 64 + (w >> 2) * 32 + h) * 8, tag, lane < 8);
    {
      __builtin_amdgcn_s_setprio(0);
      const int g = lane >> 4;
      prog_wait(sb + W_XCH + 32 + 4 * (lane & 3), 4);
      const u32 wa2 = sb + L_W + ((4 * g + (q4 >> 1)) * 18 + 16 + (q4 & 1)) * 160 + 8 * (lane & 3);
      u32x4 wx[2], wr[10];
      sfor<2>([&](auto J) { const int i = J ? 8 + (wv & 1) : wv; const u32 a = wa2 + (i / 5) * 46080 + (i % 5) * 32; wx[J] = cat(tr_read(a), tr_read(a + 5760)); });
      prog_set(sb + W_PROG + 4 * wv, 4);
      const u32 wa = sb + L_W + wv * 11520 + (18 * (g >> 1) + 4 * (g & 1) + q4) * 160 + 8 * (lane & 3), wd = sb + L_W + wv * 11520 + lane * 16;
      sfor<10>([&](auto I) { const u32 a = wa + (I / 5) * 5760 + (I % 5) * 32; wr[I] = cat(tr_read(a), tr_read(a + 1280)); });
      prog_wait(sb + W_PROG + 4 * (lane & 7), 4);
      sfor<2>([&](auto J) { const int i = J ? 8 + (wv & 1) : wv; st128(sb + L_Q + i * 1024 + lane * 16, wx[J]); });
      sfor<10>([&](auto I) { st128(wd + (I / 5) * 5760 + (I % 5) * 1024, wr[I]); });
      prog_set(sb + W_PROG + 4 * wv, 5);
    }
      if (b == 255 && wv == 4) {
#pragma unroll 1
        for (int spin = 0; spin < (1 << 18); spin++) {
          u32x4 v[9];
          sfor<9>([&](auto I) { gl128c<0>(v[I], (lane >> 4) * 2048 + (lane & 15) * 16, fb + F_U + I * 8192); });
          asm volatile("s_waitcnt vmcnt(0)" : "+v"(v[0]), "+v"(v[1]), "+v"(v[2]), "+v"(v[3]), "+v"(v[4]), "+v"(v[5]), "+v"(v[6]), "+v"(v[7]), "+v"(v[8])::"memory");
          u32 bad = 0;
#pragma unroll
          for (int i = 0; i < 9; i++) bad |= (v[i].x ^ tag.x) | (v[i].y ^ tag.y) | (v[i].z ^ tag.x) | (v[i].w ^ tag.y);
          if (!__builtin_amdgcn_ballot_w64(bad != 0)) break;
          __builtin_amdgcn_s_sleep(16);
        }
        if (lane == 0) gst64(tag, F_EP, fb);
      }
    STAMP_END();
    return;
  }
  {
    const int g = lane >> 4, l15 = lane & 15, q = l15 >> 2;
    const int r7 = lane / 9, c9 = lane - 9 * r7;
    const u32 xw = sb + L_X + cw * 2304;
    const int m16 = 16 * (rt >> 2) + 2 * (rt & 3) + 8 * (cw >> 1) + (cw & 1);
    if (__builtin_amdgcn_ballot_w64(lds_peek(sb + W_PROG + 4 * (spare ? (lane & 3) : (lane & 7))) < (u32)WOGATE)) { publish(fb, ypub, tag, lane < 16); ypub = 0; }
    prog_wait(sb + W_XCH + (spare ? 16 + 4 * (lane & 7) : 32 + 4 * (lane & 3)), 3);
    prog_wait(sb + W_PROG + 4 * (spare ? (lane & 3) : (lane & 7)), WOGATE);
    STAMP(9);
    {
      const u16* xp = x + (size_t)(16 * m16) * 576 + 72 * ct;
      dma16n<63>(xp, r7 * 1152 + 16 * c9, xw); dma16n<63>(xp + 7 * 576, r7 * 1152 + 16 * c9, xw + 1008); dma16n<63>(xp + 9 * 576, r7 * 1152 + 16 * c9, xw + 1296);
      wo_load(0, 4);
      wait_vm<7>();
      if (ypub && lane < 16) gst64(tag, ypub, fb);
      wo_load(4, YPOS);
    }
    const u32 yf = F_Y + ((rt * 8 + ct) * 80 + (lane < 18 ? 18 * cw + lane : 72 + (cw >> 1))) * 8;
    u32x2 yv;
    asm volatile("global_load_dwordx2 %0, %1, %2 sc1" : "=v"(yv) : "v"(yf), "s"(fb));
    wo_load(YPOS, 24);
    STAMP(10);
    asm volatile("s_waitcnt vmcnt(%1)" : "+v"(yv) : "i"(24 - YPOS) : "memory");
    if (__builtin_amdgcn_ballot_w64(yv.x != tag.x || yv.y != tag.y)) await<POLL_SLEEP>(fb, yf, tag);
    STAMP(11);
    {
      const char* yp = ws + WS_Y2 + (size_t)(rt * 576 + 72 * ct + 18 * cw) * 128;
      dma16c(yp, lane * 16, sb + L_Y + cw * 2304); dma16c(yp + 1024, lane * 16, sb + L_Y + cw * 2304 + 1024); dma16c(yp + 1280, lane * 16, sb + L_Y + cw * 2304 + 1280);
    }
    u32 gtu;
    gl32c<0>(gtu, (16 * m16 + l15) * 4, ws + WS_G);
    wait_vm<4>();
    prog_set(sb + W_XCH + 4 * wv, 4);
    asm volatile("s_waitcnt vmcnt(0)" : "+v"(gtu)::"memory");
    prog_set(sb + W_XCH + 4 * wv, 5);
    prog_wait(sb + W_XCH + 32 + 4 * (lane & 3), 5);
    const float gt = u2f(gtu);
    f32x4 acc[5]; u32x2 tb[5];
    const u32 xa = xw + l15 * 144 + 8 * g;
    {
      const float g1 = rbf(1.f - gt);
      const u32 ya = sb + L_Y + (4 * g + q) * 128 + 32 * cw + 8 * (lane & 3);
      sfor<5>([&](auto NT) {
        const u32x2 y = tr_read(ya + NT * 2048), xv = lds64(xa + NT * 32);
        tb[NT] = u32x2{opaque(pk2(vmul(vmul(g1, lo2f(xv.x)), lo2f(y.x)), vmul(vmul(g1, hi2f(xv.x)), hi2f(y.x)))), opaque(pk2(vmul(vmul(g1, lo2f(xv.y)), lo2f(y.y)), vmul(vmul(g1, hi2f(xv.y)), hi2f(y.y))))};
      });
    }
    const u32 wl = opaque(sb + L_W + lane * 16), wh = opaque(wl + 46080), wq = opaque(sb + L_Q + lane * 16);
    u32x4 wf[3][5];
    auto rd = [&](auto KS) {
      sfor<5>([&](auto NT) {
        constexpr int ks = KS;
        if constexpr (ks < 16) wf[ks % 3][NT] = lds128((ks < 8 ? wl : wh) + (ks & 7) * 5760 + NT * 1024);
        else wf[ks % 3][NT] = lds128(wq + (5 * (ks - 16) + NT) * 1024);
      });
    };
    STAMP(6);
    u32x4 of[16]; u32 oe[8];
    const char* ob = ws + WS_O + (size_t)m16 * 18432;
    {
      const u32 oo = F_O + ((rt * 8 + ct) * 64 + (cw >> 1) * 32 + (lane & 31)) * 8;
      prog_wait(sb + W_PROG + (spare || OGATE == 2 ? 4 * (lane & 7) : 16 * (cw >> 1) + 4 * (lane & 3)), 4);
      __builtin_amdgcn_s_sleep(ODELAY);
      u32 miss = ~0u;
      sfor<4>([&](auto G) {
#pragma unroll 1
        for (int spin = 0; (miss & (0xffu << (8 * G))) && spin < (1 << 20); spin++) {
          u32x2 v;
          asm volatile("global_load_dwordx2 %0, %1, %2 sc1\n\ts_waitcnt vmcnt(0)" : "=&v"(v) : "v"(oo), "s"(fb) : "memory");
          miss = (u32)__builtin_amdgcn_ballot_w64(v.x != tag.x || v.y != tag.y);
          if (miss & (0xffu << (8 * G))) __builtin_amdgcn_s_sleep(POLL_FAST);
        }
        sfor<4>([&](auto F) { gl128c<F * 1024>(of[4 * G + F], lane * 16, ob + G * 4096); });
      });
    }
    STAMP(12);
    __builtin_amdgcn_s_setprio(3);
    sfor<8>([&](auto F) { gl32c<(F >> 2) * 1024 + (F & 3) * 64>(oe[F], 256 * g + 4 * l15, ob + 16384); });
    prog_wait(sb + W_PROG + 4 * (lane & 7), 5);
    rd(IC(0)); rd(IC(1));
    sfor<16>([&](auto KS) {
      constexpr int ks = KS;
      SCHED();
      if constexpr (ks % 2 == 0) asm volatile("s_waitcnt vmcnt(%2)" : "+v"(of[ks]), "+v"(of[ks + 1]) : "i"(22 - ks) : "memory");
      rd(IC(ks + 2));
      sfor<5>([&](auto NT) { acc[NT] = mfma16(wf[ks % 3][NT], of[ks], ks == 0 ? f32x4{0.f, 0.f, 0.f, 0.f} : acc[NT]); });
      sfor<5>([&](auto) { __builtin_amdgcn_sched_group_barrier(0x8, 1, 0); __builtin_amdgcn_sched_group_barrier(0x100, 1, 0); });
    });
    SCHED();
    asm volatile("s_waitcnt vmcnt(0)" : "+v"(oe[0]), "+v"(oe[1]), "+v"(oe[2]), "+v"(oe[3]), "+v"(oe[4]), "+v"(oe[5]), "+v"(oe[6]), "+v"(oe[7])::"memory");
    sfor<5>([&](auto NT) { acc[NT] = mfma16(wf[1][NT], u32x4{oe[0], oe[1], oe[2], oe[3]}, acc[NT]); });
    sfor<5>([&](auto NT) { acc[NT] = mfma16(wf[2][NT], u32x4{oe[4], oe[5], oe[6], oe[7]}, acc[NT]); });
    SCHED();
    STAMP(13);
    sfor<5>([&](auto NT) {
      constexpr int nt = NT;
      if (nt < 4 || g < 2)
        st64(xa + nt * 32, u32x2{pk2(fmaf(gt, acc[nt][0], lo2f(tb[nt].x)), fmaf(gt, acc[nt][1], hi2f(tb[nt].x))), pk2(fmaf(gt, acc[nt][2], lo2f(tb[nt].y)), fmaf(gt, acc[nt][3], hi2f(tb[nt].y)))});
    });
    char* orow = (char*)(out + (size_t)(16 * m16) * 576 + 72 * ct);
    u32x4 t[3];
#pragma unroll
    for (int it = 0; it < 3; it++) t[it] = lds128(xw + it * 1008 + lane * 16);
#pragma unroll
    for (int it = 0; it < 3; it++)
      if (lane < (it < 2 ? 63 : 18)) asm volatile("global_store_dwordx4 %0, %1, %2 " OUTPOL "\n\ts_nop 1" ::"v"((7 * it + r7) * 1152 + 16 * c9), "v"(t[it]), "s"(orow) : "memory");
  }
  STAMP_END();
}
extern "C" __global__ void gm_end() {}

extern "C" void kernel_launch(void* const* d_in, const int* in_sizes, int n_in, void* d_out, int out_size, void* d_ws, size_t ws_size,
                              hipStream_t stream) {
  (void)hipFuncSetAttribute((const void*)gm_main, hipFuncAttributeMaxDynamicSharedMemorySize, L_END);
  gm_main<<<256, 768, L_END, stream>>>((const u16*)d_in[0], (const u16*)d_in[1], (char*)d_ws, (const u16*)d_in[4], (const u16*)d_in[5], (const u16*)d_in[6],
                                       (const u16*)d_in[7], (const u16*)d_in[2], (const u16*)d_in[8], (const u16*)d_in[9], (u16*)d_out);
}
```

```cpp
#include <hip/hip_runtime.h>
#include <hip/hip_bf16.h>
#include <utility>

typedef unsigned short u16;
typedef unsigned int u32;
typedef u32 u32x2 __attribute__((ext_vector_type(2)));
typedef u32 u32x4 __attribute__((ext_vector_type(4)));
typedef short s16x4 __attribute__((ext_vector_type(4)));
typedef __bf16 bf16x8 __attribute__((ext_vector_type(8)));
typedef __bf16 bf16x2 __attribute__((ext_vector_type(2)));
typedef float f32x2 __attribute__((ext_vector_type(2)));
typedef float f32x4 __attribute__((ext_vector_type(4)));
typedef float f32x16 __attribute__((ext_vector_type(16)));
#define LAS __attribute__((address_space(3)))
#define GAS __attribute__((address_space(1)))
#define DEVI __device__ __forceinline__
#define IC(n) std::integral_constant<int, (n)>{}

constexpr size_t WS_QKV = 0;
constexpr size_t WS_O   = WS_QKV + 96ull * 2048 * 40;
constexpr size_t WS_UR  = WS_O + 32ull * 2048 * 40;
constexpr size_t WS_Y2  = WS_UR + 576ull * 4096;
constexpr size_t WS_G   = WS_Y2 + 576ull * 4096;
constexpr size_t WS_F   = WS_G + 8192;
constexpr u32 F_EP = 0, F_1 = 1024, F_U = F_1 + 256 * 512, F_O = F_U + 288 * 256, F_Y = F_O + 256 * 512;

template <typename F, int... I> DEVI void sfor_impl(F&& f, std::integer_sequence<int, I...>) { (f(std::integral_constant<int, I>{}), ...); }
template <int N, typename F> DEVI void sfor(F&& f) { sfor_impl(f, std::make_integer_sequence<int, N>{}); }

DEVI u32 pk2(float a, float b) { f32x2 v = {a, b}; return __builtin_bit_cast(u32, __builtin_convertvector(v, bf16x2)); }
DEVI u32 f2u(float a) { return __builtin_bit_cast(u32, a); }
DEVI float u2f(u32 a) { return __builtin_bit_cast(float, a); }
DEVI float lo2f(u32 w) { return __builtin_bit_cast(float, w << 16); }
DEVI float hi2f(u32 w) { return __builtin_bit_cast(float, w & 0xffff0000u); }
DEVI float rbf(float a) { return lo2f(pk2(a, a)); }
DEVI f32x4 mfma16(u32x4 a, u32x4 b, f32x4 c) { return __builtin_amdgcn_mfma_f32_16x16x32_bf16(__builtin_bit_cast(bf16x8, a), __builtin_bit_cast(bf16x8, b), c, 0, 0, 0); }
DEVI f32x16 mfma32(u32x4 a, u32x4 b, f32x16 c) { return __builtin_amdgcn_mfma_f32_32x32x16_bf16(__builtin_bit_cast(bf16x8, a), __builtin_bit_cast(bf16x8, b), c, 0, 0, 0); }
DEVI f32x16 mfma32k8(u32x2 a, u32x2 b, f32x16 c) { return __builtin_amdgcn_mfma_f32_32x32x8bf16_1k(__builtin_bit_cast(s16x4, a), __builtin_bit_cast(s16x4, b), c, 0, 0, 0); }
DEVI u32x2 tr_read(u32 addr) { return __builtin_bit_cast(u32x2, __builtin_amdgcn_ds_read_tr16_b64_v4i16((LAS s16x4*)addr)); }
DEVI u32x4 lds128(u32 addr) { return *(LAS u32x4*)addr; }
DEVI u32x2 lds64(u32 addr) { return *(LAS u32x2*)addr; }
DEVI u32 lds32(u32 addr) { return *(LAS u32*)addr; }
DEVI void st128(u32 addr, u32x4 v) { *(LAS u32x4*)addr = v; }
DEVI void st64(u32 addr, u32x2 v) { *(LAS u32x2*)addr = v; }
DEVI void st32(u32 addr, u32 v) { *(LAS u32*)addr = v; }
DEVI u32x4 cat(u32x2 a, u32x2 b) { return u32x4{a.x, a.y, b.x, b.y}; }
DEVI float vfma(float a, float b, float c) { float d; asm("v_fma_f32 %0, %1, %2, %3" : "=v"(d) : "v"(a), "v"(b), "v"(c)); return d; }
DEVI float vmul(float a, float b) { float d; asm("v_mul_f32 %0, %1, %2" : "=v"(d) : "v"(a), "v"(b)); return d; }
DEVI float vadd(float a, float b) { float d; asm("v_add_f32 %0, %1, %2" : "=v"(d) : "v"(a), "v"(b)); return d; }
DEVI f32x4 vadd4(f32x4 a, f32x4 b) { return f32x4{vadd(a[0], b[0]), vadd(a[1], b[1]), vadd(a[2], b[2]), vadd(a[3], b[3])}; }
DEVI u32 opaque(u32 v) { asm volatile("" : "+v"(v)); return v; }
DEVI void dma16(const void* sbase, u32 voff, u32 ldsbase) {
  asm volatile("s_mov_b32 m0, %2\n\ts_nop 0\n\tglobal_load_lds_dwordx4 %0, %1" ::"v"(voff), "s"(sbase), "s"(ldsbase) : "memory");
}
template <int NL> DEVI void dma16n(const void* sbase, u32 voff, u32 ldsbase) {
  asm volatile("s_mov_b32 m0, %2\n\ts_mov_b32 exec_hi, %3\n\tglobal_load_lds_dwordx4 %0, %1\n\ts_mov_b32 exec_hi, -1" ::"v"(voff), "s"(sbase), "s"(ldsbase),
               "i"((1u << (NL - 32)) - 1u) : "memory");
}
template <int OFF> DEVI void gl128(u32x4& d, u32 voff, const void* sbase) { asm volatile("global_load_dwordx4 %0, %1, %2 offset:%3" : "=v"(d) : "v"(voff), "s"(sbase), "i"(OFF)); }
template <int OFF> DEVI void gl64(u32x2& d, u32 voff, const void* sbase) { asm volatile("global_load_dwordx2 %0, %1, %2 offset:%3" : "=v"(d) : "v"(voff), "s"(sbase), "i"(OFF)); }
DEVI void dma16c(const void* sbase, u32 voff, u32 ldsbase) {
  asm volatile("s_mov_b32 m0, %2\n\ts_nop 0\n\tglobal_load_lds_dwordx4 %0, %1 sc1" ::"v"(voff), "s"(sbase), "s"(ldsbase) : "memory");
}
template <int OFF> DEVI void gl128c(u32x4& d, u32 voff, const void* sbase) { asm volatile("global_load_dwordx4 %0, %1, %2 offset:%3 sc1" : "=v"(d) : "v"(voff), "s"(sbase), "i"(OFF)); }
template <int OFF> DEVI void gl32c(u32& d, u32 voff, const void* sbase) { asm volatile("global_load_dword %0, %1, %2 offset:%3 sc1" : "=v"(d) : "v"(voff), "s"(sbase), "i"(OFF)); }
DEVI void gst128(u32x4 v, u32 voff, void* sbase) { asm volatile("global_store_dwordx4 %0, %1, %2 sc1\n\ts_nop 1" ::"v"(voff), "v"(v), "s"(sbase) : "memory"); }
DEVI void gst64(u32x2 v, u32 voff, void* sbase) { asm volatile("global_store_dwordx2 %0, %1, %2 sc1" ::"v"(voff), "v"(v), "s"(sbase) : "memory"); }
DEVI void gst32(u32 v, u32 voff, void* sbase) { asm volatile("global_store_dword %0, %1, %2 sc1" ::"v"(voff), "v"(v), "s"(sbase) : "memory"); }
#define CODE_PF(from, to, ck, step, lds)                                                                                              \
  do {                                                                                                                                \
    u32 lo_, hi_, len_;                                                                                                               \
    asm volatile("s_getpc_b64 s[20:21]\n\ts_add_u32 %0, s20, " from "@rel32@lo+4\n\ts_addc_u32 %1, s21, " from "@rel32@hi+12\n\ts_mov_b32 %2, " to "-" from \
                 : "=s"(lo_), "=s"(hi_), "=s"(len_)::"s20", "s21", "scc");                                                            \
    const char* code_ = (const char*)(((unsigned long long)hi_ << 32) | lo_);                                                         \
    for (u32 o_ = (ck) * 1024; o_ < len_; o_ += (step) * 1024) { const u32 a_ = o_ + (threadIdx.x & 63) * 16; dma16(code_, a_ < len_ ? a_ : 0u, lds); } \
  } while (0)
template <int N> DEVI void wait_vm() { asm volatile("s_waitcnt vmcnt(%0)" ::"i"(N > 63 ? 63 : N) : "memory"); }
DEVI void wait_lgkm() { asm volatile("s_waitcnt lgkmcnt(0)" ::: "memory"); }
DEVI void bar() { asm volatile("s_barrier" ::: "memory"); }
DEVI u32 rfl(u32 v) { return __builtin_amdgcn_readfirstlane(v); }
#define SCHED() __builtin_amdgcn_sched_barrier(0)
#define STAMP(i)
#define STAMP_END()

constexpr float QSCALE = 0.23570226039551584f * 1.4426950408889634f;

#define POLL_SLEEP 8
#define POLL_FAST 2
#define SPDELAY 16
#define LDPOL "sc1"
#define FSPOL "sc1"
#define CVDELAY 0
#define PUBB 2
#define NMID 1
#define FLB 1
#define PUBD 0
#define PREPFIRST 2
#define OUTPOL "sc1"
#define BAL 1
#define CPRIO 3
#define WOGATE 3
#define YPOS 16
#define CMPRIO 3
#define QDELAY 8
#define OGATE 1
#define ODELAY 6
#define BAL1 4
#define BAL0 1
#define SQD 8
#define UBACK 20
#define POLLW 4
#define VDELAY 3

constexpr int L_SLOT = 32768, L_WOFF = 16384, L_STG = 94208, L_GW = 131072, L_PT = 133120, L_TILE = 17408;
constexpr int L_K = 0, L_V = 21760, L_C = 41536, L_R = L_PT + 1024, L_FL = 152320, L_DUMMY = L_FL + 256, L_Q = 153600, L_END = 163840;
constexpr int KC_STR = 4640, CS_U = 4 * KC_STR, CS_SZ = CS_U + 4096 + 128;
constexpr int L_W = L_C, L_X = L_W + 576 * 160, L_Y = L_X + 9216;
static_assert(L_Y + 9216 <= L_FL && L_C + 2 * CS_SZ <= L_STG && L_C + 4 * CS_SZ <= L_PT && L_PT + L_TILE <= L_FL, "lds map");
constexpr int W_PROG = L_FL, W_XCH = L_FL + 64, W_GO = L_FL + 128, W_TAG = L_FL + 144;

DEVI u32 lds_peek(u32 a) { return __hip_atomic_load((LAS u32*)a, __ATOMIC_RELAXED, __HIP_MEMORY_SCOPE_WORKGROUP); }
DEVI void prog_set(u32 a, u32 n) { asm volatile("" ::: "memory"); __hip_atomic_store((LAS u32*)a, n, __ATOMIC_RELAXED, __HIP_MEMORY_SCOPE_WORKGROUP); }
DEVI void prog_wait(u32 a, u32 n) { while (__builtin_amdgcn_ballot_w64(lds_peek(a) < n)) __builtin_amdgcn_s_sleep(1); asm volatile("" ::: "memory"); }
DEVI void publish(char* fb, u32 off, u32x2 tag, bool on) { wait_vm<0>(); if (on) asm volatile("global_store_dwordx2 %0, %1, %2 " FSPOL ::"v"(off), "v"(tag), "s"(fb) : "memory"); }
template <int SLP> DEVI void await(const char* fb, u32 off, u32x2 tag) {
  for (int spin = 0; spin < (1 << 20); spin++) {
    u32x2 v;
    asm volatile("global_load_dwordx2 %0, %1, %2 " LDPOL "\n\ts_waitcnt vmcnt(0)" : "=&v"(v) : "v"(off), "s"(fb) : "memory");
    if (!__builtin_amdgcn_ballot_w64(v.x != tag.x || v.y != tag.y)) break;
    __builtin_amdgcn_s_sleep(SLP);
  }
}

DEVI void prep_issue(const u16* Bk, const u16* Bv, int row0, int col, u32 tb, u32 pw, int lane) {
  const int r8 = lane >> 3, c8 = lane & 7;
  sfor<2>([&](auto I) {
    const int j = 2 * pw + I;
    int row = row0 + 8 * j + r8 - 3; row = row < 0 ? 0 : row;
    dma16(Bk + col, row * 1152 + 16 * (c8 ^ j), tb + 1024 * j);
    dma16(Bv + col, (row0 + 8 * j + r8) * 1152 + 16 * (c8 ^ j), tb + 9216 + 1024 * j);
  });
  dma16(Bk + col, (row0 + 61 + (r8 > 2 ? 2 : r8)) * 1152 + 16 * c8, tb + 8192);
}
template <typename F> DEVI void prep_compute(int row0, int col, u32 tb, u32 pw, int lane, u32x4 sh, char* ws, F&& mid) {
  const int c8 = lane & 7, cp = 8 * pw + (lane >> 3);
  const u32 o0 = tb + 1024 * c8 + 16 * ((cp >> 2) ^ c8) + 4 * (cp & 3), o1 = tb + 1024 * c8 + 16 * ((cp >> 2) ^ ((c8 + 1) & 7)) + 4 * (cp & 3);
  row0 += 8 * c8;
  u32 kk[11], vv[8];
#pragma unroll
  for (int i = 0; i < 11; i++) kk[i] = lds32((i < 8 ? o0 : o1) + 128 * i);
#pragma unroll
  for (int i = 0; i < 8; i++) vv[i] = lds32(o0 + 9216 + 128 * i);
  if (row0 == 0) { kk[0] = 0; kk[1] = 0; kk[2] = 0; }
  const u32 go = 2 * cp * 4096 + 2 * (2040 - row0);
  char* dst = ws + WS_UR + (size_t)col * 4096;
  float r[8];
  {
    const float a0 = lo2f(sh.x), a1 = hi2f(sh.x), a2 = lo2f(sh.y), a3 = hi2f(sh.y);
#pragma unroll
    for (int i = 0; i < 8; i++) r[i] = vmul(vfma(lo2f(kk[i + 3]), a0, vfma(lo2f(kk[i + 2]), a1, vfma(lo2f(kk[i + 1]), a2, vmul(lo2f(kk[i]), a3)))), lo2f(vv[i]));
    gst128(u32x4{pk2(r[7], r[6]), pk2(r[5], r[4]), pk2(r[3], r[2]), pk2(r[1], r[0])}, go, dst);
  }
  mid();
  {
    const float a0 = lo2f(sh.z), a1 = hi2f(sh.z), a2 = lo2f(sh.w), a3 = hi2f(sh.w);
#pragma unroll
    for (int i = 0; i < 8; i++) r[i] = vmul(vfma(hi2f(kk[i + 3]), a0, vfma(hi2f(kk[i + 2]), a1, vfma(hi2f(kk[i + 1]), a2, vmul(hi2f(kk[i]), a3)))), hi2f(vv[i]));
    gst128(u32x4{pk2(r[7], r[6]), pk2(r[5], r[4]), pk2(r[3], r[2]), pk2(r[1], r[0])}, go, dst + 4096);
  }
}

template <int NR> DEVI void conv_mfma(u32 cs, int rho0, int lane, f32x4 (&acc)[8]) {
  const int g = lane >> 4, l15 = lane & 15;
  u32 aa = cs + (l15 & 3) * KC_STR + 2 * (64 * (l15 >> 2) + 8 * g) + 64 * rho0;
  u32 ba = opaque(cs + CS_U + 2 * (1852 - 1792 - 4 * l15 + 8 * g) + 64 * rho0), bb = opaque(ba + 8);
  u32x4 A[8], G[8];
  sfor<5>([&](auto J) { constexpr int p = J < 4 ? J + 2 : 1; A[p] = lds128(aa + p * 512); G[p] = cat(lds64(ba + (7 - p) * 512), lds64(bb + (7 - p) * 512)); SCHED(); });
#pragma unroll 1
  for (int rho = 0; rho < NR; rho++) {
    constexpr int pa[36] = {2, 2, 3, 3, 2, 4, 3, 4, 2, 5, 1, 1, 2, 1, 3, 1, 4, 1, 5, 1, 6, 2, 0, 3, 0, 4, 0, 5, 0, 1, 0, 6, 0, 7, 0, 0};
    constexpr int pe[36] = {2, 3, 2, 3, 4, 2, 4, 3, 5, 2, 1, 2, 1, 3, 1, 4, 1, 5, 1, 6, 1, 0, 2, 0, 3, 0, 4, 0, 5, 0, 1, 0, 6, 0, 7, 0};
    constexpr int rdc[36] = {18, 19, 20, 21, 22, 23, 0, 1, 2, -1, -1, -1, -1, -1, -1, -1, -1, -1, -1, -1, -1, 6, 7, 8, 9, 10, 11, 12, 13, 14, 15, 16, 17, 3, 4, 5};
    sfor<36>([&](auto I) {
      constexpr int i = I, r = rdc[i], x = r / 3;
      if constexpr (i == 35) __builtin_amdgcn_s_setprio(CMPRIO);
      if constexpr (i == 9) { aa = opaque(aa + 64); ba = opaque(ba + 64); bb = opaque(bb + 64); }
      acc[pa[i] + pe[i]] = mfma16(A[pa[i]], G[pe[i]], acc[pa[i] + pe[i]]);
      if constexpr (r >= 0) {
        if constexpr (r % 3 == 0) A[x] = lds128(aa + x * 512);
        else if constexpr (r % 3 == 1) { const u32x2 t = lds64(ba + (7 - x) * 512); G[x].x = t.x; G[x].y = t.y; }
        else { const u32x2 t = lds64(bb + (7 - x) * 512); G[x].z = t.x; G[x].w = t.y; }
      }
      SCHED();
    });
  }
  SCHED();
}
DEVI void conv_out(f32x4 y, u32x2 u, float Dv, u32 off, char* ydst) {
  const float y0 = vfma(hi2f(u.y), Dv, y[0]), y1 = vfma(lo2f(u.y), Dv, y[1]), y2 = vfma(hi2f(u.x), Dv, y[2]), y3 = vfma(lo2f(u.x), Dv, y[3]);
  gst64(u32x2{pk2(y0, y1), pk2(y2, y3)}, off, ydst);
}

DEVI float xhalf_max(float m) { const u32 mu = f2u(m); auto r = __builtin_amdgcn_permlane32_swap(mu, mu, false, false); return fmaxf(u2f(r[0]), u2f(r[1])); }

extern "C" __global__ __launch_bounds__(768, 3) void gm_main(const u16* __restrict__ x, const u16* __restrict__ wqkv, char* __restrict__ ws,
                                                             const u16* __restrict__ Bk, const u16* __restrict__ Bv, const u16* __restrict__ shk,
                                                             const u16* __restrict__ ssmk, const u16* __restrict__ wo, const u16* __restrict__ ssmD,
                                                             const u16* __restrict__ gw, u16* __restrict__ out) {
  extern __shared__ __attribute__((aligned(16))) char smem[];
  const u32 sb = (u32)(size_t)(LAS char*)smem;
  const int b = blockIdx.x, xcd = b & 7, idx = b >> 3;
  const int lane = threadIdx.x & 63;
  const u32 wv = rfl(threadIdx.x >> 6);
  const bool spare = idx >= 28;
  char* const fb = ws + WS_F;
  const int qb = ((idx >> 2) + 1) & 7, h = 4 * xcd + (idx & 3);
  const int chb = wv >= 8 ? 64 * xcd + 2 * idx : 512 + 2 * h;
  u32x2 pre[2], fv; u32x4 cur[2];
  const u32 fo = F_U + ((chb >> 1) * 32 + 24 - 8 * (wv & 3) + (lane & 7)) * 8;
  auto build = [&]() {
    const int tid = threadIdx.x & 255, cw = wv & 3;
    const u32 cb = sb + L_C + (wv >= 8 ? 0 : 2 * CS_SZ);
    {
      const u32 z0 = opaque(cw * KC_STR + 16 * lane), z1 = opaque(CS_U + 4096 + 16 * (lane & 7)), za = cb + (lane < 24 ? z0 : z1);
      st128(za, u32x4{0u, 0u, 0u, 0u}); st128(za + CS_SZ, u32x4{0u, 0u, 0u, 0u});
    }
#pragma unroll
    for (int j = 0; j < 2; j++) {
      const u32 cs = cb + j * CS_SZ + 384 + 16 * tid;
      const u32 w0 = tid ? pre[j].x : 0u, w1 = tid ? pre[j].y : 0u, w2 = cur[j].x, w3 = cur[j].y, w4 = cur[j].z, w5 = cur[j].w;
      const u32 a01 = __builtin_amdgcn_alignbyte(w1, w0, 2), a12 = __builtin_amdgcn_alignbyte(w2, w1, 2), a23 = __builtin_amdgcn_alignbyte(w3, w2, 2),
                a34 = __builtin_amdgcn_alignbyte(w4, w3, 2), a45 = __builtin_amdgcn_alignbyte(w5, w4, 2);
      st128(cs, u32x4{a01, a12, a23, a34});
      st128(cs + KC_STR, u32x4{w1, w2, w3, w4});
      st128(cs + 2 * KC_STR, u32x4{a12, a23, a34, a45});
      st128(cs + 3 * KC_STR, cur[j]);
    }
  };
  u32 pof = 0; bool pon = false;
  STAMP(0);
  {
    const int hg = xcd & 1, ri = idx & 3, rb = 4 * (xcd >> 1) + ri, jt = idx >> 2;
    const int r8 = lane >> 3, c8 = lane & 7;
    const int rot = ((0x124924u >> (2 * (jt + ri))) & 3) * 3;
    const u32 xo = opaque((32 * wv + r8) * 1152 + 16 * (c8 ^ r8));
    asm volatile("" ::"s"(x), "s"(wqkv), "s"(Bk), "s"(Bv));
    if (wv < 4 && !spare) {
      const u32 lw = wv;
      const char* xb = (const char*)(x + (size_t)rb * 128 * 576);
      sfor<4>([&](auto I) { dma16(xb + rot * 128 + I * 9216, xo, sb + (4 * lw + I) * 1024); });
      u32 wof[2];
#pragma unroll
      for (int i = 0; i < 2; i++) {
        const int ch = (lane & 15) ^ (((lane >> 4) | (i << 2)) << 1), ln = 14 * hg + 2 * jt + (ch >> 3);
        wof[i] = ((lane >> 4) + 16 * lw) * 3456 + 128 * (ln > 26 ? 26 : ln) + 16 * (ch & 7);
      }
      const char* wb = (const char*)wqkv;
      auto issue = [&](int c) {
        const u32 sl = sb + (c & 3) * L_SLOT;
        c += rot; c = c >= 9 ? c - 9 : c;
        sfor<4>([&](auto I) { dma16(xb + c * 128 + I * 9216, xo, sl + (4 * lw + I) * 1024); });
        sfor<4>([&](auto I) { dma16(wb + (64 * c + 4 * I) * 3456, wof[I >> 1], sl + L_WOFF + (4 * lw + I) * 1024); });
      };
      sfor<4>([&](auto I) { dma16(wb + (64 * rot + 4 * I) * 3456, wof[I >> 1], sb + L_WOFF + (4 * lw + I) * 1024); });
      prep_issue(Bk, Bv, 64 * idx, 64 * xcd, sb + L_PT, lw, lane);
#pragma unroll 1
      for (int n = 1; n < 8; n++) { issue(n); wait_vm<8>(); bar(); }
      issue(8);
      wait_vm<8>(); bar();
      __builtin_amdgcn_s_sleep(2);
      {
        const char* wp = (const char*)wo + (idx == 27 ? 663552 - 24576 : idx * 23712) + lw * 6144;
        sfor<6>([&](auto I) { dma16(wp + I * 1024, lane * 16, sb + L_DUMMY); });
      }
      wait_vm<6>(); bar();
      bar();
    } else if (wv >= 8 || (spare && wv >= 4)) {
      const bool ex = wv < 8;
      const int row0 = ex ? 256 * xcd + 64 * ri : 64 * idx, col = ex ? 512 : 64 * xcd;
      const u32 tb = sb + (ex ? 0 : L_PT);
      u32x2 e;
      asm volatile("global_load_dwordx2 %0, %1, %2 sc1" : "=v"(e) : "v"(F_EP), "s"(fb));
      u32x4 sh;
      gl128<0>(sh, 8 * col + 16 * (8 * (wv & 3) + r8), shk);
      if (spare) prep_issue(Bk, Bv, row0, col, tb, wv & 3, lane);
      {
        const u32 o = (threadIdx.x & 255) * 16, op = o ? o - 8 : 0;
        sfor<2>([&](auto J) { const u16* kp = ssmk + (size_t)(chb + J) * 2048; gl128<0>(cur[J], o, kp); gl64<0>(pre[J], op, kp); });
      }
      {
        const int t3 = (4 * xcd + (idx & 3)) * 8 + (idx >> 2), k = wv & 3;
        const u32 mo = k == 0 ? F_1 + (h * 8 + qb) * 512 : k == 1 ? F_O + t3 * 512 : k == 2 ? F_Y + t3 * 640 : F_U + (chb >> 1) * 256;
        asm volatile("s_mov_b32 m0, %2\n\ts_nop 0\n\tglobal_load_lds_dword %0, %1 sc1" ::"v"(mo + (lane < (k == 3 ? 4 : k == 2 ? 10 : 8) ? lane : 0) * 64), "s"(fb), "s"(sb + L_DUMMY) : "memory");
      }
      if (wv == 8) st32(sb + L_FL + 4 * lane, 0u);
      if (spare) wait_vm<5>();
      bar();
      asm volatile("s_waitcnt vmcnt(5)" : "+v"(e), "+v"(sh)::"memory");
      e.x += 1u; e.y += e.x == 0u;
      if (wv == 8) st64(sb + W_TAG, e);
      prep_compute(row0, col, tb, wv & 3, lane, sh, ws, [&]() { if (!spare) { SCHED(); bar(); SCHED(); } });
      wait_lgkm();
      bar();
      if (!spare) sfor<PUBB>([&](auto) { bar(); });
      asm volatile("s_waitcnt vmcnt(0)" : "+v"(cur[0]), "+v"(cur[1]), "+v"(pre[0]), "+v"(pre[1])::"memory");
      publish(fb, F_U + (((col >> 1) + 8 * (wv & 3) + lane) * 32 + (row0 >> 6)) * 8, e, lane < 8);
      if (!spare) sfor<8 - NMID - PUBB - FLB>([&](auto) { bar(); });
      else { build(); if (wv >= 8) __builtin_amdgcn_s_sleep(SPDELAY); }
      asm volatile("global_load_dwordx2 %0, %1, %2 sc1" : "=v"(fv) : "v"(fo), "s"(fb));
      if (!spare) sfor<FLB>([&](auto) { bar(); });
    } else if (spare) {
      CODE_PF("gm_main", "gm_end", 4 * ri + wv, 16, sb + L_DUMMY);
      bar(); bar();
    } else {
      const int cw = wv - 4, g = lane >> 4, l15 = lane & 15, wm = cw & 1, wn = cw >> 1;
      const u32 xra = sb + (64 * wm + l15) * 128 + 16 * (g ^ (l15 & 7));
      const u32 xb[2][2] = {{opaque(xra), opaque(xra + 2 * L_SLOT)}, {opaque(xra ^ 64), opaque((xra ^ 64) + 2 * L_SLOT)}};
      u32x4 wf[2][4], xf[2][4];
      f32x4 acc[4][4];
      const int ln = 14 * hg + 2 * jt + wn;
      {
        const int r64 = 2 * rb + wm, seg = ln / 9, lc = ln - 9 * seg, hc = (64 * lc) / 18 + (lane >> 2), li = lane & 1, nx = (lane >> 1) & 1;
        pon = ln == 27 ? lane < 16 : (lane < 20 && hc < 32 && ((18 * hc + 17 * li) >> 6) == lc && (!nx || (seg > 0 && r64 < 28)));
        pof = opaque(ln == 27 ? F_Y + (((4 * (r64 >> 2) + 2 * (r64 & 1)) * 8 + lane) * 80 + 72 + ((r64 >> 1) & 1)) * 8 : F_1 + ((hc * 8 + (r64 >> 2) + nx) * 64 + seg * 16 + li * 8 + (r64 & 3) + (nx ? 0 : 4)) * 8);
      }
      __builtin_amdgcn_s_setprio(2);
      if (ln == 27) {
        dma16(gw, wm * 128 + lane * 16, sb + L_GW + wm * 128);
        wait_vm<0>();
#pragma unroll
        for (int i = 0; i < 4; i++) acc[i][0] = f32x4{0.f, 0.f, 0.f, 0.f};
#pragma unroll 1
        for (int c = 0; c < 9; c++) {
          bar();
          const int ca = c + rot >= 9 ? c + rot - 9 : c + rot;
#pragma unroll
          for (int ks = 0; ks < 2; ks++) {
            const u32x4 gv = lds128(sb + L_GW + ca * 128 + ks * 64 + 16 * g);
#pragma unroll
            for (int i = 0; i < 4; i++) acc[i][0] = mfma16(gv, lds128(xb[ks][0] + (c & 3) * L_SLOT + 2048 * i), acc[i][0]);
          }
          wait_lgkm();
        }
        bar();
        const float p = rbf(g == 0 ? acc[0][0][0] : g == 1 ? acc[1][0][0] : g == 2 ? acc[2][0][0] : acc[3][0][0]);
        gst32(f2u(rbf(1.f / (1.f + __expf(-p)))), lane * 4, ws + WS_G + (rb * 128 + 64 * wm) * 4);
      } else {
    u32 wb[4][2];
    {
      const int q = l15 >> 2, p = lane & 3, f = q | ((g & 1) << 2);
#pragma unroll
      for (int nt = 0; nt < 4; nt++) {
        const u32 a = sb + L_WOFF + (8 * g + q) * 256 + (((8 * wn + 2 * nt + (p >> 1)) ^ (f << 1)) << 4) + 8 * (p & 1);
        wb[nt][0] = opaque(a); wb[nt][1] = opaque(a + 2 * L_SLOT);
      }
    }
    auto rd = [&](auto KS, auto C) {
      constexpr int ks = KS, c = C, so = (c & 1) * L_SLOT, hi = (c >> 1) & 1;
      sfor<4>([&](auto I) {
        xf[ks][I] = lds128(xb[ks][hi] + so + 2048 * I);
        wf[ks][I] = cat(tr_read(wb[I][hi] + so + ks * 8192), tr_read(wb[I][hi] + so + ks * 8192 + 1024));
      });
    };
    auto mm = [&](auto KS, auto FIRST) {
      constexpr int ks = KS;
      sfor<16>([&](auto I) {
        constexpr int mo[16] = {0, 1, 0, 1, 2, 2, 0, 1, 2, 3, 3, 3, 0, 1, 2, 3}, no[16] = {0, 0, 1, 1, 0, 1, 2, 2, 2, 0, 1, 2, 3, 3, 3, 3};
        constexpr int mt = mo[I], nt = no[I];
        acc[mt][nt] = mfma16(xf[ks][mt], wf[ks][nt], FIRST ? f32x4{0.f, 0.f, 0.f, 0.f} : acc[mt][nt]);
      });
    };
    auto ilv = [&]() {
      sfor<12>([&](auto) { __builtin_amdgcn_sched_group_barrier(0x8, 1, 0); __builtin_amdgcn_sched_group_barrier(0x100, 1, 0); });
      __builtin_amdgcn_sched_group_barrier(0x8, 4, 0);
    };
    char* qo = ws + WS_QKV + (size_t)((2 * rb + wm) * 1728 + 64 * ln) * 128;
    SCHED();
    bar();
    STAMP(1);
    rd(IC(0), IC(0));
    sfor<9>([&](auto C) {
      constexpr int c = C;
      SCHED();
      rd(IC(1), C); mm(IC(0), IC(c == 0)); ilv();
      SCHED();
      if constexpr (c == 8) wait_lgkm();
      bar();
      SCHED();
      if constexpr (c < 8) { rd(IC(0), IC(c + 1)); mm(IC(1), IC(0)); ilv(); }
    });
    STAMP(2);
    const u32 stg = sb + L_STG + cw * 9216;
    const u32 sa = stg + l15 * 144 + 8 * g, ra = stg + r8 * 144 + 16 * c8, go = lane * 16;
    u32x4 t[4][2];
    sfor<6>([&](auto NT) {
      constexpr int nt = NT;
      if constexpr (nt < 4) sfor<4>([&](auto M) { acc[M][nt] = mfma16(xf[1][M], wf[1][nt], acc[M][nt]); });
      SCHED();
      if constexpr (nt > 0 && nt < 5) {
        sfor<4>([&](auto M) {
          const f32x4 v = acc[M][nt - 1];
          st64(sa + (nt - 1) * 2304 + 32 * M, u32x2{pk2(v[0], v[1]), pk2(v[2], v[3])});
        });
        t[nt - 1][0] = lds128(ra + (nt - 1) * 2304); t[nt - 1][1] = lds128(ra + (nt - 1) * 2304 + 1152);
      }
      if constexpr (nt > 1) { gst128(t[nt - 2][0], go, qo + (nt - 2) * 2048); gst128(t[nt - 2][1], go, qo + (nt - 2) * 2048 + 1024); }
      SCHED();
    });
      }
      STAMP(8);
    }
  }
  const int rt = 4 * xcd + (idx & 3), ct = idx >> 2;
  auto wo_load = [&](int r0, int r1) {
    const int r6 = lane / 10, c10 = lane - 10 * r6;
    const u32 vo = (6 * (wv & 3) + r6) * 1152 + 16 * (c10 > 8 ? 8 : c10);
    const char* wb = (const char*)(wo + 72 * ct);
#pragma unroll 4
    for (int i = r0; i < r1; i++) dma16n<60>(wb + i * 24 * 1152, vo, sb + L_W + (4 * i + (wv & 3)) * 960);
  };
  const u32x2 tag = lds64(sb + W_TAG);
  const int cw = wv & 3;
  u32 ypub = 0;
  if (wv >= (spare ? 4u : 8u)) {
    const int tid = threadIdx.x & 255;
    const u32 cb = sb + L_C + (wv >= 8 ? 0 : 2 * CS_SZ);
    __builtin_amdgcn_s_sleep(CVDELAY);
    asm volatile("s_waitcnt vmcnt(0)" : "+v"(fv)::"memory");
    if (__builtin_amdgcn_ballot_w64(fv.x != tag.x || fv.y != tag.y)) { if (wv < 8) await<4>(fb, fo, tag); else { __builtin_amdgcn_s_sleep(UBACK); await<UBACK>(fb, fo, tag); } }
    STAMP(2);
    sfor<2>([&](auto J) { dma16c(ws + WS_UR + (size_t)(chb + J) * 4096, tid * 16, cb + CS_U + cw * 1024 + J * CS_SZ); });
    const u32 dv = *(const __attribute__((address_space(4))) u32*)(ssmD + chb);
    f32x4 acc[8];
#pragma unroll
    for (int c = 0; c < 8; c++) { acc[c] = f32x4{0.f, 0.f, 0.f, 0.f}; asm volatile("" : "+v"(acc[c])); }
    if (!spare) build();
    STAMP(1);
    wait_vm<0>();
    prog_set(sb + W_XCH + 4 * wv, 1);
    const int tm = cw >> 1, pi = cw & 1;
    const u32 cs = cb + tm * CS_SZ;
    prog_wait(sb + W_XCH + 4 * (wv & 12) + 4 * (lane & 3), 1);
    STAMP(4);
    conv_mfma<4>(cs, 4 * pi, lane, acc);
    STAMP(5);
    __builtin_amdgcn_s_setprio(CPRIO);
    if (spare && wv >= 8) prog_wait(sb + W_XCH + 16 + 8 * tm + 4 * (lane & 1), 2);
    const u32 rA = (spare && wv >= 8 ? sb + L_C + (2 + tm) * CS_SZ : sb + L_R + tm * 8192) + lane * 16;
    if (pi) { sfor<4>([&](auto J) { st128(rA + 4096 + J * 1024, __builtin_bit_cast(u32x4, acc[J])); acc[J] = acc[4 + J]; }); }
    else sfor<4>([&](auto J) { st128(rA + J * 1024, __builtin_bit_cast(u32x4, acc[4 + J])); });
    u32x2 uu[4];
#pragma unroll
    for (int j = 0; j < 4; j++) uu[j] = lds64(cs + CS_U + 4088 - 8 * lane - 512 * (4 * pi + j));
    prog_set(sb + W_XCH + 4 * wv, 2);
    prog_wait(sb + W_XCH + 4 * (wv ^ 1), 2);
    f32x4 t[4];
#pragma unroll
    for (int j = 0; j < 4; j++) t[j] = __builtin_bit_cast(f32x4, lds128(rA + (pi ^ 1) * 4096 + j * 1024));
    wait_lgkm();
    prog_set(sb + W_XCH + 4 * wv, 3);
    const u32 yo = ((lane >> 3) & 3) * 73728 + (lane >> 5) * 64 + (lane & 7) * 8;
    char* yd = ws + WS_Y2 + (chb + tm) * 128 + pi * 1179648;
#pragma unroll
    for (int j = 0; j < 4; j++) conv_out(vadd4(acc[j], t[j]), uu[j], tm ? hi2f(dv) : lo2f(dv), yo + j * 294912, yd);
    STAMP(8);
    ypub = F_Y + (((16 * pi + lane) * 8 + (chb + tm) / 72) * 80 + (chb + tm) % 72) * 8;
    pof = ypub; pon = lane < 16;
  }
  if (wv < 8) {
    __builtin_amdgcn_s_setprio(1);
    const int c = lane & 31, hh = lane >> 5, q4 = (lane & 15) >> 2;
    const int w = spare ? wv ^ 4 : wv;
    const u32 kcol = 64 * w + (lane & 16) * 2 + 16 * (lane & 1) + 4 * (lane & 2);
    const u32 ka = opaque(sb + L_K + (8 * hh + q4) * 1088 + kcol), ka2 = opaque(sb + L_K + (16 + q4) * 1088 + kcol);
    const u32 va = opaque(sb + L_V + c * 1040 + 64 * w + 16 * hh);
    const u32 qa = opaque(sb + L_Q + q4 * 512 + 64 * w + (lane & 16) * 2 + 8 * (lane & 3)), qa0 = opaque(qa + 4096 * hh);
    const int thr = c - 8 * hh;
    const u32 lom = opaque(hh ? 0u : ~0u);
    const float qsc = u2f(opaque(f2u(QSCALE)));
    u32 msk[8], nmk[8];
    unsigned long long cnd[16];
#pragma unroll
    for (int e = 0; e < 16; e++) cnd[e] = __builtin_amdgcn_ballot_w64(16 * (e >> 3) + (e & 7) <= thr);
#pragma unroll
    for (int j = 0; j < 8; j++) { const int k0 = 16 * (j >> 2) + 2 * (j & 3); msk[j] = opaque(k0 + 1 <= thr ? 0xffffffffu : (k0 <= thr ? 0xffffu : 0u)); nmk[j] = opaque(~msk[j]); }
    f32x16 S0;
#pragma unroll
    for (int e = 0; e < 16; e++) S0[e] = u2f(opaque(spare ? 0xff800000u : 0u));
    const u32 oo = opaque((c >> 4) * 18432 + (c & 15) * 16 + hh * 256);
    const u32 fl = sb + W_PROG + 4 * (spare ? (lane & 3) : (lane & 7));
    if (wv < 3) st128(sb + (wv < 2 ? L_K + (18 + wv) * 1088 : L_V + 18 * 1040) + lane * 16, u32x4{0x3f803f80u, 0x3f803f80u, 0x3f803f80u, 0x3f803f80u});
    const char* Ag = ws + WS_QKV + (size_t)(4 * qb * 1728 + 18 * h) * 128;
    const char* Kg = Ag + 576 * 128 - (qb ? 884736 : 0);
    const u32 lo = opaque(((lane >> 3) & (qb ? 7 : 3)) * 221184 + (lane & 7) * 16), lq = opaque(((lane >> 3) & 3) * 221184 + (lane >> 5) * 128 + (lane & 7) * 16);
    const char *pq = Ag + wv * 256, *pk = Kg + wv * 128, *pe = wv < 4 ? Kg + (wv < 2 ? 16 + wv : 590 + wv) * 128 : Ag + 2048;
    u32 le = sb + (wv < 2 ? L_K + (16 + wv) * 1088 : (wv < 4 ? L_V + (14 + wv) * 1040 : L_Q + 8192)), lk = sb + L_K + wv * 1088, lv = sb + L_V + wv * 1040;
    const u32 lx = opaque(wv < 4 ? lo : lq);
    asm volatile("" : "+s"(pq), "+s"(pk), "+s"(pe), "+s"(le), "+s"(lk), "+s"(lv));
    SCHED();
    if (wv >= 4) publish(fb, pof, tag, pon);
    STAMP(3);
    if (wv == (spare ? 0u : (u32)POLLW)) {
      if (spare) { prog_wait(sb + W_XCH + 32, 1); __builtin_amdgcn_s_sleep(SQD); }
      __builtin_amdgcn_s_sleep(QDELAY);
      await<POLL_FAST>(fb, F_1 + ((h * 8 + qb) * 64 + ((lane < 48 ? lane : lane - 16) | (lane < 16 || spare ? 4 : 0))) * 8, tag);
      prog_set(sb + W_GO, 1);
    } else prog_wait(sb + W_GO, 1);
    STAMP(4);
    __builtin_amdgcn_s_setprio(BAL ? 2 : 1);
    if (!spare || wv < 4) {
    dma16c(pk, lo, lk); dma16c(pk + 1024, lo, lk + 8704); dma16c(pq, lq, sb + L_Q + wv * 1024); dma16c(pe, lx, le);
    if (spare) {
      dma16c(pk + 512, lo, lk + 4352); dma16c(pk + 1536, lo, lk + 13056); dma16c(pq + 1024, lq, sb + L_Q + 4096 + wv * 1024); dma16c(Ag + 2048, lq, sb + L_Q + 8192);
      dma16c(pk + 580 * 128, lo, lv + 4160); dma16c(pk + 588 * 128, lo, lv + 12480);
    }
    __builtin_amdgcn_s_sleep(VDELAY);
    dma16c(pk + 576 * 128, lo, lv); dma16c(pk + 584 * 128, lo, lv + 8320);
    STAMP(7);
    wait_vm<2>();
    prog_set(sb + W_PROG + 4 * wv, 2);
    }
    prog_wait(fl, 2);
    STAMP(5);
    struct KF { u32x4 a; u32x2 b; };
    struct VF { u32x4 a, b; };
    auto ldk = [&](int i) { KF k; k.a = cat(tr_read(ka + i * 64), tr_read(ka + 4352 + i * 64)); k.b = tr_read(ka2 + i * 64); return k; };
    auto ldv = [&](int i) { VF v; v.a = lds128(va + i * 64); v.b = lds128(va + i * 64 + 32); return v; };
    auto pk8 = [&](const f32x16& p, int o) { return u32x4{pk2(p[o], p[o + 1]), pk2(p[o + 2], p[o + 3]), pk2(p[o + 4], p[o + 5]), pk2(p[o + 6], p[o + 7])}; };
    const int ib = spare ? 8 - w : 1;
    u32x4 q0 = cat(tr_read(qa0), tr_read(qa0 + 2048));
    const KF k8 = ldk(8);
    u32x2 q1 = tr_read(qa + 8192);
    const KF k0 = ldk(0);
    SCHED();
    auto qs = [&](u32 v) { return pk2(vmul(lo2f(v), qsc), vmul(hi2f(v), qsc)); };
    q0 = u32x4{qs(q0.x), qs(q0.y), qs(q0.z), qs(q0.w)};
    q1.x = qs(q1.x) & lom; q1.y = 0u;
    auto qk = [&](const KF& k) { f32x16 z = {}; z = mfma32(k.a, q0, z); return mfma32k8(k.b, q1, z); };
    f32x16 Sm = {};
    Sm = mfma32(k8.a, q0, Sm); S0 = mfma32(k0.a, q0, S0);
    Sm = mfma32k8(k8.b, q1, Sm); S0 = mfma32k8(k0.b, q1, S0);
    KF kf[2]; VF vf[2], v8, v0;
    kf[1] = ldk(7); kf[0] = ldk(6);
    SCHED();
#pragma unroll
    for (int e = 0; e < 16; e++) Sm[e] = __builtin_amdgcn_inverse_ballot_w64(cnd[e]) ? Sm[e] : S0[e];
    float m = __builtin_fmaxf(__builtin_fmaxf(Sm[0], Sm[1]), Sm[2]), m2 = __builtin_fmaxf(__builtin_fmaxf(Sm[3], Sm[4]), Sm[5]);
#pragma unroll
    for (int e = 6; e < 14; e += 4) { m = fmaxf(fmaxf(m, Sm[e]), Sm[e + 1]); m2 = fmaxf(fmaxf(m2, Sm[e + 2]), Sm[e + 3]); }
    m = fmaxf(fmaxf(m, Sm[14]), Sm[15]);
    m = xhalf_max(fmaxf(m, m2));
    f32x16 O;
    float l;
#pragma unroll 1
    for (int pass = 0; pass < 2; pass++) {
      { const float r1 = lo2f(pk2(m, m)) - m; q1.y = hh ? pk2(r1 - lo2f(pk2(r1, r1)), 0.f) : pk2(-m, r1); }
      f32x16 S[2];
      {
        f32x16 p = mfma32k8(kf[1].b, u32x2{0u, q1.y}, Sm);
        S[1] = qk(kf[1]);
        SCHED();
        wait_vm<0>();
        prog_set(sb + W_PROG + 4 * wv, 3);
#pragma unroll
        for (int e = 0; e < 8; e++) p[e] = __builtin_amdgcn_exp2f(p[e]);
        SCHED();
        const u32 vfl = lds_peek(fl);
        SCHED();
#pragma unroll
        for (int e = 8; e < 16; e++) p[e] = __builtin_amdgcn_exp2f(p[e]);
        SCHED();
        if (__builtin_amdgcn_ballot_w64(vfl < 3u)) prog_wait(fl, 3);
        asm volatile("" ::: "memory");
        v8 = ldv(8); v0 = ldv(0); vf[1] = ldv(7);
        SCHED();
        const u32x4 pa = pk8(p, 0), pb = pk8(p, 8);
        O = f32x16{};
        O = mfma32(v8.a, pa & u32x4{msk[0], msk[1], msk[2], msk[3]}, O); O = mfma32(v8.b, pb & u32x4{msk[4], msk[5], msk[6], msk[7]}, O);
        O = mfma32(v0.a, pa & u32x4{nmk[0], nmk[1], nmk[2], nmk[3]}, O); O = mfma32(v0.b, pb & u32x4{nmk[4], nmk[5], nmk[6], nmk[7]}, O);
      }
      auto tile = [&](auto self, auto I) -> void {
        constexpr int i = I;
        if (i < ib) return;
        if constexpr (i == BAL1) __builtin_amdgcn_s_setprio(1);
        if constexpr (i == BAL0) __builtin_amdgcn_s_setprio(0);
        SCHED();
        f32x16& Sc = S[i & 1];
        f32x16 z = {};
        if constexpr (i > 1) z = mfma32(kf[(i - 1) & 1].a, q0, z);
        if constexpr (i > 2) kf[i & 1] = ldk(i - 2);
        if constexpr (i > 1) vf[(i - 1) & 1] = ldv(i - 1);
        SCHED();
#pragma unroll
        for (int e = 0; e < 8; e++) Sc[e] = __builtin_amdgcn_exp2f(Sc[e]);
        SCHED();
        if constexpr (i > 1) S[(i - 1) & 1] = mfma32k8(kf[(i - 1) & 1].b, q1, z);
        const u32x4 pa = pk8(Sc, 0);
        SCHED();
#pragma unroll
        for (int e = 8; e < 12; e++) Sc[e] = __builtin_amdgcn_exp2f(Sc[e]);
        SCHED();
        O = mfma32(vf[i & 1].a, pa, O);
#pragma unroll
        for (int e = 12; e < 16; e++) Sc[e] = __builtin_amdgcn_exp2f(Sc[e]);
        const u32x4 pb = pk8(Sc, 8);
        SCHED();
        O = mfma32(vf[i & 1].b, pb, O);
        if constexpr (i > 1) self(self, IC(i - 1));
      };
      tile(tile, IC(7));
      SCHED();
      { const u32 lu = f2u(O[10]); auto r = __builtin_amdgcn_permlane32_swap(lu, lu, false, false); l = u2f(r[0]); }
      if (pass == 1 || __all(l > 8.67e-19f && l < 1.15e18f)) break;
      q1.y = 0u;
#pragma unroll 1
      for (int i = ib; i < 8; i++) {
        const f32x16 s = qk(ldk(i));
#pragma unroll
        for (int e = 0; e < 16; e += 2) m = fmaxf(fmaxf(m, s[e]), s[e + 1]);
      }
      m = xhalf_max(m);
      kf[1] = ldk(7); kf[0] = ldk(6);
    }
    STAMP(6);
    float inv = __builtin_amdgcn_rcpf(l);
    asm volatile("s_nop 1" : "+v"(inv));
    char* Og = ws + WS_O + (size_t)(16 * qb + 2 * w) * 18432;
    gst128(u32x4{pk2(vmul(O[0], inv), vmul(O[1], inv)), pk2(vmul(O[2], inv), vmul(O[3], inv)), pk2(vmul(O[4], inv), vmul(O[5], inv)), pk2(vmul(O[6], inv), vmul(O[7], inv))},
           oo, Og + (h >> 1) * 1024 + (h & 1) * 512);
    if (hh == 0) gst32(pk2(vmul(O[8], inv), vmul(O[9], inv)), (c >> 4) * 18432 + (c & 15) * 4, Og + 16384 + h * 64);
    publish(fb, F_O + (((4 * qb + (w & 3)) * 8 + lane) * 64 + (w >> 2) * 32 + h) * 8, tag, lane < 8);
    {
      __builtin_amdgcn_s_setprio(0);
      const int g = lane >> 4;
      prog_wait(sb + W_XCH + 32 + 4 * (lane & 3), 4);
      const u32 wa2 = sb + L_W + ((4 * g + (q4 >> 1)) * 18 + 16 + (q4 & 1)) * 160 + 8 * (lane & 3);
      u32x4 wx[2], wr[10];
      sfor<2>([&](auto J) { const int i = J ? 8 + (wv & 1) : wv; const u32 a = wa2 + (i / 5) * 46080 + (i % 5) * 32; wx[J] = cat(tr_read(a), tr_read(a + 5760)); });
      prog_set(sb + W_PROG + 4 * wv, 4);
      const u32 wa = sb + L_W + wv * 11520 + (18 * (g >> 1) + 4 * (g & 1) + q4) * 160 + 8 * (lane & 3), wd = sb + L_W + wv * 11520 + lane * 16;
      sfor<10>([&](auto I) { const u32 a = wa + (I / 5) * 5760 + (I % 5) * 32; wr[I] = cat(tr_read(a), tr_read(a + 1280)); });
      prog_wait(sb + W_PROG + 4 * (lane & 7), 4);
      sfor<2>([&](auto J) { const int i = J ? 8 + (wv & 1) : wv; st128(sb + L_Q + i * 1024 + lane * 16, wx[J]); });
      sfor<10>([&](auto I) { st128(wd + (I / 5) * 5760 + (I % 5) * 1024, wr[I]); });
      prog_set(sb + W_PROG + 4 * wv, 5);
    }
      if (b == 255 && wv == 4) {
#pragma unroll 1
        for (int spin = 0; spin < (1 << 18); spin++) {
          u32x4 v[9];
          sfor<9>([&](auto I) { gl128c<0>(v[I], (lane >> 4) * 2048 + (lane & 15) * 16, fb + F_U + I * 8192); });
          asm volatile("s_waitcnt vmcnt(0)" : "+v"(v[0]), "+v"(v[1]), "+v"(v[2]), "+v"(v[3]), "+v"(v[4]), "+v"(v[5]), "+v"(v[6]), "+v"(v[7]), "+v"(v[8])::"memory");
          u32 bad = 0;
#pragma unroll
          for (int i = 0; i < 9; i++) bad |= (v[i].x ^ tag.x) | (v[i].y ^ tag.y) | (v[i].z ^ tag.x) | (v[i].w ^ tag.y);
          if (!__builtin_amdgcn_ballot_w64(bad != 0)) break;
          __builtin_amdgcn_s_sleep(16);
        }
        if (lane == 0) gst64(tag, F_EP, fb);
      }
    STAMP_END();
    return;
  }
  {
    const int g = lane >> 4, l15 = lane & 15, q = l15 >> 2;
    const int r7 = lane / 9, c9 = lane - 9 * r7;
    const u32 xw = sb + L_X + cw * 2304;
    const int m16 = 16 * (rt >> 2) + 2 * (rt & 3) + 8 * (cw >> 1) + (cw & 1);
    if (__builtin_amdgcn_ballot_w64(lds_peek(sb + W_PROG + 4 * (spare ? (lane & 3) : (lane & 7))) < (u32)WOGATE)) { publish(fb, ypub, tag, lane < 16); ypub = 0; }
    prog_wait(sb + W_XCH + (spare ? 16 + 4 * (lane & 7) : 32 + 4 * (lane & 3)), 3);
    prog_wait(sb + W_PROG + 4 * (spare ? (lane & 3) : (lane & 7)), WOGATE);
    STAMP(9);
    {
      const u16* xp = x + (size_t)(16 * m16) * 576 + 72 * ct;
      dma16n<63>(xp, r7 * 1152 + 16 * c9, xw); dma16n<63>(xp + 7 * 576, r7 * 1152 + 16 * c9, xw + 1008); dma16n<63>(xp + 9 * 576, r7 * 1152 + 16 * c9, xw + 1296);
      wo_load(0, 4);
      wait_vm<7>();
      if (ypub && lane < 16) gst64(tag, ypub, fb);
      wo_load(4, YPOS);
    }
    const u32 yf = F_Y + ((rt * 8 + ct) * 80 + (lane < 18 ? 18 * cw + lane : 72 + (cw >> 1))) * 8;
    u32x2 yv;
    asm volatile("global_load_dwordx2 %0, %1, %2 sc1" : "=v"(yv) : "v"(yf), "s"(fb));
    wo_load(YPOS, 24);
    STAMP(10);
    asm volatile("s_waitcnt vmcnt(%1)" : "+v"(yv) : "i"(24 - YPOS) : "memory");
    if (__builtin_amdgcn_ballot_w64(yv.x != tag.x || yv.y != tag.y)) await<POLL_SLEEP>(fb, yf, tag);
    STAMP(11);
    {
      const char* yp = ws + WS_Y2 + (size_t)(rt * 576 + 72 * ct + 18 * cw) * 128;
      dma16c(yp, lane * 16, sb + L_Y + cw * 2304); dma16c(yp + 1024, lane * 16, sb + L_Y + cw * 2304 + 1024); dma16c(yp + 1280, lane * 16, sb + L_Y + cw * 2304 + 1280);
    }
    u32 gtu;
    gl32c<0>(gtu, (16 * m16 + l15) * 4, ws + WS_G);
    wait_vm<4>();
    prog_set(sb + W_XCH + 4 * wv, 4);
    asm volatile("s_waitcnt vmcnt(0)" : "+v"(gtu)::"memory");
    prog_set(sb + W_XCH + 4 * wv, 5);
    prog_wait(sb + W_XCH + 32 + 4 * (lane & 3), 5);
    const float gt = u2f(gtu);
    f32x4 acc[5]; u32x2 tb[5];
    const u32 xa = xw + l15 * 144 + 8 * g;
    {
      const float g1 = rbf(1.f - gt);
      const u32 ya = sb + L_Y + (4 * g + q) * 128 + 32 * cw + 8 * (lane & 3);
      sfor<5>([&](auto NT) {
        const u32x2 y = tr_read(ya + NT * 2048), xv = lds64(xa + NT * 32);
        tb[NT] = u32x2{opaque(pk2(vmul(vmul(g1, lo2f(xv.x)), lo2f(y.x)), vmul(vmul(g1, hi2f(xv.x)), hi2f(y.x)))), opaque(pk2(vmul(vmul(g1, lo2f(xv.y)), lo2f(y.y)), vmul(vmul(g1, hi2f(xv.y)), hi2f(y.y))))};
      });
    }
    const u32 wl = opaque(sb + L_W + lane * 16), wh = opaque(wl + 46080), wq = opaque(sb + L_Q + lane * 16);
    u32x4 wf[3][5];
    auto rd = [&](auto KS) {
      sfor<5>([&](auto NT) {
        constexpr int ks = KS;
        if constexpr (ks < 16) wf[ks % 3][NT] = lds128((ks < 8 ? wl : wh) + (ks & 7) * 5760 + NT * 1024);
        else wf[ks % 3][NT] = lds128(wq + (5 * (ks - 16) + NT) * 1024);
      });
    };
    STAMP(6);
    u32x4 of[16]; u32 oe[8];
    const char* ob = ws + WS_O + (size_t)m16 * 18432;
    {
      const u32 oo = F_O + ((rt * 8 + ct) * 64 + (cw >> 1) * 32 + (lane & 31)) * 8;
      prog_wait(sb + W_PROG + (spare || OGATE == 2 ? 4 * (lane & 7) : 16 * (cw >> 1) + 4 * (lane & 3)), 4);
      __builtin_amdgcn_s_sleep(ODELAY);
      u32 miss = ~0u;
      sfor<4>([&](auto G) {
#pragma unroll 1
        for (int spin = 0; (miss & (0xffu << (8 * G))) && spin < (1 << 20); spin++) {
          u32x2 v;
          asm volatile("global_load_dwordx2 %0, %1, %2 sc1\n\ts_waitcnt vmcnt(0)" : "=&v"(v) : "v"(oo), "s"(fb) : "memory");
          miss = (u32)__builtin_amdgcn_ballot_w64(v.x != tag.x || v.y != tag.y);
          if (miss & (0xffu << (8 * G))) __builtin_amdgcn_s_sleep(POLL_FAST);
        }
        sfor<4>([&](auto F) { gl128c<F * 1024>(of[4 * G + F], lane * 16, ob + G * 4096); });
      });
    }
    STAMP(12);
    __builtin_amdgcn_s_setprio(3);
    sfor<8>([&](auto F) { gl32c<(F >> 2) * 1024 + (F & 3) * 64>(oe[F], 256 * g + 4 * l15, ob + 16384); });
    prog_wait(sb + W_PROG + 4 * (lane & 7), 5);
    rd(IC(0)); rd(IC(1));
    sfor<16>([&](auto KS) {
      constexpr int ks = KS;
      SCHED();
      if constexpr (ks % 2 == 0) asm volatile("s_waitcnt vmcnt(%2)" : "+v"(of[ks]), "+v"(of[ks + 1]) : "i"(22 - ks) : "memory");
      rd(IC(ks + 2));
      sfor<5>([&](auto NT) { acc[NT] = mfma16(wf[ks % 3][NT], of[ks], ks == 0 ? f32x4{0.f, 0.f, 0.f, 0.f} : acc[NT]); });
      sfor<5>([&](auto) { __builtin_amdgcn_sched_group_barrier(0x8, 1, 0); __builtin_amdgcn_sched_group_barrier(0x100, 1, 0); });
    });
    SCHED();
    asm volatile("s_waitcnt vmcnt(0)" : "+v"(oe[0]), "+v"(oe[1]), "+v"(oe[2]), "+v"(oe[3]), "+v"(oe[4]), "+v"(oe[5]), "+v"(oe[6]), "+v"(oe[7])::"memory");
    sfor<5>([&](auto NT) { acc[NT] = mfma16(wf[1][NT], u32x4{oe[0], oe[1], oe[2], oe[3]}, acc[NT]); });
    sfor<5>([&](auto NT) { acc[NT] = mfma16(wf[2][NT], u32x4{oe[4], oe[5], oe[6], oe[7]}, acc[NT]); });
    SCHED();
    STAMP(13);
    sfor<5>([&](auto NT) {
      constexpr int nt = NT;
      if (nt < 4 || g < 2)
        st64(xa + nt * 32, u32x2{pk2(fmaf(gt, acc[nt][0], lo2f(tb[nt].x)), fmaf(gt, acc[nt][1], hi2f(tb[nt].x))), pk2(fmaf(gt, acc[nt][2], lo2f(tb[nt].y)), fmaf(gt, acc[nt][3], hi2f(tb[nt].y)))});
    });
    char* orow = (char*)(out + (size_t)(16 * m16) * 576 + 72 * ct);
    u32x4 t[3];
#pragma unroll
    for (int it = 0; it < 3; it++) t[it] = lds128(xw + it * 1008 + lane * 16);
#pragma unroll
    for (int it = 0; it < 3; it++)
      if (lane < (it < 2 ? 63 : 18)) asm volatile("global_store_dwordx4 %0, %1, %2 " OUTPOL "\n\ts_nop 1" ::"v"((7 * it + r7) * 1152 + 16 * c9), "v"(t[it]), "s"(orow) : "memory");
  }
  STAMP_END();
}
extern "C" __global__ void gm_end() {}

extern "C" void kernel_launch(void* const* d_in, const int* in_sizes, int n_in, void* d_out, int out_size, void* d_ws, size_t ws_size,
                              hipStream_t stream) {
  (void)hipFuncSetAttribute((const void*)gm_main, hipFuncAttributeMaxDynamicSharedMemorySize, L_END);
  gm_main<<<256, 768, L_END, stream>>>((const u16*)d_in[0], (const u16*)d_in[1], (char*)d_ws, (const u16*)d_in[4], (const u16*)d_in[5], (const u16*)d_in[6],
                                       (const u16*)d_in[7], (const u16*)d_in[2], (const u16*)d_in[8], (const u16*)d_in[9], (u16*)d_out);
}
```

```cpp
#include <hip/hip_runtime.h>
#include <hip/hip_bf16.h>
#include <utility>

typedef unsigned short u16;
typedef unsigned int u32;
typedef u32 u32x2 __attribute__((ext_vector_type(2)));
typedef u32 u32x4 __attribute__((ext_vector_type(4)));
typedef short s16x4 __attribute__((ext_vector_type(4)));
typedef __bf16 bf16x8 __attribute__((ext_vector_type(8)));
typedef __bf16 bf16x2 __attribute__((ext_vector_type(2)));
typedef float f32x2 __attribute__((ext_vector_type(2)));
typedef float f32x4 __attribute__((ext_vector_type(4)));
typedef float f32x16 __attribute__((ext_vector_type(16)));
#define LAS __attribute__((address_space(3)))
#define DEVI __device__ __forceinline__
#define IC(n) std::integral_constant<int, (n)>{}

constexpr size_t WS_QKV = 0;
constexpr size_t WS_O   = WS_QKV + 96ull * 2048 * 40;
constexpr size_t WS_UR  = WS_O + 32ull * 2048 * 40;
constexpr size_t WS_Y2  = WS_UR + 576ull * 4096;
constexpr size_t WS_G   = WS_Y2 + 576ull * 4096;
constexpr size_t WS_F   = WS_G + 8192;
constexpr u32 F_EP = 0, F_1 = 1024, F_U = F_1 + 256 * 512, F_O = F_U + 288 * 256, F_Y = F_O + 256 * 512;

template <typename F, int... I> DEVI void sfor_impl(F&& f, std::integer_sequence<int, I...>) { (f(std::integral_constant<int, I>{}), ...); }
template <int N, typename F> DEVI void sfor(F&& f) { sfor_impl(f, std::make_integer_sequence<int, N>{}); }

DEVI u32 pk2(float a, float b) { f32x2 v = {a, b}; return __builtin_bit_cast(u32, __builtin_convertvector(v, bf16x2)); }
DEVI u32 f2u(float a) { return __builtin_bit_cast(u32, a); }
DEVI float u2f(u32 a) { return __builtin_bit_cast(float, a); }
DEVI float lo2f(u32 w) { return __builtin_bit_cast(float, w << 16); }
DEVI float hi2f(u32 w) { return __builtin_bit_cast(float, w & 0xffff0000u); }
DEVI float rbf(float a) { return lo2f(pk2(a, a)); }
DEVI f32x4 mfma16(u32x4 a, u32x4 b, f32x4 c) { return __builtin_amdgcn_mfma_f32_16x16x32_bf16(__builtin_bit_cast(bf16x8, a), __builtin_bit_cast(bf16x8, b), c, 0, 0, 0); }
DEVI f32x16 mfma32(u32x4 a, u32x4 b, f32x16 c) { return __builtin_amdgcn_mfma_f32_32x32x16_bf16(__builtin_bit_cast(bf16x8, a), __builtin_bit_cast(bf16x8, b), c, 0, 0, 0); }
DEVI f32x16 mfma32k8(u32x2 a, u32x2 b, f32x16 c) { return __builtin_amdgcn_mfma_f32_32x32x8bf16_1k(__builtin_bit_cast(s16x4, a), __builtin_bit_cast(s16x4, b), c, 0, 0, 0); }
DEVI u32x2 tr_read(u32 addr) { return __builtin_bit_cast(u32x2, __builtin_amdgcn_ds_read_tr16_b64_v4i16((LAS s16x4*)addr)); }
DEVI u32x4 lds128(u32 addr) { return *(LAS u32x4*)addr; }
DEVI u32x2 lds64(u32 addr) { return *(LAS u32x2*)addr; }
DEVI u32 lds32(u32 addr) { return *(LAS u32*)addr; }
DEVI void st128(u32 addr, u32x4 v) { *(LAS u32x4*)addr = v; }
DEVI void st64(u32 addr, u32x2 v) { *(LAS u32x2*)addr = v; }
DEVI void st32(u32 addr, u32 v) { *(LAS u32*)addr = v; }
DEVI u32x4 cat(u32x2 a, u32x2 b) { return u32x4{a.x, a.y, b.x, b.y}; }
DEVI float vfma(float a, float b, float c) { float d; asm("v_fma_f32 %0, %1, %2, %3" : "=v"(d) : "v"(a), "v"(b), "v"(c)); return d; }
DEVI float vmul(float a, float b) { float d; asm("v_mul_f32 %0, %1, %2" : "=v"(d) : "v"(a), "v"(b)); return d; }
DEVI float vadd(float a, float b) { float d; asm("v_add_f32 %0, %1, %2" : "=v"(d) : "v"(a), "v"(b)); return d; }
DEVI f32x4 vadd4(f32x4 a, f32x4 b) { return f32x4{vadd(a[0], b[0]), vadd(a[1], b[1]), vadd(a[2], b[2]), vadd(a[3], b[3])}; }
DEVI u32 opaque(u32 v) { asm volatile("" : "+v"(v)); return v; }
DEVI void dma16(const void* sbase, u32 voff, u32 ldsbase) {
  asm volatile("s_mov_b32 m0, %2\n\ts_nop 0\n\tglobal_load_lds_dwordx4 %0, %1" ::"v"(voff), "s"(sbase), "s"(ldsbase) : "memory");
}
template <int NL> DEVI void dma16n(const void* sbase, u32 voff, u32 ldsbase) {
  asm volatile("s_mov_b32 m0, %2\n\ts_mov_b32 exec_hi, %3\n\tglobal_load_lds_dwordx4 %0, %1\n\ts_mov_b32 exec_hi, -1" ::"v"(voff), "s"(sbase), "s"(ldsbase),
               "i"((1u << (NL - 32)) - 1u) : "memory");
}
template <int OFF> DEVI void gl128(u32x4& d, u32 voff, const void* sbase) { asm volatile("global_load_dwordx4 %0, %1, %2 offset:%3" : "=v"(d) : "v"(voff), "s"(sbase), "i"(OFF)); }
template <int OFF> DEVI void gl64(u32x2& d, u32 voff, const void* sbase) { asm volatile("global_load_dwordx2 %0, %1, %2 offset:%3" : "=v"(d) : "v"(voff), "s"(sbase), "i"(OFF)); }
template <int OFF> DEVI void gl128c(u32x4& d, u32 voff, const void* sbase) { asm volatile("global_load_dwordx4 %0, %1, %2 offset:%3 sc1" : "=v"(d) : "v"(voff), "s"(sbase), "i"(OFF)); }
template <int OFF> DEVI void gl32c(u32& d, u32 voff, const void* sbase) { asm volatile("global_load_dword %0, %1, %2 offset:%3 sc1" : "=v"(d) : "v"(voff), "s"(sbase), "i"(OFF)); }
DEVI void gst128(u32x4 v, u32 voff, void* sbase) { asm volatile("global_store_dwordx4 %0, %1, %2 sc1\n\ts_nop 1" ::"v"(voff), "v"(v), "s"(sbase) : "memory"); }
DEVI void gst64(u32x2 v, u32 voff, void* sbase) { asm volatile("global_store_dwordx2 %0, %1, %2 sc1" ::"v"(voff), "v"(v), "s"(sbase) : "memory"); }
DEVI void gst32(u32 v, u32 voff, void* sbase) { asm volatile("global_store_dword %0, %1, %2 sc1" ::"v"(voff), "v"(v), "s"(sbase) : "memory"); }
#define CODE_PF(from, to, ck, step, lds)                                                                                              \
  do {                                                                                                                                \
    u32 lo_, hi_, len_;                                                                                                               \
    asm volatile("s_getpc_b64 s[20:21]\n\ts_add_u32 %0, s20, " from "@rel32@lo+4\n\ts_addc_u32 %1, s21, " from "@rel32@hi+12\n\ts_mov_b32 %2, " to "-" from \
                 : "=s"(lo_), "=s"(hi_), "=s"(len_)::"s20", "s21", "scc");                                                            \
    const char* code_ = (const char*)(((unsigned long long)hi_ << 32) | lo_);                                                         \
    for (u32 o_ = (ck) * 1024; o_ < len_; o_ += (step) * 1024) { const u32 a_ = o_ + (threadIdx.x & 63) * 16; dma16(code_, a_ < len_ ? a_ : 0u, lds); } \
  } while (0)
template <int N> DEVI void wait_vm() { asm volatile("s_waitcnt vmcnt(%0)" ::"i"(N > 63 ? 63 : N) : "memory"); }
DEVI void wait_lgkm() { asm volatile("s_waitcnt lgkmcnt(0)" ::: "memory"); }
DEVI void bar() { asm volatile("s_barrier" ::: "memory"); }
DEVI u32 rfl(u32 v) { return __builtin_amdgcn_readfirstlane(v); }
#define SCHED() __builtin_amdgcn_sched_barrier(0)

constexpr float QSCALE = 0.23570226039551584f * 1.4426950408889634f;


constexpr int L_SLOT = 32768, L_WOFF = 16384, L_STG = 94208, L_GW = 131072, L_PT = 133120, L_TILE = 17408;
constexpr int L_K = 0, L_V = 21760, L_C = 41536, L_R = L_PT + 1024, L_FL = 152320, L_DUMMY = L_FL + 256, L_Q = 153600, L_END = 163840;
constexpr int KC_STR = 4640, CS_U = 4 * KC_STR, CS_SZ = CS_U + 4096 + 128;
constexpr int L_W = L_C, L_X = L_W + 576 * 160, L_Y = L_X + 9216;
static_assert(L_Y + 9216 <= L_FL && L_C + 2 * CS_SZ <= L_STG && L_C + 4 * CS_SZ <= L_PT && L_PT + L_TILE <= L_FL, "lds map");
constexpr int W_PROG = L_FL, W_XCH = L_FL + 64, W_GO = L_FL + 128, W_TAG = L_FL + 144;

DEVI u32 lds_peek(u32 a) { return __hip_atomic_load((LAS u32*)a, __ATOMIC_RELAXED, __HIP_MEMORY_SCOPE_WORKGROUP); }
DEVI void prog_set(u32 a, u32 n) { asm volatile("" ::: "memory"); __hip_atomic_store((LAS u32*)a, n, __ATOMIC_RELAXED, __HIP_MEMORY_SCOPE_WORKGROUP); }
DEVI void prog_wait(u32 a, u32 n) { while (__builtin_amdgcn_ballot_w64(lds_peek(a) < n)) __builtin_amdgcn_s_sleep(1); asm volatile("" ::: "memory"); }
DEVI void publish(char* fb, u32 off, u32x2 tag, bool on) { wait_vm<0>(); if (on) asm volatile("global_store_dwordx2 %0, %1, %2 sc1" ::"v"(off), "v"(tag), "s"(fb) : "memory"); }
template <int SLP> DEVI void await(const char* fb, u32 off, u32x2 tag) {
#pragma unroll 1
  for (int spin = 0; spin < (1 << 20); spin++) {
    u32x2 v;
    asm volatile("global_load_dwordx2 %0, %1, %2 sc1\n\ts_waitcnt vmcnt(0)" : "=&v"(v) : "v"(off), "s"(fb) : "memory");
    if (!__builtin_amdgcn_ballot_w64(v.x != tag.x || v.y != tag.y)) break;
    __builtin_amdgcn_s_sleep(SLP);
  }
}

DEVI void prep_issue(const u16* Bk, const u16* Bv, int row0, int col, u32 tb, u32 pw, int lane) {
  const int r8 = lane >> 3, c8 = lane & 7;
  sfor<2>([&](auto I) {
    const int j = 2 * pw + I;
    int row = row0 + 8 * j + r8 - 3; row = row < 0 ? 0 : row;
    dma16(Bk + col, row * 1152 + 16 * (c8 ^ j), tb + 1024 * j);
    dma16(Bv + col, (row0 + 8 * j + r8) * 1152 + 16 * (c8 ^ j), tb + 9216 + 1024 * j);
  });
  dma16(Bk + col, (row0 + 61 + (r8 > 2 ? 2 : r8)) * 1152 + 16 * c8, tb + 8192);
}
template <typename F> DEVI void prep_compute(int row0, int col, u32 tb, u32 pw, int lane, u32x4 sh, char* ws, F&& mid) {
  const int c8 = lane & 7, cp = 8 * pw + (lane >> 3);
  const u32 o0 = tb + 1024 * c8 + 16 * ((cp >> 2) ^ c8) + 4 * (cp & 3), o1 = tb + 1024 * c8 + 16 * ((cp >> 2) ^ ((c8 + 1) & 7)) + 4 * (cp & 3);
  row0 += 8 * c8;
  u32 kk[11], vv[8];
#pragma unroll
  for (int i = 0; i < 11; i++) kk[i] = lds32((i < 8 ? o0 : o1) + 128 * i);
#pragma unroll
  for (int i = 0; i < 8; i++) vv[i] = lds32(o0 + 9216 + 128 * i);
  if (row0 == 0) { kk[0] = 0; kk[1] = 0; kk[2] = 0; }
  const u32 go = 2 * cp * 4096 + 2 * (2040 - row0);
  char* dst = ws + WS_UR + (size_t)col * 4096;
  float r[8];
  {
    const float a0 = lo2f(sh.x), a1 = hi2f(sh.x), a2 = lo2f(sh.y), a3 = hi2f(sh.y);
#pragma unroll
    for (int i = 0; i < 8; i++) r[i] = vmul(vfma(lo2f(kk[i + 3]), a0, vfma(lo2f(kk[i + 2]), a1, vfma(lo2f(kk[i + 1]), a2, vmul(lo2f(kk[i]), a3)))), lo2f(vv[i]));
    gst128(u32x4{pk2(r[7], r[6]), pk2(r[5], r[4]), pk2(r[3], r[2]), pk2(r[1], r[0])}, go, dst);
  }
  mid();
  {
    const float a0 = lo2f(sh.z), a1 = hi2f(sh.z), a2 = lo2f(sh.w), a3 = hi2f(sh.w);
#pragma unroll
    for (int i = 0; i < 8; i++) r[i] = vmul(vfma(hi2f(kk[i + 3]), a0, vfma(hi2f(kk[i + 2]), a1, vfma(hi2f(kk[i + 1]), a2, vmul(hi2f(kk[i]), a3)))), hi2f(vv[i]));
    gst128(u32x4{pk2(r[7], r[6]), pk2(r[5], r[4]), pk2(r[3], r[2]), pk2(r[1], r[0])}, go, dst + 4096);
  }
}

template <int NR> DEVI void conv_mfma(u32 cs, int rho0, int lane, f32x4 (&acc)[8]) {
  const int g = lane >> 4, l15 = lane & 15;
  u32 aa = cs + (l15 & 3) * KC_STR + 2 * (64 * (l15 >> 2) + 8 * g) + 64 * rho0;
  u32 ba = opaque(cs + CS_U + 2 * (1852 - 1792 - 4 * l15 + 8 * g) + 64 * rho0), bb = opaque(ba + 8);
  u32x4 A[8], G[8];
  sfor<5>([&](auto J) { constexpr int p = J < 4 ? J + 2 : 1; A[p] = lds128(aa + p * 512); G[p] = cat(lds64(ba + (7 - p) * 512), lds64(bb + (7 - p) * 512)); SCHED(); });
#pragma unroll 1
  for (int rho = 0; rho < NR; rho++) {
    constexpr int pa[36] = {2, 2, 3, 3, 2, 4, 3, 4, 2, 5, 1, 1, 2, 1, 3, 1, 4, 1, 5, 1, 6, 2, 0, 3, 0, 4, 0, 5, 0, 1, 0, 6, 0, 7, 0, 0};
    constexpr int pe[36] = {2, 3, 2, 3, 4, 2, 4, 3, 5, 2, 1, 2, 1, 3, 1, 4, 1, 5, 1, 6, 1, 0, 2, 0, 3, 0, 4, 0, 5, 0, 1, 0, 6, 0, 7, 0};
    constexpr int rdc[36] = {18, 19, 20, 21, 22, 23, 0, 1, 2, -1, -1, -1, -1, -1, -1, -1, -1, -1, -1, -1, -1, 6, 7, 8, 9, 10, 11, 12, 13, 14, 15, 16, 17, 3, 4, 5};
    sfor<36>([&](auto I) {
      constexpr int i = I, r = rdc[i], x = r / 3;
      if constexpr (i == 35) __builtin_amdgcn_s_setprio(3);
      if constexpr (i == 9) { aa = opaque(aa + 64); ba = opaque(ba + 64); bb = opaque(bb + 64); }
      acc[pa[i] + pe[i]] = mfma16(A[pa[i]], G[pe[i]], acc[pa[i] + pe[i]]);
      if constexpr (r >= 0) {
        if constexpr (r % 3 == 0) A[x] = lds128(aa + x * 512);
        else if constexpr (r % 3 == 1) { const u32x2 t = lds64(ba + (7 - x) * 512); G[x].x = t.x; G[x].y = t.y; }
        else { const u32x2 t = lds64(bb + (7 - x) * 512); G[x].z = t.x; G[x].w = t.y; }
      }
      SCHED();
    });
  }
  SCHED();
}
DEVI void conv_out(f32x4 y, u32x2 u, float Dv, u32 off, char* ydst) {
  const float y0 = vfma(hi2f(u.y), Dv, y[0]), y1 = vfma(lo2f(u.y), Dv, y[1]), y2 = vfma(hi2f(u.x), Dv, y[2]), y3 = vfma(lo2f(u.x), Dv, y[3]);
  gst64(u32x2{pk2(y0, y1), pk2(y2, y3)}, off, ydst);
}

DEVI float xhalf_max(float m) { const u32 mu = f2u(m); auto r = __builtin_amdgcn_permlane32_swap(mu, mu, false, false); return fmaxf(u2f(r[0]), u2f(r[1])); }


extern "C" __global__ __launch_bounds__(768, 3) void gm_main(const u16* __restrict__ x, const u16* __restrict__ wqkv, char* __restrict__ ws,
                                                             const u16* __restrict__ Bk, const u16* __restrict__ Bv, const u16* __restrict__ shk,
                                                             const u16* __restrict__ ssmk, const u16* __restrict__ wo, const u16* __restrict__ ssmD,
                                                             const u16* __restrict__ gw, u16* __restrict__ out) {
  extern __shared__ __attribute__((aligned(16))) char smem[];
  const u32 sb = (u32)(size_t)(LAS char*)smem;
  const int b = blockIdx.x, xcd = b & 7, idx = b >> 3;
  const int lane = threadIdx.x & 63;
  const u32 wv = rfl(threadIdx.x >> 6);
  const bool spare = idx >= 28;
  char* const fb = ws + WS_F;
  const int qb = ((idx >> 2) + 1) & 7, h = 4 * xcd + (idx & 3);
  const int chb = wv >= 8 ? 64 * xcd + 2 * idx : 512 + 2 * h;
  u32x2 pre[2], fv; u32x4 cur[2];
  const u32 fo = F_U + ((chb >> 1) * 32 + 24 - 8 * (wv & 3) + (lane & 7)) * 8;
  u32 pof = 0; bool pon = false;
  {
    const int hg = xcd & 1, ri = idx & 3, rb = 4 * (xcd >> 1) + ri, jt = idx >> 2;
    const int r8 = lane >> 3, c8 = lane & 7;
    const int rot = ((0x124924u >> (2 * (jt + ri))) & 3) * 3;
    const u32 xo = opaque((32 * wv + r8) * 1152 + 16 * (c8 ^ r8));
    asm volatile("" ::"s"(x), "s"(wqkv), "s"(Bk), "s"(Bv));
    if (wv < 4 && !spare) {
      const u32 lw = wv;
      const char* xb = (const char*)(x + (size_t)rb * 128 * 576);
      sfor<4>([&](auto I) { dma16(xb + rot * 128 + I * 9216, xo, sb + (4 * lw + I) * 1024); });
      u32 wof[2];
#pragma unroll
      for (int i = 0; i < 2; i++) {
        const int ch = (lane & 15) ^ (((lane >> 4) | (i << 2)) << 1), ln = 14 * hg + 2 * jt + (ch >> 3);
        wof[i] = ((lane >> 4) + 16 * lw) * 3456 + 128 * (ln > 26 ? 26 : ln) + 16 * (ch & 7);
      }
      const char* wb = (const char*)wqkv;
      auto issue = [&](int c) {
        const u32 sl = sb + (c & 3) * L_SLOT;
        c += rot; c = c >= 9 ? c - 9 : c;
        sfor<4>([&](auto I) { dma16(xb + c * 128 + I * 9216, xo, sl + (4 * lw + I) * 1024); });
        sfor<4>([&](auto I) { dma16(wb + (64 * c + 4 * I) * 3456, wof[I >> 1], sl + L_WOFF + (4 * lw + I) * 1024); });
      };
      sfor<4>([&](auto I) { dma16(wb + (64 * rot + 4 * I) * 3456, wof[I >> 1], sb + L_WOFF + (4 * lw + I) * 1024); });
      prep_issue(Bk, Bv, 64 * idx, 64 * xcd, sb + L_PT, lw, lane);
#pragma unroll 1
      for (int n = 1; n < 8; n++) { issue(n); wait_vm<8>(); bar(); }
      issue(8);
      wait_vm<8>(); bar();
      __builtin_amdgcn_s_sleep(2);
      {
        const char* wp = (const char*)wo + (idx == 27 ? 663552 - 24576 : idx * 23712) + lw * 6144;
        sfor<6>([&](auto I) { dma16(wp + I * 1024, lane * 16, sb + L_DUMMY); });
      }
      wait_vm<6>(); bar();
      bar();
    } else if (wv >= 8 || (spare && wv >= 4)) {
      const bool ex = wv < 8;
      const int row0 = ex ? 256 * xcd + 64 * ri : 64 * idx, col = ex ? 512 : 64 * xcd;
      const u32 tb = sb + (ex ? 0 : L_PT);
      u32x2 e;
      asm volatile("global_load_dwordx2 %0, %1, %2 sc1" : "=v"(e) : "v"(F_EP), "s"(fb));
      u32x4 sh;
      gl128<0>(sh, 8 * col + 16 * (8 * (wv & 3) + r8), shk);
      if (spare) prep_issue(Bk, Bv, row0, col, tb, wv & 3, lane);
      {
        const u32 o = (threadIdx.x & 255) * 16, op = o ? o - 8 : 0;
        sfor<2>([&](auto J) { const u16* kp = ssmk + (size_t)(chb + J) * 2048; gl128<0>(cur[J], o, kp); gl64<0>(pre[J], op, kp); });
      }
      {
        const int t3 = (4 * xcd + (idx & 3)) * 8 + (idx >> 2), k = wv & 3;
        const u32 mo = k == 0 ? F_1 + (h * 8 + qb) * 512 : k == 1 ? F_O + t3 * 512 : k == 2 ? F_Y + t3 * 640 : F_U + (chb >> 1) * 256;
        asm volatile("s_mov_b32 m0, %2\n\ts_nop 0\n\tglobal_load_lds_dword %0, %1 sc1" ::"v"(mo + (lane < (k == 3 ? 4 : k == 2 ? 10 : 8) ? lane : 0) * 64), "s"(fb), "s"(sb + L_DUMMY) : "memory");
      }
      if (wv == 8) st32(sb + L_FL + 4 * lane, 0u);
      if (spare) wait_vm<5>();
      bar();
      asm volatile("s_waitcnt vmcnt(5)" : "+v"(e), "+v"(sh)::"memory");
      e.x += 1u; e.y += e.x == 0u;
      if (wv == 8) st64(sb + W_TAG, e);
      prep_compute(row0, col, tb, wv & 3, lane, sh, ws, [&]() { if (!spare) { SCHED(); bar(); SCHED(); } });
      wait_lgkm();
      bar();
      if (!spare) sfor<2>([&](auto) { bar(); });
      asm volatile("s_waitcnt vmcnt(0)" : "+v"(cur[0]), "+v"(cur[1]), "+v"(pre[0]), "+v"(pre[1])::"memory");
      publish(fb, F_U + (((col >> 1) + 8 * (wv & 3) + lane) * 32 + (row0 >> 6)) * 8, e, lane < 8);
      if (!spare) sfor<4>([&](auto) { bar(); });
      else if (wv >= 8) __builtin_amdgcn_s_sleep(24);
      else __builtin_amdgcn_s_sleep(16);
      asm volatile("global_load_dwordx2 %0, %1, %2 sc1" : "=v"(fv) : "v"(fo), "s"(fb));
      if (!spare) bar();
    } else if (spare) {
      CODE_PF("gm_main", "gm_end", 4 * ri + wv, 16, sb + L_DUMMY);
      bar(); bar();
    } else {
      const int cw = wv - 4, g = lane >> 4, l15 = lane & 15, wm = cw & 1, wn = cw >> 1;
      const u32 xra = sb + (64 * wm + l15) * 128 + 16 * (g ^ (l15 & 7));
      const u32 xb[2][2] = {{opaque(xra), opaque(xra + 2 * L_SLOT)}, {opaque(xra ^ 64), opaque((xra ^ 64) + 2 * L_SLOT)}};
      u32x4 wf[2][4], xf[2][4];
      f32x4 acc[4][4];
      const int ln = 14 * hg + 2 * jt + wn;
      {
        const int r64 = 2 * rb + wm, seg = ln / 9, lc = ln - 9 * seg, hc = (64 * lc) / 18 + (lane >> 2), li = lane & 1, nx = (lane >> 1) & 1;
        pon = ln == 27 ? lane < 16 : (lane < 20 && hc < 32 && ((18 * hc + 17 * li) >> 6) == lc && (!nx || (seg > 0 && r64 < 28)));
        pof = opaque(ln == 27 ? F_Y + (((4 * (r64 >> 2) + 2 * (r64 & 1)) * 8 + lane) * 80 + 72 + ((r64 >> 1) & 1)) * 8 : F_1 + ((hc * 8 + (r64 >> 2) + nx) * 64 + seg * 16 + li * 8 + (r64 & 3) + (nx ? 0 : 4)) * 8);
      }
      __builtin_amdgcn_s_setprio(2);
      if (ln == 27) {
        dma16(gw, wm * 128 + lane * 16, sb + L_GW + wm * 128);
        wait_vm<0>();
#pragma unroll
        for (int i = 0; i < 4; i++) acc[i][0] = f32x4{0.f, 0.f, 0.f, 0.f};
#pragma unroll 1
        for (int c = 0; c < 9; c++) {
          bar();
          const int ca = c + rot >= 9 ? c + rot - 9 : c + rot;
#pragma unroll
          for (int ks = 0; ks < 2; ks++) {
            const u32x4 gv = lds128(sb + L_GW + ca * 128 + ks * 64 + 16 * g);
#pragma unroll
            for (int i = 0; i < 4; i++) acc[i][0] = mfma16(gv, lds128(xb[ks][0] + (c & 3) * L_SLOT + 2048 * i), acc[i][0]);
          }
          wait_lgkm();
        }
        bar();
        const float p = rbf(g == 0 ? acc[0][0][0] : g == 1 ? acc[1][0][0] : g == 2 ? acc[2][0][0] : acc[3][0][0]);
        gst32(f2u(rbf(1.f / (1.f + __expf(-p)))), lane * 4, ws + WS_G + (rb * 128 + 64 * wm) * 4);
      } else {
        u32 wb[4][2];
        {
          const int q = l15 >> 2, p = lane & 3, f = q | ((g & 1) << 2);
#pragma unroll
          for (int nt = 0; nt < 4; nt++) {
            const u32 a = sb + L_WOFF + (8 * g + q) * 256 + (((8 * wn + 2 * nt + (p >> 1)) ^ (f << 1)) << 4) + 8 * (p & 1);
            wb[nt][0] = opaque(a); wb[nt][1] = opaque(a + 2 * L_SLOT);
          }
        }
        auto rd = [&](auto KS, auto C) {
          constexpr int ks = KS, c = C, so = (c & 1) * L_SLOT, hi = (c >> 1) & 1;
          sfor<4>([&](auto I) {
            xf[ks][I] = lds128(xb[ks][hi] + so + 2048 * I);
            wf[ks][I] = cat(tr_read(wb[I][hi] + so + ks * 8192), tr_read(wb[I][hi] + so + ks * 8192 + 1024));
          });
        };
        auto mm = [&](auto KS, auto FIRST) {
          constexpr int ks = KS;
          sfor<16>([&](auto I) {
            constexpr int mo[16] = {0, 1, 0, 1, 2, 2, 0, 1, 2, 3, 3, 3, 0, 1, 2, 3}, no[16] = {0, 0, 1, 1, 0, 1, 2, 2, 2, 0, 1, 2, 3, 3, 3, 3};
            constexpr int mt = mo[I], nt = no[I];
            acc[mt][nt] = mfma16(xf[ks][mt], wf[ks][nt], FIRST ? f32x4{0.f, 0.f, 0.f, 0.f} : acc[mt][nt]);
          });
        };
        auto ilv = [&]() {
          sfor<12>([&](auto) { __builtin_amdgcn_sched_group_barrier(0x8, 1, 0); __builtin_amdgcn_sched_group_barrier(0x100, 1, 0); });
          __builtin_amdgcn_sched_group_barrier(0x8, 4, 0);
        };
        char* qo = ws + WS_QKV + (size_t)((2 * rb + wm) * 1728 + 64 * ln) * 128;
        SCHED();
        bar();
        rd(IC(0), IC(0));
        sfor<9>([&](auto C) {
          constexpr int c = C;
          SCHED();
          rd(IC(1), C); mm(IC(0), IC(c == 0)); ilv();
          SCHED();
          if constexpr (c == 8) wait_lgkm();
          bar();
          SCHED();
          if constexpr (c < 8) { rd(IC(0), IC(c + 1)); mm(IC(1), IC(0)); ilv(); }
        });
        const u32 stg = sb + L_STG + cw * 9216;
        const u32 sa = stg + l15 * 144 + 8 * g, ra = stg + r8 * 144 + 16 * c8, go = lane * 16;
        u32x4 t[4][2];
        sfor<6>([&](auto NT) {
          constexpr int nt = NT;
          if constexpr (nt < 4) sfor<4>([&](auto M) { acc[M][nt] = mfma16(xf[1][M], wf[1][nt], acc[M][nt]); });
          SCHED();
          if constexpr (nt > 0 && nt < 5) {
            sfor<4>([&](auto M) {
              const f32x4 v = acc[M][nt - 1];
              st64(sa + (nt - 1) * 2304 + 32 * M, u32x2{pk2(v[0], v[1]), pk2(v[2], v[3])});
            });
            t[nt - 1][0] = lds128(ra + (nt - 1) * 2304); t[nt - 1][1] = lds128(ra + (nt - 1) * 2304 + 1152);
          }
          if constexpr (nt > 1) { gst128(t[nt - 2][0], go, qo + (nt - 2) * 2048); gst128(t[nt - 2][1], go, qo + (nt - 2) * 2048 + 1024); }
          SCHED();
        });
      }
    }
  }
  const int rt = 4 * xcd + (idx & 3), ct = idx >> 2;
  auto wo_load = [&](int r0, int r1) {
    const int r6 = lane / 10, c10 = lane - 10 * r6;
    const u32 vo = (6 * (wv & 3) + r6) * 1152 + 16 * (c10 > 8 ? 8 : c10);
    const char* wb = (const char*)(wo + 72 * ct);
#pragma unroll 4
    for (int i = r0; i < r1; i++) dma16n<60>(wb + i * 24 * 1152, vo, sb + L_W + (4 * i + (wv & 3)) * 960);
  };
  const u32x2 tag = lds64(sb + W_TAG);
  const int cw = wv & 3;
  u32 ypub = 0;
  if (wv >= (spare ? 4u : 8u)) {
    const int tid = threadIdx.x & 255;
    const u32 cb = sb + L_C + (wv >= 8 ? 0 : 2 * CS_SZ);
    asm volatile("s_waitcnt vmcnt(0)" : "+v"(fv)::"memory");
    if (__builtin_amdgcn_ballot_w64(fv.x != tag.x || fv.y != tag.y)) { __builtin_amdgcn_s_sleep(20); await<20>(fb, fo, tag); }
    u32x4 ur[2];
    sfor<2>([&](auto J) { gl128c<0>(ur[J], tid * 16, ws + WS_UR + (size_t)(chb + J) * 4096); });
    const u32 dv = *(const __attribute__((address_space(4))) u32*)(ssmD + chb);
    {
      const u32 z0 = opaque(cw * KC_STR + 16 * lane), z1 = opaque(CS_U + 4096 + 16 * (lane & 7)), za = cb + (lane < 24 ? z0 : z1);
      st128(za, u32x4{0u, 0u, 0u, 0u}); st128(za + CS_SZ, u32x4{0u, 0u, 0u, 0u});
    }
    f32x4 acc[8];
#pragma unroll
    for (int c = 0; c < 8; c++) { acc[c] = f32x4{0.f, 0.f, 0.f, 0.f}; asm volatile("" : "+v"(acc[c])); }
#pragma unroll
    for (int j = 0; j < 2; j++) {
      const u32 cs = cb + j * CS_SZ + 384 + 16 * tid;
      const u32 w0 = tid ? pre[j].x : 0u, w1 = tid ? pre[j].y : 0u, w2 = cur[j].x, w3 = cur[j].y, w4 = cur[j].z, w5 = cur[j].w;
      const u32 a01 = __builtin_amdgcn_alignbyte(w1, w0, 2), a12 = __builtin_amdgcn_alignbyte(w2, w1, 2), a23 = __builtin_amdgcn_alignbyte(w3, w2, 2),
                a34 = __builtin_amdgcn_alignbyte(w4, w3, 2), a45 = __builtin_amdgcn_alignbyte(w5, w4, 2);
      st128(cs, u32x4{a01, a12, a23, a34});
      st128(cs + KC_STR, u32x4{w1, w2, w3, w4});
      st128(cs + 2 * KC_STR, u32x4{a12, a23, a34, a45});
      st128(cs + 3 * KC_STR, cur[j]);
    }
    asm volatile("s_waitcnt vmcnt(0)" : "+v"(ur[0]), "+v"(ur[1])::"memory");
    st128(cb + CS_U + tid * 16, ur[0]); st128(cb + CS_SZ + CS_U + tid * 16, ur[1]);
    prog_set(sb + W_XCH + 4 * wv, 1);
    const int tm = cw >> 1, pi = cw & 1;
    const u32 cs = cb + tm * CS_SZ;
    prog_wait(sb + W_XCH + 4 * (wv & 12) + 4 * (lane & 3), 1);
    conv_mfma<4>(cs, 4 * pi, lane, acc);
    __builtin_amdgcn_s_setprio(3);
    if (spare && wv >= 8) prog_wait(sb + W_XCH + 16 + 8 * tm + 4 * (lane & 1), 2);
    const u32 rA = (spare && wv >= 8 ? sb + L_C + (2 + tm) * CS_SZ : sb + L_R + tm * 8192) + lane * 16;
    if (pi) { sfor<4>([&](auto J) { st128(rA + 4096 + J * 1024, __builtin_bit_cast(u32x4, acc[J])); acc[J] = acc[4 + J]; }); }
    else sfor<4>([&](auto J) { st128(rA + J * 1024, __builtin_bit_cast(u32x4, acc[4 + J])); });
    u32x2 uu[4];
#pragma unroll
    for (int j = 0; j < 4; j++) uu[j] = lds64(cs + CS_U + 4088 - 8 * lane - 512 * (4 * pi + j));
    prog_set(sb + W_XCH + 4 * wv, 2);
    prog_wait(sb + W_XCH + 4 * (wv ^ 1), 2);
    f32x4 t[4];
#pragma unroll
    for (int j = 0; j < 4; j++) t[j] = __builtin_bit_cast(f32x4, lds128(rA + (pi ^ 1) * 4096 + j * 1024));
    wait_lgkm();
    prog_set(sb + W_XCH + 4 * wv, 3);
    const u32 yo = ((lane >> 3) & 3) * 73728 + (lane >> 5) * 64 + (lane & 7) * 8;
    char* yd = ws + WS_Y2 + (chb + tm) * 128 + pi * 1179648;
#pragma unroll
    for (int j = 0; j < 4; j++) conv_out(vadd4(acc[j], t[j]), uu[j], tm ? hi2f(dv) : lo2f(dv), yo + j * 294912, yd);
    ypub = F_Y + (((16 * pi + lane) * 8 + (chb + tm) / 72) * 80 + (chb + tm) % 72) * 8;
    if (wv < 8) publish(fb, ypub, tag, lane < 16);
  }
  if (wv < 8) {
    __builtin_amdgcn_s_setprio(1);
    const int c = lane & 31, hh = lane >> 5, q4 = (lane & 15) >> 2;
    const int w = spare ? wv ^ 4 : wv;
    const u32 kcol = 64 * w + (lane & 16) * 2 + 16 * (lane & 1) + 4 * (lane & 2);
    const u32 ka = opaque(sb + L_K + (8 * hh + q4) * 1088 + kcol), ka2 = opaque(sb + L_K + (16 + q4) * 1088 + kcol);
    const u32 va = opaque(sb + L_V + c * 1040 + 64 * w + 16 * hh);
    const u32 qa = opaque(sb + L_Q + q4 * 512 + 64 * w + (lane & 16) * 2 + 8 * (lane & 3)), qa0 = opaque(qa + 4096 * hh);
    const int thr = c - 8 * hh;
    const u32 lom = opaque(hh ? 0u : ~0u);
    const float qsc = u2f(opaque(f2u(QSCALE)));
    u32 msk[8], nmk[8];
    unsigned long long cnd[16];
#pragma unroll
    for (int e = 0; e < 16; e++) cnd[e] = __builtin_amdgcn_ballot_w64(16 * (e >> 3) + (e & 7) <= thr);
#pragma unroll
    for (int j = 0; j < 8; j++) { const int k0 = 16 * (j >> 2) + 2 * (j & 3); msk[j] = opaque(k0 + 1 <= thr ? 0xffffffffu : (k0 <= thr ? 0xffffu : 0u)); nmk[j] = opaque(~msk[j]); }
    f32x16 S0;
#pragma unroll
    for (int e = 0; e < 16; e++) S0[e] = u2f(opaque(spare ? 0xff800000u : 0u));
    const u32 oo = opaque((c >> 4) * 18432 + (c & 15) * 16 + hh * 256);
    const u32 fl = sb + W_PROG + 4 * (spare ? (lane & 3) : (lane & 7));
    if (wv < 3) st128(sb + (wv < 2 ? L_K + (18 + wv) * 1088 : L_V + 18 * 1040) + lane * 16, u32x4{0x3f803f80u, 0x3f803f80u, 0x3f803f80u, 0x3f803f80u});
    const char* Ag = ws + WS_QKV + (size_t)(4 * qb * 1728 + 18 * h) * 128;
    const char* Kg = Ag + 576 * 128 - (qb ? 884736 : 0);
    const u32 lo = opaque(((lane >> 3) & (qb ? 7 : 3)) * 221184 + (lane & 7) * 16), lq = opaque(((lane >> 3) & 3) * 221184 + (lane >> 5) * 128 + (lane & 7) * 16);
    const char *pq = Ag + wv * 256, *pk = Kg + wv * 128, *pe = wv < 4 ? Kg + (wv < 2 ? 16 + wv : 590 + wv) * 128 : Ag + 2048;
    u32 le = sb + (wv < 2 ? L_K + (16 + wv) * 1088 : (wv < 4 ? L_V + (14 + wv) * 1040 : L_Q + 8192)), lk = sb + L_K + wv * 1088, lv = sb + L_V + wv * 1040;
    const u32 lx = opaque(wv < 4 ? lo : lq);
    asm volatile("" : "+s"(pq), "+s"(pk), "+s"(pe), "+s"(le), "+s"(lk), "+s"(lv));
    SCHED();
    if (wv >= 4 && !spare) publish(fb, pof, tag, pon);
    if (wv == (spare ? 0u : 4u)) {
      if (spare) { prog_wait(sb + W_XCH + 32, 1); __builtin_amdgcn_s_sleep(16); }
      __builtin_amdgcn_s_sleep(8);
      await<2>(fb, F_1 + ((h * 8 + qb) * 64 + ((lane < 48 ? lane : lane - 16) | (lane < 16 || spare ? 4 : 0))) * 8, tag);
      prog_set(sb + W_GO, 1);
    } else prog_wait(sb + W_GO, 1);
    __builtin_amdgcn_s_setprio(2);
    u32x4 rv[2];
    if (!spare || wv < 4) {
      const u32 l16 = lane * 16;
      if (spare) {
        u32x4 r[6];
        gl128c<512>(r[0], lo, pk); gl128c<1536>(r[1], lo, pk); gl128c<1024>(r[2], lq, pq); gl128c<2048>(r[3], lq, Ag); gl128c<0>(r[4], lo, pk + 580 * 128); gl128c<1024>(r[5], lo, pk + 580 * 128);
        asm volatile("s_waitcnt vmcnt(0)" : "+v"(r[0]), "+v"(r[1]), "+v"(r[2]), "+v"(r[3]), "+v"(r[4]), "+v"(r[5])::"memory");
        st128(lk + 4352 + l16, r[0]); st128(lk + 13056 + l16, r[1]); st128(sb + L_Q + 4096 + wv * 1024 + l16, r[2]); st128(sb + L_Q + 8192 + l16, r[3]);
        st128(lv + 4160 + l16, r[4]); st128(lv + 12480 + l16, r[5]);
      }
      u32x4 r[4];
      gl128c<0>(r[0], lo, pk); gl128c<1024>(r[1], lo, pk); gl128c<0>(r[2], lq, pq); gl128c<0>(r[3], lx, pe);
      __builtin_amdgcn_s_sleep(3);
      gl128c<0>(rv[0], lo, pk + 576 * 128); gl128c<1024>(rv[1], lo, pk + 576 * 128);
      asm volatile("s_waitcnt vmcnt(2)" : "+v"(r[0]), "+v"(r[1]), "+v"(r[2]), "+v"(r[3])::"memory");
      st128(lk + l16, r[0]); st128(lk + 8704 + l16, r[1]); st128(sb + L_Q + wv * 1024 + l16, r[2]); st128(le + l16, r[3]);
      prog_set(sb + W_PROG + 4 * wv, 2);
    }
    prog_wait(fl, 2);
    struct KF { u32x4 a; u32x2 b; };
    struct VF { u32x4 a, b; };
    auto ldk = [&](int i) { KF k; k.a = cat(tr_read(ka + i * 64), tr_read(ka + 4352 + i * 64)); k.b = tr_read(ka2 + i * 64); return k; };
    auto ldv = [&](int i) { VF v; v.a = lds128(va + i * 64); v.b = lds128(va + i * 64 + 32); return v; };
    auto pk8 = [&](const f32x16& p, int o) { return u32x4{pk2(p[o], p[o + 1]), pk2(p[o + 2], p[o + 3]), pk2(p[o + 4], p[o + 5]), pk2(p[o + 6], p[o + 7])}; };
    const int ib = spare ? 8 - w : 1;
    u32x4 q0 = cat(tr_read(qa0), tr_read(qa0 + 2048));
    const KF k8 = ldk(8);
    u32x2 q1 = tr_read(qa + 8192);
    const KF k0 = ldk(0);
    SCHED();
    auto qs = [&](u32 v) { return pk2(vmul(lo2f(v), qsc), vmul(hi2f(v), qsc)); };
    q0 = u32x4{qs(q0.x), qs(q0.y), qs(q0.z), qs(q0.w)};
    q1.x = qs(q1.x) & lom; q1.y = 0u;
    auto qk = [&](const KF& k) { f32x16 z = {}; z = mfma32(k.a, q0, z); return mfma32k8(k.b, q1, z); };
    f32x16 Sm = {};
    Sm = mfma32(k8.a, q0, Sm); S0 = mfma32(k0.a, q0, S0);
    Sm = mfma32k8(k8.b, q1, Sm); S0 = mfma32k8(k0.b, q1, S0);
    KF kf[2]; VF vf[2], v8, v0;
    kf[1] = ldk(7); kf[0] = ldk(6);
    SCHED();
#pragma unroll
    for (int e = 0; e < 16; e++) Sm[e] = __builtin_amdgcn_inverse_ballot_w64(cnd[e]) ? Sm[e] : S0[e];
    float m = __builtin_fmaxf(__builtin_fmaxf(Sm[0], Sm[1]), Sm[2]), m2 = __builtin_fmaxf(__builtin_fmaxf(Sm[3], Sm[4]), Sm[5]);
#pragma unroll
    for (int e = 6; e < 14; e += 4) { m = fmaxf(fmaxf(m, Sm[e]), Sm[e + 1]); m2 = fmaxf(fmaxf(m2, Sm[e + 2]), Sm[e + 3]); }
    m = fmaxf(fmaxf(m, Sm[14]), Sm[15]);
    m = xhalf_max(fmaxf(m, m2));
    asm volatile("s_waitcnt vmcnt(0)" : "+v"(rv[0]), "+v"(rv[1])::"memory");
    if (!spare || wv < 4) { st128(lv + lane * 16, rv[0]); st128(lv + 8320 + lane * 16, rv[1]); }
    prog_set(sb + W_PROG + 4 * wv, 3);
    f32x16 O;
    float l;
#pragma unroll 1
    for (int pass = 0; pass < 2; pass++) {
      { const float r1 = lo2f(pk2(m, m)) - m; q1.y = hh ? pk2(r1 - lo2f(pk2(r1, r1)), 0.f) : pk2(-m, r1); }
      f32x16 S[2];
      {
        f32x16 p = mfma32k8(kf[1].b, u32x2{0u, q1.y}, Sm);
        S[1] = qk(kf[1]);
        SCHED();
#pragma unroll
        for (int e = 0; e < 8; e++) p[e] = __builtin_amdgcn_exp2f(p[e]);
        SCHED();
        const u32 vfl = lds_peek(fl);
        SCHED();
#pragma unroll
        for (int e = 8; e < 16; e++) p[e] = __builtin_amdgcn_exp2f(p[e]);
        SCHED();
        if (__builtin_amdgcn_ballot_w64(vfl < 3u)) prog_wait(fl, 3);
        asm volatile("" ::: "memory");
        v8 = ldv(8); v0 = ldv(0); vf[1] = ldv(7);
        SCHED();
        const u32x4 pa = pk8(p, 0), pb = pk8(p, 8);
        O = f32x16{};
        O = mfma32(v8.a, pa & u32x4{msk[0], msk[1], msk[2], msk[3]}, O); O = mfma32(v8.b, pb & u32x4{msk[4], msk[5], msk[6], msk[7]}, O);
        O = mfma32(v0.a, pa & u32x4{nmk[0], nmk[1], nmk[2], nmk[3]}, O); O = mfma32(v0.b, pb & u32x4{nmk[4], nmk[5], nmk[6], nmk[7]}, O);
      }
      auto tile = [&](auto self, auto I) -> void {
        constexpr int i = I;
        if (i < ib) return;
        if constexpr (i == 4) __builtin_amdgcn_s_setprio(1);
        if constexpr (i == 1) __builtin_amdgcn_s_setprio(0);
        SCHED();
        f32x16& Sc = S[i & 1];
        f32x16 z = {};
        if constexpr (i > 1) z = mfma32(kf[(i - 1) & 1].a, q0, z);
        if constexpr (i > 2) kf[i & 1] = ldk(i - 2);
        if constexpr (i > 1) vf[(i - 1) & 1] = ldv(i - 1);
        SCHED();
#pragma unroll
        for (int e = 0; e < 8; e++) Sc[e] = __builtin_amdgcn_exp2f(Sc[e]);
        SCHED();
        if constexpr (i > 1) S[(i - 1) & 1] = mfma32k8(kf[(i - 1) & 1].b, q1, z);
        const u32x4 pa = pk8(Sc, 0);
        SCHED();
#pragma unroll
        for (int e = 8; e < 12; e++) Sc[e] = __builtin_amdgcn_exp2f(Sc[e]);
        SCHED();
        O = mfma32(vf[i & 1].a, pa, O);
#pragma unroll
        for (int e = 12; e < 16; e++) Sc[e] = __builtin_amdgcn_exp2f(Sc[e]);
        const u32x4 pb = pk8(Sc, 8);
        SCHED();
        O = mfma32(vf[i & 1].b, pb, O);
        if constexpr (i > 1) self(self, IC(i - 1));
      };
      tile(tile, IC(7));
      SCHED();
      { const u32 lu = f2u(O[10]); auto r = __builtin_amdgcn_permlane32_swap(lu, lu, false, false); l = u2f(r[0]); }
      if (pass == 1 || __all(l > 8.67e-19f && l < 1.15e18f)) break;
      q1.y = 0u;
#pragma unroll 1
      for (int i = ib; i < 8; i++) {
        const f32x16 s = qk(ldk(i));
#pragma unroll
        for (int e = 0; e < 16; e += 2) m = fmaxf(fmaxf(m, s[e]), s[e + 1]);
      }
      m = xhalf_max(m);
      kf[1] = ldk(7); kf[0] = ldk(6);
    }
    float inv = __builtin_amdgcn_rcpf(l);
    asm volatile("s_nop 1" : "+v"(inv));
    char* Og = ws + WS_O + (size_t)(16 * qb + 2 * w) * 18432;
    gst128(u32x4{pk2(vmul(O[0], inv), vmul(O[1], inv)), pk2(vmul(O[2], inv), vmul(O[3], inv)), pk2(vmul(O[4], inv), vmul(O[5], inv)), pk2(vmul(O[6], inv), vmul(O[7], inv))},
           oo, Og + (h >> 1) * 1024 + (h & 1) * 512);
    if (hh == 0) gst32(pk2(vmul(O[8], inv), vmul(O[9], inv)), (c >> 4) * 18432 + (c & 15) * 4, Og + 16384 + h * 64);
    publish(fb, F_O + (((4 * qb + (w & 3)) * 8 + lane) * 64 + (w >> 2) * 32 + h) * 8, tag, lane < 8);
    {
      __builtin_amdgcn_s_setprio(0);
      const int g = lane >> 4;
      prog_wait(sb + W_XCH + 32 + 4 * (lane & 3), 4);
      const u32 wa2 = sb + L_W + ((4 * g + (q4 >> 1)) * 18 + 16 + (q4 & 1)) * 160 + 8 * (lane & 3);
      u32x4 wx[2], wr[10];
      sfor<2>([&](auto J) { const int i = J ? 8 + (wv & 1) : wv; const u32 a = wa2 + (i / 5) * 46080 + (i % 5) * 32; wx[J] = cat(tr_read(a), tr_read(a + 5760)); });
      prog_set(sb + W_PROG + 4 * wv, 4);
      const u32 wa = sb + L_W + wv * 11520 + (18 * (g >> 1) + 4 * (g & 1) + q4) * 160 + 8 * (lane & 3), wd = sb + L_W + wv * 11520 + lane * 16;
      sfor<10>([&](auto I) { const u32 a = wa + (I / 5) * 5760 + (I % 5) * 32; wr[I] = cat(tr_read(a), tr_read(a + 1280)); });
      prog_wait(sb + W_PROG + 4 * (lane & 7), 4);
      sfor<2>([&](auto J) { const int i = J ? 8 + (wv & 1) : wv; st128(sb + L_Q + i * 1024 + lane * 16, wx[J]); });
      sfor<10>([&](auto I) { st128(wd + (I / 5) * 5760 + (I % 5) * 1024, wr[I]); });
      prog_set(sb + W_PROG + 4 * wv, 5);
    }
    if (b == 255 && wv == 4) {
#pragma unroll 1
      for (int spin = 0; spin < (1 << 18); spin++) {
        u32x4 v[9];
        sfor<9>([&](auto I) { gl128c<0>(v[I], (lane >> 4) * 2048 + (lane & 15) * 16, fb + F_U + I * 8192); });
        asm volatile("s_waitcnt vmcnt(0)" : "+v"(v[0]), "+v"(v[1]), "+v"(v[2]), "+v"(v[3]), "+v"(v[4]), "+v"(v[5]), "+v"(v[6]), "+v"(v[7]), "+v"(v[8])::"memory");
        u32 bad = 0;
#pragma unroll
        for (int i = 0; i < 9; i++) bad |= (v[i].x ^ tag.x) | (v[i].y ^ tag.y) | (v[i].z ^ tag.x) | (v[i].w ^ tag.y);
        if (!__builtin_amdgcn_ballot_w64(bad != 0)) break;
        __builtin_amdgcn_s_sleep(16);
      }
      if (lane == 0) gst64(tag, F_EP, fb);
    }
    return;
  }
  {
    const int g = lane >> 4, l15 = lane & 15, q = l15 >> 2;
    const int r7 = lane / 9, c9 = lane - 9 * r7;
    const u32 xw = sb + L_X + cw * 2304;
    const int m16 = 16 * (rt >> 2) + 2 * (rt & 3) + 8 * (cw >> 1) + (cw & 1);
    if (__builtin_amdgcn_ballot_w64(lds_peek(sb + W_PROG + 4 * (spare ? (lane & 3) : (lane & 7))) < 3u)) { publish(fb, ypub, tag, lane < 16); ypub = 0; }
    prog_wait(sb + W_XCH + (spare ? 16 + 4 * (lane & 7) : 32 + 4 * (lane & 3)), 3);
    prog_wait(sb + W_PROG + 4 * (spare ? (lane & 3) : (lane & 7)), 3);
    {
      const u16* xp = x + (size_t)(16 * m16) * 576 + 72 * ct;
      dma16n<63>(xp, r7 * 1152 + 16 * c9, xw); dma16n<63>(xp + 7 * 576, r7 * 1152 + 16 * c9, xw + 1008); dma16n<63>(xp + 9 * 576, r7 * 1152 + 16 * c9, xw + 1296);
      wo_load(0, 4);
      wait_vm<7>();
      if (ypub && lane < 16) gst64(tag, ypub, fb);
      wo_load(4, 16);
    }
    const u32 yf = F_Y + ((rt * 8 + ct) * 80 + (lane < 18 ? 18 * cw + lane : 72 + (cw >> 1))) * 8;
    u32x2 yv;
    asm volatile("global_load_dwordx2 %0, %1, %2 sc1" : "=v"(yv) : "v"(yf), "s"(fb));
    wo_load(16, 24);
    asm volatile("s_waitcnt vmcnt(%1)" : "+v"(yv) : "i"(8) : "memory");
    if (__builtin_amdgcn_ballot_w64(yv.x != tag.x || yv.y != tag.y)) await<8>(fb, yf, tag);
    u32x4 yr[3];
    {
      const char* yp = ws + WS_Y2 + (size_t)(rt * 576 + 72 * ct + 18 * cw) * 128;
      gl128c<0>(yr[0], lane * 16, yp); gl128c<1024>(yr[1], lane * 16, yp); gl128c<1280>(yr[2], lane * 16, yp);
    }
    u32 gtu;
    gl32c<0>(gtu, (16 * m16 + l15) * 4, ws + WS_G);
    wait_vm<4>();
    prog_set(sb + W_XCH + 4 * wv, 4);
    asm volatile("s_waitcnt vmcnt(0)" : "+v"(gtu), "+v"(yr[0]), "+v"(yr[1]), "+v"(yr[2])::"memory");
    st128(sb + L_Y + cw * 2304 + lane * 16, yr[0]); st128(sb + L_Y + cw * 2304 + 1024 + lane * 16, yr[1]); st128(sb + L_Y + cw * 2304 + 1280 + lane * 16, yr[2]);
    prog_set(sb + W_XCH + 4 * wv, 5);
    prog_wait(sb + W_XCH + 32 + 4 * (lane & 3), 5);
    const float gt = u2f(gtu);
    f32x4 acc[5]; u32x2 tb[5];
    const u32 xa = xw + l15 * 144 + 8 * g;
    {
      const float g1 = rbf(1.f - gt);
      const u32 ya = sb + L_Y + (4 * g + q) * 128 + 32 * cw + 8 * (lane & 3);
      sfor<5>([&](auto NT) {
        const u32x2 y = tr_read(ya + NT * 2048), xv = lds64(xa + NT * 32);
        tb[NT] = u32x2{opaque(pk2(vmul(vmul(g1, lo2f(xv.x)), lo2f(y.x)), vmul(vmul(g1, hi2f(xv.x)), hi2f(y.x)))), opaque(pk2(vmul(vmul(g1, lo2f(xv.y)), lo2f(y.y)), vmul(vmul(g1, hi2f(xv.y)), hi2f(y.y))))};
      });
    }
    const u32 wl = opaque(sb + L_W + lane * 16), wh = opaque(wl + 46080), wq = opaque(sb + L_Q + lane * 16);
    u32x4 wf[3][5];
    auto rd = [&](auto KS) {
      sfor<5>([&](auto NT) {
        constexpr int ks = KS;
        if constexpr (ks < 16) wf[ks % 3][NT] = lds128((ks < 8 ? wl : wh) + (ks & 7) * 5760 + NT * 1024);
        else wf[ks % 3][NT] = lds128(wq + (5 * (ks - 16) + NT) * 1024);
      });
    };
    u32x4 of[16]; u32 oe[8];
    const char* ob = ws + WS_O + (size_t)m16 * 18432;
    {
      const u32 oo = F_O + ((rt * 8 + ct) * 64 + (cw >> 1) * 32 + (lane & 31)) * 8;
      prog_wait(sb + W_PROG + (spare ? 4 * (lane & 7) : 16 * (cw >> 1) + 4 * (lane & 3)), 4);
      __builtin_amdgcn_s_sleep(6);
      u32 miss = ~0u;
      sfor<4>([&](auto G) {
#pragma unroll 1
        for (int spin = 0; (miss & (0xffu << (8 * G))) && spin < (1 << 20); spin++) {
          u32x2 v;
          asm volatile("global_load_dwordx2 %0, %1, %2 sc1\n\ts_waitcnt vmcnt(0)" : "=&v"(v) : "v"(oo), "s"(fb) : "memory");
          miss = (u32)__builtin_amdgcn_ballot_w64(v.x != tag.x || v.y != tag.y);
          if (miss & (0xffu << (8 * G))) __builtin_amdgcn_s_sleep(2);
        }
        sfor<4>([&](auto F) { gl128c<F * 1024>(of[4 * G + F], lane * 16, ob + G * 4096); });
      });
    }
    __builtin_amdgcn_s_setprio(3);
    sfor<8>([&](auto F) { gl32c<(F >> 2) * 1024 + (F & 3) * 64>(oe[F], 256 * g + 4 * l15, ob + 16384); });
    prog_wait(sb + W_PROG + 4 * (lane & 7), 5);
    rd(IC(0)); rd(IC(1));
    sfor<16>([&](auto KS) {
      constexpr int ks = KS;
      SCHED();
      if constexpr (ks % 2 == 0) asm volatile("s_waitcnt vmcnt(%2)" : "+v"(of[ks]), "+v"(of[ks + 1]) : "i"(22 - ks) : "memory");
      rd(IC(ks + 2));
      sfor<5>([&](auto NT) { acc[NT] = mfma16(wf[ks % 3][NT], of[ks], ks == 0 ? f32x4{0.f, 0.f, 0.f, 0.f} : acc[NT]); });
      sfor<5>([&](auto) { __builtin_amdgcn_sched_group_barrier(0x8, 1, 0); __builtin_amdgcn_sched_group_barrier(0x100, 1, 0); });
    });
    SCHED();
    asm volatile("s_waitcnt vmcnt(0)" : "+v"(oe[0]), "+v"(oe[1]), "+v"(oe[2]), "+v"(oe[3]), "+v"(oe[4]), "+v"(oe[5]), "+v"(oe[6]), "+v"(oe[7])::"memory");
    sfor<5>([&](auto NT) { acc[NT] = mfma16(wf[1][NT], u32x4{oe[0], oe[1], oe[2], oe[3]}, acc[NT]); });
    sfor<5>([&](auto NT) { acc[NT] = mfma16(wf[2][NT], u32x4{oe[4], oe[5], oe[6], oe[7]}, acc[NT]); });
    SCHED();
    sfor<5>([&](auto NT) {
      constexpr int nt = NT;
      if (nt < 4 || g < 2)
        st64(xa + nt * 32, u32x2{pk2(fmaf(gt, acc[nt][0], lo2f(tb[nt].x)), fmaf(gt, acc[nt][1], hi2f(tb[nt].x))), pk2(fmaf(gt, acc[nt][2], lo2f(tb[nt].y)), fmaf(gt, acc[nt][3], hi2f(tb[nt].y)))});
    });
    char* orow = (char*)(out + (size_t)(16 * m16) * 576 + 72 * ct);
    u32x4 t[3];
#pragma unroll
    for (int it = 0; it < 3; it++) t[it] = lds128(xw + it * 1008 + lane * 16);
#pragma unroll
    for (int it = 0; it < 3; it++)
      if (lane < (it < 2 ? 63 : 18)) asm volatile("global_store_dwordx4 %0, %1, %2 sc1\n\ts_nop 1" ::"v"((7 * it + r7) * 1152 + 16 * c9), "v"(t[it]), "s"(orow) : "memory");
  }
}
extern "C" __global__ void gm_end() {}

extern "C" void kernel_launch(void* const* d_in, const int* in_sizes, int n_in, void* d_out, int out_size, void* d_ws, size_t ws_size,
                              hipStream_t stream) {
  (void)hipFuncSetAttribute((const void*)gm_main, hipFuncAttributeMaxDynamicSharedMemorySize, L_END);
  gm_main<<<256, 768, L_END, stream>>>((const u16*)d_in[0], (const u16*)d_in[1], (char*)d_ws, (const u16*)d_in[4], (const u16*)d_in[5], (const u16*)d_in[6],
                                       (const u16*)d_in[7], (const u16*)d_in[2], (const u16*)d_in[8], (const u16*)d_in[9], (u16*)d_out);
}
```
